```python
import torch

def _softmax(x, axis=-1):
    return torch.softmax(x, dim=axis)
def _gelu(x):
    return torch.nn.functional.gelu(x, approximate="tanh")
def _silu(x):
    return torch.nn.functional.silu(x)
def _sigmoid(x):
    return torch.sigmoid(x)
def _layernorm(x, axis=-1, eps=1e-5):
    m = x.mean(dim=axis, keepdim=True)
    v = x.var(dim=axis, keepdim=True, unbiased=False)
    return (x - m) / torch.sqrt(v + eps)
def _scatter_add(tgt, idx, src, axis=0):
    return tgt.clone().index_add_(axis, idx, src)
def _segment_sum(src, seg, n):
    out = torch.zeros((n,) + src.shape[1:], dtype=src.dtype)
    return out.index_add_(0, seg, src)
def _pad(x, pw):
    flat = tuple(v for pair in reversed(pw) for v in pair)
    return torch.nn.functional.pad(x, flat)

kernel_name = 'standalone__attention_interact_autoint__recsys'

def setup_inputs():

    _w = (lambda a: (lambda t: t.to(torch.bfloat16) if t.is_floating_point() else t)(torch.as_tensor(a)))
    B, F = 4096, 100
    D = max(2, (128 // 2) * 2)
    H = 2
    Dh = D // H
    return {
        "e": _w((lambda *s: torch.randn(*s, dtype=torch.bfloat16) * 0.05)(B, F, D).to(float)),
        "wq": _w((lambda *s: torch.randn(*s, dtype=torch.bfloat16) * 1.6)(H, D, Dh).to(float)),
        "wk": _w((lambda *s: torch.randn(*s, dtype=torch.bfloat16) * 1.6)(H, D, Dh).to(float)),
        "wv": _w((lambda *s: torch.randn(*s, dtype=torch.bfloat16) * 0.05)(H, D, Dh).to(float)),
        "wres": _w((lambda *s: torch.randn(*s, dtype=torch.bfloat16) * 0.05)(D, D).to(float)),
    }

def reference(e, wq, wk, wv, wres):
    H, D, Dh = wq.shape[0], wq.shape[1], wq.shape[2]
    B, F = e.shape[0], e.shape[1]
    q = torch.einsum('bfd,hdk->bhfk', e, wq)
    k = torch.einsum('bfd,hdk->bhfk', e, wk)
    v = torch.einsum('bfd,hdk->bhfk', e, wv)
    scale = float(Dh) ** -0.5
    scores = torch.einsum('bhfk,bhgk->bhfg', q, k) * scale
    attn = _softmax(scores, axis=-1)
    attn = (lambda x, dt: x.to(dt))(attn, v.dtype)
    ctx = torch.einsum('bhfg,bhgk->bhfk', attn, v)
    ctx = (lambda x, *d: x.permute(*d))(ctx, 0, 2, 1, 3).reshape(B, F, H * Dh)
    res = torch.einsum('bfd,de->bfe', e, wres)
    out = ctx + res
    mask = (lambda x, dt: x.to(dt))(out > 0, out.dtype)
    return out * mask
```

```cpp
#include <hip/hip_runtime.h>
#include <stdint.h>

#define STAMP_BEGIN()
#define STAMP_END()

__global__ __launch_bounds__(256, 1)
void autoint_r_kernel(const char* __restrict__ e, const char* __restrict__ wq, const char* __restrict__ wk,
                      const char* __restrict__ wv, const char* __restrict__ wres, char* __restrict__ out,
                      char* __restrict__ ws) {
  __shared__ __attribute__((aligned(1024))) uint8_t lds_raw[163840];
  STAMP_BEGIN();
  const uint32_t wave = __builtin_amdgcn_readfirstlane(threadIdx.x >> 6);
  const uint32_t lds = (uint32_t)(uintptr_t)(__attribute__((address_space(3))) uint8_t*)lds_raw;
  const uint64_t ka = (uint64_t)(uintptr_t)__builtin_amdgcn_kernarg_segment_ptr();
  const uint32_t bid = blockIdx.x;
  asm volatile(
"s_load_dwordx8 s[8:15], %[ka], 0x0\n"
"s_load_dwordx4 s[16:19], %[ka], 0x20\n"
"s_lshl_b32 s88, %[wave], 3\n"
"s_load_dwordx2 s[22:23], %[ka], s88 offset:0x8\n"
"v_mbcnt_lo_u32_b32 v239, -1, 0\n"
"v_mbcnt_hi_u32_b32 v239, -1, v239\n"
"s_getpc_b64 s[76:77]\n"
"s_bfe_u32 s89, %[bid], 0x40003\n"
"s_lshl_b32 s89, s89, 2\n"
"s_add_u32 s89, s89, %[wave]\n"
"s_lshl_b32 s89, s89, 9\n"
"v_lshl_add_u32 v254, v239, 7, s89\n"
"s_mov_b64 exec, 0xf\n"
"global_load_dword v253, v254, s[76:77] offset:128\n"
"s_mov_b64 exec, -1\n"
"v_lshrrev_b32 v249, 4, v239\n"
"s_lshl_b32 s74, %[wave], 12\n"
"s_bfe_u32 s92, %[bid], 0x10003\n"
"s_lshl_b32 s93, s92, 14\n"
"s_add_u32 s93, s93, s74\n"
"v_lshlrev_b32 v240, 1, v249\n"
"v_xor_b32 v240, v240, v239\n"
"v_and_b32 v240, 7, v240\n"
"v_lshlrev_b32 v240, 4, v240\n"
"v_bfe_u32 v241, v239, 3, 2\n"
"v_lshrrev_b32 v242, 5, v239\n"
"v_lshl_add_u32 v241, v242, 3, v241\n"
"v_lshl_add_u32 v44, v241, 7, v240\n"
"v_add_u32 v44, s93, v44\n"
"s_bfe_u32 s88, %[bid], 0x50003\n"
"s_lshl_b32 s88, s88, 10\n"
"v_lshl_add_u32 v255, v239, 7, s88\n"
"s_and_b32 s90, %[bid], 7\n"
"s_lshl_b32 s90, s90, 4\n"
"s_lshr_b32 s91, %[bid], 4\n"
"s_add_u32 s90, s90, s91\n"
"s_lshl_b32 s90, s90, 2\n"
"s_add_u32 s90, s90, %[wave]\n"
"s_mul_i32 s91, s90, 25600\n"
"v_xor_b32 v152, v249, v239\n"
"v_lshlrev_b32 v152, 4, v152\n"
"s_mul_i32 s28, %[wave], 13056\n"
"s_add_u32 s28, s28, 66304\n"
"s_mul_i32 s29, %[wave], 10752\n"
"s_add_u32 s29, s29, 118528\n"
"s_mul_i32 s75, %[wave], 4192\n"
"s_mov_b32 s40, 192\n"
"s_mov_b32 s41, 128\n"
"s_mov_b32 s42, 64\n"
"s_mov_b32 s43, 0\n"
"s_mov_b32 s26, 104857600\n"
"s_mov_b32 s27, 0x20000\n"
"s_waitcnt lgkmcnt(0)\n"
"s_add_u32 m0, s74, 33152\n"
"s_nop 0\n"
"global_load_lds_dwordx4 v44, s[12:13] offset:0\n"
"global_load_lds_dwordx4 v44, s[12:13] offset:2048\n"
"s_add_u32 s8, s8, s91\n"
"s_addc_u32 s9, s9, 0\n"
"s_sub_u32 s24, s8, 192\n"
"s_subb_u32 s25, s9, 0\n"
"s_and_b32 s25, s25, 0xffff\n"
"s_mov_b32 s44, 4288\n"
"s_mov_b32 s45, 4224\n"
"s_mov_b32 s46, 4160\n"
"s_mov_b32 s47, 4096\n"
"s_mov_b32 s48, 8384\n"
"s_mov_b32 s49, 8320\n"
"s_mov_b32 s50, 8256\n"
"s_add_u32 m0, s74, 33664\n"
"s_nop 0\n"
"global_load_lds_dwordx4 v44, s[12:13] offset:512\n"
"s_mov_b32 s51, 8192\n"
"s_mov_b32 s52, 12480\n"
"s_mov_b32 s53, 12416\n"
"s_mov_b32 s54, 12352\n"
"s_mov_b32 s55, 12288\n"
"s_mov_b32 s56, 16576\n"
"s_mov_b32 s57, 16512\n"
"s_mov_b32 s58, 16448\n"
"s_mov_b32 s59, 16384\n"
"s_mov_b32 s60, 20672\n"
"s_mov_b32 s61, 20608\n"
"s_mov_b32 s62, 20544\n"
"global_load_lds_dwordx4 v44, s[12:13] offset:2560\n"
"s_mov_b32 s63, 20480\n"
"v_and_b32 v248, 15, v239\n"
"v_and_b32 v232, 3, v248\n"
"v_lshrrev_b32 v251, 2, v248\n"
"s_lshl_b32 s88, %[wave], 13\n"
"s_lshl_b32 s92, s92, 7\n"
"s_add_u32 s88, s88, s92\n"
"v_lshl_add_u32 v135, v241, 8, v240\n"
"v_add_u32 v135, s88, v135\n"
"v_add_u32 v252, 0x1000, v135\n"
"v_lshlrev_b32 v153, 4, v239\n"
"s_mov_b32 s65, 4096\n"
"s_mov_b64 exec, 0xff\n"
"global_load_dword v255, v255, s[22:23]\n"
"s_mov_b64 exec, -1\n"
"s_mov_b32 s66, 8192\n"
"s_mov_b32 s67, 12288\n"
"s_mov_b32 s68, 16384\n"
"s_mov_b32 s69, 20480\n"
"s_mov_b32 s70, 24576\n"
"v_bfe_u32 v240, v239, 2, 3\n"
"v_lshl_add_u32 v240, v242, 4, v240\n"
"v_lshlrev_b32 v240, 7, v240\n"
"v_lshl_add_u32 v240, v232, 3, v240\n"
"v_bfe_u32 v241, v239, 3, 2\n"
"v_xor_b32 v243, 0, v241\n"
"v_lshl_add_u32 v40, v243, 5, v240\n"
"s_add_u32 m0, s29, 0\n"
"s_mov_b32 s94, 24768\n"
"buffer_load_dwordx4 v152, s[24:27], s94 offen offset:0 lds\n"
"v_add_u32 v32, s74, v40\n"
"v_add_u32 v36, s75, v40\n"
"v_xor_b32 v243, 1, v241\n"
"v_lshl_add_u32 v41, v243, 5, v240\n"
"v_add_u32 v33, s74, v41\n"
"v_add_u32 v37, s75, v41\n"
"v_xor_b32 v243, 2, v241\n"
"v_lshl_add_u32 v42, v243, 5, v240\n"
"v_add_u32 v34, s74, v42\n"
"v_add_u32 v38, s75, v42\n"
"v_xor_b32 v243, 3, v241\n"
"v_lshl_add_u32 v43, v243, 5, v240\n"
"s_add_u32 m0, s74, 0\n"
"s_nop 0\n"
"global_load_lds_dwordx4 v44, s[10:11] offset:0\n"
"v_add_u32 v35, s74, v43\n"
"v_add_u32 v39, s75, v43\n"
"v_add_u32 v45, s74, v153\n"
"v_mul_u32_u24 v240, 1056, v232\n"
"v_lshl_add_u32 v240, v251, 4, v240\n"
"v_lshl_add_u32 v240, v249, 8, v240\n"
"v_add_u32 v46, s75, v240\n"
"v_mul_u32_u24 v240, 1088, v251\n"
"v_lshl_add_u32 v240, v232, 8, v240\n"
"v_xor_b32 v241, v249, v232\n"
"v_lshl_add_u32 v240, v241, 4, v240\n"
"v_add_u32 v154, s28, v240\n"
"s_add_u32 m0, s29, 0\n"
"s_mov_b32 s94, 13130880\n"
"buffer_load_dwordx4 v152, s[24:27], s94 offen offset:1088 lds\n"
"v_add_u32 v47, s29, v240\n"
"v_lshlrev_b32 v243, 6, v249\n"
"v_lshl_add_u32 v132, v249, 9, s29\n"
"v_lshl_add_u32 v132, v248, 4, v132\n"
"v_min_u32 v240, 1, v251\n"
"v_lshl_add_u32 v161, v240, 20, v132\n"
"v_lshl_add_u32 v158, v249, 10, s29\n"
"v_bfe_u32 v240, v239, 1, 2\n"
"v_lshl_add_u32 v158, v240, 4, v158\n"
"v_and_b32 v240, 1, v239\n"
"v_lshl_add_u32 v158, v240, 2, v158\n"
"v_bfe_u32 v240, v239, 3, 1\n"
"s_add_u32 m0, s74, 0\n"
"s_nop 0\n"
"global_load_lds_dwordx4 v44, s[10:11] offset:2048\n"
"v_lshl_add_u32 v158, v240, 20, v158\n"
"s_add_u32 s89, s29, 4096\n"
"v_lshl_add_u32 v133, v232, 4, v243\n"
"v_lshl_add_u32 v133, v251, 9, v133\n"
"v_add_u32 v133, s89, v133\n"
"v_lshl_add_u32 v156, v249, 7, s89\n"
"v_lshl_add_u32 v156, v251, 4, v156\n"
"v_and_b32 v240, 1, v239\n"
"v_lshl_add_u32 v156, v240, 3, v156\n"
"v_bfe_u32 v241, v239, 1, 1\n"
"v_lshl_add_u32 v156, v241, 20, v156\n"
"s_add_u32 s89, s74, 33152\n"
"s_add_u32 m0, s29, 0\n"
"s_mov_b32 s94, 26236992\n"
"buffer_load_dwordx4 v152, s[24:27], s94 offen offset:2176 lds\n"
"v_lshl_add_u32 v134, v249, 9, s89\n"
"v_lshl_add_u32 v134, v248, 3, v134\n"
"v_lshl_add_u32 v163, v239, 3, s89\n"
"s_add_u32 s89, s29, 8192\n"
"v_lshl_add_u32 v244, v240, 1, s89\n"
"v_lshl_add_u32 v244, v241, 4, v244\n"
"v_add_u32 v244, v243, v244\n"
"v_bfe_u32 v245, v239, 2, 1\n"
"v_lshl_add_u32 v244, v245, 3, v244\n"
"v_bfe_u32 v245, v239, 3, 1\n"
"v_lshl_add_u32 v244, v245, 8, v244\n"
"v_accvgpr_write_b32 a240, v244\n"
"s_add_u32 m0, s74, 512\n"
"s_nop 0\n"
"global_load_lds_dwordx4 v44, s[10:11] offset:512\n"
"v_add_u32 v244, s89, v153\n"
"v_accvgpr_write_b32 a241, v244\n"
"v_lshl_add_u32 v244, v248, 4, s89\n"
"v_add_u32 v244, 2048, v244\n"
"v_min_u32 v245, 1, v232\n"
"v_lshl_add_u32 v244, v245, 20, v244\n"
"v_accvgpr_write_b32 a242, v244\n"
"v_lshl_add_u32 v244, v239, 3, s89\n"
"v_mov_b32 v246, 0\n"
"v_mov_b32 v247, 0\n"
"ds_write_b64 v244, v[246:247] offset:1024\n"
"v_lshlrev_b32 v162, 3, v248\n"
"s_add_u32 m0, s29, 0\n"
"s_mov_b32 s94, 39343104\n"
"buffer_load_dwordx4 v152, s[24:27], s94 offen offset:3264 lds\n"
"v_lshl_add_u32 v155, v249, 8, v162\n"
"v_lshl_add_u32 v162, v249, 10, v162\n"
"v_lshl_add_u32 v160, v249, 2, v232\n"
"v_lshlrev_b32 v160, 2, v160\n"
"v_mul_u32_u24 v157, 0xaaaa, v240\n"
"v_add_u32 v157, 0x4444, v157\n"
"v_mov_b32 v159, 0\n"
"v_mov_b32 v243, 0x3f803f80\n"
"v_mov_b32 v242, 0x3f003f00\n"
"v_accvgpr_write_b32 a248, v243\n"
"v_accvgpr_write_b32 a249, v243\n"
"v_accvgpr_write_b32 a250, v243\n"
"v_accvgpr_write_b32 a251, v243\n"
"v_accvgpr_write_b32 a252, v242\n"
"v_accvgpr_write_b32 a253, v242\n"
"v_accvgpr_write_b32 a254, v242\n"
"v_accvgpr_write_b32 a255, v242\n"
"s_mov_b32 s72, 0x3e38aa3b\n"
"s_getpc_b64 s[80:81]\n"
".Lpc3%=:\n"
"s_add_u32 s80, s80, .Lp2f%=-.Lpc3%=\n"
"s_addc_u32 s81, s81, 0\n"
"s_and_b32 s81, s81, 0xffff\n"
"s_mov_b32 s82, .Lhot%=-.Lp2f%=\n"
"s_mov_b32 s83, 0x20000\n"
"s_add_u32 s91, s91, s92\n"
"s_add_u32 s18, s18, s91\n"
"s_addc_u32 s19, s19, 0\n"
"s_sub_u32 s84, s18, 13107200\n"
"s_subb_u32 s85, s19, 0\n"
"s_and_b32 s85, s85, 0xffff\n"
"s_mov_b32 s86, 25472\n"
"s_mov_b32 s87, 0x20000\n"
"s_bfe_u32 s90, %[bid], 0x20004\n"
"s_lshl_b32 s90, s90, 12\n"
"s_bfe_u32 s91, %[bid], 0x10003\n"
"s_lshl_b32 s91, s91, 9\n"
"s_lshl_b32 s88, %[wave], 7\n"
"s_add_u32 s88, s88, s90\n"
"s_add_u32 s88, s88, s91\n"
"s_waitcnt lgkmcnt(0)\n"
".Lhead%=:\n"
"s_mov_b64 vcc, -1\n"
"s_cmp_eq_u32 %[wave], 0\n"
"s_cbranch_scc1 .Lpad0_%=\n"
"s_cmp_eq_u32 %[wave], 1\n"
"s_cbranch_scc1 .Lpad1_%=\n"
"s_cmp_eq_u32 %[wave], 2\n"
"s_cbranch_scc1 .Lpad3_%=\n"
"s_cmp_eq_u32 %[wave], 3\n"
"s_cbranch_scc1 .Lpad4_%=\n"
".Lback0_%=:\n"
"s_mov_b64 vcc, 0\n"
"s_add_u32 m0, s74, 512\n"
"s_nop 0\n"
"global_load_lds_dwordx4 v44, s[10:11] offset:2560\n"
"s_add_u32 m0, s75, 16384\n"
"s_nop 0\n"
"global_load_lds_dwordx4 v44, s[14:15] offset:0\n"
"s_waitcnt vmcnt(10)\n"
"s_barrier\n"
"ds_read_b64_tr_b16 a[96:97], v40 offset:33152\n"
"ds_read_b64_tr_b16 a[98:99], v40 offset:34176\n"
"ds_read_b64_tr_b16 a[100:101], v40 offset:37248\n"
"ds_read_b64_tr_b16 a[102:103], v40 offset:38272\n"
"ds_read_b64_tr_b16 a[104:105], v40 offset:41344\n"
"ds_read_b64_tr_b16 a[106:107], v40 offset:42368\n"
"ds_read_b64_tr_b16 a[108:109], v40 offset:45440\n"
"ds_read_b64_tr_b16 a[110:111], v40 offset:46464\n"
"ds_read_b64_tr_b16 a[112:113], v41 offset:33152\n"
"ds_read_b64_tr_b16 a[114:115], v41 offset:34176\n"
"global_load_lds_dwordx4 v44, s[14:15] offset:2048\n"
"ds_read_b64_tr_b16 a[116:117], v41 offset:37248\n"
"ds_read_b64_tr_b16 a[118:119], v41 offset:38272\n"
"ds_read_b64_tr_b16 a[120:121], v41 offset:41344\n"
"ds_read_b64_tr_b16 a[122:123], v41 offset:42368\n"
"ds_read_b64_tr_b16 a[124:125], v41 offset:45440\n"
"ds_read_b64_tr_b16 a[126:127], v41 offset:46464\n"
"ds_read_b64_tr_b16 a[128:129], v42 offset:33152\n"
"ds_read_b64_tr_b16 a[130:131], v42 offset:34176\n"
"ds_read_b64_tr_b16 a[132:133], v42 offset:37248\n"
"ds_read_b64_tr_b16 a[134:135], v42 offset:38272\n"
"ds_read_b64_tr_b16 a[136:137], v42 offset:41344\n"
"ds_read_b64_tr_b16 a[138:139], v42 offset:42368\n"
"ds_read_b64_tr_b16 a[140:141], v42 offset:45440\n"
"ds_read_b64_tr_b16 a[142:143], v42 offset:46464\n"
"ds_read_b64_tr_b16 a[144:145], v43 offset:33152\n"
"ds_read_b64_tr_b16 a[146:147], v43 offset:34176\n"
"s_add_u32 m0, s75, 16896\n"
"s_nop 0\n"
"global_load_lds_dwordx4 v44, s[14:15] offset:512\n"
"ds_read_b64_tr_b16 a[148:149], v43 offset:37248\n"
"ds_read_b64_tr_b16 a[150:151], v43 offset:38272\n"
"ds_read_b64_tr_b16 a[152:153], v43 offset:41344\n"
"ds_read_b64_tr_b16 a[154:155], v43 offset:42368\n"
"ds_read_b64_tr_b16 a[156:157], v43 offset:45440\n"
"ds_read_b64_tr_b16 a[158:159], v43 offset:46464\n"
"global_load_lds_dwordx4 v44, s[14:15] offset:2560\n"
"s_add_u32 m0, s29, 4352\n"
"s_mov_b32 s94, 52453568\n"
"buffer_load_dwordx4 v152, s[24:27], s94 offen offset:0 lds\n"
"s_mov_b32 s94, 65559680\n"
"buffer_load_dwordx4 v152, s[24:27], s94 offen offset:1088 lds\n"
"s_mov_b32 s94, 78665792\n"
"buffer_load_dwordx4 v152, s[24:27], s94 offen offset:2176 lds\n"
"s_mov_b32 s94, 91771904\n"
"buffer_load_dwordx4 v152, s[24:27], s94 offen offset:3264 lds\n"
"s_waitcnt vmcnt(9)\n"
"ds_read_b128 v[204:207], v47 offset:0\n"
"ds_read_b128 v[208:211], v47 offset:64\n"
"ds_read_b128 v[212:215], v47 offset:128\n"
"s_add_u32 m0, s28, 0\n"
"s_nop 0\n"
"buffer_load_dwordx4 v152, s[24:27], s40 offen offset:0 lds\n"
"ds_read_b128 v[216:219], v47 offset:192\n"
"s_waitcnt vmcnt(9)\n"
"ds_read_b64_tr_b16 v[112:113], v32 offset:0\n"
"ds_read_b64_tr_b16 v[114:115], v32 offset:1024\n"
"ds_read_b64_tr_b16 v[116:117], v33 offset:0\n"
"ds_read_b64_tr_b16 v[118:119], v33 offset:1024\n"
"ds_read_b64_tr_b16 v[120:121], v34 offset:0\n"
"ds_read_b64_tr_b16 v[122:123], v34 offset:1024\n"
"ds_read_b64_tr_b16 v[124:125], v35 offset:0\n"
"ds_read_b64_tr_b16 v[126:127], v35 offset:1024\n"
"s_waitcnt lgkmcnt(12)\n"
"s_barrier\n"
"ds_read_b64_tr_b16 a[48:49], v40 offset:0\n"
"ds_read_b64_tr_b16 a[50:51], v40 offset:1024\n"
"ds_read_b64_tr_b16 a[52:53], v40 offset:4096\n"
"ds_read_b64_tr_b16 a[54:55], v40 offset:5120\n"
"ds_read_b64_tr_b16 a[56:57], v40 offset:8192\n"
"ds_read_b64_tr_b16 a[58:59], v40 offset:9216\n"
"s_waitcnt lgkmcnt(15)\n"
"v_mfma_f32_16x16x32_bf16 v[96:99], a[96:99], v[204:207], 0\n"
"ds_read_b64_tr_b16 a[60:61], v40 offset:12288\n"
"ds_read_b64_tr_b16 a[62:63], v40 offset:13312\n"
"v_mfma_f32_16x16x32_bf16 v[96:99], a[100:103], v[208:211], v[96:99]\n"
"ds_read_b64_tr_b16 a[64:65], v41 offset:0\n"
"ds_read_b64_tr_b16 a[66:67], v41 offset:1024\n"
"v_mfma_f32_16x16x32_bf16 v[96:99], a[104:107], v[212:215], v[96:99]\n"
"s_waitcnt lgkmcnt(13)\n"
"ds_read_b64_tr_b16 a[68:69], v41 offset:4096\n"
"ds_read_b64_tr_b16 a[70:71], v41 offset:5120\n"
"v_mfma_f32_16x16x32_bf16 v[96:99], a[108:111], v[216:219], v[96:99]\n"
"ds_read_b64_tr_b16 a[72:73], v41 offset:8192\n"
"ds_read_b64_tr_b16 a[74:75], v41 offset:9216\n"
"v_mfma_f32_16x16x32_bf16 v[100:103], a[112:115], v[204:207], 0\n"
"ds_read_b64_tr_b16 a[76:77], v41 offset:12288\n"
"ds_read_b64_tr_b16 a[78:79], v41 offset:13312\n"
"v_mfma_f32_16x16x32_bf16 v[100:103], a[116:119], v[208:211], v[100:103]\n"
"ds_read_b64_tr_b16 a[80:81], v42 offset:0\n"
"ds_read_b64_tr_b16 a[82:83], v42 offset:1024\n"
"v_mfma_f32_16x16x32_bf16 v[100:103], a[120:123], v[212:215], v[100:103]\n"
"ds_read_b64_tr_b16 a[84:85], v42 offset:4096\n"
"ds_read_b64_tr_b16 a[86:87], v42 offset:5120\n"
"v_mfma_f32_16x16x32_bf16 v[100:103], a[124:127], v[216:219], v[100:103]\n"
"ds_read_b64_tr_b16 a[88:89], v42 offset:8192\n"
"ds_read_b64_tr_b16 a[90:91], v42 offset:9216\n"
"v_mfma_f32_16x16x32_bf16 v[104:107], a[128:131], v[204:207], 0\n"
"ds_read_b64_tr_b16 a[92:93], v42 offset:12288\n"
"ds_read_b64_tr_b16 a[94:95], v42 offset:13312\n"
"v_mfma_f32_16x16x32_bf16 v[104:107], a[132:135], v[208:211], v[104:107]\n"
"ds_read_b64_tr_b16 a[224:225], v43 offset:0\n"
"ds_read_b64_tr_b16 a[226:227], v43 offset:1024\n"
"v_mfma_f32_16x16x32_bf16 v[104:107], a[136:139], v[212:215], v[104:107]\n"
"ds_read_b64_tr_b16 a[228:229], v43 offset:4096\n"
"ds_read_b64_tr_b16 a[230:231], v43 offset:5120\n"
"v_mfma_f32_16x16x32_bf16 v[104:107], a[140:143], v[216:219], v[104:107]\n"
"ds_read_b64_tr_b16 a[232:233], v43 offset:8192\n"
"ds_read_b64_tr_b16 a[234:235], v43 offset:9216\n"
"v_mfma_f32_16x16x32_bf16 v[108:111], a[144:147], v[204:207], 0\n"
"ds_read_b64_tr_b16 a[236:237], v43 offset:12288\n"
"ds_read_b64_tr_b16 a[238:239], v43 offset:13312\n"
"v_mfma_f32_16x16x32_bf16 v[108:111], a[148:151], v[208:211], v[108:111]\n"
"s_waitcnt vmcnt(5)\n"
"ds_read_b64_tr_b16 v[236:237], v36 offset:16384\n"
"ds_read_b64_tr_b16 v[238:239], v36 offset:17408\n"
"v_mfma_f32_16x16x32_bf16 v[108:111], a[152:155], v[212:215], v[108:111]\n"
"ds_read_b64_tr_b16 v[240:241], v37 offset:16384\n"
"ds_read_b64_tr_b16 v[242:243], v37 offset:17408\n"
"v_mfma_f32_16x16x32_bf16 v[108:111], a[156:159], v[216:219], v[108:111]\n"
"s_waitcnt lgkmcnt(13)\n"
"ds_read_b64_tr_b16 v[244:245], v38 offset:16384\n"
"ds_read_b64_tr_b16 v[246:247], v38 offset:17408\n"
"v_mfma_f32_16x16x32_bf16 v[136:139], a[48:51], v[204:207], 0\n"
"ds_read_b64_tr_b16 v[248:249], v39 offset:16384\n"
"ds_read_b64_tr_b16 v[250:251], v39 offset:17408\n"
"v_mfma_f32_16x16x32_bf16 v[136:139], a[52:55], v[208:211], v[136:139]\n"
"v_cvt_pk_bf16_f32 v96, v96, v97\n"
"v_cvt_pk_bf16_f32 v97, v98, v99\n"
"v_mfma_f32_16x16x32_bf16 v[136:139], a[56:59], v[212:215], v[136:139]\n"
"v_cvt_pk_bf16_f32 v98, v100, v101\n"
"v_cvt_pk_bf16_f32 v99, v102, v103\n"
"v_mfma_f32_16x16x32_bf16 v[136:139], a[60:63], v[216:219], v[136:139]\n"
"buffer_load_dwordx4 v152, s[24:27], s41 offen offset:1088 lds\n"
"v_cvt_pk_bf16_f32 v104, v104, v105\n"
"v_mfma_f32_16x16x32_bf16 v[140:143], a[64:67], v[204:207], 0\n"
"v_cvt_pk_bf16_f32 v105, v106, v107\n"
"v_cvt_pk_bf16_f32 v106, v108, v109\n"
"v_mfma_f32_16x16x32_bf16 v[140:143], a[68:71], v[208:211], v[140:143]\n"
"v_cvt_pk_bf16_f32 v107, v110, v111\n"
"v_lshlrev_b32 v128, 16, v112\n"
"v_mfma_f32_16x16x32_bf16 v[140:143], a[72:75], v[212:215], v[140:143]\n"
"v_mul_f32 v136, s72, v136\n"
"v_mul_f32 v137, s72, v137\n"
"v_mfma_f32_16x16x32_bf16 v[140:143], a[76:79], v[216:219], v[140:143]\n"
"v_mul_f32 v138, s72, v138\n"
"v_mul_f32 v139, s72, v139\n"
"v_mfma_f32_16x16x32_bf16 v[144:147], a[80:83], v[204:207], 0\n"
"v_cvt_pk_bf16_f32 v136, v136, v137\n"
"v_cvt_pk_bf16_f32 v138, v138, v139\n"
"v_mfma_f32_16x16x32_bf16 v[144:147], a[84:87], v[208:211], v[144:147]\n"
"v_and_b32 v112, 0xffff0000, v112\n"
"v_mul_f32 v128, s72, v128\n"
"v_mfma_f32_16x16x32_bf16 v[144:147], a[88:91], v[212:215], v[144:147]\n"
"s_waitcnt lgkmcnt(4)\n"
"v_mul_f32 v140, s72, v140\n"
"v_mul_f32 v141, s72, v141\n"
"v_mfma_f32_16x16x32_bf16 v[144:147], a[92:95], v[216:219], v[144:147]\n"
"ds_write_b128 v46, v[236:239] offset:16384\n"
"v_mfma_f32_16x16x32_bf16 v[148:151], a[224:227], v[204:207], 0\n"
"v_mul_f32 v142, s72, v142\n"
"v_mul_f32 v143, s72, v143\n"
"v_mfma_f32_16x16x32_bf16 v[148:151], a[228:231], v[208:211], v[148:151]\n"
"buffer_load_dwordx4 v152, s[24:27], s42 offen offset:2176 lds\n"
"v_cvt_pk_bf16_f32 v137, v140, v141\n"
"v_mfma_f32_16x16x32_bf16 v[148:151], a[232:235], v[212:215], v[148:151]\n"
"v_cvt_pk_bf16_f32 v139, v142, v143\n"
"v_mul_f32 v144, s72, v144\n"
"v_mfma_f32_16x16x32_bf16 v[148:151], a[236:239], v[216:219], v[148:151]\n"
"s_waitcnt lgkmcnt(1)\n"
"ds_write_b128 v46, v[240:243] offset:16448\n"
"v_mul_f32 v145, s72, v145\n"
"ds_write_b128 v46, v[244:247] offset:16512\n"
"v_mul_f32 v146, s72, v146\n"
"v_mul_f32 v147, s72, v147\n"
"s_nop 1\n"
"v_mul_f32 v148, s72, v148\n"
"v_mul_f32 v149, s72, v149\n"
"v_mul_f32 v150, s72, v150\n"
"v_mul_f32 v151, s72, v151\n"
"v_cvt_pk_bf16_f32 v144, v144, v145\n"
"v_cvt_pk_bf16_f32 v145, v148, v149\n"
"ds_write_b128 v46, v[248:251] offset:16576\n"
"v_cvt_pk_bf16_f32 v146, v146, v147\n"
"v_cvt_pk_bf16_f32 v147, v150, v151\n"
"v_mul_f32 v112, s72, v112\n"
"v_cvt_pk_bf16_f32 v112, v128, v112\n"
"v_lshlrev_b32 v129, 16, v113\n"
"v_and_b32 v113, 0xffff0000, v113\n"
"v_mul_f32 v129, s72, v129\n"
"buffer_load_dwordx4 v152, s[24:27], s43 offen offset:3264 lds\n"
"v_mul_f32 v113, s72, v113\n"
"v_cvt_pk_bf16_f32 v113, v129, v113\n"
"v_lshlrev_b32 v130, 16, v114\n"
"v_and_b32 v114, 0xffff0000, v114\n"
"v_mul_f32 v130, s72, v130\n"
"v_mul_f32 v114, s72, v114\n"
"v_cvt_pk_bf16_f32 v114, v130, v114\n"
"v_lshlrev_b32 v131, 16, v115\n"
"v_and_b32 v115, 0xffff0000, v115\n"
"v_mul_f32 v131, s72, v131\n"
"v_mul_f32 v115, s72, v115\n"
"v_cvt_pk_bf16_f32 v115, v131, v115\n"
"v_lshlrev_b32 v128, 16, v116\n"
"v_and_b32 v116, 0xffff0000, v116\n"
"v_mul_f32 v128, s72, v128\n"
"v_mul_f32 v116, s72, v116\n"
"v_cvt_pk_bf16_f32 v116, v128, v116\n"
"v_lshlrev_b32 v129, 16, v117\n"
"v_and_b32 v117, 0xffff0000, v117\n"
"v_mul_f32 v129, s72, v129\n"
"v_mul_f32 v117, s72, v117\n"
"v_cvt_pk_bf16_f32 v117, v129, v117\n"
"v_lshlrev_b32 v130, 16, v118\n"
"v_and_b32 v118, 0xffff0000, v118\n"
"v_mul_f32 v130, s72, v130\n"
"v_mul_f32 v118, s72, v118\n"
"v_cvt_pk_bf16_f32 v118, v130, v118\n"
"v_lshlrev_b32 v131, 16, v119\n"
"v_and_b32 v119, 0xffff0000, v119\n"
"v_mul_f32 v131, s72, v131\n"
"v_mul_f32 v119, s72, v119\n"
"v_cvt_pk_bf16_f32 v119, v131, v119\n"
"s_add_u32 m0, s75, 49536\n"
"s_nop 0\n"
"global_load_lds_dwordx4 v135, s[16:17] offset:0\n"
"v_lshlrev_b32 v128, 16, v120\n"
"v_and_b32 v120, 0xffff0000, v120\n"
"v_mul_f32 v128, s72, v128\n"
"v_mul_f32 v120, s72, v120\n"
"s_waitcnt vmcnt(5)\n"
"ds_read_b128 v[220:223], v47 offset:4352\n"
"ds_read_b128 v[224:227], v47 offset:4416\n"
"ds_read_b128 v[228:231], v47 offset:4480\n"
"v_cvt_pk_bf16_f32 v120, v128, v120\n"
"ds_read_b128 v[232:235], v47 offset:4544\n"
"v_lshlrev_b32 v129, 16, v121\n"
"v_and_b32 v121, 0xffff0000, v121\n"
"s_waitcnt lgkmcnt(4)\n"
"s_barrier\n"
"v_mul_f32 v129, s72, v129\n"
"ds_read_b128 a[160:163], v153 offset:16384\n"
"ds_read_b128 a[164:167], v153 offset:20576\n"
"ds_read_b128 a[168:171], v153 offset:24768\n"
"global_load_lds_dwordx4 v135, s[16:17] offset:1024\n"
"ds_read_b128 a[172:175], v153 offset:28960\n"
"v_mov_b32 v135, 0\n"
"v_mul_f32 v121, s72, v121\n"
"v_cvt_pk_bf16_f32 v121, v129, v121\n"
"ds_read_b128 a[176:179], v153 offset:17440\n"
"v_lshlrev_b32 v130, 16, v122\n"
"ds_read_b128 a[180:183], v153 offset:21632\n"
"v_and_b32 v122, 0xffff0000, v122\n"
"ds_read_b128 a[184:187], v153 offset:25824\n"
"v_mul_f32 v130, s72, v130\n"
"ds_read_b128 a[188:191], v153 offset:30016\n"
"v_mul_f32 v122, s72, v122\n"
"ds_read_b128 a[192:195], v153 offset:18496\n"
"v_cvt_pk_bf16_f32 v122, v130, v122\n"
"ds_read_b128 a[196:199], v153 offset:22688\n"
"s_waitcnt lgkmcnt(10)\n"
"v_mfma_f32_16x16x32_bf16 v[100:103], a[124:127], v[232:235], 0\n"
"ds_read_b128 a[200:203], v153 offset:26880\n"
"v_lshlrev_b32 v131, 16, v123\n"
"v_mfma_f32_16x16x32_bf16 v[100:103], a[120:123], v[228:231], v[100:103]\n"
"s_add_u32 m0, s75, 51584\n"
"s_nop 0\n"
"global_load_lds_dwordx4 v252, s[16:17] offset:0\n"
"v_mfma_f32_16x16x32_bf16 v[100:103], a[116:119], v[224:227], v[100:103]\n"
"ds_read_b128 a[204:207], v153 offset:31072\n"
"ds_read_b128 a[208:211], v153 offset:19552\n"
"v_mfma_f32_16x16x32_bf16 v[100:103], a[112:115], v[220:223], v[100:103]\n"
"s_waitcnt lgkmcnt(12)\n"
"ds_read_b128 a[212:215], v153 offset:23744\n"
"v_and_b32 v123, 0xffff0000, v123\n"
"v_mfma_f32_16x16x32_bf16 v[48:51], v[204:207], a[160:163], 0\n"
"s_waitcnt lgkmcnt(12)\n"
"ds_read_b128 a[216:219], v153 offset:27936\n"
"v_mul_f32 v131, s72, v131\n"
"v_mfma_f32_16x16x32_bf16 v[48:51], v[208:211], a[164:167], v[48:51]\n"
"s_waitcnt lgkmcnt(12)\n"
"ds_read_b128 a[220:223], v153 offset:32128\n"
"v_mul_f32 v123, s72, v123\n"
"v_mfma_f32_16x16x32_bf16 v[48:51], v[212:215], a[168:171], v[48:51]\n"
"s_waitcnt lgkmcnt(12)\n"
"v_cvt_pk_bf16_f32 v123, v131, v123\n"
"v_lshlrev_b32 v128, 16, v124\n"
"v_mfma_f32_16x16x32_bf16 v[48:51], v[216:219], a[172:175], v[48:51]\n"
"s_waitcnt lgkmcnt(11)\n"
"ds_write_b128 v132, v[104:107] offset:2048\n"
"v_mfma_f32_16x16x32_bf16 v[52:55], v[204:207], a[176:179], 0\n"
"s_waitcnt lgkmcnt(9)\n"
"v_and_b32 v124, 0xffff0000, v124\n"
"v_mul_f32 v128, s72, v128\n"
"v_mfma_f32_16x16x32_bf16 v[52:55], v[208:211], a[180:183], v[52:55]\n"
"global_load_lds_dwordx4 v252, s[16:17] offset:1024\n"
"v_cvt_pk_bf16_f32 v48, v48, v49\n"
"v_mfma_f32_16x16x32_bf16 v[52:55], v[212:215], a[184:187], v[52:55]\n"
"v_cvt_pk_bf16_f32 v49, v50, v51\n"
"v_mul_f32 v124, s72, v124\n"
"v_mfma_f32_16x16x32_bf16 v[52:55], v[216:219], a[188:191], v[52:55]\n"
"s_waitcnt lgkmcnt(6)\n"
"ds_write_b64 v134, v[48:49] offset:0\n"
"v_mfma_f32_16x16x32_bf16 v[60:63], v[204:207], a[192:195], 0\n"
"v_cvt_pk_bf16_f32 v124, v128, v124\n"
"v_mov_b32 v128, 0\n"
"v_mfma_f32_16x16x32_bf16 v[60:63], v[208:211], a[196:199], v[60:63]\n"
"v_lshlrev_b32 v129, 16, v125\n"
"v_and_b32 v125, 0xffff0000, v125\n"
"v_mfma_f32_16x16x32_bf16 v[60:63], v[212:215], a[200:203], v[60:63]\n"
"s_waitcnt lgkmcnt(3)\n"
"v_cvt_pk_bf16_f32 v52, v52, v53\n"
"v_cvt_pk_bf16_f32 v53, v54, v55\n"
"v_mfma_f32_16x16x32_bf16 v[60:63], v[216:219], a[204:207], v[60:63]\n"
"ds_write_b64 v134, v[52:53] offset:128\n"
"v_mfma_f32_16x16x32_bf16 v[64:67], v[204:207], a[208:211], 0\n"
"v_mul_f32 v129, s72, v129\n"
"v_mul_f32 v125, s72, v125\n"
"v_mfma_f32_16x16x32_bf16 v[64:67], v[208:211], a[212:215], v[64:67]\n"
"s_add_u32 m0, s28, 4352\n"
"s_nop 0\n"
"buffer_load_dwordx4 v152, s[24:27], s44 offen offset:0 lds\n"
"v_mfma_f32_16x16x32_bf16 v[64:67], v[212:215], a[216:219], v[64:67]\n"
"s_waitcnt lgkmcnt(2)\n"
"v_cvt_pk_bf16_f32 v60, v60, v61\n"
"v_cvt_pk_bf16_f32 v61, v62, v63\n"
"v_mfma_f32_16x16x32_bf16 v[64:67], v[216:219], a[220:223], v[64:67]\n"
"ds_write_b64 v134, v[60:61] offset:256\n"
"v_mfma_f32_16x16x32_bf16 v[104:107], a[140:143], v[232:235], 0\n"
"v_cvt_pk_bf16_f32 v125, v129, v125\n"
"v_mov_b32 v129, 0\n"
"v_mfma_f32_16x16x32_bf16 v[104:107], a[136:139], v[228:231], v[104:107]\n"
"v_lshlrev_b32 v130, 16, v126\n"
"v_and_b32 v126, 0xffff0000, v126\n"
"v_mfma_f32_16x16x32_bf16 v[104:107], a[132:135], v[224:227], v[104:107]\n"
"v_cvt_pk_bf16_f32 v64, v64, v65\n"
"v_cvt_pk_bf16_f32 v65, v66, v67\n"
"v_mfma_f32_16x16x32_bf16 v[104:107], a[128:131], v[220:223], v[104:107]\n"
"ds_write_b64 v134, v[64:65] offset:384\n"
"v_mfma_f32_16x16x32_bf16 v[108:111], a[156:159], v[232:235], 0\n"
"v_mul_f32 v130, s72, v130\n"
"v_mul_f32 v126, s72, v126\n"
"v_mfma_f32_16x16x32_bf16 v[108:111], a[152:155], v[228:231], v[108:111]\n"
"buffer_load_dwordx4 v152, s[24:27], s45 offen offset:1088 lds\n"
"v_cvt_pk_bf16_f32 v126, v130, v126\n"
"v_mfma_f32_16x16x32_bf16 v[108:111], a[148:151], v[224:227], v[108:111]\n"
"v_mov_b32 v130, 0\n"
"v_cvt_pk_bf16_f32 v104, v104, v105\n"
"v_mfma_f32_16x16x32_bf16 v[108:111], a[144:147], v[220:223], v[108:111]\n"
"ds_write_b128 v132, v[96:99] offset:0\n"
"v_mfma_f32_16x16x32_bf16 v[96:99], a[108:111], v[232:235], 0\n"
".Lpad0_%=:\n"
"s_cbranch_vccnz .Lpad5_%=\n"
"v_cvt_pk_bf16_f32 v105, v106, v107\n"
"v_lshlrev_b32 v131, 16, v127\n"
"v_mfma_f32_16x16x32_bf16 v[96:99], a[104:107], v[228:231], v[96:99]\n"
"s_nop 0\n"
"v_cvt_pk_bf16_f32 v106, v108, v109\n"
"v_cvt_pk_bf16_f32 v107, v110, v111\n"
"v_mfma_f32_16x16x32_bf16 v[96:99], a[100:103], v[224:227], v[96:99]\n"
"v_and_b32 v127, 0xffff0000, v127\n"
"v_mul_f32 v131, s72, v131\n"
"v_mfma_f32_16x16x32_bf16 v[96:99], a[96:99], v[220:223], v[96:99]\n"
"ds_write_b128 v133, v[144:147] offset:256\n"
"v_mfma_f32_16x16x32_bf16 v[140:143], a[76:79], v[232:235], 0\n"
"buffer_load_dwordx4 v152, s[24:27], s46 offen offset:2176 lds\n"
"v_mul_f32 v127, s72, v127\n"
"v_mfma_f32_16x16x32_bf16 v[140:143], a[72:75], v[228:231], v[140:143]\n"
"s_nop 0\n"
"v_cvt_pk_bf16_f32 v96, v96, v97\n"
"v_cvt_pk_bf16_f32 v97, v98, v99\n"
"v_mfma_f32_16x16x32_bf16 v[140:143], a[68:71], v[224:227], v[140:143]\n"
"v_cvt_pk_bf16_f32 v98, v100, v101\n"
"v_cvt_pk_bf16_f32 v99, v102, v103\n"
"v_mfma_f32_16x16x32_bf16 v[140:143], a[64:67], v[220:223], v[140:143]\n"
"s_waitcnt vmcnt(7)\n"
"ds_read_b128 a[0:3], v154 offset:0\n"
"ds_read_b128 a[4:7], v154 offset:64\n"
"v_mfma_f32_16x16x32_bf16 v[144:147], a[92:95], v[232:235], 0\n"
"ds_read_b128 a[8:11], v154 offset:128\n"
"ds_read_b128 a[12:15], v154 offset:192\n"
"v_mfma_f32_16x16x32_bf16 v[144:147], a[88:91], v[228:231], v[144:147]\n"
"v_cvt_pk_bf16_f32 v127, v131, v127\n"
"v_mov_b32 v131, 0\n"
"v_mfma_f32_16x16x32_bf16 v[144:147], a[84:87], v[224:227], v[144:147]\n"
"v_mul_f32 v140, s72, v140\n"
"v_mul_f32 v141, s72, v141\n"
"v_mfma_f32_16x16x32_bf16 v[144:147], a[80:83], v[220:223], v[144:147]\n"
"ds_write_b128 v133, v[136:139] offset:0\n"
"v_mfma_f32_16x16x32_bf16 v[136:139], a[60:63], v[232:235], 0\n"
"buffer_load_dwordx4 v152, s[24:27], s47 offen offset:3264 lds\n"
"v_mul_f32 v142, s72, v142\n"
"v_mfma_f32_16x16x32_bf16 v[136:139], a[56:59], v[228:231], v[136:139]\n"
"v_mul_f32 v143, s72, v143\n"
"v_mul_f32 v144, s72, v144\n"
"v_mfma_f32_16x16x32_bf16 v[136:139], a[52:55], v[224:227], v[136:139]\n"
"v_mul_f32 v145, s72, v145\n"
"v_mul_f32 v146, s72, v146\n"
"v_mfma_f32_16x16x32_bf16 v[136:139], a[48:51], v[220:223], v[136:139]\n"
"ds_write_b128 v132, v[96:99] offset:256\n"
"v_mfma_f32_16x16x32_bf16 v[148:151], a[236:239], v[232:235], 0\n"
"v_mul_f32 v147, s72, v147\n"
"v_cvt_pk_bf16_f32 v144, v144, v145\n"
"v_mfma_f32_16x16x32_bf16 v[148:151], a[232:235], v[228:231], v[148:151]\n"
"s_nop 0\n"
"v_mul_f32 v136, s72, v136\n"
"v_mul_f32 v137, s72, v137\n"
"v_mfma_f32_16x16x32_bf16 v[148:151], a[228:231], v[224:227], v[148:151]\n"
"v_mul_f32 v138, s72, v138\n"
"v_mul_f32 v139, s72, v139\n"
"v_mfma_f32_16x16x32_bf16 v[148:151], a[224:227], v[220:223], v[148:151]\n"
"ds_write_b128 v132, v[104:107] offset:2304\n"
"v_mfma_f32_16x16x32_bf16 v[72:75], v[232:235], a[172:175], 0\n"
"s_add_u32 m0, s28, 8704\n"
"s_nop 0\n"
"buffer_load_dwordx4 v152, s[24:27], s48 offen offset:0 lds\n"
"v_mfma_f32_16x16x32_bf16 v[72:75], v[228:231], a[168:171], v[72:75]\n"
"s_waitcnt vmcnt(5)\n"
"ds_read_b64_tr_b16 v[236:237], v36 offset:49536\n"
"ds_read_b64_tr_b16 v[238:239], v36 offset:50560\n"
"v_mfma_f32_16x16x32_bf16 v[72:75], v[224:227], a[164:167], v[72:75]\n"
"ds_read_b64_tr_b16 v[240:241], v37 offset:49536\n"
"ds_read_b64_tr_b16 v[242:243], v37 offset:50560\n"
"v_mfma_f32_16x16x32_bf16 v[72:75], v[220:223], a[160:163], v[72:75]\n"
"ds_read_b64_tr_b16 v[244:245], v38 offset:49536\n"
"ds_read_b64_tr_b16 v[246:247], v38 offset:50560\n"
"v_mfma_f32_16x16x32_bf16 v[76:79], v[232:235], a[188:191], 0\n"
"ds_read_b64_tr_b16 v[248:249], v39 offset:49536\n"
"ds_read_b64_tr_b16 v[250:251], v39 offset:50560\n"
"v_mfma_f32_16x16x32_bf16 v[76:79], v[228:231], a[184:187], v[76:79]\n"
"v_mov_b32 v132, 0\n"
"v_cvt_pk_bf16_f32 v136, v136, v137\n"
"v_mfma_f32_16x16x32_bf16 v[76:79], v[224:227], a[180:183], v[76:79]\n"
"v_cvt_pk_bf16_f32 v137, v140, v141\n"
"v_cvt_pk_bf16_f32 v138, v138, v139\n"
"v_mfma_f32_16x16x32_bf16 v[76:79], v[220:223], a[176:179], v[76:79]\n"
"ds_write_b128 v45, v[112:115] offset:0\n"
"v_mfma_f32_16x16x32_bf16 v[84:87], v[232:235], a[204:207], 0\n"
"v_cvt_pk_bf16_f32 v139, v142, v143\n"
"v_mul_f32 v148, s72, v148\n"
"v_mfma_f32_16x16x32_bf16 v[84:87], v[228:231], a[200:203], v[84:87]\n"
"ds_read_b128 v[140:143], v153 offset:22688\n"
"v_mul_f32 v149, s72, v149\n"
"v_mfma_f32_16x16x32_bf16 v[84:87], v[224:227], a[196:199], v[84:87]\n"
"v_mul_f32 v150, s72, v150\n"
"v_mul_f32 v151, s72, v151\n"
"v_mfma_f32_16x16x32_bf16 v[84:87], v[220:223], a[192:195], v[84:87]\n"
"ds_write_b128 v133, v[136:139] offset:2048\n"
"v_mfma_f32_16x16x32_bf16 v[88:91], v[232:235], a[220:223], 0\n"
"ds_read_b128 v[136:139], v153 offset:18496\n"
"v_cvt_pk_bf16_f32 v145, v148, v149\n"
"v_mfma_f32_16x16x32_bf16 v[88:91], v[228:231], a[216:219], v[88:91]\n"
"v_cvt_pk_bf16_f32 v146, v146, v147\n"
"v_cvt_pk_bf16_f32 v147, v150, v151\n"
"v_mfma_f32_16x16x32_bf16 v[88:91], v[224:227], a[212:215], v[88:91]\n"
"ds_read_b128 v[148:151], v153 offset:31072\n"
"v_cvt_pk_bf16_f32 v72, v72, v73\n"
"v_mfma_f32_16x16x32_bf16 v[88:91], v[220:223], a[208:211], v[88:91]\n"
"s_waitcnt lgkmcnt(5)\n"
"ds_write_b128 v133, v[144:147] offset:2304\n"
"v_mfma_f32_16x16x32_bf16 v[0:3], a[96:99], a[0:3], 0\n"
"v_mov_b32 v133, 0\n"
"v_cvt_pk_bf16_f32 v73, v74, v75\n"
"v_mfma_f32_16x16x32_bf16 v[0:3], a[100:103], a[4:7], v[0:3]\n"
"ds_read_b128 v[144:147], v153 offset:26880\n"
"v_cvt_pk_bf16_f32 v76, v76, v77\n"
"v_mfma_f32_16x16x32_bf16 v[0:3], a[104:107], a[8:11], v[0:3]\n"
"v_cvt_pk_bf16_f32 v77, v78, v79\n"
"v_cvt_pk_bf16_f32 v84, v84, v85\n"
"v_mfma_f32_16x16x32_bf16 v[0:3], a[108:111], a[12:15], v[0:3]\n"
"ds_write_b64 v134, v[72:73] offset:2048\n"
"v_mfma_f32_16x16x32_bf16 v[176:179], a[112:115], a[0:3], 0\n"
"v_cvt_pk_bf16_f32 v85, v86, v87\n"
"v_cvt_pk_bf16_f32 v88, v88, v89\n"
"v_mfma_f32_16x16x32_bf16 v[176:179], a[116:119], a[4:7], v[176:179]\n"
"v_cvt_pk_bf16_f32 v89, v90, v91\n"
"v_mfma_f32_16x16x32_bf16 v[176:179], a[120:123], a[8:11], v[176:179]\n"
"v_cvt_pk_bf16_f32 v0, v0, v1\n"
"v_cvt_pk_bf16_f32 v1, v2, v3\n"
"v_mfma_f32_16x16x32_bf16 v[176:179], a[124:127], a[12:15], v[176:179]\n"
"ds_write_b64 v134, v[76:77] offset:2176\n"
"v_mfma_f32_16x16x32_bf16 v[4:7], a[128:131], a[0:3], 0\n"
"buffer_load_dwordx4 v152, s[24:27], s49 offen offset:1088 lds\n"
"v_mfma_f32_16x16x32_bf16 v[4:7], a[132:135], a[4:7], v[4:7]\n"
"v_mfma_f32_16x16x32_bf16 v[4:7], a[136:139], a[8:11], v[4:7]\n"
"v_cvt_pk_bf16_f32 v2, v176, v177\n"
"v_cvt_pk_bf16_f32 v3, v178, v179\n"
"v_mfma_f32_16x16x32_bf16 v[4:7], a[140:143], a[12:15], v[4:7]\n"
"ds_write_b64 v134, v[84:85] offset:2304\n"
"v_mfma_f32_16x16x32_bf16 v[164:167], a[144:147], a[0:3], 0\n"
"v_mfma_f32_16x16x32_bf16 v[164:167], a[148:151], a[4:7], v[164:167]\n"
"v_mfma_f32_16x16x32_bf16 v[164:167], a[152:155], a[8:11], v[164:167]\n"
"v_cvt_pk_bf16_f32 v4, v4, v5\n"
"v_cvt_pk_bf16_f32 v5, v6, v7\n"
"v_mfma_f32_16x16x32_bf16 v[164:167], a[156:159], a[12:15], v[164:167]\n"
"ds_write_b64 v134, v[88:89] offset:2432\n"
"v_mfma_f32_16x16x32_bf16 v[180:183], a[48:51], a[0:3], 0\n"
"ds_read_b64 a[244:245], v163 offset:0\n"
"v_mfma_f32_16x16x32_bf16 v[180:183], a[52:55], a[4:7], v[180:183]\n"
"v_mfma_f32_16x16x32_bf16 v[180:183], a[56:59], a[8:11], v[180:183]\n"
"v_cvt_pk_bf16_f32 v6, v164, v165\n"
"v_cvt_pk_bf16_f32 v7, v166, v167\n"
"v_mfma_f32_16x16x32_bf16 v[180:183], a[60:63], a[12:15], v[180:183]\n"
"buffer_load_dwordx4 v152, s[24:27], s50 offen offset:2176 lds\n"
"ds_write_b128 v46, v[236:239] offset:49536\n"
"v_mfma_f32_16x16x32_bf16 v[176:179], a[64:67], a[0:3], 0\n"
"ds_read_b128 v[236:239], v153 offset:16384\n"
"v_mfma_f32_16x16x32_bf16 v[176:179], a[68:71], a[4:7], v[176:179]\n"
"s_nop 0\n"
"v_mul_f32 v180, s72, v180\n"
"v_mul_f32 v181, s72, v181\n"
"v_mfma_f32_16x16x32_bf16 v[176:179], a[72:75], a[8:11], v[176:179]\n"
"v_mul_f32 v182, s72, v182\n"
"v_mul_f32 v183, s72, v183\n"
"v_mfma_f32_16x16x32_bf16 v[176:179], a[76:79], a[12:15], v[176:179]\n"
"ds_write_b128 v46, v[240:243] offset:49600\n"
"v_mfma_f32_16x16x32_bf16 v[184:187], a[80:83], a[0:3], 0\n"
"ds_read_b128 v[240:243], v153 offset:20576\n"
"v_cvt_pk_bf16_f32 v180, v180, v181\n"
"v_mfma_f32_16x16x32_bf16 v[184:187], a[84:87], a[4:7], v[184:187]\n"
"v_cvt_pk_bf16_f32 v181, v182, v183\n"
"v_mul_f32 v176, s72, v176\n"
"v_mfma_f32_16x16x32_bf16 v[184:187], a[88:91], a[8:11], v[184:187]\n"
"v_mul_f32 v177, s72, v177\n"
"v_mul_f32 v178, s72, v178\n"
"v_mfma_f32_16x16x32_bf16 v[184:187], a[92:95], a[12:15], v[184:187]\n"
"ds_write_b128 v46, v[244:247] offset:49664\n"
"v_mfma_f32_16x16x32_bf16 v[168:171], a[224:227], a[0:3], 0\n"
"buffer_load_dwordx4 v152, s[24:27], s51 offen offset:3264 lds\n"
"ds_read_b128 v[244:247], v153 offset:24768\n"
"v_mfma_f32_16x16x32_bf16 v[168:171], a[228:231], a[4:7], v[168:171]\n"
"v_mul_f32 v179, s72, v179\n"
"v_cvt_pk_bf16_f32 v182, v176, v177\n"
"v_mfma_f32_16x16x32_bf16 v[168:171], a[232:235], a[8:11], v[168:171]\n"
"v_cvt_pk_bf16_f32 v183, v178, v179\n"
"v_mul_f32 v184, s72, v184\n"
"v_mfma_f32_16x16x32_bf16 v[168:171], a[236:239], a[12:15], v[168:171]\n"
"ds_write_b128 v46, v[248:251] offset:49728\n"
"v_mfma_f32_16x16x32_bf16 v[48:51], a[0:3], a[160:163], 0\n"
"ds_read_b128 v[248:251], v153 offset:28960\n"
"v_mul_f32 v185, s72, v185\n"
"v_mfma_f32_16x16x32_bf16 v[48:51], a[4:7], a[164:167], v[48:51]\n"
"v_mul_f32 v186, s72, v186\n"
"v_mul_f32 v187, s72, v187\n"
"v_mfma_f32_16x16x32_bf16 v[48:51], a[8:11], a[168:171], v[48:51]\n"
"v_cvt_pk_bf16_f32 v184, v184, v185\n"
"v_cvt_pk_bf16_f32 v185, v186, v187\n"
"v_mfma_f32_16x16x32_bf16 v[48:51], a[12:15], a[172:175], v[48:51]\n"
"ds_write_b128 v45, v[116:119] offset:1024\n"
"v_mfma_f32_16x16x32_bf16 v[60:63], a[0:3], a[176:179], 0\n"
"v_mul_f32 v168, s72, v168\n"
"v_mul_f32 v169, s72, v169\n"
"v_mfma_f32_16x16x32_bf16 v[60:63], a[4:7], a[180:183], v[60:63]\n"
"s_add_u32 m0, s28, 0\n"
"s_nop 0\n"
"buffer_load_dwordx4 v152, s[24:27], s52 offen offset:0 lds\n"
"v_mfma_f32_16x16x32_bf16 v[60:63], a[8:11], a[184:187], v[60:63]\n"
"s_waitcnt vmcnt(5)\n"
"ds_read_b128 a[16:19], v154 offset:4352\n"
"ds_read_b128 a[20:23], v154 offset:4416\n"
"v_mfma_f32_16x16x32_bf16 v[60:63], a[12:15], a[188:191], v[60:63]\n"
"ds_read_b128 a[24:27], v154 offset:4480\n"
"ds_read_b128 a[28:31], v154 offset:4544\n"
"v_mfma_f32_16x16x32_bf16 v[72:75], a[0:3], a[192:195], 0\n"
"v_mul_f32 v170, s72, v170\n"
"v_mul_f32 v171, s72, v171\n"
"v_mfma_f32_16x16x32_bf16 v[72:75], a[4:7], a[196:199], v[72:75]\n"
"v_cvt_pk_bf16_f32 v186, v168, v169\n"
"v_cvt_pk_bf16_f32 v187, v170, v171\n"
"v_mfma_f32_16x16x32_bf16 v[72:75], a[8:11], a[200:203], v[72:75]\n"
"v_cvt_pk_bf16_f32 v48, v48, v49\n"
"v_cvt_pk_bf16_f32 v49, v50, v51\n"
"v_mfma_f32_16x16x32_bf16 v[72:75], a[12:15], a[204:207], v[72:75]\n"
"ds_write_b128 v45, v[120:123] offset:2048\n"
"v_mfma_f32_16x16x32_bf16 v[84:87], a[0:3], a[208:211], 0\n"
"v_cvt_pk_bf16_f32 v60, v60, v61\n"
"v_cvt_pk_bf16_f32 v61, v62, v63\n"
"v_mfma_f32_16x16x32_bf16 v[84:87], a[4:7], a[212:215], v[84:87]\n"
"s_nop 0\n"
"v_cvt_pk_bf16_f32 v72, v72, v73\n"
"v_cvt_pk_bf16_f32 v73, v74, v75\n"
"v_mfma_f32_16x16x32_bf16 v[84:87], a[8:11], a[216:219], v[84:87]\n"
"v_mfma_f32_16x16x32_bf16 v[84:87], a[12:15], a[220:223], v[84:87]\n"
"s_waitcnt lgkmcnt(4)\n"
"buffer_load_dwordx4 v152, s[24:27], s53 offen offset:1088 lds\n"
"ds_write_b128 v45, v[124:127] offset:3072\n"
"v_mfma_f32_16x16x32_bf16 v[8:11], a[96:99], a[16:19], 0\n"
"s_waitcnt lgkmcnt(4)\n"
"v_mfma_f32_16x16x32_bf16 v[8:11], a[100:103], a[20:23], v[8:11]\n"
"s_nop 0\n"
"v_cvt_pk_bf16_f32 v84, v84, v85\n"
"v_cvt_pk_bf16_f32 v85, v86, v87\n"
"s_waitcnt lgkmcnt(2)\n"
"v_mfma_f32_16x16x32_bf16 v[8:11], a[104:107], a[24:27], v[8:11]\n"
"v_mfma_f32_16x16x32_bf16 v[8:11], a[108:111], a[28:31], v[8:11]\n"
"v_mfma_f32_16x16x32_bf16 v[176:179], a[112:115], a[16:19], 0\n"
"v_mfma_f32_16x16x32_bf16 v[176:179], a[116:119], a[20:23], v[176:179]\n"
"v_mfma_f32_16x16x32_bf16 v[176:179], a[120:123], a[24:27], v[176:179]\n"
"v_cvt_pk_bf16_f32 v8, v8, v9\n"
"v_cvt_pk_bf16_f32 v9, v10, v11\n"
"v_mfma_f32_16x16x32_bf16 v[176:179], a[124:127], a[28:31], v[176:179]\n"
"v_mfma_f32_16x16x32_bf16 v[12:15], a[128:131], a[16:19], 0\n"
"v_mfma_f32_16x16x32_bf16 v[12:15], a[132:135], a[20:23], v[12:15]\n"
"v_mfma_f32_16x16x32_bf16 v[12:15], a[136:139], a[24:27], v[12:15]\n"
"v_cvt_pk_bf16_f32 v10, v176, v177\n"
"v_cvt_pk_bf16_f32 v11, v178, v179\n"
"v_mfma_f32_16x16x32_bf16 v[12:15], a[140:143], a[28:31], v[12:15]\n"
"v_mfma_f32_16x16x32_bf16 v[168:171], a[144:147], a[16:19], 0\n"
"v_mfma_f32_16x16x32_bf16 v[168:171], a[148:151], a[20:23], v[168:171]\n"
"v_mfma_f32_16x16x32_bf16 v[168:171], a[152:155], a[24:27], v[168:171]\n"
"v_cvt_pk_bf16_f32 v12, v12, v13\n"
"v_cvt_pk_bf16_f32 v13, v14, v15\n"
"v_mfma_f32_16x16x32_bf16 v[168:171], a[156:159], a[28:31], v[168:171]\n"
"s_waitcnt lgkmcnt(0)\n"
"s_barrier\n"
"v_mfma_f32_16x16x32_bf16 v[188:191], a[48:51], a[16:19], 0\n"
"buffer_load_dwordx4 v152, s[24:27], s54 offen offset:2176 lds\n"
"s_nop 3\n"
"v_cvt_pk_bf16_f32 v14, v168, v169\n"
"v_mfma_f32_16x16x32_bf16 v[188:191], a[52:55], a[20:23], v[188:191]\n"
"v_cvt_pk_bf16_f32 v15, v170, v171\n"
"v_mfma_f32_16x16x32_bf16 v[188:191], a[56:59], a[24:27], v[188:191]\n"
"v_mfma_f32_16x16x32_bf16 v[188:191], a[60:63], a[28:31], v[188:191]\n"
"v_mfma_f32_16x16x32_bf16 v[176:179], a[64:67], a[16:19], 0\n"
"v_mfma_f32_16x16x32_bf16 v[176:179], a[68:71], a[20:23], v[176:179]\n"
"v_mfma_f32_16x16x32_bf16 v[176:179], a[72:75], a[24:27], v[176:179]\n"
"v_mul_f32 v188, s72, v188\n"
"v_mul_f32 v189, s72, v189\n"
"v_mfma_f32_16x16x32_bf16 v[176:179], a[76:79], a[28:31], v[176:179]\n"
"v_mul_f32 v190, s72, v190\n"
"v_mul_f32 v191, s72, v191\n"
"v_mfma_f32_16x16x32_bf16 v[192:195], a[80:83], a[16:19], 0\n"
"v_cvt_pk_bf16_f32 v188, v188, v189\n"
"v_cvt_pk_bf16_f32 v189, v190, v191\n"
"v_mfma_f32_16x16x32_bf16 v[192:195], a[84:87], a[20:23], v[192:195]\n"
"v_mfma_f32_16x16x32_bf16 v[192:195], a[88:91], a[24:27], v[192:195]\n"
"buffer_load_dwordx4 v152, s[24:27], s55 offen offset:3264 lds\n"
"v_mul_f32 v176, s72, v176\n"
"v_mfma_f32_16x16x32_bf16 v[192:195], a[92:95], a[28:31], v[192:195]\n"
"v_mul_f32 v177, s72, v177\n"
"v_mul_f32 v178, s72, v178\n"
"v_mfma_f32_16x16x32_bf16 v[164:167], a[224:227], a[16:19], 0\n"
"v_mul_f32 v179, s72, v179\n"
"v_cvt_pk_bf16_f32 v190, v176, v177\n"
"v_mfma_f32_16x16x32_bf16 v[164:167], a[228:231], a[20:23], v[164:167]\n"
"v_cvt_pk_bf16_f32 v191, v178, v179\n"
"v_mfma_f32_16x16x32_bf16 v[164:167], a[232:235], a[24:27], v[164:167]\n"
"v_mul_f32 v192, s72, v192\n"
"v_mul_f32 v193, s72, v193\n"
"v_mfma_f32_16x16x32_bf16 v[164:167], a[236:239], a[28:31], v[164:167]\n"
"v_mul_f32 v194, s72, v194\n"
"v_mul_f32 v195, s72, v195\n"
"v_mfma_f32_16x16x32_bf16 v[172:175], a[28:31], a[172:175], 0\n"
"v_cvt_pk_bf16_f32 v192, v192, v193\n"
"v_cvt_pk_bf16_f32 v193, v194, v195\n"
"v_mfma_f32_16x16x32_bf16 v[172:175], a[24:27], a[168:171], v[172:175]\n"
"v_mfma_f32_16x16x32_bf16 v[172:175], a[20:23], a[164:167], v[172:175]\n"
"v_mul_f32 v164, s72, v164\n"
"v_mul_f32 v165, s72, v165\n"
"v_mfma_f32_16x16x32_bf16 v[172:175], a[16:19], a[160:163], v[172:175]\n"
"v_mul_f32 v166, s72, v166\n"
"v_mul_f32 v167, s72, v167\n"
"v_mfma_f32_16x16x32_bf16 v[176:179], a[28:31], a[188:191], 0\n"
"v_cvt_pk_bf16_f32 v194, v164, v165\n"
"v_cvt_pk_bf16_f32 v195, v166, v167\n"
"v_mfma_f32_16x16x32_bf16 v[176:179], a[24:27], a[184:187], v[176:179]\n"
"s_add_u32 m0, s28, 4352\n"
"s_nop 0\n"
"buffer_load_dwordx4 v152, s[24:27], s56 offen offset:0 lds\n"
"v_mfma_f32_16x16x32_bf16 v[176:179], a[20:23], a[180:183], v[176:179]\n"
"s_waitcnt vmcnt(5)\n"
"ds_read_b128 a[32:35], v154 offset:8704\n"
"ds_read_b128 a[36:39], v154 offset:8768\n"
"v_mfma_f32_16x16x32_bf16 v[176:179], a[16:19], a[176:179], v[176:179]\n"
"ds_read_b128 a[40:43], v154 offset:8832\n"
"ds_read_b128 a[44:47], v154 offset:8896\n"
"v_cvt_pk_bf16_f32 v50, v172, v173\n"
"v_cvt_pk_bf16_f32 v51, v174, v175\n"
"v_mfma_f32_16x16x32_bf16 v[172:175], a[28:31], a[204:207], 0\n"
"v_mfma_f32_16x16x32_bf16 v[172:175], a[24:27], a[200:203], v[172:175]\n"
"v_cvt_pk_bf16_f32 v62, v176, v177\n"
"v_mfma_f32_16x16x32_bf16 v[172:175], a[20:23], a[196:199], v[172:175]\n"
"v_cvt_pk_bf16_f32 v63, v178, v179\n"
"v_mfma_f32_16x16x32_bf16 v[172:175], a[16:19], a[192:195], v[172:175]\n"
"v_mfma_f32_16x16x32_bf16 v[176:179], a[28:31], a[220:223], 0\n"
"v_mfma_f32_16x16x32_bf16 v[176:179], a[24:27], a[216:219], v[176:179]\n"
"v_mfma_f32_16x16x32_bf16 v[176:179], a[20:23], a[212:215], v[176:179]\n"
"v_cvt_pk_bf16_f32 v74, v172, v173\n"
"v_cvt_pk_bf16_f32 v75, v174, v175\n"
"v_mfma_f32_16x16x32_bf16 v[176:179], a[16:19], a[208:211], v[176:179]\n"
"s_waitcnt lgkmcnt(3)\n"
"buffer_load_dwordx4 v152, s[24:27], s57 offen offset:1088 lds\n"
"v_mfma_f32_16x16x32_bf16 v[196:199], a[48:51], a[32:35], 0\n"
"s_waitcnt lgkmcnt(2)\n"
"v_mfma_f32_16x16x32_bf16 v[196:199], a[52:55], a[36:39], v[196:199]\n"
"s_nop 0\n"
"v_cvt_pk_bf16_f32 v86, v176, v177\n"
"s_waitcnt lgkmcnt(1)\n"
"v_mfma_f32_16x16x32_bf16 v[196:199], a[56:59], a[40:43], v[196:199]\n"
"s_waitcnt lgkmcnt(0)\n"
"v_cvt_pk_bf16_f32 v87, v178, v179\n"
"v_mfma_f32_16x16x32_bf16 v[196:199], a[60:63], a[44:47], v[196:199]\n"
"v_mfma_f32_16x16x32_bf16 v[176:179], a[64:67], a[32:35], 0\n"
"v_mfma_f32_16x16x32_bf16 v[176:179], a[68:71], a[36:39], v[176:179]\n"
"v_mfma_f32_16x16x32_bf16 v[176:179], a[72:75], a[40:43], v[176:179]\n"
"v_mul_f32 v196, s72, v196\n"
"v_mul_f32 v197, s72, v197\n"
"v_mfma_f32_16x16x32_bf16 v[176:179], a[76:79], a[44:47], v[176:179]\n"
"v_mul_f32 v198, s72, v198\n"
"v_mul_f32 v199, s72, v199\n"
"v_mfma_f32_16x16x32_bf16 v[200:203], a[80:83], a[32:35], 0\n"
"v_cvt_pk_bf16_f32 v196, v196, v197\n"
"v_cvt_pk_bf16_f32 v197, v198, v199\n"
"v_mfma_f32_16x16x32_bf16 v[200:203], a[84:87], a[36:39], v[200:203]\n"
"v_mfma_f32_16x16x32_bf16 v[200:203], a[88:91], a[40:43], v[200:203]\n"
"buffer_load_dwordx4 v152, s[24:27], s58 offen offset:2176 lds\n"
"v_mul_f32 v176, s72, v176\n"
"v_mfma_f32_16x16x32_bf16 v[200:203], a[92:95], a[44:47], v[200:203]\n"
"v_mul_f32 v177, s72, v177\n"
"v_mul_f32 v178, s72, v178\n"
"v_mfma_f32_16x16x32_bf16 v[168:171], a[224:227], a[32:35], 0\n"
"ds_read_b128 a[224:227], v153 offset:0\n"
"v_mul_f32 v179, s72, v179\n"
"v_mfma_f32_16x16x32_bf16 v[168:171], a[228:231], a[36:39], v[168:171]\n"
"ds_read_b128 a[228:231], v161 offset:0\n"
"v_cvt_pk_bf16_f32 v198, v176, v177\n"
"v_mfma_f32_16x16x32_bf16 v[168:171], a[232:235], a[40:43], v[168:171]\n"
"ds_read_b128 a[232:235], v161 offset:2048\n"
"v_cvt_pk_bf16_f32 v199, v178, v179\n"
"v_mfma_f32_16x16x32_bf16 v[168:171], a[236:239], a[44:47], v[168:171]\n"
"ds_read_b64 a[236:237], v156 offset:0\n"
"ds_read_b64 a[238:239], v156 offset:64\n"
"v_mfma_f32_16x16x32_bf16 v[16:19], a[96:99], a[32:35], 0\n"
"v_mul_f32 v200, s72, v200\n"
"v_mul_f32 v201, s72, v201\n"
"v_mfma_f32_16x16x32_bf16 v[16:19], a[100:103], a[36:39], v[16:19]\n"
"v_mul_f32 v202, s72, v202\n"
"v_mul_f32 v203, s72, v203\n"
"v_mfma_f32_16x16x32_bf16 v[16:19], a[104:107], a[40:43], v[16:19]\n"
"v_cvt_pk_bf16_f32 v200, v200, v201\n"
"v_cvt_pk_bf16_f32 v201, v202, v203\n"
"v_mfma_f32_16x16x32_bf16 v[16:19], a[108:111], a[44:47], v[16:19]\n"
"v_mul_f32 v168, s72, v168\n"
"v_mul_f32 v169, s72, v169\n"
"v_mfma_f32_16x16x32_bf16 v[176:179], a[112:115], a[32:35], 0\n"
"v_mul_f32 v170, s72, v170\n"
"v_mul_f32 v171, s72, v171\n"
"v_mfma_f32_16x16x32_bf16 v[176:179], a[116:119], a[36:39], v[176:179]\n"
"v_cvt_pk_bf16_f32 v202, v168, v169\n"
"v_cvt_pk_bf16_f32 v203, v170, v171\n"
"v_mfma_f32_16x16x32_bf16 v[176:179], a[120:123], a[40:43], v[176:179]\n"
"v_cvt_pk_bf16_f32 v16, v16, v17\n"
"v_cvt_pk_bf16_f32 v17, v18, v19\n"
"v_mfma_f32_16x16x32_bf16 v[176:179], a[124:127], a[44:47], v[176:179]\n"
"v_add_u32 v156, 512, v156\n"
"v_add_u32 v161, 64, v161\n"
"v_mfma_f32_16x16x32_bf16 v[20:23], a[128:131], a[32:35], 0\n"
"v_mfma_f32_16x16x32_bf16 v[20:23], a[132:135], a[36:39], v[20:23]\n"
"v_mfma_f32_16x16x32_bf16 v[20:23], a[136:139], a[40:43], v[20:23]\n"
"v_cvt_pk_bf16_f32 v18, v176, v177\n"
"v_cvt_pk_bf16_f32 v19, v178, v179\n"
"v_mfma_f32_16x16x32_bf16 v[20:23], a[140:143], a[44:47], v[20:23]\n"
"v_mfma_f32_16x16x32_bf16 v[164:167], a[144:147], a[32:35], 0\n"
"v_mfma_f32_16x16x32_bf16 v[164:167], a[148:151], a[36:39], v[164:167]\n"
"v_mfma_f32_16x16x32_bf16 v[164:167], a[152:155], a[40:43], v[164:167]\n"
"buffer_load_dwordx4 v152, s[24:27], s59 offen offset:3264 lds\n"
"v_cvt_pk_bf16_f32 v20, v20, v21\n"
"v_mfma_f32_16x16x32_bf16 v[164:167], a[156:159], a[44:47], v[164:167]\n"
"v_cvt_pk_bf16_f32 v21, v22, v23\n"
"v_mfma_f32_16x16x32_bf16 v[52:55], a[32:35], a[160:163], 0\n"
"v_mfma_f32_16x16x32_bf16 v[52:55], a[36:39], a[164:167], v[52:55]\n"
"v_mfma_f32_16x16x32_bf16 v[52:55], a[40:43], a[168:171], v[52:55]\n"
"v_cvt_pk_bf16_f32 v22, v164, v165\n"
"v_cvt_pk_bf16_f32 v23, v166, v167\n"
"v_mfma_f32_16x16x32_bf16 v[52:55], a[44:47], a[172:175], v[52:55]\n"
"v_mfma_f32_16x16x32_bf16 v[64:67], a[32:35], a[176:179], 0\n"
"v_mfma_f32_16x16x32_bf16 v[64:67], a[36:39], a[180:183], v[64:67]\n"
"v_mfma_f32_16x16x32_bf16 v[64:67], a[40:43], a[184:187], v[64:67]\n"
"v_cvt_pk_bf16_f32 v52, v52, v53\n"
"v_cvt_pk_bf16_f32 v53, v54, v55\n"
"v_mfma_f32_16x16x32_bf16 v[64:67], a[44:47], a[188:191], v[64:67]\n"
"v_mfma_f32_16x16x32_bf16 v[76:79], a[32:35], a[192:195], 0\n"
"v_mfma_f32_16x16x32_bf16 v[76:79], a[36:39], a[196:199], v[76:79]\n"
"s_add_u32 m0, s28, 8704\n"
"s_nop 0\n"
"buffer_load_dwordx4 v152, s[24:27], s60 offen offset:0 lds\n"
"v_mfma_f32_16x16x32_bf16 v[76:79], a[40:43], a[200:203], v[76:79]\n"
"s_waitcnt vmcnt(5)\n"
"ds_read_b128 a[48:51], v154 offset:0\n"
"ds_read_b128 a[52:55], v154 offset:64\n"
"v_mfma_f32_16x16x32_bf16 v[76:79], a[44:47], a[204:207], v[76:79]\n"
"ds_read_b128 a[56:59], v154 offset:128\n"
"ds_read_b128 a[60:63], v154 offset:192\n"
"v_mfma_f32_16x16x32_bf16 v[88:91], a[32:35], a[208:211], 0\n"
"v_cvt_pk_bf16_f32 v64, v64, v65\n"
"v_cvt_pk_bf16_f32 v65, v66, v67\n"
"v_mfma_f32_16x16x32_bf16 v[88:91], a[36:39], a[212:215], v[88:91]\n"
"v_mfma_f32_16x16x32_bf16 v[88:91], a[40:43], a[216:219], v[88:91]\n"
"v_cvt_pk_bf16_f32 v76, v76, v77\n"
"v_cvt_pk_bf16_f32 v77, v78, v79\n"
"v_mfma_f32_16x16x32_bf16 v[88:91], a[44:47], a[220:223], v[88:91]\n"
"s_nop 7\n"
"v_cvt_pk_bf16_f32 v88, v88, v89\n"
"v_cvt_pk_bf16_f32 v89, v90, v91\n"
"buffer_load_dwordx4 v152, s[24:27], s61 offen offset:1088 lds\n"
"s_waitcnt lgkmcnt(3)\n"
"v_mfma_f32_16x16x32_bf16 v[24:27], a[96:99], a[48:51], 0\n"
"s_waitcnt lgkmcnt(2)\n"
"v_mfma_f32_16x16x32_bf16 v[24:27], a[100:103], a[52:55], v[24:27]\n"
"s_waitcnt lgkmcnt(1)\n"
"v_mfma_f32_16x16x32_bf16 v[24:27], a[104:107], a[56:59], v[24:27]\n"
"s_waitcnt lgkmcnt(0)\n"
"v_mfma_f32_16x16x32_bf16 v[24:27], a[108:111], a[60:63], v[24:27]\n"
"v_mfma_f32_16x16x32_bf16 v[172:175], a[60:63], a[172:175], 0\n"
"ds_read_b128 a[172:175], v153 offset:62112\n"
"v_mfma_f32_16x16x32_bf16 v[172:175], a[56:59], a[168:171], v[172:175]\n"
"ds_read_b128 a[168:171], v153 offset:57920\n"
"v_mfma_f32_16x16x32_bf16 v[172:175], a[52:55], a[164:167], v[172:175]\n"
"ds_read_b128 a[164:167], v153 offset:53728\n"
"v_cvt_pk_bf16_f32 v24, v24, v25\n"
"v_mfma_f32_16x16x32_bf16 v[172:175], a[48:51], a[160:163], v[172:175]\n"
"ds_read_b128 a[160:163], v153 offset:49536\n"
"v_cvt_pk_bf16_f32 v25, v26, v27\n"
"v_mfma_f32_16x16x32_bf16 v[176:179], a[60:63], a[188:191], 0\n"
"ds_read_b128 a[188:191], v153 offset:63168\n"
"v_mfma_f32_16x16x32_bf16 v[176:179], a[56:59], a[184:187], v[176:179]\n"
"ds_read_b128 a[184:187], v153 offset:58976\n"
"buffer_load_dwordx4 v152, s[24:27], s62 offen offset:2176 lds\n"
"v_mfma_f32_16x16x32_bf16 v[176:179], a[52:55], a[180:183], v[176:179]\n"
"ds_read_b128 a[180:183], v153 offset:54784\n"
"v_cvt_pk_bf16_f32 v54, v172, v173\n"
"v_mfma_f32_16x16x32_bf16 v[176:179], a[48:51], a[176:179], v[176:179]\n"
"ds_read_b128 a[176:179], v153 offset:50592\n"
"v_cvt_pk_bf16_f32 v55, v174, v175\n"
"v_mfma_f32_16x16x32_bf16 v[172:175], a[60:63], a[204:207], 0\n"
"ds_read_b128 a[204:207], v153 offset:64224\n"
"v_mfma_f32_16x16x32_bf16 v[172:175], a[56:59], a[200:203], v[172:175]\n"
"ds_read_b128 a[200:203], v153 offset:60032\n"
"v_mfma_f32_16x16x32_bf16 v[172:175], a[52:55], a[196:199], v[172:175]\n"
"ds_read_b128 a[196:199], v153 offset:55840\n"
"v_cvt_pk_bf16_f32 v66, v176, v177\n"
"v_mfma_f32_16x16x32_bf16 v[172:175], a[48:51], a[192:195], v[172:175]\n"
"ds_read_b128 a[192:195], v153 offset:51648\n"
"v_cvt_pk_bf16_f32 v67, v178, v179\n"
"v_mfma_f32_16x16x32_bf16 v[176:179], a[60:63], a[220:223], 0\n"
"ds_read_b128 a[220:223], v153 offset:65280\n"
"v_mfma_f32_16x16x32_bf16 v[176:179], a[56:59], a[216:219], v[176:179]\n"
"ds_read_b128 a[216:219], v153 offset:61088\n"
"v_mfma_f32_16x16x32_bf16 v[176:179], a[52:55], a[212:215], v[176:179]\n"
"ds_read_b128 a[212:215], v153 offset:56896\n"
"v_cvt_pk_bf16_f32 v78, v172, v173\n"
"v_mfma_f32_16x16x32_bf16 v[176:179], a[48:51], a[208:211], v[176:179]\n"
"ds_read_b128 a[208:211], v153 offset:52704\n"
"v_cvt_pk_bf16_f32 v79, v174, v175\n"
"v_mfma_f32_16x16x32_bf16 v[28:31], a[128:131], a[48:51], 0\n"
"buffer_load_dwordx4 v152, s[24:27], s63 offen offset:3264 lds\n"
"v_mfma_f32_16x16x32_bf16 v[28:31], a[132:135], a[52:55], v[28:31]\n"
"v_mfma_f32_16x16x32_bf16 v[28:31], a[136:139], a[56:59], v[28:31]\n"
"v_cvt_pk_bf16_f32 v90, v176, v177\n"
"v_cvt_pk_bf16_f32 v91, v178, v179\n"
"v_mfma_f32_16x16x32_bf16 v[28:31], a[140:143], a[60:63], v[28:31]\n"
"s_add_u32 s24, s24, 13107200\n"
"s_addc_u32 s25, s25, 0\n"
"s_sub_u32 s26, s26, 13107200\n"
"s_cselect_b32 s26, 0, s26\n"
"v_mfma_f32_16x16x32_bf16 v[176:179], a[112:115], a[48:51], 0\n"
"v_mfma_f32_16x16x32_bf16 v[176:179], a[116:119], a[52:55], v[176:179]\n"
"v_cvt_pk_bf16_f32 v28, v28, v29\n"
"v_mfma_f32_16x16x32_bf16 v[176:179], a[120:123], a[56:59], v[176:179]\n"
"v_cvt_pk_bf16_f32 v29, v30, v31\n"
"v_mfma_f32_16x16x32_bf16 v[176:179], a[124:127], a[60:63], v[176:179]\n"
"v_mfma_f32_16x16x32_bf16 v[168:171], a[144:147], a[48:51], 0\n"
"v_mfma_f32_16x16x32_bf16 v[168:171], a[148:151], a[52:55], v[168:171]\n"
"v_mfma_f32_16x16x32_bf16 v[168:171], a[152:155], a[56:59], v[168:171]\n"
"v_cvt_pk_bf16_f32 v26, v176, v177\n"
"v_cvt_pk_bf16_f32 v27, v178, v179\n"
"v_mfma_f32_16x16x32_bf16 v[168:171], a[156:159], a[60:63], v[168:171]\n"
"s_waitcnt lgkmcnt(0)\n"
"s_barrier\n"
"v_mfma_f32_16x16x32_bf16 v[96:99], v[204:207], a[160:163], 0\n"
"s_nop 4\n"
"v_cvt_pk_bf16_f32 v30, v168, v169\n"
"v_cvt_pk_bf16_f32 v31, v170, v171\n"
"v_mfma_f32_16x16x32_bf16 v[96:99], v[208:211], a[164:167], v[96:99]\n"
"v_mfma_f32_16x16x32_bf16 v[96:99], v[212:215], a[168:171], v[96:99]\n"
"v_mfma_f32_16x16x32_bf16 v[96:99], v[216:219], a[172:175], v[96:99]\n"
"v_mfma_f32_16x16x32_bf16 v[100:103], v[204:207], a[176:179], 0\n"
"v_mfma_f32_16x16x32_bf16 v[100:103], v[208:211], a[180:183], v[100:103]\n"
"s_add_u32 m0, s28, 0\n"
"s_nop 0\n"
"buffer_load_dwordx4 v152, s[24:27], s40 offen offset:0 lds\n"
"v_mfma_f32_16x16x32_bf16 v[100:103], v[212:215], a[184:187], v[100:103]\n"
"s_waitcnt vmcnt(5)\n"
"ds_read_b128 a[64:67], v154 offset:4352\n"
"ds_read_b128 a[68:71], v154 offset:4416\n"
"v_mfma_f32_16x16x32_bf16 v[100:103], v[216:219], a[188:191], v[100:103]\n"
"ds_read_b128 a[72:75], v154 offset:4480\n"
"ds_read_b128 a[76:79], v154 offset:4544\n"
"v_mfma_f32_16x16x32_bf16 v[104:107], v[204:207], a[192:195], 0\n"
"v_mfma_f32_16x16x32_bf16 v[104:107], v[208:211], a[196:199], v[104:107]\n"
"v_mfma_f32_16x16x32_bf16 v[104:107], v[212:215], a[200:203], v[104:107]\n"
"v_cvt_pk_bf16_f32 v96, v96, v100\n"
"v_cvt_pk_bf16_f32 v100, v97, v101\n"
"v_mfma_f32_16x16x32_bf16 v[104:107], v[216:219], a[204:207], v[104:107]\n"
"v_cvt_pk_bf16_f32 v98, v98, v102\n"
"v_cvt_pk_bf16_f32 v102, v99, v103\n"
"v_mfma_f32_16x16x32_bf16 v[108:111], v[204:207], a[208:211], 0\n"
"v_mfma_f32_16x16x32_bf16 v[108:111], v[208:211], a[212:215], v[108:111]\n"
"v_mfma_f32_16x16x32_bf16 v[108:111], v[212:215], a[216:219], v[108:111]\n"
"v_mfma_f32_16x16x32_bf16 v[108:111], v[216:219], a[220:223], v[108:111]\n"
"v_accvgpr_read_b32 v219, a240\n"
"v_mfma_f32_16x16x32_bf16 v[112:115], v[232:235], a[172:175], 0\n"
"buffer_load_dwordx4 v152, s[24:27], s41 offen offset:1088 lds\n"
"v_mfma_f32_16x16x32_bf16 v[112:115], v[228:231], a[168:171], v[112:115]\n"
"v_mfma_f32_16x16x32_bf16 v[112:115], v[224:227], a[164:167], v[112:115]\n"
"v_cvt_pk_bf16_f32 v97, v104, v108\n"
"v_cvt_pk_bf16_f32 v101, v105, v109\n"
"v_mfma_f32_16x16x32_bf16 v[112:115], v[220:223], a[160:163], v[112:115]\n"
"ds_write_b64 v134, v[96:97] offset:16384\n"
"v_mfma_f32_16x16x32_bf16 v[116:119], v[232:235], a[188:191], 0\n"
".Lpad1_%=:\n"
"s_cbranch_vccnz .Lpad6_%=\n"
"v_cvt_pk_bf16_f32 v99, v106, v110\n"
"v_cvt_pk_bf16_f32 v103, v107, v111\n"
"v_mfma_f32_16x16x32_bf16 v[116:119], v[228:231], a[184:187], v[116:119]\n"
"v_mfma_f32_16x16x32_bf16 v[116:119], v[224:227], a[180:183], v[116:119]\n"
"v_mfma_f32_16x16x32_bf16 v[116:119], v[220:223], a[176:179], v[116:119]\n"
"ds_write_b64 v134, v[100:101] offset:16512\n"
"v_mfma_f32_16x16x32_bf16 v[108:111], v[0:3], v[188:191], 0\n"
"v_mfma_f32_16x16x32_bf16 v[108:111], v[4:7], v[192:195], v[108:111]\n"
"ds_write_b64 v134, v[98:99] offset:16640\n"
"v_mfma_f32_16x16x32_bf16 v[96:99], v[0:3], v[180:183], 0\n"
"v_cvt_pk_bf16_f32 v112, v112, v116\n"
"v_cvt_pk_bf16_f32 v116, v113, v117\n"
"v_mfma_f32_16x16x32_bf16 v[96:99], v[4:7], v[184:187], v[96:99]\n"
"buffer_load_dwordx4 v152, s[24:27], s42 offen offset:2176 lds\n"
"ds_write_b64 v134, v[102:103] offset:16768\n"
"v_mfma_f32_16x16x32_bf16 v[100:103], v[16:19], v[180:183], 0\n"
"v_exp_f32 v108, v108\n"
"v_mfma_f32_16x16x32_bf16 v[100:103], v[20:23], v[184:187], v[100:103]\n"
"v_cvt_pk_bf16_f32 v114, v114, v118\n"
"v_exp_f32 v96, v96\n"
"v_mfma_f32_16x16x32_bf16 v[120:123], v[232:235], a[204:207], 0\n"
"v_exp_f32 v97, v97\n"
"v_mfma_f32_16x16x32_bf16 v[120:123], v[228:231], a[200:203], v[120:123]\n"
"v_exp_f32 v98, v98\n"
"v_mfma_f32_16x16x32_bf16 v[120:123], v[224:227], a[196:199], v[120:123]\n"
"v_exp_f32 v99, v99\n"
"v_mfma_f32_16x16x32_bf16 v[120:123], v[220:223], a[192:195], v[120:123]\n"
"v_cvt_pk_bf16_f32 v96, v96, v97\n"
"v_cvt_pk_bf16_f32 v97, v98, v99\n"
"v_mfma_f32_16x16x32_bf16 v[124:127], v[232:235], a[220:223], 0\n"
"v_exp_f32 v100, v100\n"
"v_mfma_f32_16x16x32_bf16 v[124:127], v[228:231], a[216:219], v[124:127]\n"
"v_mov_b32 v228, 0\n"
"v_mov_b32 v229, 0\n"
"v_mfma_f32_16x16x32_bf16 v[124:127], v[224:227], a[212:215], v[124:127]\n"
"v_mov_b32 v224, 0\n"
"v_mov_b32 v225, 0\n"
"v_mfma_f32_16x16x32_bf16 v[124:127], v[220:223], a[208:211], v[124:127]\n"
"buffer_load_dwordx4 v152, s[24:27], s43 offen offset:3264 lds\n"
"v_mov_b32 v220, 0\n"
"v_smfmac_f32_16x16x64_bf16 v[224:227], a[236:239], v[8:15], v157\n"
"v_mov_b32 v221, 0\n"
"v_smfmac_f32_16x16x64_bf16 v[228:231], a[236:239], v[16:23], v157\n"
"v_exp_f32 v101, v101\n"
"v_smfmac_f32_16x16x64_bf16 v[220:223], a[236:239], v[0:7], v157\n"
"s_waitcnt lgkmcnt(0)\n"
"v_cvt_pk_bf16_f32 v100, v100, v101\n"
"v_mfma_f32_16x16x32_bf16 v[32:35], a[96:99], a[64:67], 0\n"
"v_add_f32 v209, v224, v225\n"
"v_mov_b32 v224, 0\n"
"v_mfma_f32_16x16x32_bf16 v[32:35], a[100:103], a[68:71], v[32:35]\n"
"v_exp_f32 v209, v209\n"
"v_mfma_f32_16x16x32_bf16 v[32:35], a[104:107], a[72:75], v[32:35]\n"
"v_add_f32 v208, v220, v221\n"
"v_add_f32 v210, v228, v229\n"
"v_mfma_f32_16x16x32_bf16 v[32:35], a[108:111], a[76:79], v[32:35]\n"
"v_exp_f32 v208, v208\n"
"v_mfma_f32_16x16x32_bf16 v[176:179], a[112:115], a[64:67], 0\n"
"v_cvt_pk_bf16_f32 v215, v208, v209\n"
"v_mov_b32 v220, 0\n"
"v_mfma_f32_16x16x32_bf16 v[176:179], a[116:119], a[68:71], v[176:179]\n"
"ds_write_b16 v219, v215 offset:0\n"
"v_mfma_f32_16x16x32_bf16 v[176:179], a[120:123], a[72:75], v[176:179]\n"
"s_add_u32 m0, s28, 4352\n"
"s_nop 0\n"
"buffer_load_dwordx4 v152, s[24:27], s44 offen offset:0 lds\n"
"v_mfma_f32_16x16x32_bf16 v[176:179], a[124:127], a[76:79], v[176:179]\n"
"s_waitcnt vmcnt(5)\n"
"ds_read_b128 a[80:83], v154 offset:8704\n"
"ds_read_b128 a[84:87], v154 offset:8768\n"
"v_mfma_f32_16x16x32_bf16 v[36:39], a[128:131], a[64:67], 0\n"
"ds_read_b128 a[88:91], v154 offset:8832\n"
"ds_read_b128 a[92:95], v154 offset:8896\n"
"v_mfma_f32_16x16x32_bf16 v[36:39], a[132:135], a[68:71], v[36:39]\n"
"ds_write_b16_d16_hi v219, v215 offset:4\n"
"v_mfma_f32_16x16x32_bf16 v[36:39], a[136:139], a[72:75], v[36:39]\n"
"v_exp_f32 v210, v210\n"
"v_mfma_f32_16x16x32_bf16 v[36:39], a[140:143], a[76:79], v[36:39]\n"
"v_mov_b32 v221, 0\n"
"v_mov_b32 v225, 0\n"
"v_mfma_f32_16x16x32_bf16 v[164:167], a[144:147], a[64:67], 0\n"
"v_mov_b32 v228, 0\n"
"v_mov_b32 v229, 0\n"
"v_mfma_f32_16x16x32_bf16 v[164:167], a[148:151], a[68:71], v[164:167]\n"
"v_add_f32 v208, v209, v208\n"
"v_add_f32 v208, v210, v208\n"
"v_mfma_f32_16x16x32_bf16 v[164:167], a[152:155], a[72:75], v[164:167]\n"
"v_exp_f32 v102, v102\n"
"v_mfma_f32_16x16x32_bf16 v[164:167], a[156:159], a[76:79], v[164:167]\n"
"v_exp_f32 v103, v103\n"
"v_smfmac_f32_16x16x64_bf16 v[220:223], a[236:239], v[24:31], v157\n"
"v_cvt_pk_bf16_f32 v101, v102, v103\n"
"v_mfma_f32_16x16x32_bf16 v[56:59], a[64:67], v[236:239], 0\n"
"v_cvt_pk_bf16_f32 v113, v120, v124\n"
"v_cvt_pk_bf16_f32 v117, v121, v125\n"
"v_mfma_f32_16x16x32_bf16 v[56:59], a[68:71], v[240:243], v[56:59]\n"
"v_cvt_pk_bf16_f32 v118, v115, v119\n"
"v_cvt_pk_bf16_f32 v115, v122, v126\n"
"v_mfma_f32_16x16x32_bf16 v[56:59], a[72:75], v[244:247], v[56:59]\n"
"v_add_f32 v211, v220, v221\n"
"v_mov_b32 v220, 0\n"
"v_mfma_f32_16x16x32_bf16 v[56:59], a[76:79], v[248:251], v[56:59]\n"
"s_waitcnt lgkmcnt(1)\n"
"ds_write_b64 v134, v[112:113] offset:18432\n"
"v_mfma_f32_16x16x32_bf16 v[40:43], a[96:99], a[80:83], 0\n"
"v_exp_f32 v211, v211\n"
"v_mfma_f32_16x16x32_bf16 v[40:43], a[100:103], a[84:87], v[40:43]\n"
"v_cvt_pk_bf16_f32 v216, v210, v211\n"
"v_mov_b32 v221, 0\n"
"v_mfma_f32_16x16x32_bf16 v[40:43], a[104:107], a[88:91], v[40:43]\n"
"ds_write_b16 v219, v216 offset:512\n"
"v_mfma_f32_16x16x32_bf16 v[40:43], a[108:111], a[92:95], v[40:43]\n"
"ds_write_b16_d16_hi v219, v216 offset:516\n"
"v_smfmac_f32_16x16x64_bf16 v[220:223], a[236:239], a[228:235], v157\n"
"buffer_load_dwordx4 v152, s[24:27], s45 offen offset:1088 lds\n"
"v_mfma_f32_16x16x32_bf16 v[44:47], a[128:131], a[80:83], 0\n"
"ds_read_b128 a[228:231], v161 offset:0\n"
"ds_read_b128 a[232:235], v161 offset:2048\n"
"v_mfma_f32_16x16x32_bf16 v[44:47], a[132:135], a[84:87], v[44:47]\n"
"v_add_f32 v208, v211, v208\n"
"v_cvt_pk_bf16_f32 v119, v123, v127\n"
"v_mfma_f32_16x16x32_bf16 v[44:47], a[136:139], a[88:91], v[44:47]\n"
"v_add_f32 v214, v220, v221\n"
"v_cvt_pk_bf16_f32 v32, v32, v33\n"
"v_mfma_f32_16x16x32_bf16 v[44:47], a[140:143], a[92:95], v[44:47]\n"
"ds_write_b64 v134, v[116:117] offset:18560\n"
"v_mfma_f32_16x16x32_bf16 v[120:123], v[12:15], v[184:187], 0\n"
"v_exp_f32 v214, v214\n"
"v_mfma_f32_16x16x32_bf16 v[120:123], v[8:11], v[180:183], v[120:123]\n"
"ds_write_b64 v134, v[114:115] offset:18688\n"
"v_mfma_f32_16x16x32_bf16 v[124:127], v[28:31], v[184:187], 0\n"
"v_cvt_pk_bf16_f32 v218, v214, v214\n"
"v_cvt_pk_bf16_f32 v33, v34, v35\n"
"v_mfma_f32_16x16x32_bf16 v[124:127], v[24:27], v[180:183], v[124:127]\n"
"ds_write_b16 v219, v218 offset:2048\n"
"v_mfma_f32_16x16x32_bf16 v[112:115], v[16:19], v[188:191], 0\n"
"v_exp_f32 v120, v120\n"
"v_mfma_f32_16x16x32_bf16 v[112:115], v[20:23], v[192:195], v[112:115]\n"
"buffer_load_dwordx4 v152, s[24:27], s46 offen offset:2176 lds\n"
"ds_write_b64 v134, v[118:119] offset:18816\n"
"v_mfma_f32_16x16x32_bf16 v[168:171], a[144:147], a[80:83], 0\n"
"ds_read_b64 v[206:207], v163 offset:16384\n"
"v_mov_b32 v134, 0\n"
"v_mfma_f32_16x16x32_bf16 v[168:171], a[148:151], a[84:87], v[168:171]\n"
"v_exp_f32 v121, v121\n"
"v_mfma_f32_16x16x32_bf16 v[168:171], a[152:155], a[88:91], v[168:171]\n"
"v_exp_f32 v122, v122\n"
"v_mfma_f32_16x16x32_bf16 v[168:171], a[156:159], a[92:95], v[168:171]\n"
"v_exp_f32 v123, v123\n"
"v_mfma_f32_16x16x32_bf16 v[68:71], a[92:95], v[248:251], 0\n"
"ds_read_b128 v[248:251], v153 offset:30016\n"
"v_cvt_pk_bf16_f32 v98, v120, v121\n"
"v_mfma_f32_16x16x32_bf16 v[68:71], a[88:91], v[244:247], v[68:71]\n"
"ds_read_b128 v[244:247], v153 offset:25824\n"
"v_cvt_pk_bf16_f32 v99, v122, v123\n"
"v_mfma_f32_16x16x32_bf16 v[68:71], a[84:87], v[240:243], v[68:71]\n"
"ds_read_b128 v[240:243], v153 offset:21632\n"
"v_cvt_pk_bf16_f32 v34, v176, v177\n"
"v_mfma_f32_16x16x32_bf16 v[68:71], a[80:83], v[236:239], v[68:71]\n"
"ds_read_b128 v[236:239], v153 offset:17440\n"
"v_exp_f32 v124, v124\n"
"v_mfma_f32_16x16x32_bf16 v[80:83], a[64:67], v[136:139], 0\n"
"v_exp_f32 v125, v125\n"
"v_mfma_f32_16x16x32_bf16 v[80:83], a[68:71], v[140:143], v[80:83]\n"
"v_exp_f32 v126, v126\n"
"v_mfma_f32_16x16x32_bf16 v[80:83], a[72:75], v[144:147], v[80:83]\n"
"buffer_load_dwordx4 v152, s[24:27], s47 offen offset:3264 lds\n"
"v_cvt_pk_bf16_f32 v102, v124, v125\n"
"v_mfma_f32_16x16x32_bf16 v[80:83], a[76:79], v[148:151], v[80:83]\n"
"v_exp_f32 v127, v127\n"
"v_mfma_f32_16x16x32_bf16 v[92:95], a[92:95], v[148:151], 0\n"
"ds_read_b128 v[148:151], v153 offset:32128\n"
"v_cvt_pk_bf16_f32 v103, v126, v127\n"
"v_mfma_f32_16x16x32_bf16 v[92:95], a[88:91], v[144:147], v[92:95]\n"
"ds_read_b128 v[144:147], v153 offset:27936\n"
"v_cvt_pk_bf16_f32 v35, v178, v179\n"
"v_mfma_f32_16x16x32_bf16 v[92:95], a[84:87], v[140:143], v[92:95]\n"
"ds_read_b128 v[140:143], v153 offset:23744\n"
"v_cvt_pk_bf16_f32 v36, v36, v37\n"
"v_mfma_f32_16x16x32_bf16 v[92:95], a[80:83], v[136:139], v[92:95]\n"
"ds_read_b128 v[136:139], v153 offset:19552\n"
"v_cvt_pk_bf16_f32 v37, v38, v39\n"
"v_mfma_f32_16x16x32_bf16 v[104:107], v[32:35], v[180:183], 0\n"
"v_cvt_pk_bf16_f32 v38, v164, v165\n"
"v_cvt_pk_bf16_f32 v39, v166, v167\n"
"v_mfma_f32_16x16x32_bf16 v[124:127], v[12:15], v[192:195], 0\n"
"v_cvt_pk_bf16_f32 v40, v40, v41\n"
"v_cvt_pk_bf16_f32 v41, v42, v43\n"
"v_mfma_f32_16x16x32_bf16 v[124:127], v[8:11], v[188:191], v[124:127]\n"
"v_cvt_pk_bf16_f32 v44, v44, v45\n"
"v_cvt_pk_bf16_f32 v45, v46, v47\n"
"v_smfmac_f32_16x16x64_bf16 v[224:227], a[236:239], v[32:39], v157\n"
"v_cvt_pk_bf16_f32 v46, v168, v169\n"
"v_mfma_f32_16x16x32_bf16 v[104:107], v[36:39], v[184:187], v[104:107]\n"
"v_cvt_pk_bf16_f32 v47, v170, v171\n"
"v_cvt_pk_bf16_f32 v56, v56, v57\n"
"v_mfma_f32_16x16x32_bf16 v[116:119], v[32:35], v[188:191], 0\n"
"s_add_u32 m0, s28, 8704\n"
"s_nop 0\n"
"buffer_load_dwordx4 v152, s[24:27], s48 offen offset:0 lds\n"
"v_mfma_f32_16x16x32_bf16 v[120:123], v[44:47], v[184:187], 0\n"
"v_add_f32 v212, v224, v225\n"
"v_exp_f32 v212, v212\n"
"v_mfma_f32_16x16x32_bf16 v[176:179], a[112:115], a[80:83], 0\n"
"v_add_f32 v208, v212, v208\n"
"v_cvt_pk_bf16_f32 v57, v58, v59\n"
"v_mfma_f32_16x16x32_bf16 v[176:179], a[116:119], a[84:87], v[176:179]\n"
"v_exp_f32 v104, v104\n"
"v_mfma_f32_16x16x32_bf16 v[176:179], a[120:123], a[88:91], v[176:179]\n"
"v_exp_f32 v105, v105\n"
"v_mfma_f32_16x16x32_bf16 v[176:179], a[124:127], a[92:95], v[176:179]\n"
"s_waitcnt lgkmcnt(1)\n"
"v_exp_f32 v106, v106\n"
"v_mfma_f32_16x16x32_bf16 v[172:175], a[92:95], v[248:251], 0\n"
"v_exp_f32 v107, v107\n"
"v_mfma_f32_16x16x32_bf16 v[172:175], a[88:91], v[244:247], v[172:175]\n"
"v_cvt_pk_bf16_f32 v104, v104, v105\n"
"v_cvt_pk_bf16_f32 v105, v106, v107\n"
"v_mfma_f32_16x16x32_bf16 v[172:175], a[84:87], v[240:243], v[172:175]\n"
"v_cvt_pk_bf16_f32 v42, v176, v177\n"
"v_cvt_pk_bf16_f32 v43, v178, v179\n"
"v_mfma_f32_16x16x32_bf16 v[172:175], a[80:83], v[236:239], v[172:175]\n"
"v_cvt_pk_bf16_f32 v58, v68, v69\n"
"v_cvt_pk_bf16_f32 v59, v70, v71\n"
"v_smfmac_f32_16x16x64_bf16 v[228:231], a[236:239], v[40:47], v157\n"
"ds_read_b64 a[236:237], v156 offset:0\n"
"v_mfma_f32_16x16x32_bf16 v[120:123], v[40:43], v[180:183], v[120:123]\n"
"buffer_load_dwordx4 v152, s[24:27], s49 offen offset:1088 lds\n"
"ds_read_b64 a[238:239], v156 offset:64\n"
"v_mfma_f32_16x16x32_bf16 v[68:71], a[64:67], v[236:239], 0\n"
"ds_read_b128 v[236:239], v153 offset:5120\n"
"v_cvt_pk_bf16_f32 v80, v80, v81\n"
"v_mfma_f32_16x16x32_bf16 v[68:71], a[68:71], v[240:243], v[68:71]\n"
"ds_read_b128 v[240:243], v153 offset:9216\n"
"v_add_f32 v213, v228, v229\n"
"v_mfma_f32_16x16x32_bf16 v[68:71], a[72:75], v[244:247], v[68:71]\n"
"ds_read_b128 v[244:247], v153 offset:13312\n"
"v_cvt_pk_bf16_f32 v81, v82, v83\n"
"v_mfma_f32_16x16x32_bf16 v[68:71], a[76:79], v[248:251], v[68:71]\n"
"ds_read_b128 v[248:251], v153 offset:2048\n"
"v_exp_f32 v213, v213\n"
"v_mfma_f32_16x16x32_bf16 v[176:179], a[92:95], v[148:151], 0\n"
"v_cvt_pk_bf16_f32 v217, v212, v213\n"
"v_add_f32 v208, v213, v208\n"
"v_mfma_f32_16x16x32_bf16 v[176:179], a[88:91], v[144:147], v[176:179]\n"
"ds_write_b16 v219, v217 offset:1536\n"
"v_mfma_f32_16x16x32_bf16 v[176:179], a[84:87], v[140:143], v[176:179]\n"
"s_waitcnt lgkmcnt(7)\n"
"ds_write_b16_d16_hi v219, v217 offset:1540\n"
"v_mfma_f32_16x16x32_bf16 v[176:179], a[80:83], v[136:139], v[176:179]\n"
"v_accvgpr_read_b32 v219, a241\n"
"ds_read_b128 v[220:223], v219 offset:0\n"
"ds_read_b128 v[224:227], v219 offset:1024\n"
"v_accvgpr_read_b32 v219, a242\n"
"ds_read2_b32 v[228:229], v219 offset0:0 offset1:4\n"
"v_add_f32_dpp v208, v214, v208 quad_perm:[0,1,2,3] row_mask:0xf bank_mask:0x1\n"
"v_exp_f32 v120, v120\n"
"v_exp_f32 v121, v121\n"
"v_add_f32_dpp v208, v208, v208 row_ror:8 row_mask:0xf bank_mask:0xf\n"
"v_exp_f32 v122, v122\n"
"v_exp_f32 v123, v123\n"
"v_add_f32_dpp v208, v208, v208 row_ror:4 row_mask:0xf bank_mask:0xf\n"
"v_cvt_pk_bf16_f32 v106, v120, v121\n"
"v_cvt_pk_bf16_f32 v107, v122, v123\n"
"v_mfma_f32_16x16x32_bf16 v[120:123], v[28:31], v[192:195], 0\n"
"v_add_f32_dpp v208, v208, v208 row_ror:2 row_mask:0xf bank_mask:0xf\n"
"v_cvt_pk_bf16_f32 v82, v92, v93\n"
"v_mfma_f32_16x16x32_bf16 v[120:123], v[24:27], v[188:191], v[120:123]\n"
"v_add_f32_dpp v208, v208, v208 row_ror:1 row_mask:0xf bank_mask:0xf\n"
"v_rcp_f32 v209, v208\n"
"v_cvt_pk_bf16_f32 v83, v94, v95\n"
"v_mfma_f32_16x16x32_bf16 v[92:95], a[64:67], v[136:139], 0\n"
"ds_read_b128 v[136:139], v153 offset:4096\n"
"v_max3_f32 v159, v159, v208, v209\n"
"v_mfma_f32_16x16x32_bf16 v[92:95], a[68:71], v[140:143], v[92:95]\n"
"ds_read_b128 v[140:143], v153 offset:8192\n"
"v_cvt_pk_bf16_f32 v68, v68, v69\n"
"v_mfma_f32_16x16x32_bf16 v[92:95], a[72:75], v[144:147], v[92:95]\n"
"buffer_load_dwordx4 v152, s[24:27], s50 offen offset:2176 lds\n"
"ds_read2_b32 v[144:145], v158 offset0:0 offset1:2\n"
"v_mfma_f32_16x16x32_bf16 v[92:95], a[76:79], v[148:151], v[92:95]\n"
"ds_read2_b32 v[146:147], v158 offset0:128 offset1:130\n"
"v_mov_b32_dpp v158, v158 row_ror:8 row_mask:0xf bank_mask:0xf\n"
"ds_read2_b32 v[148:149], v158 offset0:0 offset1:2\n"
"ds_read2_b32 v[150:151], v158 offset0:128 offset1:130\n"
"v_cvt_pk_bf16_f32 v69, v70, v71\n"
"v_cvt_pk_bf16_f32 v70, v172, v173\n"
"v_cvt_pk_bf16_f32 v71, v174, v175\n"
"v_add_u32 v163, 512, v163\n"
"v_lshlrev_b32 v204, 16, v206\n"
"v_cvt_pk_bf16_f32 v92, v92, v93\n"
"v_cvt_pk_bf16_f32 v93, v94, v95\n"
"v_cvt_pk_bf16_f32 v94, v176, v177\n"
"v_cvt_pk_bf16_f32 v95, v178, v179\n"
"v_and_b32 v205, 0xffff0000, v206\n"
"v_lshlrev_b32 v206, 16, v207\n"
"v_and_b32 v207, 0xffff0000, v207\n"
"buffer_load_dwordx4 v152, s[24:27], s51 offen offset:3264 lds\n"
"s_waitcnt lgkmcnt(2)\n"
"v_smfmac_f32_16x16x64_bf16 v[128:131], v[144:147], v[180:187], v157\n"
"s_waitcnt lgkmcnt(0)\n"
"v_smfmac_f32_16x16x64_bf16 v[128:131], v[148:151], v[188:195], v157\n"
"ds_read_b128 v[148:151], v153 offset:1024\n"
"v_smfmac_f32_16x16x64_bf16 v[132:135], v[144:147], v[196:203], v157\n"
"ds_read_b128 v[144:147], v153 offset:12288\n"
"s_nop 3\n"
"v_add_f32 v128, v128, v129\n"
"v_add_f32 v130, v130, v131\n"
"v_exp_f32 v128, v128\n"
"v_exp_f32 v130, v130\n"
"s_nop 0\n"
"v_cvt_pk_bf16_f32 v129, v128, v130\n"
"v_add_f32 v128, v128, v130\n"
"s_nop 1\n"
"v_permlane16_swap_b32 v128, v129\n"
"buffer_load_dword a243, v162, s[80:83], s70 offen\n"
"buffer_load_dword a243, v162, s[80:83], s70 offen\n"
"buffer_load_dword a243, v162, s[80:83], s70 offen\n"
"buffer_load_dword a243, v162, s[80:83], s70 offen\n"
"s_waitcnt vmcnt(8) lgkmcnt(0)\n"
"s_mov_b32 s74, 8\n"
"s_nop 11\n"
"s_branch .Lp2b%=\n"
".p2align 12\n"
".Lp2f%=:\n"
"v_mfma_f32_16x16x32_bf16 v[56:59], a[64:67], v[248:251], 0\n"
".Lpad2_%=:\n"
"s_cbranch_vccnz .Lback0_%=\n"
"v_cvt_pk_bf16_f32 v46, v164, v165\n"
"v_cvt_pk_bf16_f32 v47, v166, v167\n"
"v_mfma_f32_16x16x32_bf16 v[56:59], a[68:71], a[224:227], v[56:59]\n"
"v_mfma_f32_16x16x32_bf16 v[56:59], a[72:75], v[136:139], v[56:59]\n"
"v_cvt_pk_bf16_f32 v54, v64, v65\n"
"v_cvt_pk_bf16_f32 v55, v66, v67\n"
"v_mfma_f32_16x16x32_bf16 v[56:59], a[76:79], v[140:143], v[56:59]\n"
"s_waitcnt lgkmcnt(0)\n"
"v_lshlrev_b32 v204, 16, v206\n"
"v_and_b32 v205, 0xffff0000, v206\n"
"v_mfma_f32_16x16x32_bf16 v[68:71], a[92:95], v[140:143], 0\n"
"ds_read_b128 v[140:143], v153 offset:21632\n"
"v_lshlrev_b32 v206, 16, v207\n"
"v_mfma_f32_16x16x32_bf16 v[68:71], a[88:91], v[136:139], v[68:71]\n"
"ds_read_b128 v[136:139], v153 offset:17440\n"
"v_and_b32 v207, 0xffff0000, v207\n"
"v_mfma_f32_16x16x32_bf16 v[68:71], a[84:87], a[224:227], v[68:71]\n"
"ds_read_b128 a[224:227], v153 offset:0\n"
"v_cvt_pk_bf16_f32 v56, v56, v57\n"
"v_mfma_f32_16x16x32_bf16 v[68:71], a[80:83], v[248:251], v[68:71]\n"
"ds_read_b128 v[248:251], v153 offset:31072\n"
"v_cvt_pk_bf16_f32 v57, v58, v59\n"
"v_smfmac_f32_16x16x64_bf16 v[220:223], a[236:239], v[0:7], v157\n"
"v_smfmac_f32_16x16x64_bf16 v[224:227], a[236:239], v[8:15], v157\n"
"v_mfma_f32_16x16x32_bf16 v[96:99], v[0:3], v[180:183], 0\n"
"v_cvt_pk_bf16_f32 v58, v68, v69\n"
"v_cvt_pk_bf16_f32 v59, v70, v71\n"
"v_mfma_f32_16x16x32_bf16 v[96:99], v[4:7], v[184:187], v[96:99]\n"
"v_add_f32 v208, v220, v221\n"
"v_mov_b32 v220, 0\n"
"v_smfmac_f32_16x16x64_bf16 v[228:231], a[236:239], v[16:23], v157\n"
"v_add_f32 v209, v224, v225\n"
"v_smfmac_f32_16x16x64_bf16 v[128:131], v[144:147], v[180:187], v157\n"
"v_mov_b32 v221, 0\n"
"v_smfmac_f32_16x16x64_bf16 v[128:131], v[148:151], v[188:195], v157\n"
"ds_read_b128 v[148:151], v153 offset:30016\n"
"v_smfmac_f32_16x16x64_bf16 v[220:223], a[236:239], v[24:31], v157\n"
"v_add_f32 v210, v228, v229\n"
"v_mfma_f32_16x16x32_bf16 v[120:123], v[12:15], v[184:187], 0\n"
"v_exp_f32 v208, v208\n"
"v_mfma_f32_16x16x32_bf16 v[120:123], v[8:11], v[180:183], v[120:123]\n"
"v_exp_f32 v209, v209\n"
"v_smfmac_f32_16x16x64_bf16 v[132:135], v[144:147], v[196:203], v157\n"
"ds_read_b128 v[144:147], v153 offset:25824\n"
"v_mfma_f32_16x16x32_bf16 v[72:75], a[0:3], v[236:239], 0\n"
"v_cvt_pk_bf16_f32 v215, v208, v209\n"
"v_add_f32 v128, v128, v129\n"
"v_mfma_f32_16x16x32_bf16 v[72:75], a[4:7], v[240:243], v[72:75]\n"
"ds_write_b16 v219, v215 offset:0\n"
"v_mfma_f32_16x16x32_bf16 v[72:75], a[8:11], v[244:247], v[72:75]\n"
"s_waitcnt lgkmcnt(3)\n"
"ds_write_b16_d16_hi v219, v215 offset:4\n"
"v_mfma_f32_16x16x32_bf16 v[72:75], a[12:15], v[248:251], v[72:75]\n"
"v_exp_f32 v210, v210\n"
"v_mfma_f32_16x16x32_bf16 v[84:87], a[28:31], v[248:251], 0\n"
"v_add_f32 v130, v130, v131\n"
"v_add_f32 v211, v220, v221\n"
"v_mfma_f32_16x16x32_bf16 v[84:87], a[24:27], v[244:247], v[84:87]\n"
"v_exp_f32 v128, v128\n"
"v_mfma_f32_16x16x32_bf16 v[84:87], a[20:23], v[240:243], v[84:87]\n"
"v_exp_f32 v130, v130\n"
"v_mfma_f32_16x16x32_bf16 v[84:87], a[16:19], v[236:239], v[84:87]\n"
"v_cvt_pk_bf16_f32 v129, v128, v130\n"
"v_add_f32 v128, v128, v130\n"
"v_mfma_f32_16x16x32_bf16 v[76:79], a[32:35], v[236:239], 0\n"
"v_exp_f32 v211, v211\n"
"v_mfma_f32_16x16x32_bf16 v[76:79], a[36:39], v[240:243], v[76:79]\n"
"v_permlane16_swap_b32 v128, v129\n"
"v_mfma_f32_16x16x32_bf16 v[76:79], a[40:43], v[244:247], v[76:79]\n"
"v_cvt_pk_bf16_f32 v216, v210, v211\n"
"v_cvt_pk_bf16_f32 v72, v72, v73\n"
"v_mfma_f32_16x16x32_bf16 v[76:79], a[44:47], v[248:251], v[76:79]\n"
"ds_write_b16 v219, v216 offset:512\n"
"v_mfma_f32_16x16x32_bf16 v[88:91], a[60:63], v[248:251], 0\n"
"ds_write_b16_d16_hi v219, v216 offset:516\n"
"v_mfma_f32_16x16x32_bf16 v[88:91], a[56:59], v[244:247], v[88:91]\n"
"v_cvt_pk_bf16_f32 v73, v74, v75\n"
"v_cvt_pk_bf16_f32 v74, v84, v85\n"
"v_mfma_f32_16x16x32_bf16 v[88:91], a[52:55], v[240:243], v[88:91]\n"
"v_cvt_pk_bf16_f32 v75, v86, v87\n"
"v_cvt_pk_bf16_f32 v76, v76, v77\n"
"v_mfma_f32_16x16x32_bf16 v[88:91], a[48:51], v[236:239], v[88:91]\n"
"v_cvt_pk_bf16_f32 v77, v78, v79\n"
"v_mov_b32 v224, 0\n"
"v_mfma_f32_16x16x32_bf16 v[80:83], a[64:67], v[236:239], 0\n"
"v_mov_b32 v225, 0\n"
"v_mov_b32 v228, 0\n"
"v_mfma_f32_16x16x32_bf16 v[80:83], a[68:71], v[240:243], v[80:83]\n"
"v_exp_f32 v96, v96\n"
"v_mfma_f32_16x16x32_bf16 v[80:83], a[72:75], v[244:247], v[80:83]\n"
"v_cvt_pk_bf16_f32 v78, v88, v89\n"
"v_cvt_pk_bf16_f32 v79, v90, v91\n"
"v_mfma_f32_16x16x32_bf16 v[80:83], a[76:79], v[248:251], v[80:83]\n"
"v_exp_f32 v97, v97\n"
"v_mfma_f32_16x16x32_bf16 v[92:95], a[92:95], v[248:251], 0\n"
"ds_read_b128 v[248:251], v153 offset:32128\n"
"v_mov_b32 v229, 0\n"
"v_mfma_f32_16x16x32_bf16 v[92:95], a[88:91], v[244:247], v[92:95]\n"
"ds_read_b128 v[244:247], v153 offset:27936\n"
"v_cvt_pk_bf16_f32 v96, v96, v97\n"
"v_mfma_f32_16x16x32_bf16 v[92:95], a[84:87], v[240:243], v[92:95]\n"
"ds_read_b128 v[240:243], v153 offset:23744\n"
"v_cvt_pk_bf16_f32 v80, v80, v81\n"
"v_mfma_f32_16x16x32_bf16 v[92:95], a[80:83], v[236:239], v[92:95]\n"
"ds_read_b128 v[236:239], v153 offset:19552\n"
"v_cvt_pk_bf16_f32 v81, v82, v83\n"
"v_smfmac_f32_16x16x64_bf16 v[224:227], a[236:239], v[32:39], v157\n"
"v_mov_b32 v220, 0\n"
"v_mfma_f32_16x16x32_bf16 v[100:103], v[16:19], v[180:183], 0\n"
"v_exp_f32 v98, v98\n"
"v_mfma_f32_16x16x32_bf16 v[100:103], v[20:23], v[184:187], v[100:103]\n"
"v_cvt_pk_bf16_f32 v82, v92, v93\n"
"v_cvt_pk_bf16_f32 v83, v94, v95\n"
"v_smfmac_f32_16x16x64_bf16 v[228:231], a[236:239], v[40:47], v157\n"
"v_add_f32 v212, v224, v225\n"
"v_mfma_f32_16x16x32_bf16 v[124:127], v[28:31], v[184:187], 0\n"
"v_exp_f32 v212, v212\n"
"v_mfma_f32_16x16x32_bf16 v[124:127], v[24:27], v[180:183], v[124:127]\n"
"v_exp_f32 v99, v99\n"
"v_mfma_f32_16x16x32_bf16 v[104:107], v[32:35], v[180:183], 0\n"
"v_add_f32 v213, v228, v229\n"
"v_cvt_pk_bf16_f32 v97, v98, v99\n"
"v_mfma_f32_16x16x32_bf16 v[104:107], v[36:39], v[184:187], v[104:107]\n"
"v_exp_f32 v213, v213\n"
"v_mfma_f32_16x16x32_bf16 v[60:63], a[0:3], v[136:139], 0\n"
"v_cvt_pk_bf16_f32 v217, v212, v213\n"
"v_mov_b32 v221, 0\n"
"v_mfma_f32_16x16x32_bf16 v[60:63], a[4:7], v[140:143], v[60:63]\n"
"s_waitcnt lgkmcnt(2)\n"
"ds_write_b16 v219, v217 offset:1536\n"
"v_mfma_f32_16x16x32_bf16 v[60:63], a[8:11], v[144:147], v[60:63]\n"
"ds_write_b16_d16_hi v219, v217 offset:1540\n"
"v_mfma_f32_16x16x32_bf16 v[60:63], a[12:15], v[148:151], v[60:63]\n"
"v_exp_f32 v120, v120\n"
"v_smfmac_f32_16x16x64_bf16 v[220:223], a[236:239], a[228:235], v157\n"
"ds_read_b64 a[236:237], v156 offset:0\n"
"v_mfma_f32_16x16x32_bf16 v[164:167], a[28:31], v[148:151], 0\n"
"ds_read_b64 a[238:239], v156 offset:64\n"
"ds_read_b128 a[228:231], v161 offset:0\n"
"v_mfma_f32_16x16x32_bf16 v[164:167], a[24:27], v[144:147], v[164:167]\n"
"ds_read_b128 a[232:235], v161 offset:2048\n"
"v_add_f32 v208, v209, v208\n"
"v_mfma_f32_16x16x32_bf16 v[164:167], a[20:23], v[140:143], v[164:167]\n"
"v_exp_f32 v121, v121\n"
"v_mfma_f32_16x16x32_bf16 v[164:167], a[16:19], v[136:139], v[164:167]\n"
"v_exp_f32 v122, v122\n"
"v_mfma_f32_16x16x32_bf16 v[64:67], a[32:35], v[136:139], 0\n"
"v_exp_f32 v123, v123\n"
"v_mfma_f32_16x16x32_bf16 v[64:67], a[36:39], v[140:143], v[64:67]\n"
"v_add_f32 v214, v220, v221\n"
"v_cvt_pk_bf16_f32 v98, v120, v121\n"
"v_mfma_f32_16x16x32_bf16 v[64:67], a[40:43], v[144:147], v[64:67]\n"
"v_exp_f32 v214, v214\n"
"v_mfma_f32_16x16x32_bf16 v[64:67], a[44:47], v[148:151], v[64:67]\n"
"v_cvt_pk_bf16_f32 v218, v214, v214\n"
"v_cvt_pk_bf16_f32 v99, v122, v123\n"
"v_mfma_f32_16x16x32_bf16 v[120:123], v[44:47], v[184:187], 0\n"
"ds_write_b16 v219, v218 offset:2048\n"
"v_mfma_f32_16x16x32_bf16 v[120:123], v[40:43], v[180:183], v[120:123]\n"
"v_exp_f32 v100, v100\n"
"v_mfma_f32_16x16x32_bf16 v[168:171], a[60:63], v[148:151], 0\n"
"v_exp_f32 v101, v101\n"
"v_mfma_f32_16x16x32_bf16 v[168:171], a[56:59], v[144:147], v[168:171]\n"
"v_accvgpr_read_b32 v219, a241\n"
"v_add_f32 v208, v210, v208\n"
"v_mfma_f32_16x16x32_bf16 v[168:171], a[52:55], v[140:143], v[168:171]\n"
"ds_read_b128 v[220:223], v219 offset:0\n"
"ds_read_b128 v[224:227], v219 offset:1024\n"
"v_mfma_f32_16x16x32_bf16 v[168:171], a[48:51], v[136:139], v[168:171]\n"
"v_accvgpr_read_b32 v219, a242\n"
"v_add_f32 v208, v211, v208\n"
"v_mfma_f32_16x16x32_bf16 v[68:71], a[64:67], v[136:139], 0\n"
"ds_read2_b32 v[228:229], v219 offset0:0 offset1:4\n"
"v_add_f32 v208, v212, v208\n"
"v_mfma_f32_16x16x32_bf16 v[68:71], a[68:71], v[140:143], v[68:71]\n"
"v_add_f32 v208, v213, v208\n"
"v_cvt_pk_bf16_f32 v60, v60, v61\n"
"v_mfma_f32_16x16x32_bf16 v[68:71], a[72:75], v[144:147], v[68:71]\n"
"v_add_f32_dpp v208, v214, v208 quad_perm:[0,1,2,3] row_mask:0xf bank_mask:0x1\n"
"v_cvt_pk_bf16_f32 v61, v62, v63\n"
"v_mfma_f32_16x16x32_bf16 v[68:71], a[76:79], v[148:151], v[68:71]\n"
"v_exp_f32 v102, v102\n"
"v_mfma_f32_16x16x32_bf16 v[172:175], a[92:95], v[148:151], 0\n"
"ds_read_b128 v[148:151], v153 offset:1024\n"
"v_add_f32_dpp v208, v208, v208 row_ror:8 row_mask:0xf bank_mask:0xf\n"
"v_mfma_f32_16x16x32_bf16 v[172:175], a[88:91], v[144:147], v[172:175]\n"
"ds_read_b128 v[144:147], v153 offset:12288\n"
"v_add_f32_dpp v208, v208, v208 row_ror:4 row_mask:0xf bank_mask:0xf\n"
"v_mfma_f32_16x16x32_bf16 v[172:175], a[84:87], v[140:143], v[172:175]\n"
"ds_read_b128 v[140:143], v153 offset:8192\n"
"v_add_f32_dpp v208, v208, v208 row_ror:2 row_mask:0xf bank_mask:0xf\n"
"v_mfma_f32_16x16x32_bf16 v[172:175], a[80:83], v[136:139], v[172:175]\n"
"ds_read_b128 v[136:139], v153 offset:4096\n"
"v_add_f32_dpp v208, v208, v208 row_ror:1 row_mask:0xf bank_mask:0xf\n"
"v_mfma_f32_16x16x32_bf16 v[108:111], v[0:3], v[188:191], 0\n"
"v_rcp_f32 v209, v208\n"
"v_mfma_f32_16x16x32_bf16 v[108:111], v[4:7], v[192:195], v[108:111]\n"
"v_exp_f32 v103, v103\n"
"v_mfma_f32_16x16x32_bf16 v[112:115], v[16:19], v[188:191], 0\n"
"v_max3_f32 v159, v159, v208, v209\n"
"v_cvt_pk_bf16_f32 v62, v164, v165\n"
"v_mfma_f32_16x16x32_bf16 v[112:115], v[20:23], v[192:195], v[112:115]\n"
"s_waitcnt lgkmcnt(5)\n"
"v_cvt_pk_bf16_f32 v63, v166, v167\n"
"v_cvt_pk_bf16_f32 v64, v64, v65\n"
"v_mfma_f32_16x16x32_bf16 v[84:87], a[0:3], v[236:239], 0\n"
"v_cvt_pk_bf16_f32 v65, v66, v67\n"
"v_cvt_pk_bf16_f32 v66, v168, v169\n"
"v_mfma_f32_16x16x32_bf16 v[84:87], a[4:7], v[240:243], v[84:87]\n"
"v_cvt_pk_bf16_f32 v67, v170, v171\n"
"v_cvt_pk_bf16_f32 v68, v68, v69\n"
"v_mfma_f32_16x16x32_bf16 v[84:87], a[8:11], v[244:247], v[84:87]\n"
"v_cvt_pk_bf16_f32 v69, v70, v71\n"
"v_cvt_pk_bf16_f32 v70, v172, v173\n"
"v_mfma_f32_16x16x32_bf16 v[84:87], a[12:15], v[248:251], v[84:87]\n"
"v_cvt_pk_bf16_f32 v71, v174, v175\n"
"v_cvt_pk_bf16_f32 v100, v100, v101\n"
"v_mfma_f32_16x16x32_bf16 v[176:179], a[28:31], v[248:251], 0\n"
"v_exp_f32 v124, v124\n"
"v_mfma_f32_16x16x32_bf16 v[176:179], a[24:27], v[244:247], v[176:179]\n"
"v_cvt_pk_bf16_f32 v101, v102, v103\n"
"v_mfma_f32_16x16x32_bf16 v[176:179], a[20:23], v[240:243], v[176:179]\n"
"v_exp_f32 v125, v125\n"
"v_mfma_f32_16x16x32_bf16 v[176:179], a[16:19], v[236:239], v[176:179]\n"
"v_exp_f32 v126, v126\n"
"v_mfma_f32_16x16x32_bf16 v[88:91], a[32:35], v[236:239], 0\n"
"v_exp_f32 v127, v127\n"
"v_mfma_f32_16x16x32_bf16 v[88:91], a[36:39], v[240:243], v[88:91]\n"
"v_cvt_pk_bf16_f32 v102, v124, v125\n"
"v_cvt_pk_bf16_f32 v103, v126, v127\n"
"v_mfma_f32_16x16x32_bf16 v[88:91], a[40:43], v[244:247], v[88:91]\n"
"v_exp_f32 v104, v104\n"
"v_mfma_f32_16x16x32_bf16 v[88:91], a[44:47], v[248:251], v[88:91]\n"
"v_exp_f32 v105, v105\n"
"v_mfma_f32_16x16x32_bf16 v[124:127], v[12:15], v[192:195], 0\n"
"v_exp_f32 v106, v106\n"
"v_mfma_f32_16x16x32_bf16 v[124:127], v[8:11], v[188:191], v[124:127]\n"
"v_exp_f32 v107, v107\n"
"v_mfma_f32_16x16x32_bf16 v[164:167], a[60:63], v[248:251], 0\n"
"v_cvt_pk_bf16_f32 v104, v104, v105\n"
"v_cvt_pk_bf16_f32 v105, v106, v107\n"
"v_mfma_f32_16x16x32_bf16 v[164:167], a[56:59], v[244:247], v[164:167]\n"
"v_exp_f32 v120, v120\n"
"v_mfma_f32_16x16x32_bf16 v[164:167], a[52:55], v[240:243], v[164:167]\n"
"v_exp_f32 v121, v121\n"
"v_mfma_f32_16x16x32_bf16 v[164:167], a[48:51], v[236:239], v[164:167]\n"
"v_exp_f32 v122, v122\n"
"v_mfma_f32_16x16x32_bf16 v[92:95], a[64:67], v[236:239], 0\n"
"v_exp_f32 v123, v123\n"
"v_mfma_f32_16x16x32_bf16 v[92:95], a[68:71], v[240:243], v[92:95]\n"
"v_cvt_pk_bf16_f32 v106, v120, v121\n"
"v_cvt_pk_bf16_f32 v107, v122, v123\n"
"v_mfma_f32_16x16x32_bf16 v[92:95], a[72:75], v[244:247], v[92:95]\n"
"v_cvt_pk_bf16_f32 v84, v84, v85\n"
"v_cvt_pk_bf16_f32 v85, v86, v87\n"
"v_mfma_f32_16x16x32_bf16 v[92:95], a[76:79], v[248:251], v[92:95]\n"
"v_cvt_pk_bf16_f32 v86, v176, v177\n"
"v_cvt_pk_bf16_f32 v87, v178, v179\n"
"v_mfma_f32_16x16x32_bf16 v[168:171], a[92:95], v[248:251], 0\n"
"ds_read_b128 v[248:251], v153 offset:2048\n"
"v_cvt_pk_bf16_f32 v88, v88, v89\n"
"v_mfma_f32_16x16x32_bf16 v[168:171], a[88:91], v[244:247], v[168:171]\n"
"ds_read_b128 v[244:247], v153 offset:13312\n"
"v_cvt_pk_bf16_f32 v89, v90, v91\n"
"v_mfma_f32_16x16x32_bf16 v[168:171], a[84:87], v[240:243], v[168:171]\n"
"ds_read_b128 v[240:243], v153 offset:9216\n"
"v_cvt_pk_bf16_f32 v90, v164, v165\n"
"v_mfma_f32_16x16x32_bf16 v[168:171], a[80:83], v[236:239], v[168:171]\n"
"ds_read_b128 v[236:239], v153 offset:5120\n"
"v_cvt_pk_bf16_f32 v91, v166, v167\n"
"v_mfma_f32_16x16x32_bf16 v[120:123], v[28:31], v[192:195], 0\n"
"v_cvt_pk_bf16_f32 v92, v92, v93\n"
"v_cvt_pk_bf16_f32 v93, v94, v95\n"
"v_mfma_f32_16x16x32_bf16 v[120:123], v[24:27], v[188:191], v[120:123]\n"
"v_exp_f32 v108, v108\n"
"v_mfma_f32_16x16x32_bf16 v[116:119], v[32:35], v[188:191], 0\n"
"v_cvt_pk_bf16_f32 v94, v168, v169\n"
"v_cvt_pk_bf16_f32 v95, v170, v171\n"
".Lp2b%=:\n"
"v_mfma_f32_16x16x32_bf16 v[116:119], v[36:39], v[192:195], v[116:119]\n"
"s_waitcnt lgkmcnt(4)\n"
"v_exp_f32 v109, v109\n"
"v_exp_f32 v110, v110\n"
"v_mfma_f32_16x16x4_4b_bf16 v[164:179], v[228:229], a[244:245], 0 cbsz:2 abid:0\n"
".Lpad3_%=:\n"
"s_cbranch_vccnz .Lpad7_%=\n"
"v_exp_f32 v111, v111\n"
"v_cvt_pk_bf16_f32 v108, v108, v109\n"
"v_exp_f32 v124, v124\n"
"v_cvt_pk_bf16_f32 v109, v110, v111\n"
"v_exp_f32 v125, v125\n"
"v_mfma_f32_16x16x32_bf16 v[252:255], a[252:255], v[96:99], v[128:131]\n"
"v_exp_f32 v126, v126\n"
"v_mfma_f32_16x16x32_bf16 v[180:183], a[224:227], a[48:51], 0\n"
"v_exp_f32 v127, v127\n"
"v_mfma_f32_16x16x32_bf16 v[180:183], v[136:139], a[52:55], v[180:183]\n"
"v_cvt_pk_bf16_f32 v110, v124, v125\n"
"v_cvt_pk_bf16_f32 v111, v126, v127\n"
"v_mfma_f32_16x16x32_bf16 v[180:183], v[140:143], a[56:59], v[180:183]\n"
"v_exp_f32 v112, v112\n"
"v_mfma_f32_16x16x32_bf16 v[180:183], v[144:147], a[60:63], v[180:183]\n"
"v_exp_f32 v113, v113\n"
"v_mfma_f32_16x16x32_bf16 v[124:127], v[44:47], v[192:195], 0\n"
"v_exp_f32 v114, v114\n"
"v_mfma_f32_16x16x32_bf16 v[124:127], v[40:43], v[188:191], v[124:127]\n"
"s_add_u32 s84, s84, 13107200\n"
"s_addc_u32 s85, s85, 0\n"
"v_smfmac_f32_16x16x64_bf16 v[164:167], v[220:223], v[48:55], v157\n"
"v_cvt_pk_bf16_f32 v112, v112, v113\n"
"v_smfmac_f32_16x16x64_bf16 v[164:167], v[224:227], v[52:59], v157\n"
"v_exp_f32 v115, v115\n"
"v_smfmac_f32_16x16x64_bf16 v[168:171], v[224:227], v[64:71], v157\n"
"v_cvt_pk_bf16_f32 v113, v114, v115\n"
"v_smfmac_f32_16x16x64_bf16 v[168:171], v[220:223], v[60:67], v157\n"
"v_exp_f32 v120, v120\n"
"v_smfmac_f32_16x16x64_bf16 v[172:175], v[220:223], v[72:79], v157\n"
"v_fmac_f32 v204, v209, v164\n"
"v_smfmac_f32_16x16x64_bf16 v[172:175], v[224:227], v[76:83], v157\n"
"v_exp_f32 v121, v121\n"
"v_smfmac_f32_16x16x64_bf16 v[176:179], v[224:227], v[88:95], v157\n"
"v_cvt_pk_bf16_f32 v114, v120, v121\n"
"v_smfmac_f32_16x16x64_bf16 v[176:179], v[220:223], v[84:91], v157\n"
"v_exp_f32 v122, v122\n"
"v_mfma_f32_16x16x4_4b_bf16 v[220:235], v[128:129], a[244:245], 0 cbsz:2 abid:1\n"
"v_exp_f32 v123, v123\n"
"v_exp_f32 v116, v116\n"
"v_cvt_pk_bf16_f32 v115, v122, v123\n"
"v_exp_f32 v117, v117\n"
"v_fmac_f32 v205, v209, v168\n"
"v_smfmac_f32_16x16x64_bf16 v[252:255], a[248:251], v[100:107], v157\n"
"v_fmac_f32 v206, v209, v172\n"
"v_mfma_f32_16x16x32_bf16 v[120:123], v[12:15], v[200:203], 0\n"
"v_exp_f32 v118, v118\n"
"v_mfma_f32_16x16x32_bf16 v[120:123], v[8:11], v[196:199], v[120:123]\n"
"v_fmac_f32 v207, v209, v176\n"
"v_fmac_f32 v204, v209, v165\n"
"v_mfma_f32_16x16x32_bf16 v[220:223], v[96:99], v[48:51], v[220:223]\n"
"v_exp_f32 v119, v119\n"
"v_mfma_f32_16x16x32_bf16 v[220:223], v[100:103], v[52:55], v[220:223]\n"
"v_fmac_f32 v205, v209, v169\n"
"v_fmac_f32 v206, v209, v173\n"
"v_mfma_f32_16x16x32_bf16 v[220:223], v[104:107], v[56:59], v[220:223]\n"
"v_fmac_f32 v207, v209, v177\n"
"v_cvt_pk_bf16_f32 v204, v204, v205\n"
"v_mfma_f32_16x16x32_bf16 v[224:227], v[104:107], v[68:71], v[224:227]\n"
"v_cvt_pk_bf16_f32 v116, v116, v117\n"
"v_cvt_pk_bf16_f32 v205, v206, v207\n"
"v_mfma_f32_16x16x32_bf16 v[224:227], v[100:103], v[64:67], v[224:227]\n"
"v_pk_max_i16 v204, v204, 0\n"
"v_pk_max_i16 v205, v205, 0\n"
"v_mfma_f32_16x16x32_bf16 v[224:227], v[96:99], v[60:63], v[224:227]\n"
"buffer_store_dwordx2 v[204:205], v155, s[84:87], s70 offen offset:0 sc0 sc1\n"
"v_mfma_f32_16x16x32_bf16 v[228:231], v[96:99], v[72:75], v[228:231]\n"
"v_exp_f32 v124, v124\n"
"v_mfma_f32_16x16x32_bf16 v[228:231], v[100:103], v[76:79], v[228:231]\n"
"v_cvt_pk_bf16_f32 v117, v118, v119\n"
"v_add_f32 v252, v252, v253\n"
"v_mfma_f32_16x16x32_bf16 v[228:231], v[104:107], v[80:83], v[228:231]\n"
"v_exp_f32 v125, v125\n"
"v_mfma_f32_16x16x32_bf16 v[232:235], v[104:107], v[92:95], v[232:235]\n"
"v_rcp_f32 v253, v252\n"
"v_mfma_f32_16x16x32_bf16 v[232:235], v[100:103], v[88:91], v[232:235]\n"
"ds_bpermute_b32 v254, v160, v253 offset:0\n"
"v_mfma_f32_16x16x32_bf16 v[232:235], v[96:99], v[84:87], v[232:235]\n"
"v_exp_f32 v126, v126\n"
"v_mfma_f32_16x16x32_bf16 v[96:99], v[0:3], v[196:199], 0\n"
"v_max3_f32 v159, v159, v252, v253\n"
"v_cvt_pk_bf16_f32 v118, v124, v125\n"
"v_mfma_f32_16x16x32_bf16 v[96:99], v[4:7], v[200:203], v[96:99]\n"
"v_exp_f32 v127, v127\n"
"v_mfma_f32_16x16x32_bf16 v[100:103], v[16:19], v[196:199], 0\n"
"v_cvt_pk_bf16_f32 v119, v126, v127\n"
"v_cvt_pk_bf16_f32 v180, v180, v181\n"
"v_mfma_f32_16x16x32_bf16 v[100:103], v[20:23], v[200:203], v[100:103]\n"
"v_exp_f32 v120, v120\n"
"v_cvt_pk_bf16_f32 v181, v182, v183\n"
"v_mfma_f32_16x16x4_4b_bf16 v[204:219], v[128:129], a[244:245], 0 cbsz:2 abid:3\n"
"v_exp_f32 v96, v96\n"
"v_exp_f32 v97, v97\n"
"v_exp_f32 v98, v98\n"
"v_exp_f32 v99, v99\n"
"v_mfma_f32_16x16x32_bf16 v[124:127], v[28:31], v[200:203], 0\n"
"v_cvt_pk_bf16_f32 v96, v96, v97\n"
"v_cvt_pk_bf16_f32 v97, v98, v99\n"
"v_mfma_f32_16x16x32_bf16 v[124:127], v[24:27], v[196:199], v[124:127]\n"
"v_exp_f32 v121, v121\n"
"v_mfma_f32_16x16x32_bf16 v[204:207], v[108:111], v[48:51], v[204:207]\n"
"v_exp_f32 v122, v122\n"
"v_mfma_f32_16x16x32_bf16 v[204:207], v[112:115], v[52:55], v[204:207]\n"
"s_waitcnt lgkmcnt(0)\n"
"v_mul_f32_dpp v220, v254, v220 quad_perm:[0,0,0,0] row_mask:0xf bank_mask:0xf\n"
"v_mul_f32_dpp v221, v254, v221 quad_perm:[1,1,1,1] row_mask:0xf bank_mask:0xf\n"
"v_mfma_f32_16x16x32_bf16 v[204:207], v[116:119], v[56:59], v[204:207]\n"
"v_mul_f32_dpp v222, v254, v222 quad_perm:[2,2,2,2] row_mask:0xf bank_mask:0xf\n"
"v_mul_f32_dpp v223, v254, v223 quad_perm:[3,3,3,3] row_mask:0xf bank_mask:0xf\n"
"v_mfma_f32_16x16x32_bf16 v[208:211], v[116:119], v[68:71], v[208:211]\n"
"v_mul_f32_dpp v224, v254, v224 quad_perm:[0,0,0,0] row_mask:0xf bank_mask:0xf\n"
"v_mul_f32_dpp v225, v254, v225 quad_perm:[1,1,1,1] row_mask:0xf bank_mask:0xf\n"
"v_mfma_f32_16x16x32_bf16 v[208:211], v[112:115], v[64:67], v[208:211]\n"
"v_mul_f32_dpp v226, v254, v226 quad_perm:[2,2,2,2] row_mask:0xf bank_mask:0xf\n"
"v_mul_f32_dpp v227, v254, v227 quad_perm:[3,3,3,3] row_mask:0xf bank_mask:0xf\n"
"v_mfma_f32_16x16x32_bf16 v[208:211], v[108:111], v[60:63], v[208:211]\n"
"v_mul_f32_dpp v228, v254, v228 quad_perm:[0,0,0,0] row_mask:0xf bank_mask:0xf\n"
"v_mul_f32_dpp v229, v254, v229 quad_perm:[1,1,1,1] row_mask:0xf bank_mask:0xf\n"
"v_mfma_f32_16x16x32_bf16 v[220:223], a[0:3], a[160:163], v[220:223]\n"
"v_mul_f32_dpp v230, v254, v230 quad_perm:[2,2,2,2] row_mask:0xf bank_mask:0xf\n"
"v_mul_f32_dpp v231, v254, v231 quad_perm:[3,3,3,3] row_mask:0xf bank_mask:0xf\n"
"v_mfma_f32_16x16x32_bf16 v[220:223], a[4:7], a[164:167], v[220:223]\n"
"v_mul_f32_dpp v232, v254, v232 quad_perm:[0,0,0,0] row_mask:0xf bank_mask:0xf\n"
"v_mul_f32_dpp v233, v254, v233 quad_perm:[1,1,1,1] row_mask:0xf bank_mask:0xf\n"
"v_mfma_f32_16x16x32_bf16 v[220:223], a[8:11], a[168:171], v[220:223]\n"
"v_mul_f32_dpp v234, v254, v234 quad_perm:[2,2,2,2] row_mask:0xf bank_mask:0xf\n"
"v_mul_f32_dpp v235, v254, v235 quad_perm:[3,3,3,3] row_mask:0xf bank_mask:0xf\n"
"v_mfma_f32_16x16x32_bf16 v[220:223], a[12:15], a[172:175], v[220:223]\n"
"v_exp_f32 v123, v123\n"
"v_mfma_f32_16x16x32_bf16 v[224:227], a[12:15], a[188:191], v[224:227]\n"
"v_cvt_pk_bf16_f32 v98, v120, v121\n"
"v_cvt_pk_bf16_f32 v99, v122, v123\n"
"v_mfma_f32_16x16x32_bf16 v[224:227], a[8:11], a[184:187], v[224:227]\n"
"v_exp_f32 v100, v100\n"
"v_mfma_f32_16x16x32_bf16 v[224:227], a[4:7], a[180:183], v[224:227]\n"
"buffer_load_dword a243, v162, s[80:83], s88 offen sc1\n"
"v_mfma_f32_16x16x32_bf16 v[224:227], a[0:3], a[176:179], v[224:227]\n"
"v_exp_f32 v101, v101\n"
"v_mfma_f32_16x16x32_bf16 v[228:231], a[0:3], a[192:195], v[228:231]\n"
"v_exp_f32 v102, v102\n"
"v_mfma_f32_16x16x32_bf16 v[228:231], a[4:7], a[196:199], v[228:231]\n"
"v_exp_f32 v103, v103\n"
"v_mfma_f32_16x16x32_bf16 v[228:231], a[8:11], a[200:203], v[228:231]\n"
"v_cvt_pk_bf16_f32 v220, v220, v224\n"
"v_cvt_pk_bf16_f32 v224, v221, v225\n"
"v_mfma_f32_16x16x32_bf16 v[228:231], a[12:15], a[204:207], v[228:231]\n"
"v_pk_max_i16 v220, v220, 0\n"
"v_pk_max_i16 v224, v224, 0\n"
"v_mfma_f32_16x16x32_bf16 v[232:235], a[12:15], a[220:223], v[232:235]\n"
"s_waitcnt vmcnt(18)\n"
"ds_read_b128 a[12:15], v154 offset:192\n"
"v_cvt_pk_bf16_f32 v222, v222, v226\n"
"v_mfma_f32_16x16x32_bf16 v[232:235], a[8:11], a[216:219], v[232:235]\n"
"ds_read_b128 a[8:11], v154 offset:128\n"
"v_cvt_pk_bf16_f32 v226, v223, v227\n"
"v_mfma_f32_16x16x32_bf16 v[232:235], a[4:7], a[212:215], v[232:235]\n"
"ds_read_b128 a[4:7], v154 offset:64\n"
"v_pk_max_i16 v222, v222, 0\n"
"v_mfma_f32_16x16x32_bf16 v[232:235], a[0:3], a[208:211], v[232:235]\n"
"ds_read_b128 a[0:3], v154 offset:0\n"
"v_pk_max_i16 v226, v226, 0\n"
"v_mfma_f32_16x16x32_bf16 v[252:255], a[252:255], v[108:111], v[128:131]\n"
"v_cvt_pk_bf16_f32 v100, v100, v101\n"
"v_cvt_pk_bf16_f32 v101, v102, v103\n"
"v_mfma_f32_16x16x32_bf16 v[104:107], v[32:35], v[196:199], 0\n"
"v_exp_f32 v124, v124\n"
"v_mfma_f32_16x16x32_bf16 v[104:107], v[36:39], v[200:203], v[104:107]\n"
"v_cvt_pk_bf16_f32 v221, v228, v232\n"
"v_cvt_pk_bf16_f32 v225, v229, v233\n"
"v_smfmac_f32_16x16x64_bf16 v[252:255], a[248:251], v[112:119], v157\n"
"v_pk_max_i16 v221, v221, 0\n"
"v_mfma_f32_16x16x32_bf16 v[212:215], v[108:111], v[72:75], v[212:215]\n"
"v_pk_max_i16 v225, v225, 0\n"
"v_cvt_pk_bf16_f32 v223, v230, v234\n"
"v_mfma_f32_16x16x32_bf16 v[212:215], v[112:115], v[76:79], v[212:215]\n"
"v_cvt_pk_bf16_f32 v227, v231, v235\n"
"v_pk_max_i16 v223, v223, 0\n"
"v_mfma_f32_16x16x32_bf16 v[212:215], v[116:119], v[80:83], v[212:215]\n"
"buffer_store_dwordx2 v[220:221], v162, s[84:87], 0 offen offset:0 sc0 sc1\n"
"v_mfma_f32_16x16x32_bf16 v[216:219], v[116:119], v[92:95], v[216:219]\n"
"v_pk_max_i16 v227, v227, 0\n"
"v_add_f32 v252, v252, v253\n"
"v_mfma_f32_16x16x32_bf16 v[216:219], v[112:115], v[88:91], v[216:219]\n"
"v_rcp_f32 v253, v252\n"
"v_mfma_f32_16x16x32_bf16 v[216:219], v[108:111], v[84:87], v[216:219]\n"
"ds_bpermute_b32 v254, v160, v253 offset:128\n"
"v_mfma_f32_16x16x32_bf16 v[120:123], v[44:47], v[200:203], 0\n"
"v_max3_f32 v159, v159, v252, v253\n"
"v_mov_b32 v128, 0\n"
"v_mfma_f32_16x16x32_bf16 v[120:123], v[40:43], v[196:199], v[120:123]\n"
"v_exp_f32 v125, v125\n"
"v_mfma_f32_16x16x32_bf16 v[188:191], v[144:147], a[76:79], 0\n"
"v_exp_f32 v126, v126\n"
"v_mfma_f32_16x16x32_bf16 v[188:191], v[140:143], a[72:75], v[188:191]\n"
"v_exp_f32 v127, v127\n"
"v_mfma_f32_16x16x32_bf16 v[188:191], v[136:139], a[68:71], v[188:191]\n"
"v_cvt_pk_bf16_f32 v102, v124, v125\n"
"v_cvt_pk_bf16_f32 v103, v126, v127\n"
"v_mfma_f32_16x16x32_bf16 v[188:191], a[224:227], a[64:67], v[188:191]\n"
"buffer_store_dwordx2 v[224:225], v162, s[84:87], 0 offen offset:256 sc0 sc1\n"
"v_mfma_f32_16x16x32_bf16 v[196:199], a[224:227], a[80:83], 0\n"
"ds_read_b128 a[224:227], v153 offset:6144\n"
"s_add_u32 m0, s28, 0\n"
"v_mfma_f32_16x16x32_bf16 v[196:199], v[136:139], a[84:87], v[196:199]\n"
"s_waitcnt lgkmcnt(2)\n"
"buffer_load_dwordx4 v152, s[24:27], s52 offen offset:0 lds\n"
"v_mfma_f32_16x16x32_bf16 v[196:199], v[140:143], a[88:91], v[196:199]\n"
"ds_read_b128 v[136:139], v153 offset:10240\n"
"ds_read_b128 v[140:143], v153 offset:14336\n"
"v_mfma_f32_16x16x32_bf16 v[196:199], v[144:147], a[92:95], v[196:199]\n"
"ds_read_b128 v[144:147], v153 offset:3072\n"
"v_cvt_pk_bf16_f32 v188, v188, v189\n"
"v_mfma_f32_16x16x32_bf16 v[184:187], v[148:151], a[48:51], 0\n"
"buffer_load_dwordx4 v152, s[24:27], s53 offen offset:1088 lds\n"
"v_mfma_f32_16x16x32_bf16 v[184:187], v[236:239], a[52:55], v[184:187]\n"
"s_waitcnt lgkmcnt(4)\n"
"v_mul_f32_dpp v204, v254, v204 quad_perm:[0,0,0,0] row_mask:0xf bank_mask:0xf\n"
"v_mul_f32_dpp v205, v254, v205 quad_perm:[1,1,1,1] row_mask:0xf bank_mask:0xf\n"
"v_mfma_f32_16x16x32_bf16 v[184:187], v[240:243], a[56:59], v[184:187]\n"
"v_mul_f32_dpp v206, v254, v206 quad_perm:[2,2,2,2] row_mask:0xf bank_mask:0xf\n"
"v_mul_f32_dpp v207, v254, v207 quad_perm:[3,3,3,3] row_mask:0xf bank_mask:0xf\n"
"v_mfma_f32_16x16x32_bf16 v[184:187], v[244:247], a[60:63], v[184:187]\n"
"buffer_store_dwordx2 v[222:223], v162, s[84:87], 0 offen offset:512 sc0 sc1\n"
"v_mfma_f32_16x16x32_bf16 v[192:195], v[244:247], a[76:79], 0\n"
"v_mul_f32_dpp v208, v254, v208 quad_perm:[0,0,0,0] row_mask:0xf bank_mask:0xf\n"
"v_mul_f32_dpp v209, v254, v209 quad_perm:[1,1,1,1] row_mask:0xf bank_mask:0xf\n"
"v_mfma_f32_16x16x32_bf16 v[192:195], v[240:243], a[72:75], v[192:195]\n"
"buffer_load_dwordx4 v152, s[24:27], s54 offen offset:2176 lds\n"
"v_mfma_f32_16x16x32_bf16 v[192:195], v[236:239], a[68:71], v[192:195]\n"
"v_mul_f32_dpp v210, v254, v210 quad_perm:[2,2,2,2] row_mask:0xf bank_mask:0xf\n"
"v_cvt_pk_bf16_f32 v189, v190, v191\n"
"v_mfma_f32_16x16x32_bf16 v[192:195], v[148:151], a[64:67], v[192:195]\n"
"v_cvt_pk_bf16_f32 v196, v196, v197\n"
"v_cvt_pk_bf16_f32 v197, v198, v199\n"
"v_mfma_f32_16x16x32_bf16 v[200:203], v[148:151], a[80:83], 0\n"
"ds_read_b128 v[148:151], v153 offset:7168\n"
"v_mul_f32_dpp v211, v254, v211 quad_perm:[3,3,3,3] row_mask:0xf bank_mask:0xf\n"
"v_mfma_f32_16x16x32_bf16 v[200:203], v[236:239], a[84:87], v[200:203]\n"
"ds_read_b128 v[236:239], v153 offset:11264\n"
"v_mul_f32_dpp v212, v254, v212 quad_perm:[0,0,0,0] row_mask:0xf bank_mask:0xf\n"
"v_mfma_f32_16x16x32_bf16 v[200:203], v[240:243], a[88:91], v[200:203]\n"
"buffer_load_dwordx4 v152, s[24:27], s55 offen offset:3264 lds\n"
"v_mfma_f32_16x16x32_bf16 v[200:203], v[244:247], a[92:95], v[200:203]\n"
"buffer_store_dwordx2 v[226:227], v162, s[84:87], 0 offen offset:768 sc0 sc1\n"
"v_mfma_f32_16x16x32_bf16 v[204:207], a[16:19], a[160:163], v[204:207]\n"
"ds_read_b128 v[240:243], v153 offset:15360\n"
"ds_read2_b32 v[244:245], v158 offset0:0 offset1:2\n"
"v_mfma_f32_16x16x32_bf16 v[204:207], a[20:23], a[164:167], v[204:207]\n"
"ds_read2_b32 v[246:247], v158 offset0:128 offset1:130\n"
"v_mul_f32_dpp v213, v254, v213 quad_perm:[1,1,1,1] row_mask:0xf bank_mask:0xf\n"
"v_mfma_f32_16x16x32_bf16 v[204:207], a[24:27], a[168:171], v[204:207]\n"
"v_mul_f32_dpp v214, v254, v214 quad_perm:[2,2,2,2] row_mask:0xf bank_mask:0xf\n"
"v_mul_f32_dpp v215, v254, v215 quad_perm:[3,3,3,3] row_mask:0xf bank_mask:0xf\n"
"v_mfma_f32_16x16x32_bf16 v[204:207], a[28:31], a[172:175], v[204:207]\n"
"v_mul_f32_dpp v216, v254, v216 quad_perm:[0,0,0,0] row_mask:0xf bank_mask:0xf\n"
"v_mul_f32_dpp v217, v254, v217 quad_perm:[1,1,1,1] row_mask:0xf bank_mask:0xf\n"
"v_mfma_f32_16x16x32_bf16 v[208:211], a[28:31], a[188:191], v[208:211]\n"
"v_mul_f32_dpp v218, v254, v218 quad_perm:[2,2,2,2] row_mask:0xf bank_mask:0xf\n"
"v_mul_f32_dpp v219, v254, v219 quad_perm:[3,3,3,3] row_mask:0xf bank_mask:0xf\n"
"v_mfma_f32_16x16x32_bf16 v[208:211], a[24:27], a[184:187], v[208:211]\n"
"v_cvt_pk_bf16_f32 v182, v184, v185\n"
"v_cvt_pk_bf16_f32 v183, v186, v187\n"
"v_mfma_f32_16x16x32_bf16 v[208:211], a[20:23], a[180:183], v[208:211]\n"
"v_cvt_pk_bf16_f32 v190, v192, v193\n"
"v_cvt_pk_bf16_f32 v191, v194, v195\n"
"v_mfma_f32_16x16x32_bf16 v[208:211], a[16:19], a[176:179], v[208:211]\n"
"v_cvt_pk_bf16_f32 v198, v200, v201\n"
"v_cvt_pk_bf16_f32 v199, v202, v203\n"
"v_mfma_f32_16x16x32_bf16 v[184:187], v[248:251], a[48:51], 0\n"
"s_waitcnt lgkmcnt(4)\n"
"v_exp_f32 v104, v104\n"
"v_mfma_f32_16x16x32_bf16 v[184:187], a[224:227], a[52:55], v[184:187]\n"
"v_exp_f32 v105, v105\n"
"v_mfma_f32_16x16x32_bf16 v[184:187], v[136:139], a[56:59], v[184:187]\n"
"v_exp_f32 v106, v106\n"
"v_mfma_f32_16x16x32_bf16 v[184:187], v[140:143], a[60:63], v[184:187]\n"
"v_exp_f32 v107, v107\n"
"v_mfma_f32_16x16x32_bf16 v[192:195], v[140:143], a[76:79], 0\n"
"v_cvt_pk_bf16_f32 v104, v104, v105\n"
"v_cvt_pk_bf16_f32 v105, v106, v107\n"
"v_mfma_f32_16x16x32_bf16 v[192:195], v[136:139], a[72:75], v[192:195]\n"
"v_exp_f32 v120, v120\n"
"v_mfma_f32_16x16x32_bf16 v[192:195], a[224:227], a[68:71], v[192:195]\n"
"v_cvt_pk_bf16_f32 v184, v184, v185\n"
"v_cvt_pk_bf16_f32 v185, v186, v187\n"
"v_mfma_f32_16x16x32_bf16 v[192:195], v[248:251], a[64:67], v[192:195]\n"
"v_exp_f32 v121, v121\n"
"v_mfma_f32_16x16x32_bf16 v[200:203], v[248:251], a[80:83], 0\n"
"v_exp_f32 v122, v122\n"
"v_mfma_f32_16x16x32_bf16 v[200:203], a[224:227], a[84:87], v[200:203]\n"
"ds_read_b128 a[224:227], v153 offset:2048\n"
"v_cvt_pk_bf16_f32 v204, v204, v208\n"
"v_mfma_f32_16x16x32_bf16 v[200:203], v[136:139], a[88:91], v[200:203]\n"
"ds_read_b128 v[136:139], v153 offset:0\n"
"v_cvt_pk_bf16_f32 v192, v192, v193\n"
"v_mfma_f32_16x16x32_bf16 v[200:203], v[140:143], a[92:95], v[200:203]\n"
"ds_read_b128 v[140:143], v153 offset:4096\n"
"v_cvt_pk_bf16_f32 v193, v194, v195\n"
"v_mfma_f32_16x16x32_bf16 v[164:167], v[144:147], a[48:51], 0\n"
".Lpad4_%=:\n"
"s_cbranch_vccnz .Lpad2_%=\n"
"v_exp_f32 v123, v123\n"
"v_mfma_f32_16x16x32_bf16 v[164:167], v[148:151], a[52:55], v[164:167]\n"
"s_waitcnt lgkmcnt(3)\n"
"v_cvt_pk_bf16_f32 v208, v205, v209\n"
"v_pk_max_i16 v204, v204, 0\n"
"v_mfma_f32_16x16x32_bf16 v[164:167], v[236:239], a[56:59], v[164:167]\n"
"v_cvt_pk_bf16_f32 v200, v200, v201\n"
"v_cvt_pk_bf16_f32 v201, v202, v203\n"
"v_mfma_f32_16x16x32_bf16 v[164:167], v[240:243], a[60:63], v[164:167]\n"
"v_pk_max_i16 v208, v208, 0\n"
"v_cvt_pk_bf16_f32 v106, v120, v121\n"
"v_mfma_f32_16x16x32_bf16 v[168:171], v[240:243], a[76:79], 0\n"
"v_cvt_pk_bf16_f32 v206, v206, v210\n"
"v_cvt_pk_bf16_f32 v210, v207, v211\n"
"v_mfma_f32_16x16x32_bf16 v[168:171], v[236:239], a[72:75], v[168:171]\n"
"v_cvt_pk_bf16_f32 v107, v122, v123\n"
"v_pk_max_i16 v206, v206, 0\n"
"v_mfma_f32_16x16x32_bf16 v[168:171], v[148:151], a[68:71], v[168:171]\n"
"v_cvt_pk_bf16_f32 v186, v164, v165\n"
"v_cvt_pk_bf16_f32 v187, v166, v167\n"
"v_mfma_f32_16x16x32_bf16 v[168:171], v[144:147], a[64:67], v[168:171]\n"
"v_mov_b32_dpp v158, v158 row_ror:8 row_mask:0xf bank_mask:0xf\n"
"v_pk_max_i16 v210, v210, 0\n"
"v_mfma_f32_16x16x32_bf16 v[172:175], v[144:147], a[80:83], 0\n"
"ds_read2_b32 v[248:249], v158 offset0:0 offset1:2\n"
"ds_read2_b32 v[250:251], v158 offset0:128 offset1:130\n"
"v_mfma_f32_16x16x32_bf16 v[172:175], v[148:151], a[84:87], v[172:175]\n"
"ds_read_b128 v[144:147], v153 offset:8192\n"
"ds_read_b128 v[148:151], v153 offset:12288\n"
"v_mfma_f32_16x16x32_bf16 v[172:175], v[236:239], a[88:91], v[172:175]\n"
"ds_read_b128 v[236:239], v153 offset:1024\n"
"v_cvt_pk_bf16_f32 v194, v168, v169\n"
"v_mfma_f32_16x16x32_bf16 v[172:175], v[240:243], a[92:95], v[172:175]\n"
"ds_read_b128 v[240:243], v153 offset:5120\n"
"v_cvt_pk_bf16_f32 v195, v170, v171\n"
"v_mfma_f32_16x16x32_bf16 v[212:215], a[16:19], a[192:195], v[212:215]\n"
"v_mov_b32 v129, 0\n"
"v_mov_b32 v130, 0\n"
"v_mfma_f32_16x16x32_bf16 v[212:215], a[20:23], a[196:199], v[212:215]\n"
"v_mov_b32 v131, 0\n"
"v_add_u32 v158, 64, v158\n"
"v_mfma_f32_16x16x32_bf16 v[212:215], a[24:27], a[200:203], v[212:215]\n"
"v_cvt_pk_bf16_f32 v202, v172, v173\n"
"v_cvt_pk_bf16_f32 v203, v174, v175\n"
"v_mfma_f32_16x16x32_bf16 v[212:215], a[28:31], a[204:207], v[212:215]\n"
"v_add_u32 v156, 512, v156\n"
"v_add_u32 v161, 64, v161\n"
"v_mfma_f32_16x16x32_bf16 v[216:219], a[28:31], a[220:223], v[216:219]\n"
"s_waitcnt vmcnt(19)\n"
"ds_read_b128 a[28:31], v154 offset:4544\n"
"v_mfma_f32_16x16x32_bf16 v[216:219], a[24:27], a[216:219], v[216:219]\n"
"ds_read_b128 a[24:27], v154 offset:4480\n"
"v_mfma_f32_16x16x32_bf16 v[216:219], a[20:23], a[212:215], v[216:219]\n"
"ds_read_b128 a[20:23], v154 offset:4416\n"
"v_mfma_f32_16x16x32_bf16 v[216:219], a[16:19], a[208:211], v[216:219]\n"
"ds_read_b128 a[16:19], v154 offset:4352\n"
"v_mfma_f32_16x16x32_bf16 v[108:111], v[0:3], v[180:183], 0\n"
"v_mfma_f32_16x16x32_bf16 v[108:111], v[4:7], v[184:187], v[108:111]\n"
"v_smfmac_f32_16x16x64_bf16 v[132:135], v[244:247], v[180:187], v157\n"
"v_cvt_pk_bf16_f32 v205, v212, v216\n"
"v_mfma_f32_16x16x32_bf16 v[124:127], v[12:15], v[184:187], 0\n"
"v_cvt_pk_bf16_f32 v209, v213, v217\n"
"v_pk_max_i16 v205, v205, 0\n"
"v_mfma_f32_16x16x32_bf16 v[124:127], v[8:11], v[180:183], v[124:127]\n"
"s_waitcnt lgkmcnt(4)\n"
"buffer_store_dwordx2 v[204:205], v162, s[84:87], s65 offen offset:0 sc0 sc1\n"
"v_smfmac_f32_16x16x64_bf16 v[128:131], v[248:251], v[188:195], v157\n"
"ds_read_b128 v[248:251], v153 offset:13312\n"
"v_smfmac_f32_16x16x64_bf16 v[128:131], v[244:247], v[196:203], v157\n"
"ds_read_b128 v[244:247], v153 offset:9216\n"
"v_mfma_f32_16x16x32_bf16 v[112:115], v[16:19], v[180:183], 0\n"
"v_pk_max_i16 v209, v209, 0\n"
"v_cvt_pk_bf16_f32 v207, v214, v218\n"
"v_mfma_f32_16x16x32_bf16 v[112:115], v[20:23], v[184:187], v[112:115]\n"
"v_cvt_pk_bf16_f32 v211, v215, v219\n"
"v_pk_max_i16 v207, v207, 0\n"
"v_mfma_f32_16x16x32_bf16 v[120:123], v[28:31], v[184:187], 0\n"
"v_pk_max_i16 v211, v211, 0\n"
"v_add_f32 v132, v132, v133\n"
"v_mfma_f32_16x16x32_bf16 v[120:123], v[24:27], v[180:183], v[120:123]\n"
"buffer_store_dwordx2 v[208:209], v162, s[84:87], s65 offen offset:256 sc0 sc1\n"
"v_mfma_f32_16x16x32_bf16 v[116:119], v[32:35], v[180:183], 0\n"
"v_add_f32 v134, v134, v135\n"
"v_add_f32 v128, v128, v129\n"
"v_mfma_f32_16x16x32_bf16 v[116:119], v[36:39], v[184:187], v[116:119]\n"
"v_exp_f32 v132, v132\n"
"s_sub_u32 s74, s74, 1\n"
"s_cmp_eq_u32 s74, 0\n"
"s_cbranch_scc1 .Lepf%=\n"
".Lp1f%=:\n"
"v_mfma_f32_16x16x32_bf16 v[164:167], a[144:147], a[0:3], 0\n"
"v_exp_f32 v134, v134\n"
"v_mfma_f32_16x16x32_bf16 v[164:167], a[148:151], a[4:7], v[164:167]\n"
"v_cvt_pk_bf16_f32 v133, v132, v134\n"
"s_add_u32 m0, s28, 4352\n"
"v_mfma_f32_16x16x32_bf16 v[164:167], a[152:155], a[8:11], v[164:167]\n"
"s_waitcnt lgkmcnt(2)\n"
"buffer_load_dwordx4 v152, s[24:27], s56 offen offset:0 lds\n"
"v_mfma_f32_16x16x32_bf16 v[164:167], a[156:159], a[12:15], v[164:167]\n"
"buffer_store_dwordx2 v[206:207], v162, s[84:87], s65 offen offset:512 sc0 sc1\n"
"v_mfma_f32_16x16x32_bf16 v[168:171], a[156:159], a[28:31], 0\n"
"v_add_f32 v132, v132, v134\n"
"v_add_f32 v130, v130, v131\n"
"v_mfma_f32_16x16x32_bf16 v[168:171], a[152:155], a[24:27], v[168:171]\n"
"v_permlane16_swap_b32 v132, v133\n"
"v_mfma_f32_16x16x32_bf16 v[168:171], a[148:151], a[20:23], v[168:171]\n"
"buffer_load_dwordx4 v152, s[24:27], s57 offen offset:1088 lds\n"
"v_mfma_f32_16x16x32_bf16 v[168:171], a[144:147], a[16:19], v[168:171]\n"
"v_exp_f32 v128, v128\n"
"v_exp_f32 v130, v130\n"
"v_mfma_f32_16x16x4_4b_bf16 v[220:235], v[132:133], a[244:245], 0 cbsz:2 abid:1\n"
"v_exp_f32 v108, v108\n"
"v_cvt_pk_bf16_f32 v129, v128, v130\n"
"v_add_f32 v128, v128, v130\n"
"v_exp_f32 v109, v109\n"
"v_exp_f32 v110, v110\n"
"v_mfma_f32_16x16x32_bf16 v[252:255], a[252:255], v[96:99], v[132:135]\n"
"buffer_store_dwordx2 v[210:211], v162, s[84:87], s65 offen offset:768 sc0 sc1\n"
"v_permlane16_swap_b32 v128, v129\n"
"v_mfma_f32_16x16x32_bf16 v[220:223], v[96:99], v[48:51], v[220:223]\n"
"buffer_load_dwordx4 v152, s[24:27], s58 offen offset:2176 lds\n"
"v_mfma_f32_16x16x32_bf16 v[220:223], v[100:103], v[52:55], v[220:223]\n"
"v_exp_f32 v111, v111\n"
"v_mfma_f32_16x16x32_bf16 v[220:223], v[104:107], v[56:59], v[220:223]\n"
"v_cvt_pk_bf16_f32 v108, v108, v109\n"
"v_cvt_pk_bf16_f32 v109, v110, v111\n"
"v_mfma_f32_16x16x32_bf16 v[224:227], v[104:107], v[68:71], v[224:227]\n"
"v_exp_f32 v124, v124\n"
"v_mfma_f32_16x16x32_bf16 v[224:227], v[100:103], v[64:67], v[224:227]\n"
"buffer_load_dwordx4 v152, s[24:27], s59 offen offset:3264 lds\n"
"v_mfma_f32_16x16x32_bf16 v[224:227], v[96:99], v[60:63], v[224:227]\n"
"v_exp_f32 v125, v125\n"
"v_smfmac_f32_16x16x64_bf16 v[252:255], a[248:251], v[100:107], v157\n"
"v_cvt_pk_bf16_f32 v110, v124, v125\n"
"v_mfma_f32_16x16x32_bf16 v[228:231], v[96:99], v[72:75], v[228:231]\n"
"v_exp_f32 v126, v126\n"
"v_mfma_f32_16x16x32_bf16 v[228:231], v[100:103], v[76:79], v[228:231]\n"
"v_exp_f32 v127, v127\n"
"v_mfma_f32_16x16x32_bf16 v[228:231], v[104:107], v[80:83], v[228:231]\n"
"v_exp_f32 v112, v112\n"
"v_mfma_f32_16x16x32_bf16 v[232:235], v[104:107], v[92:95], v[232:235]\n"
"v_cvt_pk_bf16_f32 v111, v126, v127\n"
"v_mfma_f32_16x16x32_bf16 v[232:235], v[100:103], v[88:91], v[232:235]\n"
"v_exp_f32 v113, v113\n"
"v_mfma_f32_16x16x32_bf16 v[232:235], v[96:99], v[84:87], v[232:235]\n"
"v_exp_f32 v114, v114\n"
"v_mfma_f32_16x16x32_bf16 v[124:127], v[44:47], v[184:187], 0\n"
"v_exp_f32 v115, v115\n"
"v_mfma_f32_16x16x32_bf16 v[124:127], v[40:43], v[180:183], v[124:127]\n"
"v_cvt_pk_bf16_f32 v112, v112, v113\n"
"v_cvt_pk_bf16_f32 v113, v114, v115\n"
"v_mfma_f32_16x16x32_bf16 v[96:99], v[0:3], v[188:191], 0\n"
"v_exp_f32 v120, v120\n"
"v_mfma_f32_16x16x32_bf16 v[96:99], v[4:7], v[192:195], v[96:99]\n"
"v_exp_f32 v121, v121\n"
"v_add_f32 v252, v252, v253\n"
"v_mfma_f32_16x16x4_4b_bf16 v[204:219], v[132:133], a[244:245], 0 cbsz:2 abid:3\n"
"v_exp_f32 v122, v122\n"
"v_exp_f32 v123, v123\n"
"v_cvt_pk_bf16_f32 v114, v120, v121\n"
"v_exp_f32 v116, v116\n"
"v_cvt_pk_bf16_f32 v115, v122, v123\n"
"v_mfma_f32_16x16x32_bf16 v[120:123], v[12:15], v[192:195], 0\n"
"v_exp_f32 v117, v117\n"
"v_mfma_f32_16x16x32_bf16 v[120:123], v[8:11], v[188:191], v[120:123]\n"
"v_rcp_f32 v253, v252\n"
"v_mfma_f32_16x16x32_bf16 v[204:207], v[108:111], v[48:51], v[204:207]\n"
"ds_bpermute_b32 v254, v160, v253 offset:0\n"
"v_mfma_f32_16x16x32_bf16 v[204:207], v[112:115], v[52:55], v[204:207]\n"
"v_exp_f32 v118, v118\n"
"v_mfma_f32_16x16x32_bf16 v[212:215], v[108:111], v[72:75], v[212:215]\n"
"v_max3_f32 v159, v159, v252, v253\n"
"v_cvt_pk_bf16_f32 v116, v116, v117\n"
"v_mfma_f32_16x16x32_bf16 v[212:215], v[112:115], v[76:79], v[212:215]\n"
"v_exp_f32 v119, v119\n"
"v_mfma_f32_16x16x32_bf16 v[100:103], v[16:19], v[188:191], 0\n"
"v_exp_f32 v124, v124\n"
"v_mfma_f32_16x16x32_bf16 v[100:103], v[20:23], v[192:195], v[100:103]\n"
"v_cvt_pk_bf16_f32 v117, v118, v119\n"
"v_exp_f32 v125, v125\n"
"v_mfma_f32_16x16x32_bf16 v[104:107], v[32:35], v[188:191], 0\n"
"v_exp_f32 v126, v126\n"
"v_mfma_f32_16x16x32_bf16 v[104:107], v[36:39], v[192:195], v[104:107]\n"
"v_exp_f32 v127, v127\n"
"v_mfma_f32_16x16x32_bf16 v[180:183], v[136:139], a[0:3], 0\n"
"v_cvt_pk_bf16_f32 v118, v124, v125\n"
"v_cvt_pk_bf16_f32 v119, v126, v127\n"
"v_mfma_f32_16x16x32_bf16 v[180:183], v[140:143], a[4:7], v[180:183]\n"
"v_exp_f32 v96, v96\n"
"v_mfma_f32_16x16x32_bf16 v[180:183], v[144:147], a[8:11], v[180:183]\n"
"v_exp_f32 v97, v97\n"
"v_mfma_f32_16x16x32_bf16 v[180:183], v[148:151], a[12:15], v[180:183]\n"
"s_waitcnt lgkmcnt(0)\n"
"v_mul_f32_dpp v220, v254, v220 quad_perm:[0,0,0,0] row_mask:0xf bank_mask:0xf\n"
"v_mul_f32_dpp v221, v254, v221 quad_perm:[1,1,1,1] row_mask:0xf bank_mask:0xf\n"
"v_mfma_f32_16x16x32_bf16 v[204:207], v[116:119], v[56:59], v[204:207]\n"
"v_mul_f32_dpp v222, v254, v222 quad_perm:[2,2,2,2] row_mask:0xf bank_mask:0xf\n"
"v_mul_f32_dpp v223, v254, v223 quad_perm:[3,3,3,3] row_mask:0xf bank_mask:0xf\n"
"v_mfma_f32_16x16x32_bf16 v[208:211], v[116:119], v[68:71], v[208:211]\n"
"v_mul_f32_dpp v224, v254, v224 quad_perm:[0,0,0,0] row_mask:0xf bank_mask:0xf\n"
"v_mul_f32_dpp v225, v254, v225 quad_perm:[1,1,1,1] row_mask:0xf bank_mask:0xf\n"
"v_mfma_f32_16x16x32_bf16 v[208:211], v[112:115], v[64:67], v[208:211]\n"
"v_mul_f32_dpp v226, v254, v226 quad_perm:[2,2,2,2] row_mask:0xf bank_mask:0xf\n"
"v_mul_f32_dpp v227, v254, v227 quad_perm:[3,3,3,3] row_mask:0xf bank_mask:0xf\n"
"v_mfma_f32_16x16x32_bf16 v[208:211], v[108:111], v[60:63], v[208:211]\n"
"v_mul_f32_dpp v228, v254, v228 quad_perm:[0,0,0,0] row_mask:0xf bank_mask:0xf\n"
"v_mul_f32_dpp v229, v254, v229 quad_perm:[1,1,1,1] row_mask:0xf bank_mask:0xf\n"
"v_mfma_f32_16x16x32_bf16 v[220:223], a[32:35], a[160:163], v[220:223]\n"
"v_mul_f32_dpp v230, v254, v230 quad_perm:[2,2,2,2] row_mask:0xf bank_mask:0xf\n"
"v_mul_f32_dpp v231, v254, v231 quad_perm:[3,3,3,3] row_mask:0xf bank_mask:0xf\n"
"v_mfma_f32_16x16x32_bf16 v[220:223], a[36:39], a[164:167], v[220:223]\n"
"v_mul_f32_dpp v232, v254, v232 quad_perm:[0,0,0,0] row_mask:0xf bank_mask:0xf\n"
"v_mul_f32_dpp v233, v254, v233 quad_perm:[1,1,1,1] row_mask:0xf bank_mask:0xf\n"
"v_mfma_f32_16x16x32_bf16 v[220:223], a[40:43], a[168:171], v[220:223]\n"
"v_mul_f32_dpp v234, v254, v234 quad_perm:[2,2,2,2] row_mask:0xf bank_mask:0xf\n"
"v_mul_f32_dpp v235, v254, v235 quad_perm:[3,3,3,3] row_mask:0xf bank_mask:0xf\n"
"v_mfma_f32_16x16x32_bf16 v[220:223], a[44:47], a[172:175], v[220:223]\n"
"v_exp_f32 v98, v98\n"
"v_mfma_f32_16x16x32_bf16 v[252:255], a[252:255], v[108:111], v[132:135]\n"
"v_exp_f32 v99, v99\n"
"v_mfma_f32_16x16x32_bf16 v[224:227], a[44:47], a[188:191], v[224:227]\n"
"v_cvt_pk_bf16_f32 v96, v96, v97\n"
"v_cvt_pk_bf16_f32 v97, v98, v99\n"
"v_mfma_f32_16x16x32_bf16 v[224:227], a[40:43], a[184:187], v[224:227]\n"
"v_exp_f32 v120, v120\n"
"v_mfma_f32_16x16x32_bf16 v[224:227], a[36:39], a[180:183], v[224:227]\n"
"v_exp_f32 v121, v121\n"
"v_mfma_f32_16x16x32_bf16 v[224:227], a[32:35], a[176:179], v[224:227]\n"
"v_exp_f32 v122, v122\n"
"v_mfma_f32_16x16x32_bf16 v[228:231], a[32:35], a[192:195], v[228:231]\n"
"v_exp_f32 v123, v123\n"
"v_mfma_f32_16x16x32_bf16 v[228:231], a[36:39], a[196:199], v[228:231]\n"
"v_cvt_pk_bf16_f32 v98, v120, v121\n"
"v_cvt_pk_bf16_f32 v99, v122, v123\n"
"v_mfma_f32_16x16x32_bf16 v[228:231], a[40:43], a[200:203], v[228:231]\n"
"v_cvt_pk_bf16_f32 v220, v220, v224\n"
"v_cvt_pk_bf16_f32 v224, v221, v225\n"
"v_mfma_f32_16x16x32_bf16 v[228:231], a[44:47], a[204:207], v[228:231]\n"
"v_pk_max_i16 v220, v220, 0\n"
"v_pk_max_i16 v224, v224, 0\n"
"v_smfmac_f32_16x16x64_bf16 v[252:255], a[248:251], v[112:119], v157\n"
"v_cvt_pk_bf16_f32 v222, v222, v226\n"
"v_mfma_f32_16x16x32_bf16 v[232:235], a[44:47], a[220:223], v[232:235]\n"
"s_waitcnt vmcnt(19)\n"
"ds_read_b128 a[44:47], v154 offset:8896\n"
"v_cvt_pk_bf16_f32 v226, v223, v227\n"
"v_mfma_f32_16x16x32_bf16 v[232:235], a[40:43], a[216:219], v[232:235]\n"
"ds_read_b128 a[40:43], v154 offset:8832\n"
"v_pk_max_i16 v222, v222, 0\n"
"v_mfma_f32_16x16x32_bf16 v[232:235], a[36:39], a[212:215], v[232:235]\n"
"ds_read_b128 a[36:39], v154 offset:8768\n"
"v_add_f32 v252, v252, v253\n"
"v_mfma_f32_16x16x32_bf16 v[232:235], a[32:35], a[208:211], v[232:235]\n"
"ds_read_b128 a[32:35], v154 offset:8704\n"
"v_pk_max_i16 v226, v226, 0\n"
"v_mfma_f32_16x16x32_bf16 v[212:215], v[116:119], v[80:83], v[212:215]\n"
"v_rcp_f32 v253, v252\n"
"v_mfma_f32_16x16x32_bf16 v[216:219], v[116:119], v[92:95], v[216:219]\n"
"ds_bpermute_b32 v254, v160, v253 offset:128\n"
"v_mfma_f32_16x16x32_bf16 v[216:219], v[112:115], v[88:91], v[216:219]\n"
"v_cvt_pk_bf16_f32 v221, v228, v232\n"
"v_cvt_pk_bf16_f32 v225, v229, v233\n"
"v_mfma_f32_16x16x32_bf16 v[216:219], v[108:111], v[84:87], v[216:219]\n"
"v_pk_max_i16 v221, v221, 0\n"
"v_max3_f32 v159, v159, v252, v253\n"
"v_mfma_f32_16x16x32_bf16 v[124:127], v[28:31], v[192:195], 0\n"
"v_pk_max_i16 v225, v225, 0\n"
"v_cvt_pk_bf16_f32 v223, v230, v234\n"
"v_mfma_f32_16x16x32_bf16 v[124:127], v[24:27], v[188:191], v[124:127]\n"
"buffer_store_dwordx2 v[220:221], v162, s[84:87], s66 offen offset:0 sc0 sc1\n"
"v_mfma_f32_16x16x32_bf16 v[120:123], v[44:47], v[192:195], 0\n"
"v_cvt_pk_bf16_f32 v227, v231, v235\n"
"v_pk_max_i16 v223, v223, 0\n"
"v_mfma_f32_16x16x32_bf16 v[120:123], v[40:43], v[188:191], v[120:123]\n"
"v_pk_max_i16 v227, v227, 0\n"
"v_exp_f32 v100, v100\n"
"v_mfma_f32_16x16x32_bf16 v[108:111], v[0:3], v[196:199], 0\n"
"v_exp_f32 v101, v101\n"
"v_mfma_f32_16x16x32_bf16 v[108:111], v[4:7], v[200:203], v[108:111]\n"
"v_exp_f32 v102, v102\n"
"v_mfma_f32_16x16x32_bf16 v[112:115], v[16:19], v[196:199], 0\n"
"v_exp_f32 v103, v103\n"
"v_mfma_f32_16x16x32_bf16 v[112:115], v[20:23], v[200:203], v[112:115]\n"
"buffer_store_dwordx2 v[224:225], v162, s[84:87], s66 offen offset:256 sc0 sc1\n"
"v_mfma_f32_16x16x32_bf16 v[116:119], v[32:35], v[196:199], 0\n"
"s_waitcnt lgkmcnt(0)\n"
"v_mul_f32_dpp v204, v254, v204 quad_perm:[0,0,0,0] row_mask:0xf bank_mask:0xf\n"
"v_mul_f32_dpp v205, v254, v205 quad_perm:[1,1,1,1] row_mask:0xf bank_mask:0xf\n"
"v_mfma_f32_16x16x32_bf16 v[116:119], v[36:39], v[200:203], v[116:119]\n"
"v_mul_f32_dpp v206, v254, v206 quad_perm:[2,2,2,2] row_mask:0xf bank_mask:0xf\n"
"v_mul_f32_dpp v207, v254, v207 quad_perm:[3,3,3,3] row_mask:0xf bank_mask:0xf\n"
"v_mfma_f32_16x16x32_bf16 v[0:3], a[96:99], a[0:3], 0\n"
"v_mul_f32_dpp v208, v254, v208 quad_perm:[0,0,0,0] row_mask:0xf bank_mask:0xf\n"
"s_add_u32 m0, s28, 8704\n"
"v_mfma_f32_16x16x32_bf16 v[0:3], a[100:103], a[4:7], v[0:3]\n"
"buffer_load_dwordx4 v152, s[24:27], s60 offen offset:0 lds\n"
"v_mfma_f32_16x16x32_bf16 v[0:3], a[104:107], a[8:11], v[0:3]\n"
"v_mul_f32_dpp v209, v254, v209 quad_perm:[1,1,1,1] row_mask:0xf bank_mask:0xf\n"
"v_mul_f32_dpp v210, v254, v210 quad_perm:[2,2,2,2] row_mask:0xf bank_mask:0xf\n"
"v_mfma_f32_16x16x32_bf16 v[0:3], a[108:111], a[12:15], v[0:3]\n"
"buffer_store_dwordx2 v[222:223], v162, s[84:87], s66 offen offset:512 sc0 sc1\n"
"v_mfma_f32_16x16x32_bf16 v[204:207], a[48:51], a[160:163], v[204:207]\n"
"v_mul_f32_dpp v211, v254, v211 quad_perm:[3,3,3,3] row_mask:0xf bank_mask:0xf\n"
"v_mul_f32_dpp v212, v254, v212 quad_perm:[0,0,0,0] row_mask:0xf bank_mask:0xf\n"
"v_mfma_f32_16x16x32_bf16 v[204:207], a[52:55], a[164:167], v[204:207]\n"
"buffer_load_dwordx4 v152, s[24:27], s61 offen offset:1088 lds\n"
"v_mfma_f32_16x16x32_bf16 v[204:207], a[56:59], a[168:171], v[204:207]\n"
"v_mul_f32_dpp v213, v254, v213 quad_perm:[1,1,1,1] row_mask:0xf bank_mask:0xf\n"
"v_mul_f32_dpp v214, v254, v214 quad_perm:[2,2,2,2] row_mask:0xf bank_mask:0xf\n"
"v_mfma_f32_16x16x32_bf16 v[204:207], a[60:63], a[172:175], v[204:207]\n"
"v_mul_f32_dpp v215, v254, v215 quad_perm:[3,3,3,3] row_mask:0xf bank_mask:0xf\n"
"v_mul_f32_dpp v216, v254, v216 quad_perm:[0,0,0,0] row_mask:0xf bank_mask:0xf\n"
"v_mfma_f32_16x16x32_bf16 v[208:211], a[60:63], a[188:191], v[208:211]\n"
"v_mul_f32_dpp v217, v254, v217 quad_perm:[1,1,1,1] row_mask:0xf bank_mask:0xf\n"
"v_mul_f32_dpp v218, v254, v218 quad_perm:[2,2,2,2] row_mask:0xf bank_mask:0xf\n"
"v_mfma_f32_16x16x32_bf16 v[208:211], a[56:59], a[184:187], v[208:211]\n"
"v_mul_f32_dpp v219, v254, v219 quad_perm:[3,3,3,3] row_mask:0xf bank_mask:0xf\n"
"v_cvt_pk_bf16_f32 v100, v100, v101\n"
"v_mfma_f32_16x16x32_bf16 v[208:211], a[52:55], a[180:183], v[208:211]\n"
"buffer_load_dwordx4 v152, s[24:27], s62 offen offset:2176 lds\n"
"v_mfma_f32_16x16x32_bf16 v[208:211], a[48:51], a[176:179], v[208:211]\n"
"buffer_store_dwordx2 v[226:227], v162, s[84:87], s66 offen offset:768 sc0 sc1\n"
"v_mfma_f32_16x16x32_bf16 v[212:215], a[48:51], a[192:195], v[212:215]\n"
"v_exp_f32 v124, v124\n"
"v_mfma_f32_16x16x32_bf16 v[212:215], a[52:55], a[196:199], v[212:215]\n"
"v_cvt_pk_bf16_f32 v101, v102, v103\n"
"v_mfma_f32_16x16x32_bf16 v[212:215], a[56:59], a[200:203], v[212:215]\n"
"v_exp_f32 v125, v125\n"
"v_mfma_f32_16x16x32_bf16 v[212:215], a[60:63], a[204:207], v[212:215]\n"
"buffer_load_dwordx4 v152, s[24:27], s63 offen offset:3264 lds\n"
"v_mfma_f32_16x16x32_bf16 v[216:219], a[60:63], a[220:223], v[216:219]\n"
"v_exp_f32 v126, v126\n"
"v_mfma_f32_16x16x32_bf16 v[216:219], a[56:59], a[216:219], v[216:219]\n"
"s_waitcnt vmcnt(17)\n"
"ds_read_b128 a[56:59], v154 offset:128\n"
"ds_read_b128 a[60:63], v154 offset:192\n"
"v_mfma_f32_16x16x32_bf16 v[216:219], a[52:55], a[212:215], v[216:219]\n"
"ds_read_b128 a[52:55], v154 offset:64\n"
"v_cvt_pk_bf16_f32 v204, v204, v208\n"
"v_mfma_f32_16x16x32_bf16 v[216:219], a[48:51], a[208:211], v[216:219]\n"
"ds_read_b128 a[48:51], v154 offset:0\n"
"v_cvt_pk_bf16_f32 v208, v205, v209\n"
"v_pk_max_i16 v204, v204, 0\n"
"v_mfma_f32_16x16x4_4b_bf16 v[220:235], v[128:129], a[244:245], 0 cbsz:2 abid:1\n"
"s_add_u32 s24, s24, 13107200\n"
"s_addc_u32 s25, s25, 0\n"
"s_sub_u32 s26, s26, 13107200\n"
"s_cselect_b32 s26, 0, s26\n"
"v_exp_f32 v127, v127\n"
"v_pk_max_i16 v208, v208, 0\n"
"v_cvt_pk_bf16_f32 v205, v212, v216\n"
"v_mfma_f32_16x16x32_bf16 v[252:255], a[252:255], v[96:99], v[128:131]\n"
"v_cvt_pk_bf16_f32 v209, v213, v217\n"
"v_pk_max_i16 v205, v205, 0\n"
"v_mfma_f32_16x16x32_bf16 v[16:19], a[96:99], a[32:35], 0\n"
"v_pk_max_i16 v209, v209, 0\n"
"v_cvt_pk_bf16_f32 v206, v206, v210\n"
"v_mfma_f32_16x16x32_bf16 v[16:19], a[100:103], a[36:39], v[16:19]\n"
"v_cvt_pk_bf16_f32 v210, v207, v211\n"
"v_cvt_pk_bf16_f32 v207, v214, v218\n"
"v_mfma_f32_16x16x32_bf16 v[16:19], a[104:107], a[40:43], v[16:19]\n"
"v_cvt_pk_bf16_f32 v211, v215, v219\n"
"v_pk_max_i16 v206, v206, 0\n"
"v_mfma_f32_16x16x32_bf16 v[16:19], a[108:111], a[44:47], v[16:19]\n"
"buffer_store_dwordx2 v[204:205], v162, s[84:87], s67 offen offset:0 sc0 sc1\n"
"v_mfma_f32_16x16x32_bf16 v[220:223], v[96:99], v[48:51], v[220:223]\n"
"v_cvt_pk_bf16_f32 v102, v124, v125\n"
"v_pk_max_i16 v207, v207, 0\n"
"v_mfma_f32_16x16x32_bf16 v[228:231], v[96:99], v[72:75], v[228:231]\n"
"v_exp_f32 v104, v104\n"
"v_mfma_f32_16x16x32_bf16 v[4:7], a[112:115], a[0:3], 0\n"
"v_pk_max_i16 v210, v210, 0\n"
"v_cvt_pk_bf16_f32 v103, v126, v127\n"
"v_mfma_f32_16x16x32_bf16 v[4:7], a[116:119], a[4:7], v[4:7]\n"
"v_pk_max_i16 v211, v211, 0\n"
"v_mfma_f32_16x16x32_bf16 v[4:7], a[120:123], a[8:11], v[4:7]\n"
"v_exp_f32 v105, v105\n"
"v_mfma_f32_16x16x32_bf16 v[4:7], a[124:127], a[12:15], v[4:7]\n"
"buffer_store_dwordx2 v[208:209], v162, s[84:87], s67 offen offset:256 sc0 sc1\n"
"v_mfma_f32_16x16x32_bf16 v[124:127], v[12:15], v[200:203], 0\n"
"v_exp_f32 v106, v106\n"
"v_mfma_f32_16x16x32_bf16 v[124:127], v[8:11], v[196:199], v[124:127]\n"
"v_exp_f32 v107, v107\n"
"v_mfma_f32_16x16x32_bf16 v[220:223], v[100:103], v[52:55], v[220:223]\n"
"v_cvt_pk_bf16_f32 v104, v104, v105\n"
"s_add_u32 m0, s28, 0\n"
"v_mfma_f32_16x16x32_bf16 v[228:231], v[100:103], v[76:79], v[228:231]\n"
"s_waitcnt lgkmcnt(0)\n"
"buffer_load_dwordx4 v152, s[24:27], s40 offen offset:0 lds\n"
"v_mfma_f32_16x16x32_bf16 v[8:11], a[108:111], a[28:31], 0\n"
"v_exp_f32 v120, v120\n"
"v_mfma_f32_16x16x32_bf16 v[8:11], a[104:107], a[24:27], v[8:11]\n"
"v_cvt_pk_bf16_f32 v105, v106, v107\n"
"v_mfma_f32_16x16x32_bf16 v[8:11], a[100:103], a[20:23], v[8:11]\n"
"v_exp_f32 v121, v121\n"
"v_mfma_f32_16x16x32_bf16 v[8:11], a[96:99], a[16:19], v[8:11]\n"
"buffer_store_dwordx2 v[206:207], v162, s[84:87], s67 offen offset:512 sc0 sc1\n"
"v_mfma_f32_16x16x32_bf16 v[12:15], a[124:127], a[28:31], 0\n"
"buffer_load_dwordx4 v152, s[24:27], s41 offen offset:1088 lds\n"
"v_mfma_f32_16x16x32_bf16 v[12:15], a[120:123], a[24:27], v[12:15]\n"
"v_exp_f32 v122, v122\n"
"v_mfma_f32_16x16x32_bf16 v[12:15], a[116:119], a[20:23], v[12:15]\n"
"v_exp_f32 v123, v123\n"
"v_mfma_f32_16x16x32_bf16 v[12:15], a[112:115], a[16:19], v[12:15]\n"
"v_cvt_pk_bf16_f32 v106, v120, v121\n"
"v_cvt_pk_bf16_f32 v107, v122, v123\n"
"v_mfma_f32_16x16x32_bf16 v[120:123], v[28:31], v[200:203], 0\n"
"buffer_load_dwordx4 v152, s[24:27], s42 offen offset:2176 lds\n"
"v_mfma_f32_16x16x32_bf16 v[120:123], v[24:27], v[196:199], v[120:123]\n"
"buffer_store_dwordx2 v[210:211], v162, s[84:87], s67 offen offset:768 sc0 sc1\n"
"v_mfma_f32_16x16x32_bf16 v[220:223], v[104:107], v[56:59], v[220:223]\n"
"v_exp_f32 v108, v108\n"
"v_mfma_f32_16x16x32_bf16 v[224:227], v[104:107], v[68:71], v[224:227]\n"
"v_exp_f32 v109, v109\n"
"v_mfma_f32_16x16x32_bf16 v[224:227], v[100:103], v[64:67], v[224:227]\n"
"v_exp_f32 v110, v110\n"
"v_mfma_f32_16x16x32_bf16 v[224:227], v[96:99], v[60:63], v[224:227]\n"
"buffer_load_dwordx4 v152, s[24:27], s43 offen offset:3264 lds\n"
"v_smfmac_f32_16x16x64_bf16 v[252:255], a[248:251], v[100:107], v157\n"
"v_cvt_pk_bf16_f32 v108, v108, v109\n"
"v_mfma_f32_16x16x32_bf16 v[228:231], v[104:107], v[80:83], v[228:231]\n"
"v_exp_f32 v111, v111\n"
"v_mfma_f32_16x16x32_bf16 v[232:235], v[104:107], v[92:95], v[232:235]\n"
"v_exp_f32 v124, v124\n"
"v_mfma_f32_16x16x32_bf16 v[232:235], v[100:103], v[88:91], v[232:235]\n"
"v_cvt_pk_bf16_f32 v109, v110, v111\n"
"v_add_f32 v252, v252, v253\n"
"v_mfma_f32_16x16x32_bf16 v[232:235], v[96:99], v[84:87], v[232:235]\n"
"v_exp_f32 v125, v125\n"
"v_mfma_f32_16x16x32_bf16 v[20:23], a[112:115], a[32:35], 0\n"
"v_exp_f32 v126, v126\n"
"v_mfma_f32_16x16x32_bf16 v[20:23], a[116:119], a[36:39], v[20:23]\n"
"v_exp_f32 v127, v127\n"
"v_mfma_f32_16x16x32_bf16 v[20:23], a[120:123], a[40:43], v[20:23]\n"
"v_rcp_f32 v253, v252\n"
"v_mfma_f32_16x16x32_bf16 v[20:23], a[124:127], a[44:47], v[20:23]\n"
"ds_bpermute_b32 v254, v160, v253 offset:0\n"
"v_mfma_f32_16x16x32_bf16 v[172:175], a[144:147], a[32:35], 0\n"
"v_max3_f32 v159, v159, v252, v253\n"
"v_cvt_pk_bf16_f32 v110, v124, v125\n"
"v_mfma_f32_16x16x32_bf16 v[172:175], a[148:151], a[36:39], v[172:175]\n"
"v_exp_f32 v112, v112\n"
"v_mfma_f32_16x16x32_bf16 v[172:175], a[152:155], a[40:43], v[172:175]\n"
"v_cvt_pk_bf16_f32 v111, v126, v127\n"
"v_cvt_pk_bf16_f32 v0, v0, v1\n"
"v_mfma_f32_16x16x32_bf16 v[172:175], a[156:159], a[44:47], v[172:175]\n"
"v_exp_f32 v113, v113\n"
"v_mfma_f32_16x16x32_bf16 v[124:127], v[44:47], v[200:203], 0\n"
"v_exp_f32 v114, v114\n"
"v_mfma_f32_16x16x32_bf16 v[124:127], v[40:43], v[196:199], v[124:127]\n"
"v_exp_f32 v115, v115\n"
"v_cvt_pk_bf16_f32 v112, v112, v113\n"
"v_mfma_f32_16x16x4_4b_bf16 v[204:219], v[128:129], a[244:245], 0 cbsz:2 abid:3\n"
"ds_read_b64 a[244:245], v163 offset:0\n"
"v_exp_f32 v120, v120\n"
"v_cvt_pk_bf16_f32 v1, v2, v3\n"
"v_cvt_pk_bf16_f32 v8, v8, v9\n"
"v_cvt_pk_bf16_f32 v9, v10, v11\n"
"v_cvt_pk_bf16_f32 v16, v16, v17\n"
"v_cvt_pk_bf16_f32 v17, v18, v19\n"
"v_mfma_f32_16x16x32_bf16 v[24:27], a[96:99], a[48:51], 0\n"
"v_cvt_pk_bf16_f32 v113, v114, v115\n"
"v_cvt_pk_bf16_f32 v2, v4, v5\n"
"v_mfma_f32_16x16x32_bf16 v[24:27], a[100:103], a[52:55], v[24:27]\n"
"v_exp_f32 v121, v121\n"
"v_mfma_f32_16x16x32_bf16 v[24:27], a[104:107], a[56:59], v[24:27]\n"
"s_waitcnt lgkmcnt(1)\n"
"v_mul_f32_dpp v220, v254, v220 quad_perm:[0,0,0,0] row_mask:0xf bank_mask:0xf\n"
"v_mul_f32_dpp v221, v254, v221 quad_perm:[1,1,1,1] row_mask:0xf bank_mask:0xf\n"
"v_mfma_f32_16x16x32_bf16 v[24:27], a[108:111], a[60:63], v[24:27]\n"
"v_mul_f32_dpp v222, v254, v222 quad_perm:[2,2,2,2] row_mask:0xf bank_mask:0xf\n"
"v_mul_f32_dpp v223, v254, v223 quad_perm:[3,3,3,3] row_mask:0xf bank_mask:0xf\n"
"v_mfma_f32_16x16x32_bf16 v[204:207], v[108:111], v[48:51], v[204:207]\n"
"v_mul_f32_dpp v224, v254, v224 quad_perm:[0,0,0,0] row_mask:0xf bank_mask:0xf\n"
"v_mul_f32_dpp v225, v254, v225 quad_perm:[1,1,1,1] row_mask:0xf bank_mask:0xf\n"
"v_mfma_f32_16x16x32_bf16 v[220:223], a[64:67], a[160:163], v[220:223]\n"
"v_mul_f32_dpp v226, v254, v226 quad_perm:[2,2,2,2] row_mask:0xf bank_mask:0xf\n"
"v_mul_f32_dpp v227, v254, v227 quad_perm:[3,3,3,3] row_mask:0xf bank_mask:0xf\n"
"v_mfma_f32_16x16x32_bf16 v[220:223], a[68:71], a[164:167], v[220:223]\n"
"v_mul_f32_dpp v228, v254, v228 quad_perm:[0,0,0,0] row_mask:0xf bank_mask:0xf\n"
"v_mul_f32_dpp v229, v254, v229 quad_perm:[1,1,1,1] row_mask:0xf bank_mask:0xf\n"
"v_mfma_f32_16x16x32_bf16 v[220:223], a[72:75], a[168:171], v[220:223]\n"
"v_mul_f32_dpp v230, v254, v230 quad_perm:[2,2,2,2] row_mask:0xf bank_mask:0xf\n"
"v_mul_f32_dpp v231, v254, v231 quad_perm:[3,3,3,3] row_mask:0xf bank_mask:0xf\n"
"v_mfma_f32_16x16x32_bf16 v[220:223], a[76:79], a[172:175], v[220:223]\n"
"v_mul_f32_dpp v232, v254, v232 quad_perm:[0,0,0,0] row_mask:0xf bank_mask:0xf\n"
"v_mul_f32_dpp v233, v254, v233 quad_perm:[1,1,1,1] row_mask:0xf bank_mask:0xf\n"
"v_mfma_f32_16x16x32_bf16 v[224:227], a[76:79], a[188:191], v[224:227]\n"
"v_mul_f32_dpp v234, v254, v234 quad_perm:[2,2,2,2] row_mask:0xf bank_mask:0xf\n"
"v_mul_f32_dpp v235, v254, v235 quad_perm:[3,3,3,3] row_mask:0xf bank_mask:0xf\n"
"v_mfma_f32_16x16x32_bf16 v[224:227], a[72:75], a[184:187], v[224:227]\n"
"v_exp_f32 v122, v122\n"
"v_mfma_f32_16x16x32_bf16 v[224:227], a[68:71], a[180:183], v[224:227]\n"
"v_cvt_pk_bf16_f32 v3, v6, v7\n"
"v_cvt_pk_bf16_f32 v10, v12, v13\n"
"v_mfma_f32_16x16x32_bf16 v[224:227], a[64:67], a[176:179], v[224:227]\n"
"v_cvt_pk_bf16_f32 v11, v14, v15\n"
"v_cvt_pk_bf16_f32 v18, v20, v21\n"
"v_mfma_f32_16x16x32_bf16 v[4:7], a[128:131], a[0:3], 0\n"
"v_cvt_pk_bf16_f32 v19, v22, v23\n"
"v_cvt_pk_bf16_f32 v114, v120, v121\n"
"v_mfma_f32_16x16x32_bf16 v[4:7], a[132:135], a[4:7], v[4:7]\n"
"v_exp_f32 v123, v123\n"
"v_mfma_f32_16x16x32_bf16 v[4:7], a[136:139], a[8:11], v[4:7]\n"
"v_exp_f32 v116, v116\n"
"v_mfma_f32_16x16x32_bf16 v[4:7], a[140:143], a[12:15], v[4:7]\n"
"v_cvt_pk_bf16_f32 v115, v122, v123\n"
"v_cvt_pk_bf16_f32 v220, v220, v224\n"
"v_mfma_f32_16x16x32_bf16 v[12:15], a[140:143], a[28:31], 0\n"
"v_exp_f32 v117, v117\n"
"v_mfma_f32_16x16x32_bf16 v[12:15], a[136:139], a[24:27], v[12:15]\n"
"v_exp_f32 v118, v118\n"
"v_mfma_f32_16x16x32_bf16 v[12:15], a[132:135], a[20:23], v[12:15]\n"
"v_cvt_pk_bf16_f32 v4, v4, v5\n"
"v_cvt_pk_bf16_f32 v5, v6, v7\n"
"v_mfma_f32_16x16x32_bf16 v[12:15], a[128:131], a[16:19], v[12:15]\n"
"v_exp_f32 v119, v119\n"
"v_mfma_f32_16x16x32_bf16 v[20:23], a[128:131], a[32:35], 0\n"
"v_cvt_pk_bf16_f32 v6, v164, v165\n"
"v_cvt_pk_bf16_f32 v7, v166, v167\n"
"v_mfma_f32_16x16x32_bf16 v[20:23], a[132:135], a[36:39], v[20:23]\n"
"v_cvt_pk_bf16_f32 v116, v116, v117\n"
"v_cvt_pk_bf16_f32 v117, v118, v119\n"
"v_mfma_f32_16x16x32_bf16 v[20:23], a[136:139], a[40:43], v[20:23]\n"
"v_cvt_pk_bf16_f32 v12, v12, v13\n"
"v_cvt_pk_bf16_f32 v13, v14, v15\n"
"v_mfma_f32_16x16x32_bf16 v[20:23], a[140:143], a[44:47], v[20:23]\n"
"v_cvt_pk_bf16_f32 v14, v168, v169\n"
"v_cvt_pk_bf16_f32 v15, v170, v171\n"
"v_mfma_f32_16x16x32_bf16 v[228:231], a[64:67], a[192:195], v[228:231]\n"
"v_exp_f32 v124, v124\n"
"v_mfma_f32_16x16x32_bf16 v[228:231], a[68:71], a[196:199], v[228:231]\n"
"v_exp_f32 v125, v125\n"
"v_mfma_f32_16x16x32_bf16 v[228:231], a[72:75], a[200:203], v[228:231]\n"
"v_cvt_pk_bf16_f32 v20, v20, v21\n"
"v_cvt_pk_bf16_f32 v21, v22, v23\n"
"v_mfma_f32_16x16x32_bf16 v[228:231], a[76:79], a[204:207], v[228:231]\n"
"v_cvt_pk_bf16_f32 v22, v172, v173\n"
"v_cvt_pk_bf16_f32 v23, v174, v175\n"
"v_mfma_f32_16x16x32_bf16 v[232:235], a[76:79], a[220:223], v[232:235]\n"
"s_waitcnt vmcnt(16)\n"
"ds_read_b128 a[76:79], v154 offset:4544\n"
"v_cvt_pk_bf16_f32 v224, v221, v225\n"
"v_mfma_f32_16x16x32_bf16 v[232:235], a[72:75], a[216:219], v[232:235]\n"
"ds_read_b128 a[72:75], v154 offset:4480\n"
"v_pk_max_i16 v220, v220, 0\n"
"v_mfma_f32_16x16x32_bf16 v[232:235], a[68:71], a[212:215], v[232:235]\n"
"ds_read_b128 a[68:71], v154 offset:4416\n"
"v_pk_max_i16 v224, v224, 0\n"
"v_mfma_f32_16x16x32_bf16 v[232:235], a[64:67], a[208:211], v[232:235]\n"
"ds_read_b128 a[64:67], v154 offset:4352\n"
"v_cvt_pk_bf16_f32 v222, v222, v226\n"
"v_mfma_f32_16x16x32_bf16 v[252:255], a[252:255], v[108:111], v[128:131]\n"
"v_exp_f32 v126, v126\n"
"v_mfma_f32_16x16x32_bf16 v[204:207], v[112:115], v[52:55], v[204:207]\n"
"v_exp_f32 v127, v127\n"
"v_mfma_f32_16x16x32_bf16 v[212:215], v[108:111], v[72:75], v[212:215]\n"
"v_cvt_pk_bf16_f32 v221, v228, v232\n"
"v_cvt_pk_bf16_f32 v225, v229, v233\n"
"v_mfma_f32_16x16x32_bf16 v[212:215], v[112:115], v[76:79], v[212:215]\n"
"v_pk_max_i16 v221, v221, 0\n"
"v_pk_max_i16 v225, v225, 0\n"
"v_mfma_f32_16x16x32_bf16 v[176:179], a[112:115], a[48:51], 0\n"
".Lpad5_%=:\n"
"s_cbranch_vccnz .Lback0_%=\n"
"v_cvt_pk_bf16_f32 v226, v223, v227\n"
"v_cvt_pk_bf16_f32 v223, v230, v234\n"
"v_mfma_f32_16x16x32_bf16 v[176:179], a[116:119], a[52:55], v[176:179]\n"
"v_cvt_pk_bf16_f32 v227, v231, v235\n"
"v_pk_max_i16 v222, v222, 0\n"
"v_mfma_f32_16x16x32_bf16 v[176:179], a[120:123], a[56:59], v[176:179]\n"
"v_pk_max_i16 v223, v223, 0\n"
"v_pk_max_i16 v226, v226, 0\n"
"v_mfma_f32_16x16x32_bf16 v[176:179], a[124:127], a[60:63], v[176:179]\n"
"s_waitcnt lgkmcnt(3)\n"
"buffer_store_dwordx2 v[220:221], v162, s[84:87], s68 offen offset:0 sc0 sc1\n"
"v_mfma_f32_16x16x32_bf16 v[32:35], a[108:111], a[76:79], 0\n"
"s_waitcnt lgkmcnt(2)\n"
"v_pk_max_i16 v227, v227, 0\n"
"v_cvt_pk_bf16_f32 v118, v124, v125\n"
"v_mfma_f32_16x16x32_bf16 v[32:35], a[104:107], a[72:75], v[32:35]\n"
"s_waitcnt lgkmcnt(1)\n"
"v_cvt_pk_bf16_f32 v119, v126, v127\n"
"v_cvt_pk_bf16_f32 v24, v24, v25\n"
"v_mfma_f32_16x16x32_bf16 v[32:35], a[100:103], a[68:71], v[32:35]\n"
"s_waitcnt lgkmcnt(0)\n"
"v_cvt_pk_bf16_f32 v25, v26, v27\n"
"v_cvt_pk_bf16_f32 v26, v176, v177\n"
"v_mfma_f32_16x16x32_bf16 v[32:35], a[96:99], a[64:67], v[32:35]\n"
"v_cvt_pk_bf16_f32 v27, v178, v179\n"
"v_cvt_pk_bf16_f32 v180, v180, v181\n"
"v_mfma_f32_16x16x32_bf16 v[204:207], v[116:119], v[56:59], v[204:207]\n"
"v_cvt_pk_bf16_f32 v181, v182, v183\n"
"v_mov_b32 v220, 0\n"
"v_mfma_f32_16x16x32_bf16 v[208:211], v[116:119], v[68:71], v[208:211]\n"
"v_mov_b32 v221, 0\n"
"v_mov_b32 v228, 0\n"
"v_mfma_f32_16x16x32_bf16 v[208:211], v[112:115], v[64:67], v[208:211]\n"
"v_cvt_pk_bf16_f32 v32, v32, v33\n"
"v_cvt_pk_bf16_f32 v33, v34, v35\n"
"v_mfma_f32_16x16x32_bf16 v[208:211], v[108:111], v[60:63], v[208:211]\n"
"buffer_store_dwordx2 v[224:225], v162, s[84:87], s68 offen offset:256 sc0 sc1\n"
"v_smfmac_f32_16x16x64_bf16 v[252:255], a[248:251], v[112:119], v157\n"
"s_add_u32 m0, s28, 4352\n"
"v_mfma_f32_16x16x32_bf16 v[212:215], v[116:119], v[80:83], v[212:215]\n"
"buffer_load_dwordx4 v152, s[24:27], s44 offen offset:0 lds\n"
"v_mfma_f32_16x16x32_bf16 v[216:219], v[116:119], v[92:95], v[216:219]\n"
"v_mov_b32 v224, 0\n"
"v_mov_b32 v225, 0\n"
"v_mfma_f32_16x16x32_bf16 v[216:219], v[112:115], v[88:91], v[216:219]\n"
"v_add_f32 v252, v252, v253\n"
"v_mov_b32 v229, 0\n"
"v_mfma_f32_16x16x32_bf16 v[216:219], v[108:111], v[84:87], v[216:219]\n"
"buffer_load_dwordx4 v152, s[24:27], s45 offen offset:1088 lds\n"
"v_mfma_f32_16x16x32_bf16 v[172:175], a[124:127], a[76:79], 0\n"
"v_rcp_f32 v253, v252\n"
"v_mfma_f32_16x16x32_bf16 v[172:175], a[120:123], a[72:75], v[172:175]\n"
"ds_bpermute_b32 v254, v160, v253 offset:128\n"
"v_mfma_f32_16x16x32_bf16 v[172:175], a[116:119], a[68:71], v[172:175]\n"
"v_max3_f32 v159, v159, v252, v253\n"
"v_mov_b32 v128, 0\n"
"v_mfma_f32_16x16x32_bf16 v[172:175], a[112:115], a[64:67], v[172:175]\n"
"buffer_store_dwordx2 v[222:223], v162, s[84:87], s68 offen offset:512 sc0 sc1\n"
"v_mfma_f32_16x16x32_bf16 v[28:31], a[128:131], a[48:51], 0\n"
"buffer_load_dwordx4 v152, s[24:27], s46 offen offset:2176 lds\n"
"v_mfma_f32_16x16x32_bf16 v[28:31], a[132:135], a[52:55], v[28:31]\n"
"v_mov_b32 v129, 0\n"
"v_mov_b32 v130, 0\n"
"v_mfma_f32_16x16x32_bf16 v[28:31], a[136:139], a[56:59], v[28:31]\n"
"v_cvt_pk_bf16_f32 v34, v172, v173\n"
"v_cvt_pk_bf16_f32 v35, v174, v175\n"
"v_mfma_f32_16x16x32_bf16 v[28:31], a[140:143], a[60:63], v[28:31]\n"
"v_mov_b32 v131, 0\n"
"v_mov_b32 v132, 0\n"
"v_mfma_f32_16x16x32_bf16 v[36:39], a[140:143], a[76:79], 0\n"
"buffer_load_dwordx4 v152, s[24:27], s47 offen offset:3264 lds\n"
"v_mfma_f32_16x16x32_bf16 v[36:39], a[136:139], a[72:75], v[36:39]\n"
"v_mov_b32 v133, 0\n"
"v_mov_b32 v134, 0\n"
"v_mfma_f32_16x16x32_bf16 v[36:39], a[132:135], a[68:71], v[36:39]\n"
"v_cvt_pk_bf16_f32 v28, v28, v29\n"
"v_cvt_pk_bf16_f32 v29, v30, v31\n"
"v_mfma_f32_16x16x32_bf16 v[36:39], a[128:131], a[64:67], v[36:39]\n"
"buffer_store_dwordx2 v[226:227], v162, s[84:87], s68 offen offset:768 sc0 sc1\n"
"v_mfma_f32_16x16x32_bf16 v[188:191], v[148:151], a[28:31], 0\n"
"s_waitcnt lgkmcnt(0)\n"
"v_mul_f32_dpp v204, v254, v204 quad_perm:[0,0,0,0] row_mask:0xf bank_mask:0xf\n"
"v_mul_f32_dpp v205, v254, v205 quad_perm:[1,1,1,1] row_mask:0xf bank_mask:0xf\n"
"v_mfma_f32_16x16x32_bf16 v[188:191], v[144:147], a[24:27], v[188:191]\n"
"v_mul_f32_dpp v206, v254, v206 quad_perm:[2,2,2,2] row_mask:0xf bank_mask:0xf\n"
"v_mul_f32_dpp v207, v254, v207 quad_perm:[3,3,3,3] row_mask:0xf bank_mask:0xf\n"
"v_mfma_f32_16x16x32_bf16 v[188:191], v[140:143], a[20:23], v[188:191]\n"
"v_cvt_pk_bf16_f32 v36, v36, v37\n"
"v_cvt_pk_bf16_f32 v37, v38, v39\n"
"v_mfma_f32_16x16x32_bf16 v[188:191], v[136:139], a[16:19], v[188:191]\n"
"v_mul_f32_dpp v208, v254, v208 quad_perm:[0,0,0,0] row_mask:0xf bank_mask:0xf\n"
"v_mul_f32_dpp v209, v254, v209 quad_perm:[1,1,1,1] row_mask:0xf bank_mask:0xf\n"
"v_mfma_f32_16x16x32_bf16 v[196:199], v[136:139], a[32:35], 0\n"
"ds_read_b128 v[136:139], v153 offset:6144\n"
"v_mul_f32_dpp v210, v254, v210 quad_perm:[2,2,2,2] row_mask:0xf bank_mask:0xf\n"
"v_mfma_f32_16x16x32_bf16 v[196:199], v[140:143], a[36:39], v[196:199]\n"
"ds_read_b128 v[140:143], v153 offset:10240\n"
"v_mul_f32_dpp v211, v254, v211 quad_perm:[3,3,3,3] row_mask:0xf bank_mask:0xf\n"
"v_mfma_f32_16x16x32_bf16 v[196:199], v[144:147], a[40:43], v[196:199]\n"
"ds_read_b128 v[144:147], v153 offset:14336\n"
"v_cvt_pk_bf16_f32 v188, v188, v189\n"
"v_mfma_f32_16x16x32_bf16 v[196:199], v[148:151], a[44:47], v[196:199]\n"
"ds_read_b128 v[148:151], v153 offset:3072\n"
"v_cvt_pk_bf16_f32 v189, v190, v191\n"
"v_mfma_f32_16x16x32_bf16 v[164:167], a[144:147], a[48:51], 0\n"
"v_mul_f32_dpp v212, v254, v212 quad_perm:[0,0,0,0] row_mask:0xf bank_mask:0xf\n"
"v_mul_f32_dpp v213, v254, v213 quad_perm:[1,1,1,1] row_mask:0xf bank_mask:0xf\n"
"v_mfma_f32_16x16x32_bf16 v[164:167], a[148:151], a[52:55], v[164:167]\n"
"v_mul_f32_dpp v214, v254, v214 quad_perm:[2,2,2,2] row_mask:0xf bank_mask:0xf\n"
"v_mul_f32_dpp v215, v254, v215 quad_perm:[3,3,3,3] row_mask:0xf bank_mask:0xf\n"
"v_mfma_f32_16x16x32_bf16 v[164:167], a[152:155], a[56:59], v[164:167]\n"
"v_cvt_pk_bf16_f32 v196, v196, v197\n"
"v_cvt_pk_bf16_f32 v197, v198, v199\n"
"v_mfma_f32_16x16x32_bf16 v[164:167], a[156:159], a[60:63], v[164:167]\n"
"v_mul_f32_dpp v216, v254, v216 quad_perm:[0,0,0,0] row_mask:0xf bank_mask:0xf\n"
"v_mul_f32_dpp v217, v254, v217 quad_perm:[1,1,1,1] row_mask:0xf bank_mask:0xf\n"
"v_mfma_f32_16x16x32_bf16 v[168:171], a[156:159], a[76:79], 0\n"
"v_mul_f32_dpp v218, v254, v218 quad_perm:[2,2,2,2] row_mask:0xf bank_mask:0xf\n"
"v_mul_f32_dpp v219, v254, v219 quad_perm:[3,3,3,3] row_mask:0xf bank_mask:0xf\n"
"v_mfma_f32_16x16x32_bf16 v[168:171], a[152:155], a[72:75], v[168:171]\n"
"v_mov_b32 v135, 0\n"
"v_mfma_f32_16x16x32_bf16 v[168:171], a[148:151], a[68:71], v[168:171]\n"
"v_cvt_pk_bf16_f32 v30, v164, v165\n"
"v_cvt_pk_bf16_f32 v31, v166, v167\n"
"v_mfma_f32_16x16x32_bf16 v[168:171], a[144:147], a[64:67], v[168:171]\n"
"v_mfma_f32_16x16x32_bf16 v[184:187], v[236:239], a[0:3], 0\n"
"v_mfma_f32_16x16x32_bf16 v[184:187], v[240:243], a[4:7], v[184:187]\n"
"v_mfma_f32_16x16x32_bf16 v[184:187], v[244:247], a[8:11], v[184:187]\n"
"v_cvt_pk_bf16_f32 v38, v168, v169\n"
"v_cvt_pk_bf16_f32 v39, v170, v171\n"
"v_mfma_f32_16x16x32_bf16 v[184:187], v[248:251], a[12:15], v[184:187]\n"
"v_mfma_f32_16x16x32_bf16 v[192:195], v[248:251], a[28:31], 0\n"
"v_mfma_f32_16x16x32_bf16 v[192:195], v[244:247], a[24:27], v[192:195]\n"
"v_mfma_f32_16x16x32_bf16 v[192:195], v[240:243], a[20:23], v[192:195]\n"
"v_cvt_pk_bf16_f32 v182, v184, v185\n"
"v_cvt_pk_bf16_f32 v183, v186, v187\n"
"v_mfma_f32_16x16x32_bf16 v[192:195], v[236:239], a[16:19], v[192:195]\n"
"v_mfma_f32_16x16x32_bf16 v[200:203], v[236:239], a[32:35], 0\n"
"ds_read_b128 v[236:239], v153 offset:7168\n"
"v_mfma_f32_16x16x32_bf16 v[200:203], v[240:243], a[36:39], v[200:203]\n"
"ds_read_b128 v[240:243], v153 offset:11264\n"
"v_mfma_f32_16x16x32_bf16 v[200:203], v[244:247], a[40:43], v[200:203]\n"
"ds_read_b128 v[244:247], v153 offset:15360\n"
"v_cvt_pk_bf16_f32 v190, v192, v193\n"
"v_mfma_f32_16x16x32_bf16 v[200:203], v[248:251], a[44:47], v[200:203]\n"
"ds_read_b128 v[248:251], v153 offset:16384\n"
"v_cvt_pk_bf16_f32 v191, v194, v195\n"
"v_mfma_f32_16x16x32_bf16 v[184:187], a[224:227], a[0:3], 0\n"
"s_waitcnt lgkmcnt(4)\n"
"v_mfma_f32_16x16x32_bf16 v[184:187], v[136:139], a[4:7], v[184:187]\n"
"v_mfma_f32_16x16x32_bf16 v[184:187], v[140:143], a[8:11], v[184:187]\n"
"v_cvt_pk_bf16_f32 v198, v200, v201\n"
"v_cvt_pk_bf16_f32 v199, v202, v203\n"
"v_mfma_f32_16x16x32_bf16 v[184:187], v[144:147], a[12:15], v[184:187]\n"
"v_mfma_f32_16x16x32_bf16 v[192:195], v[144:147], a[28:31], 0\n"
"v_mfma_f32_16x16x32_bf16 v[192:195], v[140:143], a[24:27], v[192:195]\n"
"v_mfma_f32_16x16x32_bf16 v[192:195], v[136:139], a[20:23], v[192:195]\n"
"v_cvt_pk_bf16_f32 v184, v184, v185\n"
"v_cvt_pk_bf16_f32 v185, v186, v187\n"
"v_mfma_f32_16x16x32_bf16 v[192:195], a[224:227], a[16:19], v[192:195]\n"
"v_mfma_f32_16x16x32_bf16 v[200:203], a[224:227], a[32:35], 0\n"
"ds_read_b128 a[224:227], v153 offset:20576\n"
"v_mfma_f32_16x16x32_bf16 v[200:203], v[136:139], a[36:39], v[200:203]\n"
"ds_read_b128 v[136:139], v153 offset:24768\n"
"v_mfma_f32_16x16x32_bf16 v[200:203], v[140:143], a[40:43], v[200:203]\n"
"ds_read_b128 v[140:143], v153 offset:28960\n"
"v_cvt_pk_bf16_f32 v192, v192, v193\n"
"v_mfma_f32_16x16x32_bf16 v[200:203], v[144:147], a[44:47], v[200:203]\n"
"ds_read2_b32 v[144:145], v158 offset0:0 offset1:2\n"
"ds_read2_b32 v[146:147], v158 offset0:128 offset1:130\n"
"v_mfma_f32_16x16x32_bf16 v[204:207], a[80:83], a[160:163], v[204:207]\n"
"v_cvt_pk_bf16_f32 v193, v194, v195\n"
"v_mov_b32_dpp v158, v158 row_ror:8 row_mask:0xf bank_mask:0xf\n"
"v_mfma_f32_16x16x32_bf16 v[204:207], a[84:87], a[164:167], v[204:207]\n"
"v_mfma_f32_16x16x32_bf16 v[204:207], a[88:91], a[168:171], v[204:207]\n"
"v_cvt_pk_bf16_f32 v200, v200, v201\n"
"v_cvt_pk_bf16_f32 v201, v202, v203\n"
"v_mfma_f32_16x16x32_bf16 v[204:207], a[92:95], a[172:175], v[204:207]\n"
"v_mfma_f32_16x16x32_bf16 v[164:167], v[148:151], a[0:3], 0\n"
"s_waitcnt lgkmcnt(5)\n"
"v_mfma_f32_16x16x32_bf16 v[164:167], v[236:239], a[4:7], v[164:167]\n"
"v_mfma_f32_16x16x32_bf16 v[164:167], v[240:243], a[8:11], v[164:167]\n"
"v_mfma_f32_16x16x32_bf16 v[164:167], v[244:247], a[12:15], v[164:167]\n"
"v_mfma_f32_16x16x32_bf16 v[168:171], v[244:247], a[28:31], 0\n"
"v_mfma_f32_16x16x32_bf16 v[168:171], v[240:243], a[24:27], v[168:171]\n"
"v_mfma_f32_16x16x32_bf16 v[168:171], v[236:239], a[20:23], v[168:171]\n"
"v_cvt_pk_bf16_f32 v186, v164, v165\n"
"v_cvt_pk_bf16_f32 v187, v166, v167\n"
"v_mfma_f32_16x16x32_bf16 v[168:171], v[148:151], a[16:19], v[168:171]\n"
"v_mfma_f32_16x16x32_bf16 v[172:175], v[148:151], a[32:35], 0\n"
"ds_read2_b32 v[148:149], v158 offset0:0 offset1:2\n"
"ds_read2_b32 v[150:151], v158 offset0:128 offset1:130\n"
"v_mfma_f32_16x16x32_bf16 v[172:175], v[236:239], a[36:39], v[172:175]\n"
"ds_read_b128 v[236:239], v153 offset:18496\n"
"v_mfma_f32_16x16x32_bf16 v[172:175], v[240:243], a[40:43], v[172:175]\n"
"ds_read_b128 v[240:243], v153 offset:22688\n"
"v_cvt_pk_bf16_f32 v194, v168, v169\n"
"v_mfma_f32_16x16x32_bf16 v[172:175], v[244:247], a[44:47], v[172:175]\n"
"ds_read_b128 v[244:247], v153 offset:26880\n"
"v_cvt_pk_bf16_f32 v195, v170, v171\n"
"v_mfma_f32_16x16x32_bf16 v[208:211], a[92:95], a[188:191], v[208:211]\n"
"v_mfma_f32_16x16x32_bf16 v[208:211], a[88:91], a[184:187], v[208:211]\n"
"v_mfma_f32_16x16x32_bf16 v[208:211], a[84:87], a[180:183], v[208:211]\n"
"v_cvt_pk_bf16_f32 v202, v172, v173\n"
"v_cvt_pk_bf16_f32 v203, v174, v175\n"
"v_mfma_f32_16x16x32_bf16 v[208:211], a[80:83], a[176:179], v[208:211]\n"
"v_mfma_f32_16x16x32_bf16 v[212:215], a[80:83], a[192:195], v[212:215]\n"
"v_mfma_f32_16x16x32_bf16 v[212:215], a[84:87], a[196:199], v[212:215]\n"
"v_mfma_f32_16x16x32_bf16 v[212:215], a[88:91], a[200:203], v[212:215]\n"
"v_cvt_pk_bf16_f32 v204, v204, v208\n"
"v_cvt_pk_bf16_f32 v208, v205, v209\n"
"v_mfma_f32_16x16x32_bf16 v[212:215], a[92:95], a[204:207], v[212:215]\n"
"v_pk_max_i16 v204, v204, 0\n"
"v_pk_max_i16 v208, v208, 0\n"
"v_mfma_f32_16x16x32_bf16 v[216:219], a[92:95], a[220:223], v[216:219]\n"
"s_waitcnt vmcnt(16)\n"
"ds_read_b128 a[92:95], v154 offset:8896\n"
"v_cvt_pk_bf16_f32 v206, v206, v210\n"
"v_mfma_f32_16x16x32_bf16 v[216:219], a[88:91], a[216:219], v[216:219]\n"
"ds_read_b128 a[88:91], v154 offset:8832\n"
"v_cvt_pk_bf16_f32 v210, v207, v211\n"
"v_mfma_f32_16x16x32_bf16 v[216:219], a[84:87], a[212:215], v[216:219]\n"
"ds_read_b128 a[84:87], v154 offset:8768\n"
"v_pk_max_i16 v206, v206, 0\n"
"v_mfma_f32_16x16x32_bf16 v[216:219], a[80:83], a[208:211], v[216:219]\n"
"ds_read_b128 a[80:83], v154 offset:8704\n"
"v_pk_max_i16 v210, v210, 0\n"
"v_mfma_f32_16x16x32_bf16 v[48:51], a[0:3], v[248:251], 0\n"
"s_waitcnt lgkmcnt(4)\n"
"v_mfma_f32_16x16x32_bf16 v[48:51], a[4:7], a[224:227], v[48:51]\n"
"v_mfma_f32_16x16x32_bf16 v[48:51], a[8:11], v[136:139], v[48:51]\n"
"v_cvt_pk_bf16_f32 v205, v212, v216\n"
"v_cvt_pk_bf16_f32 v209, v213, v217\n"
"v_mfma_f32_16x16x32_bf16 v[48:51], a[12:15], v[140:143], v[48:51]\n"
"v_pk_max_i16 v205, v205, 0\n"
"v_pk_max_i16 v209, v209, 0\n"
"v_mfma_f32_16x16x32_bf16 v[60:63], a[28:31], v[140:143], 0\n"
"v_cvt_pk_bf16_f32 v207, v214, v218\n"
"v_cvt_pk_bf16_f32 v211, v215, v219\n"
"v_mfma_f32_16x16x32_bf16 v[60:63], a[24:27], v[136:139], v[60:63]\n"
"v_pk_max_i16 v207, v207, 0\n"
"v_pk_max_i16 v211, v211, 0\n"
"v_mfma_f32_16x16x32_bf16 v[60:63], a[20:23], a[224:227], v[60:63]\n"
"v_cvt_pk_bf16_f32 v48, v48, v49\n"
"v_cvt_pk_bf16_f32 v49, v50, v51\n"
"v_mfma_f32_16x16x32_bf16 v[60:63], a[16:19], v[248:251], v[60:63]\n"
"buffer_store_dwordx2 v[204:205], v162, s[84:87], s69 offen offset:0 sc0 sc1\n"
"v_mfma_f32_16x16x32_bf16 v[52:55], a[32:35], v[248:251], 0\n"
"v_accvgpr_read_b32 v219, a240\n"
"v_mfma_f32_16x16x32_bf16 v[52:55], a[36:39], a[224:227], v[52:55]\n"
"v_mfma_f32_16x16x32_bf16 v[52:55], a[40:43], v[136:139], v[52:55]\n"
"v_cvt_pk_bf16_f32 v50, v60, v61\n"
"v_cvt_pk_bf16_f32 v51, v62, v63\n"
"v_mfma_f32_16x16x32_bf16 v[52:55], a[44:47], v[140:143], v[52:55]\n"
"s_waitcnt lgkmcnt(0)\n"
"v_mfma_f32_16x16x32_bf16 v[40:43], a[96:99], a[80:83], 0\n"
"v_mfma_f32_16x16x32_bf16 v[40:43], a[100:103], a[84:87], v[40:43]\n"
"v_mfma_f32_16x16x32_bf16 v[40:43], a[104:107], a[88:91], v[40:43]\n"
"v_cvt_pk_bf16_f32 v52, v52, v53\n"
"v_cvt_pk_bf16_f32 v53, v54, v55\n"
"v_mfma_f32_16x16x32_bf16 v[40:43], a[108:111], a[92:95], v[40:43]\n"
"buffer_store_dwordx2 v[208:209], v162, s[84:87], s69 offen offset:256 sc0 sc1\n"
"v_mfma_f32_16x16x32_bf16 v[176:179], a[112:115], a[80:83], 0\n"
"s_add_u32 m0, s28, 8704\n"
"v_mfma_f32_16x16x32_bf16 v[176:179], a[116:119], a[84:87], v[176:179]\n"
"buffer_load_dwordx4 v152, s[24:27], s48 offen offset:0 lds\n"
"v_mfma_f32_16x16x32_bf16 v[176:179], a[120:123], a[88:91], v[176:179]\n"
"v_cvt_pk_bf16_f32 v40, v40, v41\n"
"v_cvt_pk_bf16_f32 v41, v42, v43\n"
"v_mfma_f32_16x16x32_bf16 v[176:179], a[124:127], a[92:95], v[176:179]\n"
"v_mfma_f32_16x16x32_bf16 v[44:47], a[128:131], a[80:83], 0\n"
"buffer_load_dwordx4 v152, s[24:27], s49 offen offset:1088 lds\n"
"v_mfma_f32_16x16x32_bf16 v[44:47], a[132:135], a[84:87], v[44:47]\n"
"v_mfma_f32_16x16x32_bf16 v[44:47], a[136:139], a[88:91], v[44:47]\n"
"v_cvt_pk_bf16_f32 v42, v176, v177\n"
"v_cvt_pk_bf16_f32 v43, v178, v179\n"
"v_mfma_f32_16x16x32_bf16 v[44:47], a[140:143], a[92:95], v[44:47]\n"
"buffer_store_dwordx2 v[206:207], v162, s[84:87], s69 offen offset:512 sc0 sc1\n"
"v_mfma_f32_16x16x32_bf16 v[164:167], a[144:147], a[80:83], 0\n"
"s_nop 0\n"
"ds_read_b64 v[206:207], v163 offset:16384\n"
"v_add_u32 v163, 512, v163\n"
"v_mfma_f32_16x16x32_bf16 v[164:167], a[148:151], a[84:87], v[164:167]\n"
"buffer_load_dwordx4 v152, s[24:27], s50 offen offset:2176 lds\n"
"v_mfma_f32_16x16x32_bf16 v[164:167], a[152:155], a[88:91], v[164:167]\n"
"v_cvt_pk_bf16_f32 v44, v44, v45\n"
"v_cvt_pk_bf16_f32 v45, v46, v47\n"
"v_mfma_f32_16x16x32_bf16 v[164:167], a[156:159], a[92:95], v[164:167]\n"
"v_mfma_f32_16x16x32_bf16 v[64:67], a[60:63], v[140:143], 0\n"
"v_mfma_f32_16x16x32_bf16 v[64:67], a[56:59], v[136:139], v[64:67]\n"
"v_mfma_f32_16x16x32_bf16 v[64:67], a[52:55], a[224:227], v[64:67]\n"
"buffer_load_dwordx4 v152, s[24:27], s51 offen offset:3264 lds\n"
"v_mfma_f32_16x16x32_bf16 v[64:67], a[48:51], v[248:251], v[64:67]\n"
"buffer_store_dwordx2 v[210:211], v162, s[84:87], s69 offen offset:768 sc0 sc1\n"
"s_branch .Lp2f%=\n"
".Lepf%=:\n"
"v_mfma_f32_16x16x32_bf16 v[164:167], a[80:83], a[160:163], 0\n"
".Lpad6_%=:\n"
"s_cbranch_vccnz .Lback0_%=\n"
"v_exp_f32 v134, v134\n"
"v_mfma_f32_16x16x32_bf16 v[164:167], a[84:87], a[164:167], v[164:167]\n"
"v_cvt_pk_bf16_f32 v133, v132, v134\n"
"v_add_f32 v132, v132, v134\n"
"v_mfma_f32_16x16x32_bf16 v[164:167], a[88:91], a[168:171], v[164:167]\n"
"v_exp_f32 v108, v108\n"
"v_mfma_f32_16x16x32_bf16 v[164:167], a[92:95], a[172:175], v[164:167]\n"
"buffer_store_dwordx2 v[206:207], v162, s[84:87], s65 offen offset:512 sc0 sc1\n"
"v_permlane16_swap_b32 v132, v133\n"
"v_mfma_f32_16x16x32_bf16 v[168:171], a[92:95], a[188:191], 0\n"
"v_exp_f32 v109, v109\n"
"v_mfma_f32_16x16x32_bf16 v[168:171], a[88:91], a[184:187], v[168:171]\n"
"v_exp_f32 v110, v110\n"
"v_mfma_f32_16x16x32_bf16 v[168:171], a[84:87], a[180:183], v[168:171]\n"
"v_exp_f32 v111, v111\n"
"v_mfma_f32_16x16x32_bf16 v[168:171], a[80:83], a[176:179], v[168:171]\n"
"buffer_store_dwordx2 v[210:211], v162, s[84:87], s65 offen offset:768 sc0 sc1\n"
"v_exp_f32 v124, v124\n"
"v_mfma_f32_16x16x32_bf16 v[252:255], a[252:255], v[96:99], v[132:135]\n"
"v_exp_f32 v125, v125\n"
"v_cvt_pk_bf16_f32 v108, v108, v109\n"
"v_mfma_f32_16x16x4_4b_bf16 v[220:235], v[132:133], a[244:245], 0 cbsz:2 abid:1\n"
"v_exp_f32 v126, v126\n"
"v_exp_f32 v127, v127\n"
"v_cvt_pk_bf16_f32 v109, v110, v111\n"
"v_cvt_pk_bf16_f32 v110, v124, v125\n"
"v_cvt_pk_bf16_f32 v111, v126, v127\n"
"v_exp_f32 v112, v112\n"
"v_smfmac_f32_16x16x64_bf16 v[252:255], a[248:251], v[100:107], v157\n"
"v_exp_f32 v113, v113\n"
"v_mfma_f32_16x16x32_bf16 v[220:223], v[96:99], v[48:51], v[220:223]\n"
"v_exp_f32 v116, v116\n"
"v_mfma_f32_16x16x32_bf16 v[220:223], v[100:103], v[52:55], v[220:223]\n"
"v_exp_f32 v117, v117\n"
"v_mfma_f32_16x16x32_bf16 v[220:223], v[104:107], v[56:59], v[220:223]\n"
"v_add_f32 v252, v252, v253\n"
"v_rcp_f32 v253, v252\n"
"v_mfma_f32_16x16x32_bf16 v[224:227], v[104:107], v[68:71], v[224:227]\n"
"ds_bpermute_b32 v254, v160, v253 offset:0\n"
"v_mfma_f32_16x16x32_bf16 v[224:227], v[100:103], v[64:67], v[224:227]\n"
"v_max3_f32 v159, v159, v252, v253\n"
"v_mfma_f32_16x16x32_bf16 v[224:227], v[96:99], v[60:63], v[224:227]\n"
"v_exp_f32 v114, v114\n"
"v_mfma_f32_16x16x32_bf16 v[228:231], v[96:99], v[72:75], v[228:231]\n"
"v_exp_f32 v115, v115\n"
"v_mfma_f32_16x16x32_bf16 v[228:231], v[100:103], v[76:79], v[228:231]\n"
"v_exp_f32 v118, v118\n"
"v_mfma_f32_16x16x32_bf16 v[228:231], v[104:107], v[80:83], v[228:231]\n"
"v_exp_f32 v119, v119\n"
"v_mfma_f32_16x16x32_bf16 v[232:235], v[104:107], v[92:95], v[232:235]\n"
"v_exp_f32 v120, v120\n"
"v_mfma_f32_16x16x32_bf16 v[232:235], v[100:103], v[88:91], v[232:235]\n"
"v_exp_f32 v121, v121\n"
"v_mfma_f32_16x16x32_bf16 v[232:235], v[96:99], v[84:87], v[232:235]\n"
"v_exp_f32 v122, v122\n"
"v_mfma_f32_16x16x32_bf16 v[124:127], v[44:47], v[184:187], 0\n"
"v_exp_f32 v123, v123\n"
"v_mfma_f32_16x16x32_bf16 v[124:127], v[40:43], v[180:183], v[124:127]\n"
"v_cvt_pk_bf16_f32 v112, v112, v113\n"
"v_cvt_pk_bf16_f32 v116, v116, v117\n"
"v_cvt_pk_bf16_f32 v113, v114, v115\n"
"v_mfma_f32_16x16x4_4b_bf16 v[204:219], v[132:133], a[244:245], 0 cbsz:2 abid:3\n"
"s_waitcnt lgkmcnt(0)\n"
"v_mul_f32_dpp v220, v254, v220 quad_perm:[0,0,0,0] row_mask:0xf bank_mask:0xf\n"
"v_mul_f32_dpp v221, v254, v221 quad_perm:[1,1,1,1] row_mask:0xf bank_mask:0xf\n"
"v_mul_f32_dpp v222, v254, v222 quad_perm:[2,2,2,2] row_mask:0xf bank_mask:0xf\n"
"v_mul_f32_dpp v223, v254, v223 quad_perm:[3,3,3,3] row_mask:0xf bank_mask:0xf\n"
"v_mul_f32_dpp v224, v254, v224 quad_perm:[0,0,0,0] row_mask:0xf bank_mask:0xf\n"
"v_mul_f32_dpp v225, v254, v225 quad_perm:[1,1,1,1] row_mask:0xf bank_mask:0xf\n"
"v_mul_f32_dpp v226, v254, v226 quad_perm:[2,2,2,2] row_mask:0xf bank_mask:0xf\n"
"v_mul_f32_dpp v227, v254, v227 quad_perm:[3,3,3,3] row_mask:0xf bank_mask:0xf\n"
"v_mfma_f32_16x16x32_bf16 v[96:99], v[0:3], v[188:191], 0\n"
"v_mul_f32_dpp v228, v254, v228 quad_perm:[0,0,0,0] row_mask:0xf bank_mask:0xf\n"
"v_mul_f32_dpp v229, v254, v229 quad_perm:[1,1,1,1] row_mask:0xf bank_mask:0xf\n"
"v_mfma_f32_16x16x32_bf16 v[96:99], v[4:7], v[192:195], v[96:99]\n"
"v_mul_f32_dpp v230, v254, v230 quad_perm:[2,2,2,2] row_mask:0xf bank_mask:0xf\n"
"v_mul_f32_dpp v231, v254, v231 quad_perm:[3,3,3,3] row_mask:0xf bank_mask:0xf\n"
"v_mfma_f32_16x16x32_bf16 v[204:207], v[108:111], v[48:51], v[204:207]\n"
"v_mul_f32_dpp v232, v254, v232 quad_perm:[0,0,0,0] row_mask:0xf bank_mask:0xf\n"
"v_mul_f32_dpp v233, v254, v233 quad_perm:[1,1,1,1] row_mask:0xf bank_mask:0xf\n"
"v_mfma_f32_16x16x32_bf16 v[212:215], v[108:111], v[72:75], v[212:215]\n"
"v_mul_f32_dpp v234, v254, v234 quad_perm:[2,2,2,2] row_mask:0xf bank_mask:0xf\n"
"v_mul_f32_dpp v235, v254, v235 quad_perm:[3,3,3,3] row_mask:0xf bank_mask:0xf\n"
"v_mfma_f32_16x16x32_bf16 v[252:255], a[252:255], v[108:111], v[132:135]\n"
"v_exp_f32 v124, v124\n"
"v_mfma_f32_16x16x32_bf16 v[100:103], v[16:19], v[188:191], 0\n"
"v_exp_f32 v125, v125\n"
"v_mfma_f32_16x16x32_bf16 v[100:103], v[20:23], v[192:195], v[100:103]\n"
"v_exp_f32 v126, v126\n"
"v_mfma_f32_16x16x32_bf16 v[104:107], v[32:35], v[188:191], 0\n"
"v_exp_f32 v127, v127\n"
"v_mfma_f32_16x16x32_bf16 v[104:107], v[36:39], v[192:195], v[104:107]\n"
"v_cvt_pk_bf16_f32 v117, v118, v119\n"
"v_cvt_pk_bf16_f32 v114, v120, v121\n"
"v_mfma_f32_16x16x32_bf16 v[220:223], a[32:35], a[160:163], v[220:223]\n"
"v_cvt_pk_bf16_f32 v115, v122, v123\n"
"v_cvt_pk_bf16_f32 v118, v124, v125\n"
"v_mfma_f32_16x16x32_bf16 v[220:223], a[36:39], a[164:167], v[220:223]\n"
"v_cvt_pk_bf16_f32 v119, v126, v127\n"
"v_add_f32 v130, v130, v131\n"
"v_mfma_f32_16x16x32_bf16 v[220:223], a[40:43], a[168:171], v[220:223]\n"
"v_exp_f32 v96, v96\n"
"v_mfma_f32_16x16x32_bf16 v[220:223], a[44:47], a[172:175], v[220:223]\n"
"v_exp_f32 v97, v97\n"
"v_smfmac_f32_16x16x64_bf16 v[252:255], a[248:251], v[112:119], v157\n"
"v_cvt_pk_bf16_f32 v96, v96, v97\n"
"v_mfma_f32_16x16x32_bf16 v[120:123], v[12:15], v[192:195], 0\n"
"v_exp_f32 v98, v98\n"
"v_mfma_f32_16x16x32_bf16 v[120:123], v[8:11], v[188:191], v[120:123]\n"
"v_exp_f32 v99, v99\n"
"v_mfma_f32_16x16x32_bf16 v[208:211], v[116:119], v[68:71], v[208:211]\n"
"v_add_f32 v252, v252, v253\n"
"v_cvt_pk_bf16_f32 v97, v98, v99\n"
"v_mfma_f32_16x16x32_bf16 v[208:211], v[112:115], v[64:67], v[208:211]\n"
"v_rcp_f32 v253, v252\n"
"v_mfma_f32_16x16x32_bf16 v[208:211], v[108:111], v[60:63], v[208:211]\n"
"ds_bpermute_b32 v254, v160, v253 offset:128\n"
"v_mfma_f32_16x16x32_bf16 v[216:219], v[116:119], v[92:95], v[216:219]\n"
"v_exp_f32 v120, v120\n"
"v_mfma_f32_16x16x32_bf16 v[216:219], v[112:115], v[88:91], v[216:219]\n"
"v_exp_f32 v121, v121\n"
"v_mfma_f32_16x16x32_bf16 v[216:219], v[108:111], v[84:87], v[216:219]\n"
"v_exp_f32 v122, v122\n"
"v_mfma_f32_16x16x32_bf16 v[204:207], v[112:115], v[52:55], v[204:207]\n"
"v_exp_f32 v123, v123\n"
"v_mfma_f32_16x16x32_bf16 v[204:207], v[116:119], v[56:59], v[204:207]\n"
"v_cvt_pk_bf16_f32 v98, v120, v121\n"
"v_cvt_pk_bf16_f32 v99, v122, v123\n"
"v_mfma_f32_16x16x32_bf16 v[212:215], v[112:115], v[76:79], v[212:215]\n"
"v_exp_f32 v128, v128\n"
"v_mfma_f32_16x16x32_bf16 v[212:215], v[116:119], v[80:83], v[212:215]\n"
"v_exp_f32 v130, v130\n"
"v_mfma_f32_16x16x32_bf16 v[124:127], v[28:31], v[192:195], 0\n"
"v_cvt_pk_bf16_f32 v129, v128, v130\n"
"v_add_f32 v128, v128, v130\n"
"v_mfma_f32_16x16x32_bf16 v[124:127], v[24:27], v[188:191], v[124:127]\n"
"v_max3_f32 v159, v159, v252, v253\n"
"v_permlane16_swap_b32 v128, v129\n"
"v_mfma_f32_16x16x32_bf16 v[120:123], v[44:47], v[192:195], 0\n"
"v_exp_f32 v100, v100\n"
"v_mfma_f32_16x16x32_bf16 v[120:123], v[40:43], v[188:191], v[120:123]\n"
"s_waitcnt lgkmcnt(0)\n"
"v_mul_f32_dpp v204, v254, v204 quad_perm:[0,0,0,0] row_mask:0xf bank_mask:0xf\n"
"v_mul_f32_dpp v205, v254, v205 quad_perm:[1,1,1,1] row_mask:0xf bank_mask:0xf\n"
"v_mfma_f32_16x16x32_bf16 v[224:227], a[44:47], a[188:191], v[224:227]\n"
"v_mul_f32_dpp v206, v254, v206 quad_perm:[2,2,2,2] row_mask:0xf bank_mask:0xf\n"
"v_mul_f32_dpp v207, v254, v207 quad_perm:[3,3,3,3] row_mask:0xf bank_mask:0xf\n"
"v_mfma_f32_16x16x32_bf16 v[224:227], a[40:43], a[184:187], v[224:227]\n"
"v_mul_f32_dpp v208, v254, v208 quad_perm:[0,0,0,0] row_mask:0xf bank_mask:0xf\n"
"v_mul_f32_dpp v209, v254, v209 quad_perm:[1,1,1,1] row_mask:0xf bank_mask:0xf\n"
"v_mfma_f32_16x16x32_bf16 v[224:227], a[36:39], a[180:183], v[224:227]\n"
"v_mul_f32_dpp v210, v254, v210 quad_perm:[2,2,2,2] row_mask:0xf bank_mask:0xf\n"
"v_mul_f32_dpp v211, v254, v211 quad_perm:[3,3,3,3] row_mask:0xf bank_mask:0xf\n"
"v_mfma_f32_16x16x32_bf16 v[224:227], a[32:35], a[176:179], v[224:227]\n"
"v_mul_f32_dpp v212, v254, v212 quad_perm:[0,0,0,0] row_mask:0xf bank_mask:0xf\n"
"v_mul_f32_dpp v213, v254, v213 quad_perm:[1,1,1,1] row_mask:0xf bank_mask:0xf\n"
"v_mfma_f32_16x16x32_bf16 v[228:231], a[32:35], a[192:195], v[228:231]\n"
"v_mul_f32_dpp v214, v254, v214 quad_perm:[2,2,2,2] row_mask:0xf bank_mask:0xf\n"
"v_mul_f32_dpp v215, v254, v215 quad_perm:[3,3,3,3] row_mask:0xf bank_mask:0xf\n"
"v_mfma_f32_16x16x32_bf16 v[228:231], a[36:39], a[196:199], v[228:231]\n"
"v_mul_f32_dpp v216, v254, v216 quad_perm:[0,0,0,0] row_mask:0xf bank_mask:0xf\n"
"v_mul_f32_dpp v217, v254, v217 quad_perm:[1,1,1,1] row_mask:0xf bank_mask:0xf\n"
"v_mfma_f32_16x16x32_bf16 v[228:231], a[40:43], a[200:203], v[228:231]\n"
"v_mul_f32_dpp v218, v254, v218 quad_perm:[2,2,2,2] row_mask:0xf bank_mask:0xf\n"
"v_mul_f32_dpp v219, v254, v219 quad_perm:[3,3,3,3] row_mask:0xf bank_mask:0xf\n"
"v_mfma_f32_16x16x32_bf16 v[228:231], a[44:47], a[204:207], v[228:231]\n"
"v_exp_f32 v101, v101\n"
"v_mfma_f32_16x16x32_bf16 v[252:255], a[252:255], v[96:99], v[128:131]\n"
"v_exp_f32 v104, v104\n"
"v_mfma_f32_16x16x32_bf16 v[232:235], a[44:47], a[220:223], v[232:235]\n"
"v_exp_f32 v105, v105\n"
"v_mfma_f32_16x16x32_bf16 v[232:235], a[40:43], a[216:219], v[232:235]\n"
"v_exp_f32 v102, v102\n"
"v_mfma_f32_16x16x32_bf16 v[232:235], a[36:39], a[212:215], v[232:235]\n"
"v_exp_f32 v103, v103\n"
"v_mfma_f32_16x16x32_bf16 v[232:235], a[32:35], a[208:211], v[232:235]\n"
"v_exp_f32 v106, v106\n"
"v_mfma_f32_16x16x32_bf16 v[108:111], v[0:3], v[196:199], 0\n"
"v_exp_f32 v107, v107\n"
"v_mfma_f32_16x16x32_bf16 v[108:111], v[4:7], v[200:203], v[108:111]\n"
"v_exp_f32 v124, v124\n"
"v_mfma_f32_16x16x32_bf16 v[112:115], v[16:19], v[196:199], 0\n"
"v_exp_f32 v125, v125\n"
"v_mfma_f32_16x16x32_bf16 v[112:115], v[20:23], v[200:203], v[112:115]\n"
"v_exp_f32 v126, v126\n"
"v_mfma_f32_16x16x32_bf16 v[116:119], v[32:35], v[196:199], 0\n"
"v_exp_f32 v127, v127\n"
"v_mfma_f32_16x16x32_bf16 v[116:119], v[36:39], v[200:203], v[116:119]\n"
"v_exp_f32 v120, v120\n"
"v_mfma_f32_16x16x32_bf16 v[204:207], a[48:51], a[160:163], v[204:207]\n"
"v_exp_f32 v121, v121\n"
"v_mfma_f32_16x16x32_bf16 v[204:207], a[52:55], a[164:167], v[204:207]\n"
"v_exp_f32 v122, v122\n"
"v_mfma_f32_16x16x32_bf16 v[204:207], a[56:59], a[168:171], v[204:207]\n"
"v_exp_f32 v123, v123\n"
"v_mfma_f32_16x16x32_bf16 v[204:207], a[60:63], a[172:175], v[204:207]\n"
"v_cvt_pk_bf16_f32 v100, v100, v101\n"
"v_cvt_pk_bf16_f32 v104, v104, v105\n"
"v_mfma_f32_16x16x32_bf16 v[208:211], a[60:63], a[188:191], v[208:211]\n"
"v_cvt_pk_bf16_f32 v101, v102, v103\n"
"v_cvt_pk_bf16_f32 v105, v106, v107\n"
"v_mfma_f32_16x16x32_bf16 v[208:211], a[56:59], a[184:187], v[208:211]\n"
"v_cvt_pk_bf16_f32 v102, v124, v125\n"
"v_cvt_pk_bf16_f32 v103, v126, v127\n"
"v_mfma_f32_16x16x32_bf16 v[208:211], a[52:55], a[180:183], v[208:211]\n"
"v_cvt_pk_bf16_f32 v106, v120, v121\n"
"v_cvt_pk_bf16_f32 v107, v122, v123\n"
"v_mfma_f32_16x16x32_bf16 v[208:211], a[48:51], a[176:179], v[208:211]\n"
"v_cvt_pk_bf16_f32 v220, v220, v224\n"
"v_cvt_pk_bf16_f32 v222, v222, v226\n"
"v_smfmac_f32_16x16x64_bf16 v[252:255], a[248:251], v[100:107], v157\n"
"v_cvt_pk_bf16_f32 v224, v221, v225\n"
"v_mfma_f32_16x16x32_bf16 v[124:127], v[12:15], v[200:203], 0\n"
"v_cvt_pk_bf16_f32 v226, v223, v227\n"
"v_cvt_pk_bf16_f32 v221, v228, v232\n"
"v_mfma_f32_16x16x32_bf16 v[124:127], v[8:11], v[196:199], v[124:127]\n"
"v_cvt_pk_bf16_f32 v225, v229, v233\n"
"v_cvt_pk_bf16_f32 v223, v230, v234\n"
"v_mfma_f32_16x16x32_bf16 v[120:123], v[28:31], v[200:203], 0\n"
"v_add_f32 v252, v252, v253\n"
"v_cvt_pk_bf16_f32 v227, v231, v235\n"
"v_mfma_f32_16x16x32_bf16 v[120:123], v[24:27], v[196:199], v[120:123]\n"
"v_rcp_f32 v253, v252\n"
"v_mfma_f32_16x16x32_bf16 v[212:215], a[48:51], a[192:195], v[212:215]\n"
"ds_bpermute_b32 v254, v160, v253 offset:0\n"
"v_mfma_f32_16x16x32_bf16 v[212:215], a[52:55], a[196:199], v[212:215]\n"
"v_pk_max_i16 v220, v220, 0\n"
"v_pk_max_i16 v221, v221, 0\n"
"v_mfma_f32_16x16x32_bf16 v[212:215], a[56:59], a[200:203], v[212:215]\n"
"v_pk_max_i16 v224, v224, 0\n"
"v_pk_max_i16 v225, v225, 0\n"
"v_mfma_f32_16x16x32_bf16 v[212:215], a[60:63], a[204:207], v[212:215]\n"
"buffer_store_dwordx2 v[220:221], v162, s[84:87], s66 offen offset:0 sc0 sc1\n"
"v_pk_max_i16 v222, v222, 0\n"
"v_pk_max_i16 v223, v223, 0\n"
"v_mfma_f32_16x16x32_bf16 v[216:219], a[60:63], a[220:223], v[216:219]\n"
"v_pk_max_i16 v226, v226, 0\n"
"v_pk_max_i16 v227, v227, 0\n"
"v_mfma_f32_16x16x32_bf16 v[216:219], a[56:59], a[216:219], v[216:219]\n"
"v_exp_f32 v108, v108\n"
"v_mfma_f32_16x16x32_bf16 v[216:219], a[52:55], a[212:215], v[216:219]\n"
"v_exp_f32 v109, v109\n"
"v_mfma_f32_16x16x32_bf16 v[216:219], a[48:51], a[208:211], v[216:219]\n"
"buffer_store_dwordx2 v[224:225], v162, s[84:87], s66 offen offset:256 sc0 sc1\n"
"v_exp_f32 v110, v110\n"
"v_mfma_f32_16x16x32_bf16 v[172:175], a[80:83], a[192:195], 0\n"
"v_exp_f32 v111, v111\n"
"v_mfma_f32_16x16x32_bf16 v[172:175], a[84:87], a[196:199], v[172:175]\n"
"v_exp_f32 v124, v124\n"
"v_mfma_f32_16x16x32_bf16 v[172:175], a[88:91], a[200:203], v[172:175]\n"
"v_exp_f32 v125, v125\n"
"v_mfma_f32_16x16x32_bf16 v[172:175], a[92:95], a[204:207], v[172:175]\n"
"buffer_store_dwordx2 v[222:223], v162, s[84:87], s66 offen offset:512 sc0 sc1\n"
"v_exp_f32 v126, v126\n"
"v_mfma_f32_16x16x32_bf16 v[176:179], a[92:95], a[220:223], 0\n"
"v_exp_f32 v127, v127\n"
"v_mfma_f32_16x16x32_bf16 v[176:179], a[88:91], a[216:219], v[176:179]\n"
"v_cvt_pk_bf16_f32 v108, v108, v109\n"
"v_cvt_pk_bf16_f32 v109, v110, v111\n"
"v_mfma_f32_16x16x32_bf16 v[176:179], a[84:87], a[212:215], v[176:179]\n"
"v_cvt_pk_bf16_f32 v110, v124, v125\n"
"v_cvt_pk_bf16_f32 v111, v126, v127\n"
"v_mfma_f32_16x16x32_bf16 v[176:179], a[80:83], a[208:211], v[176:179]\n"
"buffer_store_dwordx2 v[226:227], v162, s[84:87], s66 offen offset:768 sc0 sc1\n"
"v_exp_f32 v112, v112\n"
"v_mfma_f32_16x16x32_bf16 v[124:127], v[44:47], v[200:203], 0\n"
".Lpad7_%=:\n"
"s_cbranch_vccnz .Lback0_%=\n"
"v_exp_f32 v113, v113\n"
"v_mfma_f32_16x16x32_bf16 v[124:127], v[40:43], v[196:199], v[124:127]\n"
"v_exp_f32 v116, v116\n"
"v_exp_f32 v117, v117\n"
"v_mfma_f32_16x16x4_4b_bf16 v[220:235], v[128:129], a[244:245], 0 cbsz:2 abid:1\n"
"v_exp_f32 v114, v114\n"
"v_exp_f32 v115, v115\n"
"v_exp_f32 v118, v118\n"
"v_exp_f32 v119, v119\n"
"v_mfma_f32_16x16x32_bf16 v[132:135], a[252:255], v[108:111], v[128:131]\n"
"v_exp_f32 v120, v120\n"
"v_exp_f32 v121, v121\n"
"v_mfma_f32_16x16x32_bf16 v[220:223], v[96:99], v[48:51], v[220:223]\n"
"v_exp_f32 v122, v122\n"
"v_mfma_f32_16x16x32_bf16 v[220:223], v[100:103], v[52:55], v[220:223]\n"
"v_exp_f32 v123, v123\n"
"v_mfma_f32_16x16x32_bf16 v[220:223], v[104:107], v[56:59], v[220:223]\n"
"v_exp_f32 v124, v124\n"
"v_mfma_f32_16x16x32_bf16 v[224:227], v[104:107], v[68:71], v[224:227]\n"
"v_exp_f32 v125, v125\n"
"v_mfma_f32_16x16x32_bf16 v[224:227], v[100:103], v[64:67], v[224:227]\n"
"v_exp_f32 v126, v126\n"
"v_mfma_f32_16x16x32_bf16 v[224:227], v[96:99], v[60:63], v[224:227]\n"
"v_exp_f32 v127, v127\n"
"v_mfma_f32_16x16x32_bf16 v[228:231], v[96:99], v[72:75], v[228:231]\n"
"v_cvt_pk_bf16_f32 v112, v112, v113\n"
"v_cvt_pk_bf16_f32 v116, v116, v117\n"
"v_mfma_f32_16x16x32_bf16 v[228:231], v[100:103], v[76:79], v[228:231]\n"
"v_cvt_pk_bf16_f32 v113, v114, v115\n"
"v_cvt_pk_bf16_f32 v117, v118, v119\n"
"v_mfma_f32_16x16x32_bf16 v[228:231], v[104:107], v[80:83], v[228:231]\n"
"v_cvt_pk_bf16_f32 v114, v120, v121\n"
"v_cvt_pk_bf16_f32 v115, v122, v123\n"
"v_mfma_f32_16x16x32_bf16 v[232:235], v[104:107], v[92:95], v[232:235]\n"
"v_cvt_pk_bf16_f32 v118, v124, v125\n"
"v_cvt_pk_bf16_f32 v119, v126, v127\n"
"v_mfma_f32_16x16x32_bf16 v[232:235], v[100:103], v[88:91], v[232:235]\n"
"s_waitcnt lgkmcnt(0)\n"
"v_mul_f32_dpp v220, v254, v220 quad_perm:[0,0,0,0] row_mask:0xf bank_mask:0xf\n"
"v_mul_f32_dpp v221, v254, v221 quad_perm:[1,1,1,1] row_mask:0xf bank_mask:0xf\n"
"v_mfma_f32_16x16x32_bf16 v[232:235], v[96:99], v[84:87], v[232:235]\n"
"v_mul_f32_dpp v222, v254, v222 quad_perm:[2,2,2,2] row_mask:0xf bank_mask:0xf\n"
"v_mul_f32_dpp v223, v254, v223 quad_perm:[3,3,3,3] row_mask:0xf bank_mask:0xf\n"
"v_smfmac_f32_16x16x64_bf16 v[132:135], a[248:251], v[112:119], v157\n"
"v_mul_f32_dpp v224, v254, v224 quad_perm:[0,0,0,0] row_mask:0xf bank_mask:0xf\n"
"v_mfma_f32_16x16x32_bf16 v[220:223], a[64:67], a[160:163], v[220:223]\n"
"v_mul_f32_dpp v225, v254, v225 quad_perm:[1,1,1,1] row_mask:0xf bank_mask:0xf\n"
"v_mul_f32_dpp v226, v254, v226 quad_perm:[2,2,2,2] row_mask:0xf bank_mask:0xf\n"
"v_mfma_f32_16x16x32_bf16 v[220:223], a[68:71], a[164:167], v[220:223]\n"
"v_mul_f32_dpp v227, v254, v227 quad_perm:[3,3,3,3] row_mask:0xf bank_mask:0xf\n"
"v_mul_f32_dpp v228, v254, v228 quad_perm:[0,0,0,0] row_mask:0xf bank_mask:0xf\n"
"v_mfma_f32_16x16x32_bf16 v[220:223], a[72:75], a[168:171], v[220:223]\n"
"v_mul_f32_dpp v229, v254, v229 quad_perm:[1,1,1,1] row_mask:0xf bank_mask:0xf\n"
"v_mul_f32_dpp v230, v254, v230 quad_perm:[2,2,2,2] row_mask:0xf bank_mask:0xf\n"
"v_mfma_f32_16x16x32_bf16 v[220:223], a[76:79], a[172:175], v[220:223]\n"
"v_mul_f32_dpp v231, v254, v231 quad_perm:[3,3,3,3] row_mask:0xf bank_mask:0xf\n"
"v_mul_f32_dpp v232, v254, v232 quad_perm:[0,0,0,0] row_mask:0xf bank_mask:0xf\n"
"v_mfma_f32_16x16x32_bf16 v[224:227], a[76:79], a[188:191], v[224:227]\n"
"v_mul_f32_dpp v233, v254, v233 quad_perm:[1,1,1,1] row_mask:0xf bank_mask:0xf\n"
"v_mul_f32_dpp v234, v254, v234 quad_perm:[2,2,2,2] row_mask:0xf bank_mask:0xf\n"
"v_mfma_f32_16x16x32_bf16 v[224:227], a[72:75], a[184:187], v[224:227]\n"
"v_mul_f32_dpp v235, v254, v235 quad_perm:[3,3,3,3] row_mask:0xf bank_mask:0xf\n"
"v_cvt_pk_bf16_f32 v204, v204, v208\n"
"v_mfma_f32_16x16x32_bf16 v[224:227], a[68:71], a[180:183], v[224:227]\n"
"v_cvt_pk_bf16_f32 v206, v206, v210\n"
"v_cvt_pk_bf16_f32 v208, v205, v209\n"
"v_mfma_f32_16x16x32_bf16 v[224:227], a[64:67], a[176:179], v[224:227]\n"
"v_cvt_pk_bf16_f32 v210, v207, v211\n"
"v_cvt_pk_bf16_f32 v205, v212, v216\n"
"v_mfma_f32_16x16x32_bf16 v[228:231], a[64:67], a[192:195], v[228:231]\n"
"v_cvt_pk_bf16_f32 v209, v213, v217\n"
"v_cvt_pk_bf16_f32 v207, v214, v218\n"
"v_mfma_f32_16x16x32_bf16 v[228:231], a[68:71], a[196:199], v[228:231]\n"
"v_cvt_pk_bf16_f32 v211, v215, v219\n"
"v_add_f32 v132, v132, v133\n"
"v_mfma_f32_16x16x32_bf16 v[228:231], a[72:75], a[200:203], v[228:231]\n"
"v_pk_max_i16 v204, v204, 0\n"
"v_pk_max_i16 v205, v205, 0\n"
"v_mfma_f32_16x16x32_bf16 v[228:231], a[76:79], a[204:207], v[228:231]\n"
"buffer_store_dwordx2 v[204:205], v162, s[84:87], s67 offen offset:0 sc0 sc1\n"
"v_pk_max_i16 v208, v208, 0\n"
"v_pk_max_i16 v209, v209, 0\n"
"v_mfma_f32_16x16x32_bf16 v[232:235], a[76:79], a[220:223], v[232:235]\n"
"v_pk_max_i16 v206, v206, 0\n"
"v_pk_max_i16 v207, v207, 0\n"
"v_mfma_f32_16x16x32_bf16 v[232:235], a[72:75], a[216:219], v[232:235]\n"
"v_pk_max_i16 v210, v210, 0\n"
"v_pk_max_i16 v211, v211, 0\n"
"v_mfma_f32_16x16x32_bf16 v[232:235], a[68:71], a[212:215], v[232:235]\n"
"v_rcp_f32 v133, v132\n"
"v_mfma_f32_16x16x32_bf16 v[232:235], a[64:67], a[208:211], v[232:235]\n"
"buffer_store_dwordx2 v[208:209], v162, s[84:87], s67 offen offset:256 sc0 sc1\n"
"ds_bpermute_b32 v134, v160, v133 offset:128\n"
"v_cvt_pk_bf16_f32 v220, v220, v224\n"
"v_cvt_pk_bf16_f32 v222, v222, v226\n"
"v_cvt_pk_bf16_f32 v224, v221, v225\n"
"v_cvt_pk_bf16_f32 v226, v223, v227\n"
"v_pk_max_i16 v220, v220, 0\n"
"v_pk_max_i16 v224, v224, 0\n"
"v_pk_max_i16 v222, v222, 0\n"
"v_pk_max_i16 v226, v226, 0\n"
"v_cvt_pk_bf16_f32 v221, v228, v232\n"
"v_cvt_pk_bf16_f32 v225, v229, v233\n"
"v_cvt_pk_bf16_f32 v223, v230, v234\n"
"v_cvt_pk_bf16_f32 v227, v231, v235\n"
"v_pk_max_i16 v221, v221, 0\n"
"v_pk_max_i16 v225, v225, 0\n"
"v_pk_max_i16 v223, v223, 0\n"
"v_pk_max_i16 v227, v227, 0\n"
"v_max3_f32 v159, v159, v252, v253\n"
"v_max3_f32 v159, v159, v132, v133\n"
"buffer_store_dwordx2 v[206:207], v162, s[84:87], s67 offen offset:512 sc0 sc1\n"
"buffer_store_dwordx2 v[210:211], v162, s[84:87], s67 offen offset:768 sc0 sc1\n"
"s_nop 1\n"
"v_mfma_f32_16x16x4_4b_bf16 v[204:219], v[128:129], a[244:245], 0 cbsz:2 abid:3\n"
"buffer_store_dwordx2 v[220:221], v162, s[84:87], s68 offen offset:0 sc0 sc1\n"
"s_nop 5\n"
"v_mfma_f32_16x16x32_bf16 v[204:207], v[108:111], v[48:51], v[204:207]\n"
"s_nop 1\n"
"v_mfma_f32_16x16x32_bf16 v[204:207], v[112:115], v[52:55], v[204:207]\n"
"v_mfma_f32_16x16x32_bf16 v[204:207], v[116:119], v[56:59], v[204:207]\n"
"buffer_store_dwordx2 v[224:225], v162, s[84:87], s68 offen offset:256 sc0 sc1\n"
"v_mfma_f32_16x16x32_bf16 v[208:211], v[116:119], v[68:71], v[208:211]\n"
"v_mfma_f32_16x16x32_bf16 v[208:211], v[112:115], v[64:67], v[208:211]\n"
"v_mfma_f32_16x16x32_bf16 v[208:211], v[108:111], v[60:63], v[208:211]\n"
"s_waitcnt lgkmcnt(0)\n"
"buffer_store_dwordx2 v[222:223], v162, s[84:87], s68 offen offset:512 sc0 sc1\n"
"v_fmac_f32_dpp v164, v134, v204 quad_perm:[0,0,0,0] row_mask:0xf bank_mask:0xf\n"
"v_fmac_f32_dpp v166, v134, v206 quad_perm:[2,2,2,2] row_mask:0xf bank_mask:0xf\n"
"v_mfma_f32_16x16x32_bf16 v[212:215], v[108:111], v[72:75], v[212:215]\n"
"v_fmac_f32_dpp v165, v134, v205 quad_perm:[1,1,1,1] row_mask:0xf bank_mask:0xf\n"
"v_fmac_f32_dpp v167, v134, v207 quad_perm:[3,3,3,3] row_mask:0xf bank_mask:0xf\n"
"v_mfma_f32_16x16x32_bf16 v[212:215], v[112:115], v[76:79], v[212:215]\n"
"v_mfma_f32_16x16x32_bf16 v[212:215], v[116:119], v[80:83], v[212:215]\n"
"buffer_store_dwordx2 v[226:227], v162, s[84:87], s68 offen offset:768 sc0 sc1\n"
"v_fmac_f32_dpp v168, v134, v208 quad_perm:[0,0,0,0] row_mask:0xf bank_mask:0xf\n"
"v_fmac_f32_dpp v170, v134, v210 quad_perm:[2,2,2,2] row_mask:0xf bank_mask:0xf\n"
"v_mfma_f32_16x16x32_bf16 v[216:219], v[116:119], v[92:95], v[216:219]\n"
"v_fmac_f32_dpp v169, v134, v209 quad_perm:[1,1,1,1] row_mask:0xf bank_mask:0xf\n"
"v_fmac_f32_dpp v171, v134, v211 quad_perm:[3,3,3,3] row_mask:0xf bank_mask:0xf\n"
"v_mfma_f32_16x16x32_bf16 v[216:219], v[112:115], v[88:91], v[216:219]\n"
"v_cvt_pk_bf16_f32 v164, v164, v168\n"
"v_cvt_pk_bf16_f32 v166, v166, v170\n"
"v_mfma_f32_16x16x32_bf16 v[216:219], v[108:111], v[84:87], v[216:219]\n"
"v_fmac_f32_dpp v172, v134, v212 quad_perm:[0,0,0,0] row_mask:0xf bank_mask:0xf\n"
"v_fmac_f32_dpp v173, v134, v213 quad_perm:[1,1,1,1] row_mask:0xf bank_mask:0xf\n"
"v_fmac_f32_dpp v174, v134, v214 quad_perm:[2,2,2,2] row_mask:0xf bank_mask:0xf\n"
"v_fmac_f32_dpp v175, v134, v215 quad_perm:[3,3,3,3] row_mask:0xf bank_mask:0xf\n"
"v_cvt_pk_bf16_f32 v168, v165, v169\n"
"v_cvt_pk_bf16_f32 v170, v167, v171\n"
"v_pk_max_i16 v164, v164, 0\n"
"v_pk_max_i16 v168, v168, 0\n"
"v_pk_max_i16 v166, v166, 0\n"
"v_pk_max_i16 v170, v170, 0\n"
"v_fmac_f32_dpp v176, v134, v216 quad_perm:[0,0,0,0] row_mask:0xf bank_mask:0xf\n"
"v_fmac_f32_dpp v177, v134, v217 quad_perm:[1,1,1,1] row_mask:0xf bank_mask:0xf\n"
"v_fmac_f32_dpp v178, v134, v218 quad_perm:[2,2,2,2] row_mask:0xf bank_mask:0xf\n"
"v_fmac_f32_dpp v179, v134, v219 quad_perm:[3,3,3,3] row_mask:0xf bank_mask:0xf\n"
"v_cvt_pk_bf16_f32 v165, v172, v176\n"
"v_cvt_pk_bf16_f32 v169, v173, v177\n"
"v_cvt_pk_bf16_f32 v167, v174, v178\n"
"v_cvt_pk_bf16_f32 v171, v175, v179\n"
"v_pk_max_i16 v165, v165, 0\n"
"v_pk_max_i16 v169, v169, 0\n"
"v_pk_max_i16 v167, v167, 0\n"
"v_pk_max_i16 v171, v171, 0\n"
"buffer_store_dwordx2 v[164:165], v162, s[84:87], s69 offen offset:0 sc0 sc1\n"
"buffer_store_dwordx2 v[168:169], v162, s[84:87], s69 offen offset:256 sc0 sc1\n"
"buffer_store_dwordx2 v[166:167], v162, s[84:87], s69 offen offset:512 sc0 sc1\n"
"buffer_store_dwordx2 v[170:171], v162, s[84:87], s69 offen offset:768 sc0 sc1\n"
"v_mov_b32 v0, 0x4e800000\n"
"v_cmp_nle_f32 vcc, v159, v0\n"
"s_nop 4\n"
"s_cmp_eq_u64 vcc, 0\n"
"s_cbranch_scc0 .Lhot%=\n"
"s_endpgm\n"
".Lhot%=:\n"
"s_waitcnt vmcnt(0) lgkmcnt(0)\n"
"s_sub_u32 s24, s8, 192\n"
"s_subb_u32 s25, s9, 0\n"
"s_and_b32 s25, s25, 0xffff\n"
"s_mov_b32 s26, 104857600\n"
"s_mov_b32 s27, 0x20000\n"
"s_sub_u32 s84, s18, 13107200\n"
"s_subb_u32 s85, s19, 0\n"
"s_and_b32 s85, s85, 0xffff\n"
"s_mov_b32 s86, 25472\n"
"s_mov_b32 s87, 0x20000\n"
"v_lshrrev_b32 v0, 8, v153\n"
"v_lshl_add_u32 v158, v0, 9, s29\n"
"v_and_b32 v0, 0x30, v153\n"
"v_add_u32 v158, v0, v158\n"
"s_add_u32 s89, s29, 4096\n"
"v_lshrrev_b32 v156, 2, v153\n"
"v_and_b32 v0, 12, v156\n"
"v_and_b32 v156, 0xf0, v156\n"
"v_lshl_add_u32 v156, v0, 20, v156\n"
"v_add_u32 v156, s89, v156\n"
"s_lshl_b32 s89, %[wave], 12\n"
"s_add_u32 s89, s89, 33152\n"
"v_lshrrev_b32 v163, 1, v153\n"
"v_add_u32 v163, s89, v163\n"
"s_nop 11\n"
"ds_read_b128 v[248:251], v153 offset:0\n"
"ds_read_b128 a[224:227], v153 offset:4096\n"
"ds_read_b128 v[236:239], v153 offset:8192\n"
"ds_read_b128 v[240:243], v153 offset:12288\n"
"ds_read_b128 v[244:247], v153 offset:1024\n"
"ds_read_b128 a[228:231], v158 offset:0\n"
"ds_read_b128 a[232:235], v158 offset:2048\n"
"ds_read_b64 a[244:245], v163 offset:0\n"
"ds_read_b32 a236, v156 offset:0\n"
"ds_read_b32 a237, v156 offset:8\n"
"ds_read_b32 a238, v156 offset:4\n"
"ds_read_b32 a239, v156 offset:12\n"
"ds_read_b32 a240, v156 offset:256\n"
"ds_read_b32 a241, v156 offset:264\n"
"ds_read_b32 a242, v156 offset:260\n"
"ds_read_b32 a243, v156 offset:268\n"
"s_add_u32 m0, s28, 0\n"
"v_add_u32 v156, 512, v156\n"
"buffer_load_dwordx4 v152, s[24:27], s40 offen offset:0 lds\n"
"buffer_load_dwordx4 v152, s[24:27], s41 offen offset:1088 lds\n"
"buffer_load_dwordx4 v152, s[24:27], s42 offen offset:2176 lds\n"
"buffer_load_dwordx4 v152, s[24:27], s43 offen offset:3264 lds\n"
"s_add_u32 m0, s28, 4352\n"
"s_nop 0\n"
"buffer_load_dwordx4 v152, s[24:27], s44 offen offset:0 lds\n"
"buffer_load_dwordx4 v152, s[24:27], s45 offen offset:1088 lds\n"
"s_waitcnt vmcnt(2)\n"
"ds_read_b128 a[0:3], v154 offset:0\n"
"ds_read_b128 a[4:7], v154 offset:64\n"
"buffer_load_dwordx4 v152, s[24:27], s46 offen offset:2176 lds\n"
"ds_read_b128 a[8:11], v154 offset:128\n"
"ds_read_b128 a[12:15], v154 offset:192\n"
"s_waitcnt lgkmcnt(3)\n"
"v_mfma_f32_16x16x32_bf16 v[0:3], a[96:99], a[0:3], 0\n"
"s_waitcnt lgkmcnt(2)\n"
"buffer_load_dwordx4 v152, s[24:27], s47 offen offset:3264 lds\n"
"s_add_u32 m0, s28, 8704\n"
"v_mfma_f32_16x16x32_bf16 v[0:3], a[100:103], a[4:7], v[0:3]\n"
"s_waitcnt lgkmcnt(1)\n"
"buffer_load_dwordx4 v152, s[24:27], s48 offen offset:0 lds\n"
"v_mfma_f32_16x16x32_bf16 v[0:3], a[104:107], a[8:11], v[0:3]\n"
"s_waitcnt lgkmcnt(0)\n"
"buffer_load_dwordx4 v152, s[24:27], s49 offen offset:1088 lds\n"
"v_mfma_f32_16x16x32_bf16 v[0:3], a[108:111], a[12:15], v[0:3]\n"
"buffer_load_dwordx4 v152, s[24:27], s50 offen offset:2176 lds\n"
"v_mfma_f32_16x16x32_bf16 v[4:7], a[112:115], a[0:3], 0\n"
"buffer_load_dwordx4 v152, s[24:27], s51 offen offset:3264 lds\n"
"v_mfma_f32_16x16x32_bf16 v[4:7], a[116:119], a[4:7], v[4:7]\n"
"v_mfma_f32_16x16x32_bf16 v[4:7], a[120:123], a[8:11], v[4:7]\n"
"v_cvt_pk_bf16_f32 v0, v0, v1\n"
"v_cvt_pk_bf16_f32 v1, v2, v3\n"
"v_mfma_f32_16x16x32_bf16 v[4:7], a[124:127], a[12:15], v[4:7]\n"
"v_mfma_f32_16x16x32_bf16 v[164:167], a[144:147], a[0:3], 0\n"
"v_mfma_f32_16x16x32_bf16 v[164:167], a[148:151], a[4:7], v[164:167]\n"
"s_add_u32 m0, s28, 0\n"
"v_mfma_f32_16x16x32_bf16 v[164:167], a[152:155], a[8:11], v[164:167]\n"
"buffer_load_dwordx4 v152, s[24:27], s52 offen offset:0 lds\n"
"v_cvt_pk_bf16_f32 v2, v4, v5\n"
"v_mfma_f32_16x16x32_bf16 v[164:167], a[156:159], a[12:15], v[164:167]\n"
"buffer_load_dwordx4 v152, s[24:27], s53 offen offset:1088 lds\n"
"v_cvt_pk_bf16_f32 v3, v6, v7\n"
"v_mfma_f32_16x16x32_bf16 v[4:7], a[128:131], a[0:3], 0\n"
"buffer_load_dwordx4 v152, s[24:27], s54 offen offset:2176 lds\n"
"v_mfma_f32_16x16x32_bf16 v[4:7], a[132:135], a[4:7], v[4:7]\n"
"buffer_load_dwordx4 v152, s[24:27], s55 offen offset:3264 lds\n"
"v_mfma_f32_16x16x32_bf16 v[4:7], a[136:139], a[8:11], v[4:7]\n"
"v_mfma_f32_16x16x32_bf16 v[4:7], a[140:143], a[12:15], v[4:7]\n"
"v_mfma_f32_16x16x32_bf16 v[180:183], v[248:251], a[0:3], 0\n"
"v_mfma_f32_16x16x32_bf16 v[180:183], a[224:227], a[4:7], v[180:183]\n"
"v_mfma_f32_16x16x32_bf16 v[180:183], v[236:239], a[8:11], v[180:183]\n"
"v_cvt_pk_bf16_f32 v4, v4, v5\n"
"v_cvt_pk_bf16_f32 v5, v6, v7\n"
"v_mfma_f32_16x16x32_bf16 v[180:183], v[240:243], a[12:15], v[180:183]\n"
"v_cvt_pk_bf16_f32 v6, v164, v165\n"
"v_cvt_pk_bf16_f32 v7, v166, v167\n"
"v_mfma_f32_16x16x32_bf16 v[184:187], v[244:247], a[0:3], 0\n"
"s_nop 4\n"
"v_cvt_pk_bf16_f32 v180, v180, v181\n"
"v_cvt_pk_bf16_f32 v181, v182, v183\n"
"s_waitcnt vmcnt(8)\n"
"ds_read_b128 a[16:19], v154 offset:4352\n"
"ds_read_b128 a[20:23], v154 offset:4416\n"
"ds_read_b128 a[24:27], v154 offset:4480\n"
"ds_read_b128 a[28:31], v154 offset:4544\n"
"s_waitcnt vmcnt(4)\n"
"ds_read_b128 a[32:35], v154 offset:8704\n"
"ds_read_b128 a[36:39], v154 offset:8768\n"
"ds_read_b128 a[40:43], v154 offset:8832\n"
"ds_read_b128 a[44:47], v154 offset:8896\n"
"s_waitcnt lgkmcnt(4)\n"
"v_mfma_f32_16x16x32_bf16 v[8:11], a[108:111], a[28:31], 0\n"
"v_mfma_f32_16x16x32_bf16 v[8:11], a[104:107], a[24:27], v[8:11]\n"
"s_waitcnt vmcnt(0)\n"
"ds_read_b128 a[48:51], v154 offset:0\n"
"ds_read_b128 a[52:55], v154 offset:64\n"
"v_mfma_f32_16x16x32_bf16 v[8:11], a[100:103], a[20:23], v[8:11]\n"
"ds_read_b128 a[56:59], v154 offset:128\n"
"ds_read_b128 a[60:63], v154 offset:192\n"
"v_mfma_f32_16x16x32_bf16 v[8:11], a[96:99], a[16:19], v[8:11]\n"
"s_waitcnt lgkmcnt(7)\n"
"v_mfma_f32_16x16x32_bf16 v[16:19], a[96:99], a[32:35], 0\n"
"s_waitcnt lgkmcnt(6)\n"
"v_mfma_f32_16x16x32_bf16 v[16:19], a[100:103], a[36:39], v[16:19]\n"
"s_waitcnt lgkmcnt(5)\n"
"v_mfma_f32_16x16x32_bf16 v[16:19], a[104:107], a[40:43], v[16:19]\n"
"s_waitcnt lgkmcnt(4)\n"
"v_cvt_pk_bf16_f32 v8, v8, v9\n"
"v_cvt_pk_bf16_f32 v9, v10, v11\n"
"v_mfma_f32_16x16x32_bf16 v[16:19], a[108:111], a[44:47], v[16:19]\n"
"v_mfma_f32_16x16x32_bf16 v[12:15], a[124:127], a[28:31], 0\n"
"s_add_u32 m0, s28, 4352\n"
"v_mfma_f32_16x16x32_bf16 v[12:15], a[120:123], a[24:27], v[12:15]\n"
"buffer_load_dwordx4 v152, s[24:27], s56 offen offset:0 lds\n"
"v_mfma_f32_16x16x32_bf16 v[12:15], a[116:119], a[20:23], v[12:15]\n"
"buffer_load_dwordx4 v152, s[24:27], s57 offen offset:1088 lds\n"
"v_cvt_pk_bf16_f32 v16, v16, v17\n"
"v_mfma_f32_16x16x32_bf16 v[12:15], a[112:115], a[16:19], v[12:15]\n"
"buffer_load_dwordx4 v152, s[24:27], s58 offen offset:2176 lds\n"
"v_cvt_pk_bf16_f32 v17, v18, v19\n"
"v_mfma_f32_16x16x32_bf16 v[20:23], a[112:115], a[32:35], 0\n"
"buffer_load_dwordx4 v152, s[24:27], s59 offen offset:3264 lds\n"
"s_add_u32 m0, s28, 8704\n"
"v_mfma_f32_16x16x32_bf16 v[20:23], a[116:119], a[36:39], v[20:23]\n"
"buffer_load_dwordx4 v152, s[24:27], s60 offen offset:0 lds\n"
"v_mfma_f32_16x16x32_bf16 v[20:23], a[120:123], a[40:43], v[20:23]\n"
"buffer_load_dwordx4 v152, s[24:27], s61 offen offset:1088 lds\n"
"v_cvt_pk_bf16_f32 v10, v12, v13\n"
"v_mfma_f32_16x16x32_bf16 v[20:23], a[124:127], a[44:47], v[20:23]\n"
"buffer_load_dwordx4 v152, s[24:27], s62 offen offset:2176 lds\n"
"v_cvt_pk_bf16_f32 v11, v14, v15\n"
"v_mfma_f32_16x16x32_bf16 v[12:15], a[140:143], a[28:31], 0\n"
"buffer_load_dwordx4 v152, s[24:27], s63 offen offset:3264 lds\n"
"v_mfma_f32_16x16x32_bf16 v[12:15], a[136:139], a[24:27], v[12:15]\n"
"v_mfma_f32_16x16x32_bf16 v[12:15], a[132:135], a[20:23], v[12:15]\n"
"v_cvt_pk_bf16_f32 v18, v20, v21\n"
"v_cvt_pk_bf16_f32 v19, v22, v23\n"
"v_mfma_f32_16x16x32_bf16 v[12:15], a[128:131], a[16:19], v[12:15]\n"
"s_add_u32 s24, s24, 13107200\n"
"s_addc_u32 s25, s25, 0\n"
"s_sub_u32 s26, s26, 13107200\n"
"s_cselect_b32 s26, 0, s26\n"
"v_mfma_f32_16x16x32_bf16 v[20:23], a[128:131], a[32:35], 0\n"
"v_mfma_f32_16x16x32_bf16 v[20:23], a[132:135], a[36:39], v[20:23]\n"
"s_add_u32 m0, s28, 0\n"
"v_cvt_pk_bf16_f32 v12, v12, v13\n"
"v_mfma_f32_16x16x32_bf16 v[20:23], a[136:139], a[40:43], v[20:23]\n"
"s_waitcnt lgkmcnt(0)\n"
"buffer_load_dwordx4 v152, s[24:27], s40 offen offset:0 lds\n"
"v_cvt_pk_bf16_f32 v13, v14, v15\n"
"v_mfma_f32_16x16x32_bf16 v[20:23], a[140:143], a[44:47], v[20:23]\n"
"buffer_load_dwordx4 v152, s[24:27], s41 offen offset:1088 lds\n"
"v_mfma_f32_16x16x32_bf16 v[168:171], a[156:159], a[28:31], 0\n"
"buffer_load_dwordx4 v152, s[24:27], s42 offen offset:2176 lds\n"
"v_mfma_f32_16x16x32_bf16 v[168:171], a[152:155], a[24:27], v[168:171]\n"
"buffer_load_dwordx4 v152, s[24:27], s43 offen offset:3264 lds\n"
"v_mfma_f32_16x16x32_bf16 v[168:171], a[148:151], a[20:23], v[168:171]\n"
"v_cvt_pk_bf16_f32 v20, v20, v21\n"
"v_cvt_pk_bf16_f32 v21, v22, v23\n"
"v_mfma_f32_16x16x32_bf16 v[168:171], a[144:147], a[16:19], v[168:171]\n"
"v_mfma_f32_16x16x32_bf16 v[172:175], a[144:147], a[32:35], 0\n"
"v_mfma_f32_16x16x32_bf16 v[172:175], a[148:151], a[36:39], v[172:175]\n"
"v_mfma_f32_16x16x32_bf16 v[172:175], a[152:155], a[40:43], v[172:175]\n"
"v_cvt_pk_bf16_f32 v14, v168, v169\n"
"v_cvt_pk_bf16_f32 v15, v170, v171\n"
"v_mfma_f32_16x16x32_bf16 v[172:175], a[156:159], a[44:47], v[172:175]\n"
"v_mfma_f32_16x16x32_bf16 v[24:27], a[96:99], a[48:51], 0\n"
"v_mfma_f32_16x16x32_bf16 v[24:27], a[100:103], a[52:55], v[24:27]\n"
"v_mfma_f32_16x16x32_bf16 v[24:27], a[104:107], a[56:59], v[24:27]\n"
"v_cvt_pk_bf16_f32 v22, v172, v173\n"
"v_cvt_pk_bf16_f32 v23, v174, v175\n"
"v_mfma_f32_16x16x32_bf16 v[24:27], a[108:111], a[60:63], v[24:27]\n"
"v_mfma_f32_16x16x32_bf16 v[176:179], a[112:115], a[48:51], 0\n"
"v_mfma_f32_16x16x32_bf16 v[176:179], a[116:119], a[52:55], v[176:179]\n"
"v_mfma_f32_16x16x32_bf16 v[176:179], a[120:123], a[56:59], v[176:179]\n"
"v_cvt_pk_bf16_f32 v24, v24, v25\n"
"v_cvt_pk_bf16_f32 v25, v26, v27\n"
"v_mfma_f32_16x16x32_bf16 v[176:179], a[124:127], a[60:63], v[176:179]\n"
"v_mfma_f32_16x16x32_bf16 v[188:191], v[240:243], a[28:31], 0\n"
"v_mfma_f32_16x16x32_bf16 v[188:191], v[236:239], a[24:27], v[188:191]\n"
"v_mfma_f32_16x16x32_bf16 v[188:191], a[224:227], a[20:23], v[188:191]\n"
"v_cvt_pk_bf16_f32 v26, v176, v177\n"
"v_cvt_pk_bf16_f32 v27, v178, v179\n"
"v_mfma_f32_16x16x32_bf16 v[188:191], v[248:251], a[16:19], v[188:191]\n"
"v_mfma_f32_16x16x32_bf16 v[196:199], v[248:251], a[32:35], 0\n"
"ds_read_b128 v[248:251], v153 offset:5120\n"
"v_mfma_f32_16x16x32_bf16 v[196:199], a[224:227], a[36:39], v[196:199]\n"
"ds_read_b128 a[224:227], v153 offset:9216\n"
"v_mfma_f32_16x16x32_bf16 v[196:199], v[236:239], a[40:43], v[196:199]\n"
"ds_read_b128 v[236:239], v153 offset:13312\n"
"v_cvt_pk_bf16_f32 v188, v188, v189\n"
"v_mfma_f32_16x16x32_bf16 v[196:199], v[240:243], a[44:47], v[196:199]\n"
"ds_read_b128 v[240:243], v153 offset:2048\n"
"v_cvt_pk_bf16_f32 v189, v190, v191\n"
"v_mfma_f32_16x16x32_bf16 v[28:31], a[128:131], a[48:51], 0\n"
"v_mfma_f32_16x16x32_bf16 v[28:31], a[132:135], a[52:55], v[28:31]\n"
"v_mfma_f32_16x16x32_bf16 v[28:31], a[136:139], a[56:59], v[28:31]\n"
"v_cvt_pk_bf16_f32 v196, v196, v197\n"
"v_cvt_pk_bf16_f32 v197, v198, v199\n"
"v_mfma_f32_16x16x32_bf16 v[28:31], a[140:143], a[60:63], v[28:31]\n"
"v_mfma_f32_16x16x32_bf16 v[200:203], v[244:247], a[32:35], 0\n"
"v_mfma_f32_16x16x32_bf16 v[164:167], a[144:147], a[48:51], 0\n"
"v_mfma_f32_16x16x32_bf16 v[164:167], a[148:151], a[52:55], v[164:167]\n"
"v_cvt_pk_bf16_f32 v28, v28, v29\n"
"v_cvt_pk_bf16_f32 v29, v30, v31\n"
"v_mfma_f32_16x16x32_bf16 v[164:167], a[152:155], a[56:59], v[164:167]\n"
"v_mfma_f32_16x16x32_bf16 v[164:167], a[156:159], a[60:63], v[164:167]\n"
"s_waitcnt lgkmcnt(3)\n"
"v_mfma_f32_16x16x32_bf16 v[184:187], v[248:251], a[4:7], v[184:187]\n"
"s_waitcnt lgkmcnt(2)\n"
"v_mfma_f32_16x16x32_bf16 v[184:187], a[224:227], a[8:11], v[184:187]\n"
"s_waitcnt lgkmcnt(1)\n"
"v_mfma_f32_16x16x32_bf16 v[184:187], v[236:239], a[12:15], v[184:187]\n"
"v_cvt_pk_bf16_f32 v30, v164, v165\n"
"v_cvt_pk_bf16_f32 v31, v166, v167\n"
"v_mfma_f32_16x16x32_bf16 v[192:195], v[236:239], a[28:31], 0\n"
"v_mfma_f32_16x16x32_bf16 v[192:195], a[224:227], a[24:27], v[192:195]\n"
"v_mfma_f32_16x16x32_bf16 v[192:195], v[248:251], a[20:23], v[192:195]\n"
"v_cvt_pk_bf16_f32 v182, v184, v185\n"
"v_cvt_pk_bf16_f32 v183, v186, v187\n"
"v_mfma_f32_16x16x32_bf16 v[192:195], v[244:247], a[16:19], v[192:195]\n"
"ds_read_b128 v[244:247], v153 offset:6144\n"
"v_mfma_f32_16x16x32_bf16 v[200:203], v[248:251], a[36:39], v[200:203]\n"
"ds_read_b128 v[248:251], v153 offset:10240\n"
"v_mfma_f32_16x16x32_bf16 v[200:203], a[224:227], a[40:43], v[200:203]\n"
"ds_read_b128 a[224:227], v153 offset:14336\n"
"v_mfma_f32_16x16x32_bf16 v[200:203], v[236:239], a[44:47], v[200:203]\n"
"s_waitcnt lgkmcnt(3)\n"
"ds_read_b128 v[236:239], v153 offset:3072\n"
"v_cvt_pk_bf16_f32 v190, v192, v193\n"
"v_mfma_f32_16x16x32_bf16 v[184:187], v[240:243], a[0:3], 0\n"
"v_cvt_pk_bf16_f32 v191, v194, v195\n"
"v_mfma_f32_16x16x32_bf16 v[120:123], a[228:231], v[180:183], 0\n"
"ds_read_b128 a[228:231], v158 offset:0\n"
"v_mfma_f32_16x16x32_bf16 v[96:99], v[0:3], v[180:183], 0\n"
"v_cvt_pk_bf16_f32 v198, v200, v201\n"
"v_cvt_pk_bf16_f32 v199, v202, v203\n"
"v_mfma_f32_16x16x32_bf16 v[200:203], v[240:243], a[32:35], 0\n"
"v_mfma_f32_16x16x32_bf16 v[104:107], v[16:19], v[180:183], 0\n"
"s_waitcnt lgkmcnt(4)\n"
"v_mfma_f32_16x16x32_bf16 v[184:187], v[244:247], a[4:7], v[184:187]\n"
"s_waitcnt lgkmcnt(3)\n"
"v_mfma_f32_16x16x32_bf16 v[184:187], v[248:251], a[8:11], v[184:187]\n"
"s_waitcnt lgkmcnt(2)\n"
"v_mfma_f32_16x16x32_bf16 v[184:187], a[224:227], a[12:15], v[184:187]\n"
"v_mfma_f32_16x16x32_bf16 v[192:195], a[224:227], a[28:31], 0\n"
"v_mfma_f32_16x16x32_bf16 v[192:195], v[248:251], a[24:27], v[192:195]\n"
"v_mfma_f32_16x16x32_bf16 v[192:195], v[244:247], a[20:23], v[192:195]\n"
"v_cvt_pk_bf16_f32 v184, v184, v185\n"
"v_cvt_pk_bf16_f32 v185, v186, v187\n"
"v_mfma_f32_16x16x32_bf16 v[192:195], v[240:243], a[16:19], v[192:195]\n"
"ds_read_b128 v[240:243], v153 offset:7168\n"
"v_mfma_f32_16x16x32_bf16 v[200:203], v[244:247], a[36:39], v[200:203]\n"
"ds_read_b128 v[244:247], v153 offset:11264\n"
"v_mfma_f32_16x16x32_bf16 v[200:203], v[248:251], a[40:43], v[200:203]\n"
"ds_read_b128 v[248:251], v153 offset:15360\n"
"v_mfma_f32_16x16x32_bf16 v[200:203], a[224:227], a[44:47], v[200:203]\n"
"s_waitcnt lgkmcnt(3)\n"
"ds_read_b128 a[224:227], v153 offset:18496\n"
"v_cvt_pk_bf16_f32 v192, v192, v193\n"
"v_mfma_f32_16x16x32_bf16 v[164:167], v[236:239], a[0:3], 0\n"
"v_cvt_pk_bf16_f32 v193, v194, v195\n"
"s_nop 2\n"
"v_cvt_pk_bf16_f32 v200, v200, v201\n"
"v_cvt_pk_bf16_f32 v201, v202, v203\n"
"s_waitcnt lgkmcnt(3)\n"
"v_mfma_f32_16x16x32_bf16 v[164:167], v[240:243], a[4:7], v[164:167]\n"
"s_waitcnt lgkmcnt(2)\n"
"v_mfma_f32_16x16x32_bf16 v[164:167], v[244:247], a[8:11], v[164:167]\n"
"s_waitcnt lgkmcnt(1)\n"
"v_mfma_f32_16x16x32_bf16 v[164:167], v[248:251], a[12:15], v[164:167]\n"
"s_nop 7\n"
"v_cvt_pk_bf16_f32 v186, v164, v165\n"
"v_cvt_pk_bf16_f32 v187, v166, v167\n"
"s_nop 1\n"
"v_mfma_f32_16x16x32_bf16 v[120:123], a[232:235], v[184:187], v[120:123]\n"
"ds_read_b128 a[232:235], v158 offset:2048\n"
"v_mfma_f32_16x16x32_bf16 v[96:99], v[4:7], v[184:187], v[96:99]\n"
"v_mfma_f32_16x16x32_bf16 v[100:103], v[12:15], v[184:187], 0\n"
"v_mfma_f32_16x16x32_bf16 v[100:103], v[8:11], v[180:183], v[100:103]\n"
"v_max3_f32 v157, v120, v121, v122\n"
"v_mfma_f32_16x16x32_bf16 v[104:107], v[20:23], v[184:187], v[104:107]\n"
"v_max3_f32 v159, v123, v96, v97\n"
"v_max3_f32 v157, v157, v98, v99\n"
"v_mfma_f32_16x16x32_bf16 v[108:111], v[28:31], v[184:187], 0\n"
"v_mfma_f32_16x16x32_bf16 v[108:111], v[24:27], v[180:183], v[108:111]\n"
"v_max3_f32 v159, v159, v100, v101\n"
"v_max3_f32 v157, v157, v102, v103\n"
"v_max3_f32 v159, v159, v104, v105\n"
"v_max3_f32 v157, v157, v106, v107\n"
"s_nop 3\n"
"v_max3_f32 v159, v159, v108, v109\n"
"s_waitcnt vmcnt(8)\n"
"ds_read_b128 a[64:67], v154 offset:4352\n"
"ds_read_b128 a[68:71], v154 offset:4416\n"
"ds_read_b128 a[72:75], v154 offset:4480\n"
"ds_read_b128 a[76:79], v154 offset:4544\n"
"s_waitcnt vmcnt(4)\n"
"ds_read_b128 a[80:83], v154 offset:8704\n"
"ds_read_b128 a[84:87], v154 offset:8768\n"
"ds_read_b128 a[88:91], v154 offset:8832\n"
"ds_read_b128 a[92:95], v154 offset:8896\n"
"s_waitcnt lgkmcnt(4)\n"
"v_mfma_f32_16x16x32_bf16 v[32:35], a[108:111], a[76:79], 0\n"
"v_mfma_f32_16x16x32_bf16 v[32:35], a[104:107], a[72:75], v[32:35]\n"
"v_mfma_f32_16x16x32_bf16 v[32:35], a[100:103], a[68:71], v[32:35]\n"
"v_mfma_f32_16x16x32_bf16 v[32:35], a[96:99], a[64:67], v[32:35]\n"
"v_mfma_f32_16x16x32_bf16 v[172:175], a[124:127], a[76:79], 0\n"
"v_mfma_f32_16x16x32_bf16 v[172:175], a[120:123], a[72:75], v[172:175]\n"
"v_mfma_f32_16x16x32_bf16 v[172:175], a[116:119], a[68:71], v[172:175]\n"
"v_cvt_pk_bf16_f32 v32, v32, v33\n"
"v_cvt_pk_bf16_f32 v33, v34, v35\n"
"v_mfma_f32_16x16x32_bf16 v[172:175], a[112:115], a[64:67], v[172:175]\n"
"v_mfma_f32_16x16x32_bf16 v[36:39], a[140:143], a[76:79], 0\n"
"s_add_u32 m0, s28, 4352\n"
"v_mfma_f32_16x16x32_bf16 v[36:39], a[136:139], a[72:75], v[36:39]\n"
"buffer_load_dwordx4 v152, s[24:27], s44 offen offset:0 lds\n"
"v_mfma_f32_16x16x32_bf16 v[36:39], a[132:135], a[68:71], v[36:39]\n"
"buffer_load_dwordx4 v152, s[24:27], s45 offen offset:1088 lds\n"
"v_cvt_pk_bf16_f32 v34, v172, v173\n"
"v_mfma_f32_16x16x32_bf16 v[36:39], a[128:131], a[64:67], v[36:39]\n"
"buffer_load_dwordx4 v152, s[24:27], s46 offen offset:2176 lds\n"
"v_cvt_pk_bf16_f32 v35, v174, v175\n"
"v_mfma_f32_16x16x32_bf16 v[168:171], a[156:159], a[76:79], 0\n"
"buffer_load_dwordx4 v152, s[24:27], s47 offen offset:3264 lds\n"
"v_mfma_f32_16x16x32_bf16 v[168:171], a[152:155], a[72:75], v[168:171]\n"
"v_mfma_f32_16x16x32_bf16 v[168:171], a[148:151], a[68:71], v[168:171]\n"
"v_cvt_pk_bf16_f32 v36, v36, v37\n"
"v_cvt_pk_bf16_f32 v37, v38, v39\n"
"v_mfma_f32_16x16x32_bf16 v[168:171], a[144:147], a[64:67], v[168:171]\n"
"v_mfma_f32_16x16x32_bf16 v[172:175], v[236:239], a[32:35], 0\n"
"v_mfma_f32_16x16x32_bf16 v[172:175], v[240:243], a[36:39], v[172:175]\n"
"v_mfma_f32_16x16x32_bf16 v[172:175], v[244:247], a[40:43], v[172:175]\n"
"v_cvt_pk_bf16_f32 v38, v168, v169\n"
"v_cvt_pk_bf16_f32 v39, v170, v171\n"
"v_mfma_f32_16x16x32_bf16 v[172:175], v[248:251], a[44:47], v[172:175]\n"
"v_mfma_f32_16x16x32_bf16 v[168:171], v[248:251], a[28:31], 0\n"
"ds_read_b128 v[248:251], v153 offset:28960\n"
"v_mfma_f32_16x16x32_bf16 v[168:171], v[244:247], a[24:27], v[168:171]\n"
"ds_read_b128 v[244:247], v153 offset:24768\n"
"v_mfma_f32_16x16x32_bf16 v[168:171], v[240:243], a[20:23], v[168:171]\n"
"ds_read_b128 v[240:243], v153 offset:20576\n"
"v_cvt_pk_bf16_f32 v202, v172, v173\n"
"v_mfma_f32_16x16x32_bf16 v[168:171], v[236:239], a[16:19], v[168:171]\n"
"s_waitcnt lgkmcnt(3)\n"
"ds_read_b128 v[236:239], v153 offset:16384\n"
"v_cvt_pk_bf16_f32 v203, v174, v175\n"
"v_mfma_f32_16x16x32_bf16 v[40:43], a[96:99], a[80:83], 0\n"
"v_mfma_f32_16x16x32_bf16 v[40:43], a[100:103], a[84:87], v[40:43]\n"
"v_mfma_f32_16x16x32_bf16 v[40:43], a[104:107], a[88:91], v[40:43]\n"
"v_cvt_pk_bf16_f32 v194, v168, v169\n"
"v_cvt_pk_bf16_f32 v195, v170, v171\n"
"v_mfma_f32_16x16x32_bf16 v[40:43], a[108:111], a[92:95], v[40:43]\n"
"s_waitcnt vmcnt(0) lgkmcnt(0)\n"
"s_mov_b32 s74, 8\n"
"s_nop 11\n"
".Lp2s%=:\n"
"v_mfma_f32_16x16x32_bf16 v[176:179], a[112:115], a[80:83], 0\n"
"s_add_u32 m0, s28, 8704\n"
"v_max3_f32 v157, v157, v110, v111\n"
"v_mfma_f32_16x16x32_bf16 v[176:179], a[116:119], a[84:87], v[176:179]\n"
"buffer_load_dwordx4 v152, s[24:27], s48 offen offset:0 lds\n"
"v_mfma_f32_16x16x32_bf16 v[176:179], a[120:123], a[88:91], v[176:179]\n"
"buffer_load_dwordx4 v152, s[24:27], s49 offen offset:1088 lds\n"
"v_cvt_pk_bf16_f32 v40, v40, v41\n"
"v_mfma_f32_16x16x32_bf16 v[176:179], a[124:127], a[92:95], v[176:179]\n"
"buffer_load_dwordx4 v152, s[24:27], s50 offen offset:2176 lds\n"
"v_cvt_pk_bf16_f32 v41, v42, v43\n"
"v_mfma_f32_16x16x32_bf16 v[44:47], a[128:131], a[80:83], 0\n"
"buffer_load_dwordx4 v152, s[24:27], s51 offen offset:3264 lds\n"
"v_mfma_f32_16x16x32_bf16 v[44:47], a[132:135], a[84:87], v[44:47]\n"
"v_mfma_f32_16x16x32_bf16 v[44:47], a[136:139], a[88:91], v[44:47]\n"
"v_cvt_pk_bf16_f32 v42, v176, v177\n"
"v_cvt_pk_bf16_f32 v43, v178, v179\n"
"v_mfma_f32_16x16x32_bf16 v[44:47], a[140:143], a[92:95], v[44:47]\n"
"v_mfma_f32_16x16x32_bf16 v[164:167], a[144:147], a[80:83], 0\n"
"v_mfma_f32_16x16x32_bf16 v[164:167], a[148:151], a[84:87], v[164:167]\n"
"v_mfma_f32_16x16x32_bf16 v[164:167], a[152:155], a[88:91], v[164:167]\n"
"v_cvt_pk_bf16_f32 v44, v44, v45\n"
"v_cvt_pk_bf16_f32 v45, v46, v47\n"
"v_mfma_f32_16x16x32_bf16 v[164:167], a[156:159], a[92:95], v[164:167]\n"
"v_mfma_f32_16x16x32_bf16 v[112:115], v[32:35], v[180:183], 0\n"
"v_mfma_f32_16x16x32_bf16 v[112:115], v[36:39], v[184:187], v[112:115]\n"
"v_mfma_f32_16x16x32_bf16 v[48:51], a[0:3], v[236:239], 0\n"
"v_cvt_pk_bf16_f32 v46, v164, v165\n"
"v_cvt_pk_bf16_f32 v47, v166, v167\n"
"v_mfma_f32_16x16x32_bf16 v[48:51], a[4:7], v[240:243], v[48:51]\n"
"v_mfma_f32_16x16x32_bf16 v[48:51], a[8:11], v[244:247], v[48:51]\n"
"v_max3_f32 v159, v159, v112, v113\n"
"v_max3_f32 v157, v157, v114, v115\n"
"v_mfma_f32_16x16x32_bf16 v[48:51], a[12:15], v[248:251], v[48:51]\n"
"v_mfma_f32_16x16x32_bf16 v[116:119], v[44:47], v[184:187], 0\n"
"v_mfma_f32_16x16x32_bf16 v[116:119], v[40:43], v[180:183], v[116:119]\n"
"v_mfma_f32_16x16x32_bf16 v[60:63], a[28:31], v[248:251], 0\n"
"v_cvt_pk_bf16_f32 v48, v48, v49\n"
"v_cvt_pk_bf16_f32 v49, v50, v51\n"
"v_mfma_f32_16x16x32_bf16 v[60:63], a[24:27], v[244:247], v[60:63]\n"
"v_mfma_f32_16x16x32_bf16 v[60:63], a[20:23], v[240:243], v[60:63]\n"
"v_max3_f32 v159, v159, v116, v117\n"
"v_max3_f32 v157, v157, v118, v119\n"
"v_mfma_f32_16x16x32_bf16 v[60:63], a[16:19], v[236:239], v[60:63]\n"
"v_max_f32 v157, v157, v159\n"
"v_mov_b32 v161, v157\n"
"v_mfma_f32_16x16x32_bf16 v[52:55], a[32:35], v[236:239], 0\n"
"v_mfma_f32_16x16x32_bf16 v[52:55], a[36:39], v[240:243], v[52:55]\n"
"v_permlane32_swap_b32 v157, v161\n"
"v_mfma_f32_16x16x32_bf16 v[52:55], a[40:43], v[244:247], v[52:55]\n"
"v_cvt_pk_bf16_f32 v50, v60, v61\n"
"v_cvt_pk_bf16_f32 v51, v62, v63\n"
"v_mfma_f32_16x16x32_bf16 v[52:55], a[44:47], v[248:251], v[52:55]\n"
"v_max_f32 v157, v157, v161\n"
"v_mov_b32 v161, v157\n"
"v_mfma_f32_16x16x32_bf16 v[64:67], a[60:63], v[248:251], 0\n"
"v_mfma_f32_16x16x32_bf16 v[64:67], a[56:59], v[244:247], v[64:67]\n"
"v_permlane16_swap_b32 v157, v161\n"
"v_mfma_f32_16x16x32_bf16 v[64:67], a[52:55], v[240:243], v[64:67]\n"
"v_cvt_pk_bf16_f32 v52, v52, v53\n"
"v_cvt_pk_bf16_f32 v53, v54, v55\n"
"v_mfma_f32_16x16x32_bf16 v[64:67], a[48:51], v[236:239], v[64:67]\n"
"v_max_f32 v157, v157, v161\n"
"v_sub_f32 v120, v120, v157\n"
"v_mfma_f32_16x16x32_bf16 v[56:59], a[64:67], v[236:239], 0\n"
"v_sub_f32 v121, v121, v157\n"
"v_sub_f32 v122, v122, v157\n"
"v_mfma_f32_16x16x32_bf16 v[56:59], a[68:71], v[240:243], v[56:59]\n"
"v_sub_f32 v123, v123, v157\n"
"v_sub_f32 v96, v96, v157\n"
"v_mfma_f32_16x16x32_bf16 v[56:59], a[72:75], v[244:247], v[56:59]\n"
"v_cvt_pk_bf16_f32 v54, v64, v65\n"
"v_cvt_pk_bf16_f32 v55, v66, v67\n"
"v_mfma_f32_16x16x32_bf16 v[56:59], a[76:79], v[248:251], v[56:59]\n"
"v_exp_f32 v161, v122\n"
"v_mfma_f32_16x16x32_bf16 v[68:71], a[92:95], v[248:251], 0\n"
"ds_read_b128 v[248:251], v153 offset:17440\n"
"v_sub_f32 v97, v97, v157\n"
"v_mfma_f32_16x16x32_bf16 v[68:71], a[88:91], v[244:247], v[68:71]\n"
"ds_read_b128 v[244:247], v153 offset:31072\n"
"v_sub_f32 v98, v98, v157\n"
"v_mfma_f32_16x16x32_bf16 v[68:71], a[84:87], v[240:243], v[68:71]\n"
"ds_read_b128 v[240:243], v153 offset:26880\n"
"v_cvt_pk_bf16_f32 v56, v56, v57\n"
"v_mfma_f32_16x16x32_bf16 v[68:71], a[80:83], v[236:239], v[68:71]\n"
"s_waitcnt lgkmcnt(3)\n"
"ds_read_b128 v[236:239], v153 offset:22688\n"
"v_cvt_pk_bf16_f32 v57, v58, v59\n"
"v_mfma_f32_16x16x32_bf16 v[148:151], a[228:231], v[188:191], 0\n"
"ds_read_b128 a[228:231], v158 offset:0\n"
"v_sub_f32 v99, v99, v157\n"
"v_mfma_f32_16x16x32_bf16 v[148:151], a[232:235], v[192:195], v[148:151]\n"
"ds_read_b128 a[232:235], v158 offset:2048\n"
"v_exp_f32 v120, v120\n"
"v_mfma_f32_16x16x32_bf16 v[124:127], v[0:3], v[188:191], 0\n"
"v_cvt_pk_bf16_f32 v58, v68, v69\n"
"v_cvt_pk_bf16_f32 v59, v70, v71\n"
"v_mfma_f32_16x16x32_bf16 v[124:127], v[4:7], v[192:195], v[124:127]\n"
"v_exp_f32 v121, v121\n"
"v_mfma_f32_16x16x32_bf16 v[128:131], v[12:15], v[192:195], 0\n"
"v_exp_f32 v123, v123\n"
"v_mfma_f32_16x16x32_bf16 v[128:131], v[8:11], v[188:191], v[128:131]\n"
"v_cvt_pk_bf16_f32 v122, v120, v121\n"
"v_add_f32 v120, v120, v121\n"
"v_mfma_f32_16x16x32_bf16 v[132:135], v[16:19], v[188:191], 0\n"
"v_add_f32 v121, v161, v123\n"
"v_cvt_pk_bf16_f32 v123, v161, v123\n"
"v_mfma_f32_16x16x32_bf16 v[132:135], v[20:23], v[192:195], v[132:135]\n"
"v_exp_f32 v96, v96\n"
"v_mfma_f32_16x16x32_bf16 v[136:139], v[28:31], v[192:195], 0\n"
"v_exp_f32 v97, v97\n"
"v_mfma_f32_16x16x32_bf16 v[136:139], v[24:27], v[188:191], v[136:139]\n"
"v_exp_f32 v98, v98\n"
"v_mfma_f32_16x16x32_bf16 v[140:143], v[32:35], v[188:191], 0\n"
"v_exp_f32 v99, v99\n"
"v_mfma_f32_16x16x32_bf16 v[140:143], v[36:39], v[192:195], v[140:143]\n"
"v_sub_f32 v100, v100, v157\n"
"v_sub_f32 v101, v101, v157\n"
"v_mfma_f32_16x16x32_bf16 v[144:147], v[44:47], v[192:195], 0\n"
"v_exp_f32 v100, v100\n"
"v_mfma_f32_16x16x32_bf16 v[144:147], v[40:43], v[188:191], v[144:147]\n"
"v_cvt_pk_bf16_f32 v96, v96, v97\n"
"v_cvt_pk_bf16_f32 v97, v98, v99\n"
"v_mfma_f32_16x16x32_bf16 v[72:75], a[0:3], a[224:227], 0\n"
"s_waitcnt lgkmcnt(0)\n"
"v_sub_f32 v102, v102, v157\n"
"v_sub_f32 v103, v103, v157\n"
"v_mfma_f32_16x16x32_bf16 v[72:75], a[4:7], v[236:239], v[72:75]\n"
"v_exp_f32 v101, v101\n"
"v_mfma_f32_16x16x32_bf16 v[72:75], a[8:11], v[240:243], v[72:75]\n"
"v_exp_f32 v102, v102\n"
"v_mfma_f32_16x16x32_bf16 v[72:75], a[12:15], v[244:247], v[72:75]\n"
"v_exp_f32 v103, v103\n"
"v_mfma_f32_16x16x32_bf16 v[84:87], a[28:31], v[244:247], 0\n"
"v_sub_f32 v104, v104, v157\n"
"v_sub_f32 v105, v105, v157\n"
"v_mfma_f32_16x16x32_bf16 v[84:87], a[24:27], v[240:243], v[84:87]\n"
"v_exp_f32 v104, v104\n"
"v_mfma_f32_16x16x32_bf16 v[84:87], a[20:23], v[236:239], v[84:87]\n"
"v_cvt_pk_bf16_f32 v72, v72, v73\n"
"v_cvt_pk_bf16_f32 v73, v74, v75\n"
"v_mfma_f32_16x16x32_bf16 v[84:87], a[16:19], a[224:227], v[84:87]\n"
"v_cvt_pk_bf16_f32 v98, v100, v101\n"
"v_cvt_pk_bf16_f32 v99, v102, v103\n"
"v_mfma_f32_16x16x32_bf16 v[76:79], a[32:35], a[224:227], 0\n"
"v_sub_f32 v106, v106, v157\n"
"v_sub_f32 v107, v107, v157\n"
"v_mfma_f32_16x16x32_bf16 v[76:79], a[36:39], v[236:239], v[76:79]\n"
"v_exp_f32 v105, v105\n"
"v_mfma_f32_16x16x32_bf16 v[76:79], a[40:43], v[240:243], v[76:79]\n"
"v_cvt_pk_bf16_f32 v74, v84, v85\n"
"v_cvt_pk_bf16_f32 v75, v86, v87\n"
"v_mfma_f32_16x16x32_bf16 v[76:79], a[44:47], v[244:247], v[76:79]\n"
"v_exp_f32 v106, v106\n"
"v_mfma_f32_16x16x32_bf16 v[88:91], a[60:63], v[244:247], 0\n"
"v_exp_f32 v107, v107\n"
"v_mfma_f32_16x16x32_bf16 v[88:91], a[56:59], v[240:243], v[88:91]\n"
"v_sub_f32 v108, v108, v157\n"
"v_sub_f32 v109, v109, v157\n"
"v_mfma_f32_16x16x32_bf16 v[88:91], a[52:55], v[236:239], v[88:91]\n"
"v_cvt_pk_bf16_f32 v76, v76, v77\n"
"v_cvt_pk_bf16_f32 v77, v78, v79\n"
"v_mfma_f32_16x16x32_bf16 v[88:91], a[48:51], a[224:227], v[88:91]\n"
"v_exp_f32 v108, v108\n"
"v_mfma_f32_16x16x32_bf16 v[80:83], a[64:67], a[224:227], 0\n"
"v_cvt_pk_bf16_f32 v104, v104, v105\n"
"v_cvt_pk_bf16_f32 v105, v106, v107\n"
"v_mfma_f32_16x16x32_bf16 v[80:83], a[68:71], v[236:239], v[80:83]\n"
"v_sub_f32 v110, v110, v157\n"
"v_sub_f32 v111, v111, v157\n"
"v_mfma_f32_16x16x32_bf16 v[80:83], a[72:75], v[240:243], v[80:83]\n"
"v_cvt_pk_bf16_f32 v78, v88, v89\n"
"v_cvt_pk_bf16_f32 v79, v90, v91\n"
"v_mfma_f32_16x16x32_bf16 v[80:83], a[76:79], v[244:247], v[80:83]\n"
"v_exp_f32 v109, v109\n"
"v_mfma_f32_16x16x32_bf16 v[92:95], a[92:95], v[244:247], 0\n"
"ds_read_b128 v[244:247], v153 offset:19552\n"
"v_sub_f32 v112, v112, v157\n"
"v_mfma_f32_16x16x32_bf16 v[92:95], a[88:91], v[240:243], v[92:95]\n"
"ds_read_b128 v[240:243], v153 offset:30016\n"
"v_sub_f32 v113, v113, v157\n"
"v_mfma_f32_16x16x32_bf16 v[92:95], a[84:87], v[236:239], v[92:95]\n"
"ds_read_b128 v[236:239], v153 offset:25824\n"
"v_cvt_pk_bf16_f32 v80, v80, v81\n"
"v_mfma_f32_16x16x32_bf16 v[92:95], a[80:83], a[224:227], v[92:95]\n"
"ds_read_b128 a[224:227], v153 offset:21632\n"
"v_cvt_pk_bf16_f32 v81, v82, v83\n"
"v_mfma_f32_16x16x32_bf16 v[60:63], a[0:3], v[248:251], 0\n"
"v_exp_f32 v110, v110\n"
"v_mfma_f32_16x16x32_bf16 v[64:67], a[32:35], v[248:251], 0\n"
"v_exp_f32 v111, v111\n"
"v_mfma_f32_16x16x32_bf16 v[68:71], a[64:67], v[248:251], 0\n"
"v_cvt_pk_bf16_f32 v82, v92, v93\n"
"v_cvt_pk_bf16_f32 v83, v94, v95\n"
"v_cvt_pk_bf16_f32 v106, v108, v109\n"
"v_mfma_f32_16x16x4_4b_bf16 v[204:219], v[122:123], a[244:245], 0 cbsz:2 abid:0\n"
"v_exp_f32 v112, v112\n"
"v_cvt_pk_bf16_f32 v107, v110, v111\n"
"v_sub_f32 v114, v114, v157\n"
"v_exp_f32 v113, v113\n"
"v_sub_f32 v115, v115, v157\n"
"v_sub_f32 v116, v116, v157\n"
"v_mfma_f32_16x16x32_bf16 v[252:255], a[252:255], v[96:99], v[120:123]\n"
"v_exp_f32 v114, v114\n"
"v_mfma_f32_16x16x32_bf16 v[252:255], a[252:255], v[104:107], v[252:255]\n"
"s_waitcnt lgkmcnt(3)\n"
"v_exp_f32 v115, v115\n"
"v_mfma_f32_16x16x32_bf16 v[84:87], a[0:3], v[244:247], 0\n"
"s_waitcnt lgkmcnt(2)\n"
"v_sub_f32 v117, v117, v157\n"
"v_cvt_pk_bf16_f32 v112, v112, v113\n"
"v_mfma_f32_16x16x32_bf16 v[164:167], a[28:31], v[240:243], 0\n"
"s_waitcnt lgkmcnt(1)\n"
"v_exp_f32 v116, v116\n"
"v_mfma_f32_16x16x32_bf16 v[164:167], a[24:27], v[236:239], v[164:167]\n"
"s_waitcnt lgkmcnt(0)\n"
"v_cvt_pk_bf16_f32 v113, v114, v115\n"
"v_sub_f32 v118, v118, v157\n"
"v_mfma_f32_16x16x32_bf16 v[164:167], a[20:23], a[224:227], v[164:167]\n"
"v_exp_f32 v117, v117\n"
"v_mfma_f32_16x16x32_bf16 v[164:167], a[16:19], v[248:251], v[164:167]\n"
"v_sub_f32 v119, v119, v157\n"
"v_cvt_pk_bf16_f32 v114, v116, v117\n"
"v_mfma_f32_16x16x32_bf16 v[60:63], a[4:7], a[224:227], v[60:63]\n"
"v_exp_f32 v118, v118\n"
"v_mfma_f32_16x16x32_bf16 v[60:63], a[8:11], v[236:239], v[60:63]\n"
"v_exp_f32 v119, v119\n"
"v_mfma_f32_16x16x32_bf16 v[60:63], a[12:15], v[240:243], v[60:63]\n"
"v_cvt_pk_bf16_f32 v115, v118, v119\n"
"v_max3_f32 v157, v148, v149, v150\n"
"v_mfma_f32_16x16x32_bf16 v[64:67], a[36:39], a[224:227], v[64:67]\n"
"s_add_u32 s84, s84, 13107200\n"
"s_addc_u32 s85, s85, 0\n"
"v_mfma_f32_16x16x32_bf16 v[64:67], a[40:43], v[236:239], v[64:67]\n"
"v_max3_f32 v159, v151, v124, v125\n"
"v_max3_f32 v157, v157, v126, v127\n"
"v_mfma_f32_16x16x32_bf16 v[64:67], a[44:47], v[240:243], v[64:67]\n"
"v_cvt_pk_bf16_f32 v60, v60, v61\n"
"v_cvt_pk_bf16_f32 v61, v62, v63\n"
"v_mfma_f32_16x16x32_bf16 v[168:171], a[60:63], v[240:243], 0\n"
"v_cvt_pk_bf16_f32 v62, v164, v165\n"
"v_cvt_pk_bf16_f32 v63, v166, v167\n"
"v_mfma_f32_16x16x32_bf16 v[168:171], a[56:59], v[236:239], v[168:171]\n"
"v_max3_f32 v159, v159, v128, v129\n"
"v_max3_f32 v157, v157, v130, v131\n"
"v_mfma_f32_16x16x32_bf16 v[168:171], a[52:55], a[224:227], v[168:171]\n"
"v_cvt_pk_bf16_f32 v64, v64, v65\n"
"v_cvt_pk_bf16_f32 v65, v66, v67\n"
"v_mfma_f32_16x16x32_bf16 v[168:171], a[48:51], v[248:251], v[168:171]\n"
"v_max3_f32 v159, v159, v132, v133\n"
"v_max3_f32 v157, v157, v134, v135\n"
"v_mfma_f32_16x16x32_bf16 v[68:71], a[68:71], a[224:227], v[68:71]\n"
"v_max3_f32 v159, v159, v136, v137\n"
"v_max3_f32 v157, v157, v138, v139\n"
"v_mfma_f32_16x16x32_bf16 v[68:71], a[72:75], v[236:239], v[68:71]\n"
"v_max3_f32 v159, v159, v140, v141\n"
"v_max3_f32 v157, v157, v142, v143\n"
"v_mfma_f32_16x16x32_bf16 v[68:71], a[76:79], v[240:243], v[68:71]\n"
"v_cvt_pk_bf16_f32 v66, v168, v169\n"
"v_cvt_pk_bf16_f32 v67, v170, v171\n"
"v_mfma_f32_16x16x32_bf16 v[172:175], a[92:95], v[240:243], 0\n"
"ds_read_b128 v[240:243], v153 offset:0\n"
"v_max3_f32 v159, v159, v144, v145\n"
"v_mfma_f32_16x16x32_bf16 v[172:175], a[88:91], v[236:239], v[172:175]\n"
"ds_read_b128 v[236:239], v153 offset:32128\n"
"v_max3_f32 v157, v157, v146, v147\n"
"v_mfma_f32_16x16x32_bf16 v[172:175], a[84:87], a[224:227], v[172:175]\n"
"ds_read_b128 a[224:227], v153 offset:27936\n"
"v_cvt_pk_bf16_f32 v68, v68, v69\n"
"v_mfma_f32_16x16x32_bf16 v[172:175], a[80:83], v[248:251], v[172:175]\n"
"ds_read_b128 v[248:251], v153 offset:23744\n"
"v_cvt_pk_bf16_f32 v69, v70, v71\n"
"v_mfma_f32_16x16x32_bf16 v[88:91], a[32:35], v[244:247], 0\n"
"v_max_f32 v157, v157, v159\n"
"v_mov_b32 v161, v157\n"
"v_mfma_f32_16x16x32_bf16 v[92:95], a[64:67], v[244:247], 0\n"
"s_nop 0\n"
"v_permlane32_swap_b32 v157, v161\n"
"v_mfma_f32_16x16x32_bf16 v[252:255], a[252:255], v[112:115], v[252:255]\n"
"v_cvt_pk_bf16_f32 v70, v172, v173\n"
"v_cvt_pk_bf16_f32 v71, v174, v175\n"
"v_mfma_f32_16x16x32_bf16 v[204:207], v[96:99], v[48:51], v[204:207]\n"
"v_max_f32 v157, v157, v161\n"
"v_mov_b32 v161, v157\n"
"v_mfma_f32_16x16x32_bf16 v[204:207], v[104:107], v[52:55], v[204:207]\n"
"v_mfma_f32_16x16x32_bf16 v[204:207], v[112:115], v[56:59], v[204:207]\n"
"v_permlane16_swap_b32 v157, v161\n"
"v_mfma_f32_16x16x32_bf16 v[208:211], v[112:115], v[68:71], v[208:211]\n"
"v_max_f32 v157, v157, v161\n"
"v_sub_f32 v148, v148, v157\n"
"v_mfma_f32_16x16x32_bf16 v[208:211], v[104:107], v[64:67], v[208:211]\n"
"v_sub_f32 v149, v149, v157\n"
"v_sub_f32 v150, v150, v157\n"
"v_mfma_f32_16x16x32_bf16 v[208:211], v[96:99], v[60:63], v[208:211]\n"
"s_waitcnt lgkmcnt(2)\n"
"v_sub_f32 v151, v151, v157\n"
"v_sub_f32 v124, v124, v157\n"
"v_mfma_f32_16x16x32_bf16 v[176:179], a[28:31], v[236:239], 0\n"
"s_waitcnt lgkmcnt(1)\n"
"v_exp_f32 v161, v150\n"
"v_mfma_f32_16x16x32_bf16 v[176:179], a[24:27], a[224:227], v[176:179]\n"
"s_waitcnt lgkmcnt(0)\n"
"v_exp_f32 v148, v148\n"
"v_mfma_f32_16x16x32_bf16 v[176:179], a[20:23], v[248:251], v[176:179]\n"
"v_exp_f32 v149, v149\n"
"v_mfma_f32_16x16x32_bf16 v[176:179], a[16:19], v[244:247], v[176:179]\n"
"v_exp_f32 v151, v151\n"
"v_mfma_f32_16x16x32_bf16 v[84:87], a[4:7], v[248:251], v[84:87]\n"
"v_cvt_pk_bf16_f32 v150, v148, v149\n"
"v_add_f32 v148, v148, v149\n"
"v_mfma_f32_16x16x32_bf16 v[84:87], a[8:11], a[224:227], v[84:87]\n"
"v_add_f32 v149, v161, v151\n"
"v_cvt_pk_bf16_f32 v151, v161, v151\n"
"v_mfma_f32_16x16x32_bf16 v[84:87], a[12:15], v[236:239], v[84:87]\n"
"v_sub_f32 v125, v125, v157\n"
"v_sub_f32 v126, v126, v157\n"
"v_mfma_f32_16x16x32_bf16 v[88:91], a[36:39], v[248:251], v[88:91]\n"
"v_exp_f32 v124, v124\n"
"v_mfma_f32_16x16x32_bf16 v[88:91], a[40:43], a[224:227], v[88:91]\n"
"v_exp_f32 v125, v125\n"
"v_mfma_f32_16x16x32_bf16 v[88:91], a[44:47], v[236:239], v[88:91]\n"
"v_cvt_pk_bf16_f32 v84, v84, v85\n"
"v_cvt_pk_bf16_f32 v85, v86, v87\n"
"v_mfma_f32_16x16x32_bf16 v[164:167], a[60:63], v[236:239], 0\n"
"v_cvt_pk_bf16_f32 v86, v176, v177\n"
"v_cvt_pk_bf16_f32 v87, v178, v179\n"
"v_mfma_f32_16x16x32_bf16 v[164:167], a[56:59], a[224:227], v[164:167]\n"
"v_sub_f32 v127, v127, v157\n"
"v_sub_f32 v128, v128, v157\n"
"v_mfma_f32_16x16x32_bf16 v[164:167], a[52:55], v[248:251], v[164:167]\n"
"v_cvt_pk_bf16_f32 v88, v88, v89\n"
"v_cvt_pk_bf16_f32 v89, v90, v91\n"
"v_mfma_f32_16x16x32_bf16 v[164:167], a[48:51], v[244:247], v[164:167]\n"
"v_exp_f32 v126, v126\n"
"v_mfma_f32_16x16x32_bf16 v[92:95], a[68:71], v[248:251], v[92:95]\n"
"v_exp_f32 v127, v127\n"
"v_mfma_f32_16x16x32_bf16 v[92:95], a[72:75], a[224:227], v[92:95]\n"
"v_sub_f32 v129, v129, v157\n"
"v_cvt_pk_bf16_f32 v124, v124, v125\n"
"v_mfma_f32_16x16x32_bf16 v[92:95], a[76:79], v[236:239], v[92:95]\n"
"v_cvt_pk_bf16_f32 v90, v164, v165\n"
"v_cvt_pk_bf16_f32 v91, v166, v167\n"
"v_mfma_f32_16x16x32_bf16 v[168:171], a[92:95], v[236:239], 0\n"
"ds_read_b128 v[236:239], v153 offset:1024\n"
"v_cvt_pk_bf16_f32 v125, v126, v127\n"
"v_mfma_f32_16x16x32_bf16 v[168:171], a[88:91], a[224:227], v[168:171]\n"
"ds_read_b128 a[224:227], v153 offset:12288\n"
"v_sub_f32 v130, v130, v157\n"
"v_mfma_f32_16x16x32_bf16 v[168:171], a[84:87], v[248:251], v[168:171]\n"
"ds_read_b128 v[248:251], v153 offset:8192\n"
"v_cvt_pk_bf16_f32 v92, v92, v93\n"
"v_mfma_f32_16x16x32_bf16 v[168:171], a[80:83], v[244:247], v[168:171]\n"
"ds_read_b128 v[244:247], v153 offset:4096\n"
"v_cvt_pk_bf16_f32 v93, v94, v95\n"
"v_mfma_f32_16x16x32_bf16 v[212:215], v[96:99], v[72:75], v[212:215]\n"
"v_exp_f32 v128, v128\n"
"v_mfma_f32_16x16x32_bf16 v[212:215], v[104:107], v[76:79], v[212:215]\n"
"v_exp_f32 v129, v129\n"
"v_mfma_f32_16x16x32_bf16 v[212:215], v[112:115], v[80:83], v[212:215]\n"
"v_cvt_pk_bf16_f32 v94, v168, v169\n"
"v_cvt_pk_bf16_f32 v95, v170, v171\n"
"v_mfma_f32_16x16x32_bf16 v[120:123], a[228:231], v[196:199], 0\n"
"ds_read_b128 a[228:231], v158 offset:0\n"
"v_sub_f32 v131, v131, v157\n"
"v_mfma_f32_16x16x32_bf16 v[120:123], a[232:235], v[200:203], v[120:123]\n"
"ds_read_b128 a[232:235], v158 offset:2048\n"
"v_sub_f32 v132, v132, v157\n"
"v_mfma_f32_16x16x32_bf16 v[216:219], v[112:115], v[92:95], v[216:219]\n"
"v_exp_f32 v130, v130\n"
"v_mfma_f32_16x16x32_bf16 v[216:219], v[104:107], v[88:91], v[216:219]\n"
"v_exp_f32 v131, v131\n"
"v_mfma_f32_16x16x32_bf16 v[216:219], v[96:99], v[84:87], v[216:219]\n"
"v_sub_f32 v133, v133, v157\n"
"v_cvt_pk_bf16_f32 v126, v128, v129\n"
"v_mfma_f32_16x16x32_bf16 v[96:99], v[0:3], v[196:199], 0\n"
"v_exp_f32 v132, v132\n"
"v_mfma_f32_16x16x32_bf16 v[96:99], v[4:7], v[200:203], v[96:99]\n"
"v_cvt_pk_bf16_f32 v127, v130, v131\n"
"v_sub_f32 v134, v134, v157\n"
"v_mfma_f32_16x16x32_bf16 v[100:103], v[12:15], v[200:203], 0\n"
"v_exp_f32 v133, v133\n"
"v_mfma_f32_16x16x32_bf16 v[100:103], v[8:11], v[196:199], v[100:103]\n"
"v_sub_f32 v135, v135, v157\n"
"v_sub_f32 v136, v136, v157\n"
"v_mfma_f32_16x16x32_bf16 v[104:107], v[16:19], v[196:199], 0\n"
"v_exp_f32 v134, v134\n"
"v_mfma_f32_16x16x32_bf16 v[104:107], v[20:23], v[200:203], v[104:107]\n"
"v_exp_f32 v135, v135\n"
"v_mfma_f32_16x16x32_bf16 v[108:111], v[28:31], v[200:203], 0\n"
"v_sub_f32 v137, v137, v157\n"
"v_cvt_pk_bf16_f32 v132, v132, v133\n"
"v_mfma_f32_16x16x32_bf16 v[108:111], v[24:27], v[196:199], v[108:111]\n"
"v_exp_f32 v136, v136\n"
"v_mfma_f32_16x16x32_bf16 v[112:115], v[32:35], v[196:199], 0\n"
"v_cvt_pk_bf16_f32 v133, v134, v135\n"
"v_sub_f32 v138, v138, v157\n"
"v_mfma_f32_16x16x32_bf16 v[112:115], v[36:39], v[200:203], v[112:115]\n"
"v_exp_f32 v137, v137\n"
"v_mfma_f32_16x16x32_bf16 v[116:119], v[44:47], v[200:203], 0\n"
"v_sub_f32 v139, v139, v157\n"
"v_sub_f32 v140, v140, v157\n"
"v_mfma_f32_16x16x32_bf16 v[116:119], v[40:43], v[196:199], v[116:119]\n"
"v_exp_f32 v138, v138\n"
"v_sub_f32 v141, v141, v157\n"
"v_mfma_f32_16x16x4_4b_bf16 v[220:235], v[150:151], a[244:245], 0 cbsz:2 abid:0\n"
"v_exp_f32 v139, v139\n"
"v_exp_f32 v140, v140\n"
"v_cvt_pk_bf16_f32 v134, v136, v137\n"
"v_cvt_pk_bf16_f32 v135, v138, v139\n"
"v_sub_f32 v142, v142, v157\n"
"v_sub_f32 v143, v143, v157\n"
"v_mfma_f32_16x16x32_bf16 v[128:131], v[12:15], a[240:243], 0\n"
"v_exp_f32 v141, v141\n"
"v_mfma_f32_16x16x32_bf16 v[128:131], v[8:11], a[236:239], v[128:131]\n"
"v_exp_f32 v142, v142\n"
"v_mfma_f32_16x16x32_bf16 v[220:223], v[124:127], v[48:51], v[220:223]\n"
"v_exp_f32 v143, v143\n"
"v_mfma_f32_16x16x32_bf16 v[220:223], v[132:135], v[52:55], v[220:223]\n"
"v_sub_f32 v144, v144, v157\n"
"v_sub_f32 v145, v145, v157\n"
"v_mfma_f32_16x16x32_bf16 v[228:231], v[124:127], v[72:75], v[228:231]\n"
"v_exp_f32 v144, v144\n"
"v_mfma_f32_16x16x32_bf16 v[228:231], v[132:135], v[76:79], v[228:231]\n"
"v_cvt_pk_bf16_f32 v140, v140, v141\n"
"v_cvt_pk_bf16_f32 v141, v142, v143\n"
"v_mfma_f32_16x16x32_bf16 v[136:139], v[28:31], a[240:243], 0\n"
"v_sub_f32 v146, v146, v157\n"
"v_sub_f32 v147, v147, v157\n"
"v_mfma_f32_16x16x32_bf16 v[136:139], v[24:27], a[236:239], v[136:139]\n"
"v_exp_f32 v145, v145\n"
"v_mfma_f32_16x16x32_bf16 v[180:183], v[240:243], a[48:51], 0\n"
"s_waitcnt lgkmcnt(0)\n"
"v_exp_f32 v146, v146\n"
"v_mfma_f32_16x16x32_bf16 v[180:183], v[244:247], a[52:55], v[180:183]\n"
"v_exp_f32 v147, v147\n"
"v_mfma_f32_16x16x32_bf16 v[180:183], v[248:251], a[56:59], v[180:183]\n"
"v_cvt_pk_bf16_f32 v142, v144, v145\n"
"v_cvt_pk_bf16_f32 v143, v146, v147\n"
"v_mfma_f32_16x16x32_bf16 v[180:183], a[224:227], a[60:63], v[180:183]\n"
"v_add_f32 v252, v252, v253\n"
"v_max3_f32 v157, v120, v121, v122\n"
"v_mfma_f32_16x16x32_bf16 v[220:223], v[140:143], v[56:59], v[220:223]\n"
"v_rcp_f32 v253, v252\n"
"v_mfma_f32_16x16x32_bf16 v[224:227], v[140:143], v[68:71], v[224:227]\n"
"ds_bpermute_b32 v254, v160, v253 offset:0\n"
"v_mfma_f32_16x16x32_bf16 v[224:227], v[132:135], v[64:67], v[224:227]\n"
"v_max3_f32 v159, v123, v96, v97\n"
"v_max3_f32 v157, v157, v98, v99\n"
"v_mfma_f32_16x16x32_bf16 v[224:227], v[124:127], v[60:63], v[224:227]\n"
"v_max3_f32 v159, v159, v100, v101\n"
"v_max3_f32 v157, v157, v102, v103\n"
"v_mfma_f32_16x16x32_bf16 v[228:231], v[140:143], v[80:83], v[228:231]\n"
"v_max3_f32 v159, v159, v104, v105\n"
"v_max3_f32 v157, v157, v106, v107\n"
"v_mfma_f32_16x16x32_bf16 v[232:235], v[140:143], v[92:95], v[232:235]\n"
"v_max3_f32 v159, v159, v108, v109\n"
"v_max3_f32 v157, v157, v110, v111\n"
"v_mfma_f32_16x16x32_bf16 v[232:235], v[132:135], v[88:91], v[232:235]\n"
"v_max3_f32 v159, v159, v112, v113\n"
"v_max3_f32 v157, v157, v114, v115\n"
"v_mfma_f32_16x16x32_bf16 v[232:235], v[124:127], v[84:87], v[232:235]\n"
"v_max3_f32 v159, v159, v116, v117\n"
"v_max3_f32 v157, v157, v118, v119\n"
"v_mfma_f32_16x16x32_bf16 v[144:147], v[44:47], a[240:243], 0\n"
"v_max_f32 v157, v157, v159\n"
"v_mov_b32 v161, v157\n"
"v_mfma_f32_16x16x32_bf16 v[144:147], v[40:43], a[236:239], v[144:147]\n"
"v_cvt_pk_bf16_f32 v180, v180, v181\n"
"v_cvt_pk_bf16_f32 v181, v182, v183\n"
"v_mfma_f32_16x16x32_bf16 v[188:191], a[224:227], a[76:79], 0\n"
"v_permlane32_swap_b32 v157, v161\n"
"v_mfma_f32_16x16x32_bf16 v[188:191], v[248:251], a[72:75], v[188:191]\n"
"v_max_f32 v157, v157, v161\n"
"v_mov_b32 v161, v157\n"
"v_mfma_f32_16x16x32_bf16 v[188:191], v[244:247], a[68:71], v[188:191]\n"
"s_waitcnt lgkmcnt(0)\n"
"v_mul_f32_dpp v204, v254, v204 quad_perm:[0,0,0,0] row_mask:0xf bank_mask:0xf\n"
"v_mul_f32_dpp v205, v254, v205 quad_perm:[1,1,1,1] row_mask:0xf bank_mask:0xf\n"
"v_mfma_f32_16x16x32_bf16 v[188:191], v[240:243], a[64:67], v[188:191]\n"
"v_mul_f32_dpp v206, v254, v206 quad_perm:[2,2,2,2] row_mask:0xf bank_mask:0xf\n"
"v_mul_f32_dpp v207, v254, v207 quad_perm:[3,3,3,3] row_mask:0xf bank_mask:0xf\n"
"v_mfma_f32_16x16x32_bf16 v[196:199], v[240:243], a[80:83], 0\n"
"ds_read_b128 v[240:243], v153 offset:5120\n"
"v_mul_f32_dpp v208, v254, v208 quad_perm:[0,0,0,0] row_mask:0xf bank_mask:0xf\n"
"v_mfma_f32_16x16x32_bf16 v[196:199], v[244:247], a[84:87], v[196:199]\n"
"ds_read_b128 v[244:247], v153 offset:9216\n"
"v_mul_f32_dpp v209, v254, v209 quad_perm:[1,1,1,1] row_mask:0xf bank_mask:0xf\n"
"v_mfma_f32_16x16x32_bf16 v[196:199], v[248:251], a[88:91], v[196:199]\n"
"ds_read_b128 v[248:251], v153 offset:13312\n"
"v_mul_f32_dpp v210, v254, v210 quad_perm:[2,2,2,2] row_mask:0xf bank_mask:0xf\n"
"v_mfma_f32_16x16x32_bf16 v[196:199], a[224:227], a[92:95], v[196:199]\n"
"ds_read_b128 a[224:227], v153 offset:3072\n"
"v_mul_f32_dpp v211, v254, v211 quad_perm:[3,3,3,3] row_mask:0xf bank_mask:0xf\n"
"v_mfma_f32_16x16x32_bf16 v[204:207], a[0:3], a[160:163], v[204:207]\n"
"v_mul_f32_dpp v212, v254, v212 quad_perm:[0,0,0,0] row_mask:0xf bank_mask:0xf\n"
"v_mul_f32_dpp v213, v254, v213 quad_perm:[1,1,1,1] row_mask:0xf bank_mask:0xf\n"
"v_mfma_f32_16x16x32_bf16 v[204:207], a[4:7], a[164:167], v[204:207]\n"
"v_mul_f32_dpp v214, v254, v214 quad_perm:[2,2,2,2] row_mask:0xf bank_mask:0xf\n"
"v_mul_f32_dpp v215, v254, v215 quad_perm:[3,3,3,3] row_mask:0xf bank_mask:0xf\n"
"v_mfma_f32_16x16x32_bf16 v[204:207], a[8:11], a[168:171], v[204:207]\n"
"v_mul_f32_dpp v216, v254, v216 quad_perm:[0,0,0,0] row_mask:0xf bank_mask:0xf\n"
"v_mul_f32_dpp v217, v254, v217 quad_perm:[1,1,1,1] row_mask:0xf bank_mask:0xf\n"
"v_mfma_f32_16x16x32_bf16 v[204:207], a[12:15], a[172:175], v[204:207]\n"
"v_mul_f32_dpp v218, v254, v218 quad_perm:[2,2,2,2] row_mask:0xf bank_mask:0xf\n"
"v_mul_f32_dpp v219, v254, v219 quad_perm:[3,3,3,3] row_mask:0xf bank_mask:0xf\n"
"v_mfma_f32_16x16x32_bf16 v[208:211], a[12:15], a[188:191], v[208:211]\n"
"v_permlane16_swap_b32 v157, v161\n"
"v_mfma_f32_16x16x32_bf16 v[208:211], a[8:11], a[184:187], v[208:211]\n"
"v_max_f32 v157, v157, v161\n"
"v_sub_f32 v120, v120, v157\n"
"v_mfma_f32_16x16x32_bf16 v[208:211], a[4:7], a[180:183], v[208:211]\n"
"v_sub_f32 v121, v121, v157\n"
"v_sub_f32 v122, v122, v157\n"
"v_mfma_f32_16x16x32_bf16 v[208:211], a[0:3], a[176:179], v[208:211]\n"
"v_sub_f32 v123, v123, v157\n"
"v_sub_f32 v96, v96, v157\n"
"v_mfma_f32_16x16x32_bf16 v[212:215], a[0:3], a[192:195], v[212:215]\n"
"v_exp_f32 v161, v122\n"
"v_mfma_f32_16x16x32_bf16 v[212:215], a[4:7], a[196:199], v[212:215]\n"
"v_exp_f32 v120, v120\n"
"v_mfma_f32_16x16x32_bf16 v[212:215], a[8:11], a[200:203], v[212:215]\n"
"v_cvt_pk_bf16_f32 v204, v204, v208\n"
"v_cvt_pk_bf16_f32 v208, v205, v209\n"
"v_mfma_f32_16x16x32_bf16 v[212:215], a[12:15], a[204:207], v[212:215]\n"
"v_pk_max_i16 v204, v204, 0\n"
"v_pk_max_i16 v208, v208, 0\n"
"v_mfma_f32_16x16x32_bf16 v[216:219], a[12:15], a[220:223], v[216:219]\n"
"s_waitcnt vmcnt(16)\n"
"ds_read_b128 a[12:15], v154 offset:192\n"
"v_cvt_pk_bf16_f32 v206, v206, v210\n"
"v_mfma_f32_16x16x32_bf16 v[216:219], a[8:11], a[216:219], v[216:219]\n"
"ds_read_b128 a[8:11], v154 offset:128\n"
"v_cvt_pk_bf16_f32 v210, v207, v211\n"
"v_mfma_f32_16x16x32_bf16 v[216:219], a[4:7], a[212:215], v[216:219]\n"
"ds_read_b128 a[4:7], v154 offset:64\n"
"v_pk_max_i16 v206, v206, 0\n"
"v_mfma_f32_16x16x32_bf16 v[216:219], a[0:3], a[208:211], v[216:219]\n"
"ds_read_b128 a[0:3], v154 offset:0\n"
"v_pk_max_i16 v210, v210, 0\n"
"v_mfma_f32_16x16x32_bf16 v[252:255], a[252:255], v[124:127], v[148:151]\n"
"v_exp_f32 v121, v121\n"
"v_mfma_f32_16x16x32_bf16 v[252:255], a[252:255], v[132:135], v[252:255]\n"
"v_exp_f32 v123, v123\n"
"v_mfma_f32_16x16x32_bf16 v[252:255], a[252:255], v[140:143], v[252:255]\n"
"v_cvt_pk_bf16_f32 v205, v212, v216\n"
"v_cvt_pk_bf16_f32 v209, v213, v217\n"
"v_mfma_f32_16x16x32_bf16 v[148:151], a[228:231], a[236:239], 0\n"
"ds_read_b128 a[228:231], v158 offset:0\n"
"v_pk_max_i16 v205, v205, 0\n"
"v_mfma_f32_16x16x32_bf16 v[148:151], a[232:235], a[240:243], v[148:151]\n"
"buffer_store_dwordx2 v[204:205], v162, s[84:87], 0 offen offset:0 sc0 sc1\n"
"v_mfma_f32_16x16x32_bf16 v[124:127], v[0:3], a[236:239], 0\n"
"ds_read_b128 a[232:235], v158 offset:2048\n"
"v_pk_max_i16 v209, v209, 0\n"
"v_mfma_f32_16x16x32_bf16 v[124:127], v[4:7], a[240:243], v[124:127]\n"
"buffer_store_dwordx2 v[208:209], v162, s[84:87], 0 offen offset:256 sc0 sc1\n"
"v_mfma_f32_16x16x32_bf16 v[132:135], v[16:19], a[236:239], 0\n"
"v_cvt_pk_bf16_f32 v207, v214, v218\n"
"v_cvt_pk_bf16_f32 v211, v215, v219\n"
"v_mfma_f32_16x16x32_bf16 v[132:135], v[20:23], a[240:243], v[132:135]\n"
"v_pk_max_i16 v207, v207, 0\n"
"v_pk_max_i16 v211, v211, 0\n"
"v_mfma_f32_16x16x32_bf16 v[140:143], v[32:35], a[236:239], 0\n"
"ds_read_b32 a236, v156 offset:0\n"
"ds_read_b32 a237, v156 offset:8\n"
"v_mfma_f32_16x16x32_bf16 v[140:143], v[36:39], a[240:243], v[140:143]\n"
"buffer_store_dwordx2 v[206:207], v162, s[84:87], 0 offen offset:512 sc0 sc1\n"
"v_mfma_f32_16x16x32_bf16 v[184:187], v[236:239], a[48:51], 0\n"
"s_waitcnt lgkmcnt(4)\n"
"ds_read_b32 a238, v156 offset:4\n"
"ds_read_b32 a239, v156 offset:12\n"
"v_mfma_f32_16x16x32_bf16 v[184:187], v[240:243], a[52:55], v[184:187]\n"
"ds_read_b32 a240, v156 offset:256\n"
"ds_read_b32 a241, v156 offset:264\n"
"v_mfma_f32_16x16x32_bf16 v[184:187], v[244:247], a[56:59], v[184:187]\n"
"ds_read_b32 a242, v156 offset:260\n"
"ds_read_b32 a243, v156 offset:268\n"
"v_mfma_f32_16x16x32_bf16 v[184:187], v[248:251], a[60:63], v[184:187]\n"
"buffer_store_dwordx2 v[210:211], v162, s[84:87], 0 offen offset:768 sc0 sc1\n"
"v_mfma_f32_16x16x32_bf16 v[192:195], v[248:251], a[76:79], 0\n"
"s_add_u32 m0, s28, 0\n"
"v_cvt_pk_bf16_f32 v122, v120, v121\n"
"v_mfma_f32_16x16x32_bf16 v[192:195], v[244:247], a[72:75], v[192:195]\n"
"buffer_load_dwordx4 v152, s[24:27], s52 offen offset:0 lds\n"
"v_add_f32 v120, v120, v121\n"
"v_mfma_f32_16x16x32_bf16 v[192:195], v[240:243], a[68:71], v[192:195]\n"
"buffer_load_dwordx4 v152, s[24:27], s53 offen offset:1088 lds\n"
"v_add_f32 v121, v161, v123\n"
"v_mfma_f32_16x16x32_bf16 v[192:195], v[236:239], a[64:67], v[192:195]\n"
"buffer_load_dwordx4 v152, s[24:27], s54 offen offset:2176 lds\n"
"v_cvt_pk_bf16_f32 v123, v161, v123\n"
"v_mfma_f32_16x16x32_bf16 v[200:203], v[236:239], a[80:83], 0\n"
"buffer_load_dwordx4 v152, s[24:27], s55 offen offset:3264 lds\n"
"ds_read_b128 v[236:239], v153 offset:2048\n"
"v_mfma_f32_16x16x32_bf16 v[200:203], v[240:243], a[84:87], v[200:203]\n"
"ds_read_b128 v[240:243], v153 offset:6144\n"
"v_sub_f32 v97, v97, v157\n"
"v_mfma_f32_16x16x32_bf16 v[200:203], v[244:247], a[88:91], v[200:203]\n"
"ds_read_b128 v[244:247], v153 offset:10240\n"
"v_add_f32 v252, v252, v253\n"
"v_mfma_f32_16x16x32_bf16 v[200:203], v[248:251], a[92:95], v[200:203]\n"
"ds_read_b128 v[248:251], v153 offset:14336\n"
"v_exp_f32 v96, v96\n"
"v_mfma_f32_16x16x4_4b_bf16 v[204:219], v[122:123], a[244:245], 0 cbsz:2 abid:0\n"
"v_sub_f32 v98, v98, v157\n"
"v_exp_f32 v97, v97\n"
"v_rcp_f32 v253, v252\n"
"v_sub_f32 v99, v99, v157\n"
"v_exp_f32 v98, v98\n"
"v_mfma_f32_16x16x32_bf16 v[164:167], a[224:227], a[48:51], 0\n"
"ds_bpermute_b32 v254, v160, v253 offset:0\n"
"v_mfma_f32_16x16x32_bf16 v[172:175], a[224:227], a[80:83], 0\n"
"v_exp_f32 v99, v99\n"
"v_sub_f32 v100, v100, v157\n"
"v_sub_f32 v101, v101, v157\n"
"v_exp_f32 v100, v100\n"
"v_cvt_pk_bf16_f32 v96, v96, v97\n"
"v_cvt_pk_bf16_f32 v97, v98, v99\n"
"v_sub_f32 v102, v102, v157\n"
"v_exp_f32 v101, v101\n"
"v_sub_f32 v103, v103, v157\n"
"v_exp_f32 v102, v102\n"
"v_exp_f32 v103, v103\n"
"v_sub_f32 v104, v104, v157\n"
"v_sub_f32 v105, v105, v157\n"
"v_exp_f32 v104, v104\n"
"v_cvt_pk_bf16_f32 v98, v100, v101\n"
"v_cvt_pk_bf16_f32 v99, v102, v103\n"
"v_sub_f32 v106, v106, v157\n"
"v_exp_f32 v105, v105\n"
"v_mfma_f32_16x16x32_bf16 v[204:207], v[96:99], v[48:51], v[204:207]\n"
"v_sub_f32 v107, v107, v157\n"
"v_sub_f32 v108, v108, v157\n"
"v_mfma_f32_16x16x32_bf16 v[212:215], v[96:99], v[72:75], v[212:215]\n"
"v_exp_f32 v106, v106\n"
"v_exp_f32 v107, v107\n"
"v_sub_f32 v109, v109, v157\n"
"v_exp_f32 v108, v108\n"
"v_cvt_pk_bf16_f32 v104, v104, v105\n"
"s_waitcnt lgkmcnt(0)\n"
"v_mul_f32_dpp v220, v254, v220 quad_perm:[0,0,0,0] row_mask:0xf bank_mask:0xf\n"
"v_mul_f32_dpp v221, v254, v221 quad_perm:[1,1,1,1] row_mask:0xf bank_mask:0xf\n"
"v_mul_f32_dpp v222, v254, v222 quad_perm:[2,2,2,2] row_mask:0xf bank_mask:0xf\n"
"v_mul_f32_dpp v223, v254, v223 quad_perm:[3,3,3,3] row_mask:0xf bank_mask:0xf\n"
"v_mul_f32_dpp v224, v254, v224 quad_perm:[0,0,0,0] row_mask:0xf bank_mask:0xf\n"
"v_mul_f32_dpp v225, v254, v225 quad_perm:[1,1,1,1] row_mask:0xf bank_mask:0xf\n"
"v_mul_f32_dpp v226, v254, v226 quad_perm:[2,2,2,2] row_mask:0xf bank_mask:0xf\n"
"v_mfma_f32_16x16x32_bf16 v[220:223], a[16:19], a[160:163], v[220:223]\n"
"v_mul_f32_dpp v227, v254, v227 quad_perm:[3,3,3,3] row_mask:0xf bank_mask:0xf\n"
"v_mul_f32_dpp v228, v254, v228 quad_perm:[0,0,0,0] row_mask:0xf bank_mask:0xf\n"
"v_mfma_f32_16x16x32_bf16 v[220:223], a[20:23], a[164:167], v[220:223]\n"
"v_mul_f32_dpp v229, v254, v229 quad_perm:[1,1,1,1] row_mask:0xf bank_mask:0xf\n"
"v_mul_f32_dpp v230, v254, v230 quad_perm:[2,2,2,2] row_mask:0xf bank_mask:0xf\n"
"v_mfma_f32_16x16x32_bf16 v[220:223], a[24:27], a[168:171], v[220:223]\n"
"v_mul_f32_dpp v231, v254, v231 quad_perm:[3,3,3,3] row_mask:0xf bank_mask:0xf\n"
"v_mul_f32_dpp v232, v254, v232 quad_perm:[0,0,0,0] row_mask:0xf bank_mask:0xf\n"
"v_mfma_f32_16x16x32_bf16 v[220:223], a[28:31], a[172:175], v[220:223]\n"
"v_mul_f32_dpp v233, v254, v233 quad_perm:[1,1,1,1] row_mask:0xf bank_mask:0xf\n"
"v_cvt_pk_bf16_f32 v105, v106, v107\n"
"v_mfma_f32_16x16x32_bf16 v[224:227], a[28:31], a[188:191], v[224:227]\n"
"v_mul_f32_dpp v234, v254, v234 quad_perm:[2,2,2,2] row_mask:0xf bank_mask:0xf\n"
"v_sub_f32 v110, v110, v157\n"
"v_mfma_f32_16x16x32_bf16 v[224:227], a[24:27], a[184:187], v[224:227]\n"
"v_exp_f32 v109, v109\n"
"v_mfma_f32_16x16x32_bf16 v[224:227], a[20:23], a[180:183], v[224:227]\n"
"v_mul_f32_dpp v235, v254, v235 quad_perm:[3,3,3,3] row_mask:0xf bank_mask:0xf\n"
"v_sub_f32 v111, v111, v157\n"
"v_mfma_f32_16x16x32_bf16 v[224:227], a[16:19], a[176:179], v[224:227]\n"
"v_exp_f32 v110, v110\n"
"v_mfma_f32_16x16x32_bf16 v[228:231], a[16:19], a[192:195], v[228:231]\n"
"v_exp_f32 v111, v111\n"
"v_mfma_f32_16x16x32_bf16 v[228:231], a[20:23], a[196:199], v[228:231]\n"
"v_sub_f32 v112, v112, v157\n"
"v_sub_f32 v113, v113, v157\n"
"v_mfma_f32_16x16x32_bf16 v[228:231], a[24:27], a[200:203], v[228:231]\n"
"v_exp_f32 v112, v112\n"
"v_mfma_f32_16x16x32_bf16 v[228:231], a[28:31], a[204:207], v[228:231]\n"
"v_cvt_pk_bf16_f32 v106, v108, v109\n"
"v_cvt_pk_bf16_f32 v107, v110, v111\n"
"v_mfma_f32_16x16x32_bf16 v[232:235], a[28:31], a[220:223], v[232:235]\n"
"s_waitcnt vmcnt(17)\n"
"ds_read_b128 a[28:31], v154 offset:4544\n"
"v_sub_f32 v114, v114, v157\n"
"v_mfma_f32_16x16x32_bf16 v[232:235], a[24:27], a[216:219], v[232:235]\n"
"ds_read_b128 a[24:27], v154 offset:4480\n"
"v_sub_f32 v115, v115, v157\n"
"v_mfma_f32_16x16x32_bf16 v[232:235], a[20:23], a[212:215], v[232:235]\n"
"ds_read_b128 a[20:23], v154 offset:4416\n"
"v_sub_f32 v116, v116, v157\n"
"v_mfma_f32_16x16x32_bf16 v[232:235], a[16:19], a[208:211], v[232:235]\n"
"ds_read_b128 a[16:19], v154 offset:4352\n"
"v_sub_f32 v117, v117, v157\n"
"v_mfma_f32_16x16x32_bf16 v[252:255], a[252:255], v[96:99], v[120:123]\n"
"v_exp_f32 v113, v113\n"
"v_mfma_f32_16x16x32_bf16 v[252:255], a[252:255], v[104:107], v[252:255]\n"
"v_exp_f32 v114, v114\n"
"v_mfma_f32_16x16x32_bf16 v[204:207], v[104:107], v[52:55], v[204:207]\n"
"v_exp_f32 v115, v115\n"
"v_mfma_f32_16x16x32_bf16 v[212:215], v[104:107], v[76:79], v[212:215]\n"
"v_exp_f32 v116, v116\n"
"v_cvt_pk_bf16_f32 v112, v112, v113\n"
"v_cvt_pk_bf16_f32 v113, v114, v115\n"
"v_sub_f32 v118, v118, v157\n"
"v_exp_f32 v117, v117\n"
"v_sub_f32 v119, v119, v157\n"
"v_exp_f32 v118, v118\n"
"v_exp_f32 v119, v119\n"
"v_cvt_pk_bf16_f32 v220, v220, v224\n"
"v_cvt_pk_bf16_f32 v224, v221, v225\n"
"v_cvt_pk_bf16_f32 v221, v228, v232\n"
"v_cvt_pk_bf16_f32 v225, v229, v233\n"
"v_pk_max_i16 v220, v220, 0\n"
"v_pk_max_i16 v221, v221, 0\n"
"v_cvt_pk_bf16_f32 v114, v116, v117\n"
"v_cvt_pk_bf16_f32 v115, v118, v119\n"
"v_pk_max_i16 v224, v224, 0\n"
"v_pk_max_i16 v225, v225, 0\n"
"v_cvt_pk_bf16_f32 v222, v222, v226\n"
"v_mfma_f32_16x16x32_bf16 v[252:255], a[252:255], v[112:115], v[252:255]\n"
"buffer_store_dwordx2 v[220:221], v162, s[84:87], s65 offen offset:0 sc0 sc1\n"
"v_mfma_f32_16x16x32_bf16 v[204:207], v[112:115], v[56:59], v[204:207]\n"
"buffer_store_dwordx2 v[224:225], v162, s[84:87], s65 offen offset:256 sc0 sc1\n"
"v_mfma_f32_16x16x32_bf16 v[208:211], v[112:115], v[68:71], v[208:211]\n"
"v_cvt_pk_bf16_f32 v226, v223, v227\n"
"v_cvt_pk_bf16_f32 v223, v230, v234\n"
"v_mfma_f32_16x16x32_bf16 v[208:211], v[104:107], v[64:67], v[208:211]\n"
"v_cvt_pk_bf16_f32 v227, v231, v235\n"
"v_pk_max_i16 v222, v222, 0\n"
"v_mfma_f32_16x16x32_bf16 v[208:211], v[96:99], v[60:63], v[208:211]\n"
"v_pk_max_i16 v223, v223, 0\n"
"v_pk_max_i16 v226, v226, 0\n"
"v_mfma_f32_16x16x32_bf16 v[212:215], v[112:115], v[80:83], v[212:215]\n"
"buffer_store_dwordx2 v[222:223], v162, s[84:87], s65 offen offset:512 sc0 sc1\n"
"v_mfma_f32_16x16x32_bf16 v[216:219], v[112:115], v[92:95], v[216:219]\n"
"s_nop 0\n"
"ds_read_b64 v[222:223], v163 offset:16384\n"
"s_add_u32 m0, s28, 4352\n"
"v_mfma_f32_16x16x32_bf16 v[216:219], v[104:107], v[88:91], v[216:219]\n"
"s_waitcnt lgkmcnt(1)\n"
"buffer_load_dwordx4 v152, s[24:27], s56 offen offset:0 lds\n"
"v_pk_max_i16 v227, v227, 0\n"
"v_mfma_f32_16x16x32_bf16 v[216:219], v[96:99], v[84:87], v[216:219]\n"
"buffer_load_dwordx4 v152, s[24:27], s57 offen offset:1088 lds\n"
"v_cvt_pk_bf16_f32 v188, v188, v189\n"
"v_cvt_pk_bf16_f32 v189, v190, v191\n"
"v_cvt_pk_bf16_f32 v196, v196, v197\n"
"v_cvt_pk_bf16_f32 v197, v198, v199\n"
"v_cvt_pk_bf16_f32 v182, v184, v185\n"
"v_cvt_pk_bf16_f32 v183, v186, v187\n"
"v_mfma_f32_16x16x32_bf16 v[184:187], v[236:239], a[48:51], 0\n"
"buffer_load_dwordx4 v152, s[24:27], s58 offen offset:2176 lds\n"
"v_cvt_pk_bf16_f32 v190, v192, v193\n"
"v_mfma_f32_16x16x32_bf16 v[184:187], v[240:243], a[52:55], v[184:187]\n"
"buffer_load_dwordx4 v152, s[24:27], s59 offen offset:3264 lds\n"
"v_cvt_pk_bf16_f32 v191, v194, v195\n"
"v_mfma_f32_16x16x32_bf16 v[184:187], v[244:247], a[56:59], v[184:187]\n"
"v_cvt_pk_bf16_f32 v198, v200, v201\n"
"v_cvt_pk_bf16_f32 v199, v202, v203\n"
"v_mfma_f32_16x16x32_bf16 v[184:187], v[248:251], a[60:63], v[184:187]\n"
"buffer_store_dwordx2 v[226:227], v162, s[84:87], s65 offen offset:768 sc0 sc1\n"
"v_mfma_f32_16x16x32_bf16 v[192:195], v[248:251], a[76:79], 0\n"
"v_max3_f32 v157, v148, v149, v150\n"
"v_max3_f32 v159, v151, v124, v125\n"
"v_mfma_f32_16x16x32_bf16 v[192:195], v[244:247], a[72:75], v[192:195]\n"
"v_max3_f32 v157, v157, v126, v127\n"
"v_max3_f32 v159, v159, v128, v129\n"
"v_mfma_f32_16x16x32_bf16 v[192:195], v[240:243], a[68:71], v[192:195]\n"
"v_max3_f32 v157, v157, v130, v131\n"
"v_add_u32 v163, 512, v163\n"
"v_mfma_f32_16x16x32_bf16 v[192:195], v[236:239], a[64:67], v[192:195]\n"
"v_max3_f32 v159, v159, v132, v133\n"
"v_max3_f32 v157, v157, v134, v135\n"
"v_mfma_f32_16x16x32_bf16 v[200:203], v[236:239], a[80:83], 0\n"
"ds_read_b128 v[236:239], v153 offset:7168\n"
"v_max3_f32 v159, v159, v136, v137\n"
"v_mfma_f32_16x16x32_bf16 v[200:203], v[240:243], a[84:87], v[200:203]\n"
"ds_read_b128 v[240:243], v153 offset:11264\n"
"v_max3_f32 v157, v157, v138, v139\n"
"v_mfma_f32_16x16x32_bf16 v[200:203], v[244:247], a[88:91], v[200:203]\n"
"ds_read_b128 v[244:247], v153 offset:15360\n"
"v_max3_f32 v159, v159, v140, v141\n"
"v_mfma_f32_16x16x32_bf16 v[200:203], v[248:251], a[92:95], v[200:203]\n"
"ds_read_b128 v[248:251], v153 offset:0\n"
"v_max3_f32 v157, v157, v142, v143\n"
"v_mfma_f32_16x16x32_bf16 v[120:123], a[228:231], v[180:183], 0\n"
"ds_read_b128 a[228:231], v158 offset:0\n"
"v_max3_f32 v159, v159, v144, v145\n"
"v_mfma_f32_16x16x32_bf16 v[96:99], v[0:3], v[180:183], 0\n"
"v_cvt_pk_bf16_f32 v184, v184, v185\n"
"v_cvt_pk_bf16_f32 v185, v186, v187\n"
"v_mfma_f32_16x16x32_bf16 v[104:107], v[16:19], v[180:183], 0\n"
"v_cvt_pk_bf16_f32 v192, v192, v193\n"
"v_cvt_pk_bf16_f32 v193, v194, v195\n"
"v_mfma_f32_16x16x32_bf16 v[112:115], v[32:35], v[180:183], 0\n"
"v_cvt_pk_bf16_f32 v200, v200, v201\n"
"v_cvt_pk_bf16_f32 v201, v202, v203\n"
"v_max3_f32 v157, v157, v146, v147\n"
"v_max_f32 v157, v157, v159\n"
"v_mov_b32 v161, v157\n"
"v_add_f32 v252, v252, v253\n"
"v_rcp_f32 v253, v252\n"
"v_permlane32_swap_b32 v157, v161\n"
"v_max_f32 v157, v157, v161\n"
"v_mov_b32 v161, v157\n"
"s_waitcnt lgkmcnt(5)\n"
"v_lshlrev_b32 v220, 16, v222\n"
"v_and_b32 v221, 0xffff0000, v222\n"
"s_waitcnt lgkmcnt(4)\n"
"v_mfma_f32_16x16x32_bf16 v[164:167], v[236:239], a[52:55], v[164:167]\n"
"s_waitcnt lgkmcnt(3)\n"
"ds_bpermute_b32 v254, v160, v253 offset:0\n"
"v_mfma_f32_16x16x32_bf16 v[164:167], v[240:243], a[56:59], v[164:167]\n"
"s_waitcnt lgkmcnt(3)\n"
"v_permlane16_swap_b32 v157, v161\n"
"v_mfma_f32_16x16x32_bf16 v[164:167], v[244:247], a[60:63], v[164:167]\n"
"v_max_f32 v157, v157, v161\n"
"v_sub_f32 v148, v148, v157\n"
"v_mfma_f32_16x16x32_bf16 v[168:171], v[244:247], a[76:79], 0\n"
"v_sub_f32 v149, v149, v157\n"
"v_sub_f32 v150, v150, v157\n"
"v_mfma_f32_16x16x32_bf16 v[168:171], v[240:243], a[72:75], v[168:171]\n"
"v_sub_f32 v151, v151, v157\n"
"v_sub_f32 v124, v124, v157\n"
"v_mfma_f32_16x16x32_bf16 v[168:171], v[236:239], a[68:71], v[168:171]\n"
"v_exp_f32 v161, v150\n"
"v_mfma_f32_16x16x32_bf16 v[168:171], a[224:227], a[64:67], v[168:171]\n"
"ds_read_b128 a[224:227], v153 offset:4096\n"
"v_cvt_pk_bf16_f32 v186, v164, v165\n"
"v_mfma_f32_16x16x32_bf16 v[172:175], v[236:239], a[84:87], v[172:175]\n"
"ds_read_b128 v[236:239], v153 offset:8192\n"
"v_cvt_pk_bf16_f32 v187, v166, v167\n"
"v_mfma_f32_16x16x32_bf16 v[172:175], v[240:243], a[88:91], v[172:175]\n"
"ds_read_b128 v[240:243], v153 offset:12288\n"
"v_sub_f32 v125, v125, v157\n"
"v_mfma_f32_16x16x32_bf16 v[172:175], v[244:247], a[92:95], v[172:175]\n"
"ds_read_b128 v[244:247], v153 offset:1024\n"
"v_cvt_pk_bf16_f32 v194, v168, v169\n"
"v_mfma_f32_16x16x32_bf16 v[120:123], a[232:235], v[184:187], v[120:123]\n"
"ds_read_b128 a[232:235], v158 offset:2048\n"
"v_cvt_pk_bf16_f32 v195, v170, v171\n"
"v_mfma_f32_16x16x32_bf16 v[96:99], v[4:7], v[184:187], v[96:99]\n"
"v_exp_f32 v148, v148\n"
"v_mfma_f32_16x16x32_bf16 v[100:103], v[12:15], v[184:187], 0\n"
"v_exp_f32 v149, v149\n"
"v_mfma_f32_16x16x32_bf16 v[100:103], v[8:11], v[180:183], v[100:103]\n"
"v_exp_f32 v151, v151\n"
"v_mfma_f32_16x16x32_bf16 v[104:107], v[20:23], v[184:187], v[104:107]\n"
"v_cvt_pk_bf16_f32 v202, v172, v173\n"
"v_cvt_pk_bf16_f32 v203, v174, v175\n"
"v_mfma_f32_16x16x32_bf16 v[108:111], v[28:31], v[184:187], 0\n"
"v_cvt_pk_bf16_f32 v150, v148, v149\n"
"v_add_f32 v148, v148, v149\n"
"v_mfma_f32_16x16x32_bf16 v[108:111], v[24:27], v[180:183], v[108:111]\n"
"v_add_f32 v149, v161, v151\n"
"v_cvt_pk_bf16_f32 v151, v161, v151\n"
"v_mfma_f32_16x16x32_bf16 v[112:115], v[36:39], v[184:187], v[112:115]\n"
"v_exp_f32 v124, v124\n"
"v_mfma_f32_16x16x32_bf16 v[116:119], v[44:47], v[184:187], 0\n"
"v_sub_f32 v126, v126, v157\n"
"v_sub_f32 v127, v127, v157\n"
"v_mfma_f32_16x16x32_bf16 v[116:119], v[40:43], v[180:183], v[116:119]\n"
"s_waitcnt lgkmcnt(3)\n"
"v_exp_f32 v125, v125\n"
"v_mul_f32_dpp v204, v254, v204 quad_perm:[0,0,0,0] row_mask:0xf bank_mask:0xf\n"
"v_mfma_f32_16x16x4_4b_bf16 v[164:179], v[150:151], a[244:245], 0 cbsz:2 abid:0\n"
"v_exp_f32 v126, v126\n"
"v_mul_f32_dpp v205, v254, v205 quad_perm:[1,1,1,1] row_mask:0xf bank_mask:0xf\n"
"v_mul_f32_dpp v206, v254, v206 quad_perm:[2,2,2,2] row_mask:0xf bank_mask:0xf\n"
"v_exp_f32 v127, v127\n"
"v_mul_f32_dpp v207, v254, v207 quad_perm:[3,3,3,3] row_mask:0xf bank_mask:0xf\n"
"v_sub_f32 v128, v128, v157\n"
"s_sub_u32 s74, s74, 1\n"
"s_cmp_eq_u32 s74, 0\n"
"s_cbranch_scc1 .Leps%=\n"
".Lp1s%=:\n"
"v_mfma_f32_16x16x32_bf16 v[180:183], v[248:251], a[0:3], 0\n"
"v_mul_f32_dpp v208, v254, v208 quad_perm:[0,0,0,0] row_mask:0xf bank_mask:0xf\n"
"v_mul_f32_dpp v209, v254, v209 quad_perm:[1,1,1,1] row_mask:0xf bank_mask:0xf\n"
"v_mfma_f32_16x16x32_bf16 v[180:183], a[224:227], a[4:7], v[180:183]\n"
"v_mul_f32_dpp v210, v254, v210 quad_perm:[2,2,2,2] row_mask:0xf bank_mask:0xf\n"
"v_sub_f32 v129, v129, v157\n"
"v_mfma_f32_16x16x32_bf16 v[180:183], v[236:239], a[8:11], v[180:183]\n"
"s_waitcnt lgkmcnt(0)\n"
"v_exp_f32 v128, v128\n"
"v_mfma_f32_16x16x32_bf16 v[180:183], v[240:243], a[12:15], v[180:183]\n"
"v_mul_f32_dpp v211, v254, v211 quad_perm:[3,3,3,3] row_mask:0xf bank_mask:0xf\n"
"v_cvt_pk_bf16_f32 v124, v124, v125\n"
"v_mfma_f32_16x16x32_bf16 v[204:207], a[32:35], a[160:163], v[204:207]\n"
"v_mul_f32_dpp v212, v254, v212 quad_perm:[0,0,0,0] row_mask:0xf bank_mask:0xf\n"
"v_cvt_pk_bf16_f32 v125, v126, v127\n"
"v_mfma_f32_16x16x32_bf16 v[204:207], a[36:39], a[164:167], v[204:207]\n"
"v_sub_f32 v130, v130, v157\n"
"v_mul_f32_dpp v213, v254, v213 quad_perm:[1,1,1,1] row_mask:0xf bank_mask:0xf\n"
"v_mfma_f32_16x16x32_bf16 v[204:207], a[40:43], a[168:171], v[204:207]\n"
"v_exp_f32 v129, v129\n"
"v_mfma_f32_16x16x32_bf16 v[204:207], a[44:47], a[172:175], v[204:207]\n"
"v_mul_f32_dpp v214, v254, v214 quad_perm:[2,2,2,2] row_mask:0xf bank_mask:0xf\n"
"v_mul_f32_dpp v215, v254, v215 quad_perm:[3,3,3,3] row_mask:0xf bank_mask:0xf\n"
"v_mfma_f32_16x16x32_bf16 v[208:211], a[44:47], a[188:191], v[208:211]\n"
"v_sub_f32 v131, v131, v157\n"
"v_mul_f32_dpp v216, v254, v216 quad_perm:[0,0,0,0] row_mask:0xf bank_mask:0xf\n"
"v_mfma_f32_16x16x32_bf16 v[208:211], a[40:43], a[184:187], v[208:211]\n"
"v_exp_f32 v130, v130\n"
"v_mfma_f32_16x16x32_bf16 v[208:211], a[36:39], a[180:183], v[208:211]\n"
"v_mul_f32_dpp v217, v254, v217 quad_perm:[1,1,1,1] row_mask:0xf bank_mask:0xf\n"
"v_mul_f32_dpp v218, v254, v218 quad_perm:[2,2,2,2] row_mask:0xf bank_mask:0xf\n"
"v_mfma_f32_16x16x32_bf16 v[208:211], a[32:35], a[176:179], v[208:211]\n"
"v_exp_f32 v131, v131\n"
"v_mfma_f32_16x16x32_bf16 v[212:215], a[32:35], a[192:195], v[212:215]\n"
"v_mul_f32_dpp v219, v254, v219 quad_perm:[3,3,3,3] row_mask:0xf bank_mask:0xf\n"
"v_sub_f32 v132, v132, v157\n"
"v_mfma_f32_16x16x32_bf16 v[212:215], a[36:39], a[196:199], v[212:215]\n"
"v_sub_f32 v133, v133, v157\n"
"v_cvt_pk_bf16_f32 v126, v128, v129\n"
"v_mfma_f32_16x16x32_bf16 v[212:215], a[40:43], a[200:203], v[212:215]\n"
"v_exp_f32 v132, v132\n"
"v_mfma_f32_16x16x32_bf16 v[212:215], a[44:47], a[204:207], v[212:215]\n"
"v_cvt_pk_bf16_f32 v127, v130, v131\n"
"v_sub_f32 v134, v134, v157\n"
"v_mfma_f32_16x16x32_bf16 v[216:219], a[44:47], a[220:223], v[216:219]\n"
"s_waitcnt vmcnt(16)\n"
"ds_read_b128 a[44:47], v154 offset:8896\n"
"v_sub_f32 v135, v135, v157\n"
"v_mfma_f32_16x16x32_bf16 v[216:219], a[40:43], a[216:219], v[216:219]\n"
"ds_read_b128 a[40:43], v154 offset:8832\n"
"v_sub_f32 v136, v136, v157\n"
"v_mfma_f32_16x16x32_bf16 v[216:219], a[36:39], a[212:215], v[216:219]\n"
"ds_read_b128 a[36:39], v154 offset:8768\n"
"v_sub_f32 v137, v137, v157\n"
"v_mfma_f32_16x16x32_bf16 v[216:219], a[32:35], a[208:211], v[216:219]\n"
"ds_read_b128 a[32:35], v154 offset:8704\n"
"v_sub_f32 v138, v138, v157\n"
"v_mfma_f32_16x16x32_bf16 v[252:255], a[252:255], v[124:127], v[148:151]\n"
"v_exp_f32 v133, v133\n"
"v_mfma_f32_16x16x32_bf16 v[164:167], v[124:127], v[48:51], v[164:167]\n"
"v_exp_f32 v134, v134\n"
"v_mfma_f32_16x16x32_bf16 v[172:175], v[124:127], v[72:75], v[172:175]\n"
"v_exp_f32 v135, v135\n"
"v_mfma_f32_16x16x32_bf16 v[148:151], a[228:231], v[188:191], 0\n"
"ds_read_b128 a[228:231], v158 offset:0\n"
"v_cvt_pk_bf16_f32 v132, v132, v133\n"
"v_mfma_f32_16x16x32_bf16 v[148:151], a[232:235], v[192:195], v[148:151]\n"
"ds_read_b128 a[232:235], v158 offset:2048\n"
"v_cvt_pk_bf16_f32 v133, v134, v135\n"
"v_mfma_f32_16x16x32_bf16 v[128:131], v[12:15], v[192:195], 0\n"
"v_exp_f32 v136, v136\n"
"v_mfma_f32_16x16x32_bf16 v[128:131], v[8:11], v[188:191], v[128:131]\n"
"v_exp_f32 v137, v137\n"
"v_mfma_f32_16x16x32_bf16 v[184:187], v[244:247], a[0:3], 0\n"
"v_sub_f32 v139, v139, v157\n"
"v_exp_f32 v138, v138\n"
"v_exp_f32 v139, v139\n"
"v_sub_f32 v140, v140, v157\n"
"v_sub_f32 v141, v141, v157\n"
"v_exp_f32 v140, v140\n"
"v_cvt_pk_bf16_f32 v134, v136, v137\n"
"v_cvt_pk_bf16_f32 v135, v138, v139\n"
"v_mfma_f32_16x16x32_bf16 v[136:139], v[28:31], v[192:195], 0\n"
"v_cvt_pk_bf16_f32 v204, v204, v208\n"
"v_sub_f32 v142, v142, v157\n"
"v_mfma_f32_16x16x32_bf16 v[136:139], v[24:27], v[188:191], v[136:139]\n"
"v_exp_f32 v141, v141\n"
"v_mfma_f32_16x16x32_bf16 v[252:255], a[252:255], v[132:135], v[252:255]\n"
"v_cvt_pk_bf16_f32 v208, v205, v209\n"
"v_cvt_pk_bf16_f32 v205, v212, v216\n"
"v_mfma_f32_16x16x32_bf16 v[164:167], v[132:135], v[52:55], v[164:167]\n"
"v_cvt_pk_bf16_f32 v209, v213, v217\n"
"v_sub_f32 v143, v143, v157\n"
"v_mfma_f32_16x16x32_bf16 v[172:175], v[132:135], v[76:79], v[172:175]\n"
"v_pk_max_i16 v204, v204, 0\n"
"v_exp_f32 v142, v142\n"
"s_add_u32 m0, s28, 8704\n"
"v_pk_max_i16 v205, v205, 0\n"
"v_pk_max_i16 v208, v208, 0\n"
"v_exp_f32 v143, v143\n"
"v_pk_max_i16 v209, v209, 0\n"
"v_sub_f32 v144, v144, v157\n"
"v_cvt_pk_bf16_f32 v206, v206, v210\n"
"v_cvt_pk_bf16_f32 v210, v207, v211\n"
"v_cvt_pk_bf16_f32 v207, v214, v218\n"
"v_sub_f32 v145, v145, v157\n"
"v_cvt_pk_bf16_f32 v211, v215, v219\n"
"v_exp_f32 v144, v144\n"
"v_pk_max_i16 v206, v206, 0\n"
"v_cvt_pk_bf16_f32 v140, v140, v141\n"
"v_pk_max_i16 v207, v207, 0\n"
"v_cvt_pk_bf16_f32 v141, v142, v143\n"
"v_sub_f32 v146, v146, v157\n"
"v_exp_f32 v145, v145\n"
"v_pk_max_i16 v210, v210, 0\n"
"v_pk_max_i16 v211, v211, 0\n"
"v_sub_f32 v147, v147, v157\n"
"v_exp_f32 v146, v146\n"
"v_exp_f32 v147, v147\n"
"v_cvt_pk_bf16_f32 v142, v144, v145\n"
"v_max3_f32 v157, v120, v121, v122\n"
"v_cvt_pk_bf16_f32 v143, v146, v147\n"
"v_mfma_f32_16x16x32_bf16 v[144:147], v[44:47], v[192:195], 0\n"
"s_waitcnt lgkmcnt(0)\n"
"buffer_load_dwordx4 v152, s[24:27], s60 offen offset:0 lds\n"
"v_lshlrev_b32 v222, 16, v223\n"
"v_mfma_f32_16x16x32_bf16 v[144:147], v[40:43], v[188:191], v[144:147]\n"
"buffer_load_dwordx4 v152, s[24:27], s61 offen offset:1088 lds\n"
"v_and_b32 v223, 0xffff0000, v223\n"
"v_mfma_f32_16x16x32_bf16 v[252:255], a[252:255], v[140:143], v[252:255]\n"
"buffer_load_dwordx4 v152, s[24:27], s62 offen offset:2176 lds\n"
"v_max3_f32 v159, v123, v96, v97\n"
"v_mfma_f32_16x16x32_bf16 v[164:167], v[140:143], v[56:59], v[164:167]\n"
"buffer_load_dwordx4 v152, s[24:27], s63 offen offset:3264 lds\n"
"v_max3_f32 v157, v157, v98, v99\n"
"v_mfma_f32_16x16x32_bf16 v[168:171], v[140:143], v[68:71], v[168:171]\n"
"v_max3_f32 v159, v159, v100, v101\n"
"v_max3_f32 v157, v157, v102, v103\n"
"v_mfma_f32_16x16x32_bf16 v[168:171], v[132:135], v[64:67], v[168:171]\n"
"v_max3_f32 v159, v159, v104, v105\n"
"v_max3_f32 v157, v157, v106, v107\n"
"v_mfma_f32_16x16x32_bf16 v[168:171], v[124:127], v[60:63], v[168:171]\n"
"buffer_store_dwordx2 v[204:205], v162, s[84:87], s66 offen offset:0 sc0 sc1\n"
"v_mfma_f32_16x16x32_bf16 v[172:175], v[140:143], v[80:83], v[172:175]\n"
"buffer_store_dwordx2 v[208:209], v162, s[84:87], s66 offen offset:256 sc0 sc1\n"
"v_mfma_f32_16x16x32_bf16 v[176:179], v[140:143], v[92:95], v[176:179]\n"
"v_max3_f32 v159, v159, v108, v109\n"
"v_max3_f32 v157, v157, v110, v111\n"
"v_mfma_f32_16x16x32_bf16 v[176:179], v[132:135], v[88:91], v[176:179]\n"
"v_max3_f32 v159, v159, v112, v113\n"
"v_max3_f32 v157, v157, v114, v115\n"
"v_mfma_f32_16x16x32_bf16 v[176:179], v[124:127], v[84:87], v[176:179]\n"
"buffer_store_dwordx2 v[206:207], v162, s[84:87], s66 offen offset:512 sc0 sc1\n"
"v_mfma_f32_16x16x32_bf16 v[124:127], v[0:3], v[188:191], 0\n"
"v_max3_f32 v159, v159, v116, v117\n"
"v_max3_f32 v157, v157, v118, v119\n"
"v_mfma_f32_16x16x32_bf16 v[124:127], v[4:7], v[192:195], v[124:127]\n"
"buffer_store_dwordx2 v[210:211], v162, s[84:87], s66 offen offset:768 sc0 sc1\n"
"v_mfma_f32_16x16x32_bf16 v[132:135], v[16:19], v[188:191], 0\n"
"v_max_f32 v157, v157, v159\n"
"v_mov_b32 v161, v157\n"
"v_mfma_f32_16x16x32_bf16 v[132:135], v[20:23], v[192:195], v[132:135]\n"
"v_add_f32 v252, v252, v253\n"
"v_add_u32 v158, 64, v158\n"
"v_mfma_f32_16x16x32_bf16 v[140:143], v[32:35], v[188:191], 0\n"
"v_permlane32_swap_b32 v157, v161\n"
"v_mfma_f32_16x16x32_bf16 v[140:143], v[36:39], v[192:195], v[140:143]\n"
"v_max_f32 v157, v157, v161\n"
"v_mov_b32 v161, v157\n"
"v_mfma_f32_16x16x32_bf16 v[188:191], v[240:243], a[28:31], 0\n"
"v_rcp_f32 v253, v252\n"
"v_mfma_f32_16x16x32_bf16 v[188:191], v[236:239], a[24:27], v[188:191]\n"
"ds_bpermute_b32 v254, v160, v253 offset:0\n"
"v_mfma_f32_16x16x32_bf16 v[188:191], a[224:227], a[20:23], v[188:191]\n"
"v_permlane16_swap_b32 v157, v161\n"
"v_mfma_f32_16x16x32_bf16 v[188:191], v[248:251], a[16:19], v[188:191]\n"
"v_max_f32 v157, v157, v161\n"
"v_sub_f32 v120, v120, v157\n"
"v_sub_f32 v121, v121, v157\n"
"v_sub_f32 v122, v122, v157\n"
"v_sub_f32 v123, v123, v157\n"
"v_exp_f32 v161, v122\n"
"v_exp_f32 v120, v120\n"
"v_exp_f32 v121, v121\n"
"v_exp_f32 v123, v123\n"
"v_cvt_pk_bf16_f32 v122, v120, v121\n"
"v_add_f32 v120, v120, v121\n"
"v_add_f32 v121, v161, v123\n"
"v_cvt_pk_bf16_f32 v123, v161, v123\n"
"v_sub_f32 v96, v96, v157\n"
"v_sub_f32 v97, v97, v157\n"
"v_exp_f32 v96, v96\n"
"v_mfma_f32_16x16x4_4b_bf16 v[204:219], v[122:123], a[244:245], 0 cbsz:2 abid:0\n"
"v_sub_f32 v98, v98, v157\n"
"v_exp_f32 v97, v97\n"
"v_sub_f32 v99, v99, v157\n"
"v_exp_f32 v98, v98\n"
"v_exp_f32 v99, v99\n"
"v_sub_f32 v100, v100, v157\n"
"v_sub_f32 v101, v101, v157\n"
"v_exp_f32 v100, v100\n"
"v_cvt_pk_bf16_f32 v96, v96, v97\n"
"s_waitcnt lgkmcnt(0)\n"
"v_fmac_f32_dpp v220, v254, v164 quad_perm:[0,0,0,0] row_mask:0xf bank_mask:0xf\n"
"v_mfma_f32_16x16x32_bf16 v[164:167], a[144:147], a[0:3], 0\n"
"v_fmac_f32_dpp v221, v254, v168 quad_perm:[0,0,0,0] row_mask:0xf bank_mask:0xf\n"
"v_fmac_f32_dpp v222, v254, v172 quad_perm:[0,0,0,0] row_mask:0xf bank_mask:0xf\n"
"v_mfma_f32_16x16x32_bf16 v[164:167], a[148:151], a[4:7], v[164:167]\n"
"v_fmac_f32_dpp v223, v254, v176 quad_perm:[0,0,0,0] row_mask:0xf bank_mask:0xf\n"
"v_cvt_pk_bf16_f32 v220, v220, v221\n"
"v_mfma_f32_16x16x32_bf16 v[164:167], a[152:155], a[8:11], v[164:167]\n"
"v_cvt_pk_bf16_f32 v221, v222, v223\n"
"v_cvt_pk_bf16_f32 v97, v98, v99\n"
"v_mfma_f32_16x16x32_bf16 v[164:167], a[156:159], a[12:15], v[164:167]\n"
"v_pk_max_i16 v220, v220, 0\n"
"v_sub_f32 v102, v102, v157\n"
"v_mfma_f32_16x16x32_bf16 v[168:171], a[156:159], a[28:31], 0\n"
"v_pk_max_i16 v221, v221, 0\n"
"v_sub_f32 v103, v103, v157\n"
"v_mfma_f32_16x16x32_bf16 v[168:171], a[152:155], a[24:27], v[168:171]\n"
"v_exp_f32 v101, v101\n"
"v_mfma_f32_16x16x32_bf16 v[168:171], a[148:151], a[20:23], v[168:171]\n"
"v_exp_f32 v102, v102\n"
"v_mfma_f32_16x16x32_bf16 v[168:171], a[144:147], a[16:19], v[168:171]\n"
"buffer_store_dwordx2 v[220:221], v155, s[84:87], s70 offen offset:0 sc0 sc1\n"
"v_mfma_f32_16x16x32_bf16 v[172:175], a[144:147], a[32:35], 0\n"
"v_exp_f32 v103, v103\n"
"v_mfma_f32_16x16x32_bf16 v[172:175], a[148:151], a[36:39], v[172:175]\n"
"v_sub_f32 v104, v104, v157\n"
"v_sub_f32 v105, v105, v157\n"
"v_mfma_f32_16x16x32_bf16 v[172:175], a[152:155], a[40:43], v[172:175]\n"
"v_exp_f32 v104, v104\n"
"v_mfma_f32_16x16x32_bf16 v[172:175], a[156:159], a[44:47], v[172:175]\n"
"v_cvt_pk_bf16_f32 v98, v100, v101\n"
"v_cvt_pk_bf16_f32 v99, v102, v103\n"
"v_mfma_f32_16x16x32_bf16 v[100:103], v[12:15], v[200:203], 0\n"
"v_sub_f32 v106, v106, v157\n"
"v_sub_f32 v107, v107, v157\n"
"v_mfma_f32_16x16x32_bf16 v[100:103], v[8:11], v[196:199], v[100:103]\n"
"v_exp_f32 v105, v105\n"
"v_mfma_f32_16x16x32_bf16 v[252:255], a[252:255], v[96:99], v[120:123]\n"
"v_exp_f32 v106, v106\n"
"v_mfma_f32_16x16x32_bf16 v[204:207], v[96:99], v[48:51], v[204:207]\n"
"v_exp_f32 v107, v107\n"
"v_mfma_f32_16x16x32_bf16 v[212:215], v[96:99], v[72:75], v[212:215]\n"
"v_sub_f32 v108, v108, v157\n"
"v_sub_f32 v109, v109, v157\n"
"v_mfma_f32_16x16x32_bf16 v[120:123], a[228:231], v[196:199], 0\n"
"ds_read_b128 a[228:231], v158 offset:0\n"
"v_cvt_pk_bf16_f32 v104, v104, v105\n"
"v_mfma_f32_16x16x32_bf16 v[120:123], a[232:235], v[200:203], v[120:123]\n"
"ds_read_b128 a[232:235], v158 offset:2048\n"
"v_cvt_pk_bf16_f32 v105, v106, v107\n"
"v_mfma_f32_16x16x32_bf16 v[8:11], a[108:111], a[28:31], 0\n"
"v_exp_f32 v108, v108\n"
"v_mfma_f32_16x16x32_bf16 v[8:11], a[104:107], a[24:27], v[8:11]\n"
"v_sub_f32 v110, v110, v157\n"
"v_sub_f32 v111, v111, v157\n"
"v_mfma_f32_16x16x32_bf16 v[8:11], a[100:103], a[20:23], v[8:11]\n"
"v_exp_f32 v109, v109\n"
"v_mfma_f32_16x16x32_bf16 v[8:11], a[96:99], a[16:19], v[8:11]\n"
"v_exp_f32 v110, v110\n"
"v_mfma_f32_16x16x32_bf16 v[12:15], a[124:127], a[28:31], 0\n"
"v_exp_f32 v111, v111\n"
"v_mfma_f32_16x16x32_bf16 v[12:15], a[120:123], a[24:27], v[12:15]\n"
"v_sub_f32 v112, v112, v157\n"
"v_sub_f32 v113, v113, v157\n"
"v_mfma_f32_16x16x32_bf16 v[12:15], a[116:119], a[20:23], v[12:15]\n"
"v_exp_f32 v112, v112\n"
"v_mfma_f32_16x16x32_bf16 v[12:15], a[112:115], a[16:19], v[12:15]\n"
"v_cvt_pk_bf16_f32 v106, v108, v109\n"
"v_cvt_pk_bf16_f32 v107, v110, v111\n"
"v_mfma_f32_16x16x32_bf16 v[108:111], v[28:31], v[200:203], 0\n"
"v_sub_f32 v114, v114, v157\n"
"v_sub_f32 v115, v115, v157\n"
"v_mfma_f32_16x16x32_bf16 v[108:111], v[24:27], v[196:199], v[108:111]\n"
"v_exp_f32 v113, v113\n"
"v_mfma_f32_16x16x32_bf16 v[252:255], a[252:255], v[104:107], v[252:255]\n"
"v_exp_f32 v114, v114\n"
"v_mfma_f32_16x16x32_bf16 v[204:207], v[104:107], v[52:55], v[204:207]\n"
"v_exp_f32 v115, v115\n"
"v_mfma_f32_16x16x32_bf16 v[212:215], v[104:107], v[76:79], v[212:215]\n"
"v_sub_f32 v116, v116, v157\n"
"v_sub_f32 v117, v117, v157\n"
"v_exp_f32 v116, v116\n"
"v_cvt_pk_bf16_f32 v112, v112, v113\n"
"v_cvt_pk_bf16_f32 v113, v114, v115\n"
"v_sub_f32 v118, v118, v157\n"
"v_exp_f32 v117, v117\n"
"v_sub_f32 v119, v119, v157\n"
"v_exp_f32 v118, v118\n"
"v_exp_f32 v119, v119\n"
"v_cvt_pk_bf16_f32 v114, v116, v117\n"
"v_max3_f32 v157, v148, v149, v150\n"
"v_cvt_pk_bf16_f32 v115, v118, v119\n"
"v_mfma_f32_16x16x32_bf16 v[116:119], v[44:47], v[200:203], 0\n"
"v_max3_f32 v159, v151, v124, v125\n"
"v_max3_f32 v157, v157, v126, v127\n"
"v_mfma_f32_16x16x32_bf16 v[116:119], v[40:43], v[196:199], v[116:119]\n"
"v_max3_f32 v159, v159, v128, v129\n"
"v_max3_f32 v157, v157, v130, v131\n"
"v_mfma_f32_16x16x32_bf16 v[252:255], a[252:255], v[112:115], v[252:255]\n"
"v_max3_f32 v159, v159, v132, v133\n"
"v_max3_f32 v157, v157, v134, v135\n"
"v_mfma_f32_16x16x32_bf16 v[204:207], v[112:115], v[56:59], v[204:207]\n"
"v_max3_f32 v159, v159, v136, v137\n"
"v_max3_f32 v157, v157, v138, v139\n"
"v_mfma_f32_16x16x32_bf16 v[208:211], v[112:115], v[68:71], v[208:211]\n"
"v_max3_f32 v159, v159, v140, v141\n"
"v_max3_f32 v157, v157, v142, v143\n"
"v_mfma_f32_16x16x32_bf16 v[208:211], v[104:107], v[64:67], v[208:211]\n"
"v_max3_f32 v159, v159, v144, v145\n"
"v_max3_f32 v157, v157, v146, v147\n"
"v_mfma_f32_16x16x32_bf16 v[208:211], v[96:99], v[60:63], v[208:211]\n"
"v_max_f32 v157, v157, v159\n"
"v_mov_b32 v161, v157\n"
"v_mfma_f32_16x16x32_bf16 v[212:215], v[112:115], v[80:83], v[212:215]\n"
"v_add_f32 v252, v252, v253\n"
"v_cvt_pk_bf16_f32 v8, v8, v9\n"
"v_mfma_f32_16x16x32_bf16 v[216:219], v[112:115], v[92:95], v[216:219]\n"
"v_permlane32_swap_b32 v157, v161\n"
"v_mfma_f32_16x16x32_bf16 v[216:219], v[104:107], v[88:91], v[216:219]\n"
"v_max_f32 v157, v157, v161\n"
"v_mov_b32 v161, v157\n"
"v_mfma_f32_16x16x32_bf16 v[216:219], v[96:99], v[84:87], v[216:219]\n"
"v_rcp_f32 v253, v252\n"
"v_mfma_f32_16x16x32_bf16 v[96:99], v[0:3], v[196:199], 0\n"
"ds_bpermute_b32 v254, v160, v253 offset:0\n"
"v_mfma_f32_16x16x32_bf16 v[96:99], v[4:7], v[200:203], v[96:99]\n"
"v_permlane16_swap_b32 v157, v161\n"
"v_mfma_f32_16x16x32_bf16 v[104:107], v[16:19], v[196:199], 0\n"
"v_max_f32 v157, v157, v161\n"
"v_sub_f32 v148, v148, v157\n"
"v_mfma_f32_16x16x32_bf16 v[104:107], v[20:23], v[200:203], v[104:107]\n"
"v_sub_f32 v149, v149, v157\n"
"v_sub_f32 v150, v150, v157\n"
"v_mfma_f32_16x16x32_bf16 v[112:115], v[32:35], v[196:199], 0\n"
"v_sub_f32 v151, v151, v157\n"
"v_sub_f32 v124, v124, v157\n"
"v_mfma_f32_16x16x32_bf16 v[112:115], v[36:39], v[200:203], v[112:115]\n"
"v_exp_f32 v161, v150\n"
"v_mfma_f32_16x16x32_bf16 v[0:3], a[96:99], a[0:3], 0\n"
"v_exp_f32 v148, v148\n"
"v_mfma_f32_16x16x32_bf16 v[0:3], a[100:103], a[4:7], v[0:3]\n"
"v_exp_f32 v149, v149\n"
"v_mfma_f32_16x16x32_bf16 v[0:3], a[104:107], a[8:11], v[0:3]\n"
"v_exp_f32 v151, v151\n"
"v_mfma_f32_16x16x32_bf16 v[0:3], a[108:111], a[12:15], v[0:3]\n"
"v_cvt_pk_bf16_f32 v150, v148, v149\n"
"v_add_f32 v148, v148, v149\n"
"v_mfma_f32_16x16x32_bf16 v[16:19], a[96:99], a[32:35], 0\n"
"v_add_f32 v149, v161, v151\n"
"v_cvt_pk_bf16_f32 v151, v161, v151\n"
"v_mfma_f32_16x16x32_bf16 v[16:19], a[100:103], a[36:39], v[16:19]\n"
"v_sub_f32 v125, v125, v157\n"
"v_sub_f32 v126, v126, v157\n"
"v_mfma_f32_16x16x32_bf16 v[16:19], a[104:107], a[40:43], v[16:19]\n"
"v_exp_f32 v124, v124\n"
"v_mfma_f32_16x16x32_bf16 v[16:19], a[108:111], a[44:47], v[16:19]\n"
"v_exp_f32 v125, v125\n"
"v_mfma_f32_16x16x32_bf16 v[4:7], a[112:115], a[0:3], 0\n"
"v_sub_f32 v127, v127, v157\n"
"v_sub_f32 v128, v128, v157\n"
"v_mfma_f32_16x16x32_bf16 v[4:7], a[116:119], a[4:7], v[4:7]\n"
"v_exp_f32 v126, v126\n"
"v_mfma_f32_16x16x32_bf16 v[4:7], a[120:123], a[8:11], v[4:7]\n"
"v_exp_f32 v127, v127\n"
"v_mfma_f32_16x16x32_bf16 v[4:7], a[124:127], a[12:15], v[4:7]\n"
"v_sub_f32 v129, v129, v157\n"
"v_cvt_pk_bf16_f32 v124, v124, v125\n"
"v_mfma_f32_16x16x32_bf16 v[20:23], a[112:115], a[32:35], 0\n"
"v_exp_f32 v128, v128\n"
"v_mfma_f32_16x16x32_bf16 v[20:23], a[116:119], a[36:39], v[20:23]\n"
"v_cvt_pk_bf16_f32 v125, v126, v127\n"
"v_sub_f32 v130, v130, v157\n"
"v_mfma_f32_16x16x32_bf16 v[20:23], a[120:123], a[40:43], v[20:23]\n"
"v_exp_f32 v129, v129\n"
"v_mfma_f32_16x16x32_bf16 v[20:23], a[124:127], a[44:47], v[20:23]\n"
"v_sub_f32 v131, v131, v157\n"
"v_exp_f32 v130, v130\n"
"v_mfma_f32_16x16x4_4b_bf16 v[220:235], v[150:151], a[244:245], 0 cbsz:2 abid:0\n"
"v_exp_f32 v131, v131\n"
"v_sub_f32 v132, v132, v157\n"
"v_sub_f32 v133, v133, v157\n"
"v_exp_f32 v132, v132\n"
"v_cvt_pk_bf16_f32 v126, v128, v129\n"
"v_cvt_pk_bf16_f32 v127, v130, v131\n"
"v_mfma_f32_16x16x32_bf16 v[196:199], v[248:251], a[32:35], 0\n"
"ds_read_b128 v[248:251], v153 offset:5120\n"
"v_sub_f32 v134, v134, v157\n"
"v_mfma_f32_16x16x32_bf16 v[196:199], a[224:227], a[36:39], v[196:199]\n"
"ds_read_b128 a[224:227], v153 offset:9216\n"
"v_sub_f32 v135, v135, v157\n"
"v_mfma_f32_16x16x32_bf16 v[196:199], v[236:239], a[40:43], v[196:199]\n"
"ds_read_b128 v[236:239], v153 offset:13312\n"
"v_sub_f32 v136, v136, v157\n"
"v_mfma_f32_16x16x32_bf16 v[196:199], v[240:243], a[44:47], v[196:199]\n"
"ds_read_b128 v[240:243], v153 offset:2048\n"
"v_sub_f32 v137, v137, v157\n"
"v_mfma_f32_16x16x32_bf16 v[220:223], v[124:127], v[48:51], v[220:223]\n"
"v_exp_f32 v133, v133\n"
"v_mfma_f32_16x16x32_bf16 v[228:231], v[124:127], v[72:75], v[228:231]\n"
"v_exp_f32 v134, v134\n"
"v_mfma_f32_16x16x32_bf16 v[200:203], v[244:247], a[32:35], 0\n"
"v_exp_f32 v135, v135\n"
"v_exp_f32 v136, v136\n"
"v_cvt_pk_bf16_f32 v132, v132, v133\n"
"v_cvt_pk_bf16_f32 v133, v134, v135\n"
"v_sub_f32 v138, v138, v157\n"
"v_exp_f32 v137, v137\n"
"v_sub_f32 v139, v139, v157\n"
"v_exp_f32 v138, v138\n"
"s_waitcnt lgkmcnt(4)\n"
"v_mul_f32_dpp v204, v254, v204 quad_perm:[0,0,0,0] row_mask:0xf bank_mask:0xf\n"
"v_exp_f32 v139, v139\n"
"v_mul_f32_dpp v205, v254, v205 quad_perm:[1,1,1,1] row_mask:0xf bank_mask:0xf\n"
"v_sub_f32 v140, v140, v157\n"
"v_mul_f32_dpp v206, v254, v206 quad_perm:[2,2,2,2] row_mask:0xf bank_mask:0xf\n"
"s_waitcnt lgkmcnt(3)\n"
"v_mfma_f32_16x16x32_bf16 v[184:187], v[248:251], a[4:7], v[184:187]\n"
"s_waitcnt lgkmcnt(2)\n"
"v_mul_f32_dpp v207, v254, v207 quad_perm:[3,3,3,3] row_mask:0xf bank_mask:0xf\n"
"v_mul_f32_dpp v208, v254, v208 quad_perm:[0,0,0,0] row_mask:0xf bank_mask:0xf\n"
"v_mfma_f32_16x16x32_bf16 v[184:187], a[224:227], a[8:11], v[184:187]\n"
"s_waitcnt lgkmcnt(1)\n"
"v_sub_f32 v141, v141, v157\n"
"v_mul_f32_dpp v209, v254, v209 quad_perm:[1,1,1,1] row_mask:0xf bank_mask:0xf\n"
"v_mfma_f32_16x16x32_bf16 v[184:187], v[236:239], a[12:15], v[184:187]\n"
"v_exp_f32 v140, v140\n"
"v_mfma_f32_16x16x32_bf16 v[204:207], a[48:51], a[160:163], v[204:207]\n"
"v_cvt_pk_bf16_f32 v134, v136, v137\n"
"v_mul_f32_dpp v210, v254, v210 quad_perm:[2,2,2,2] row_mask:0xf bank_mask:0xf\n"
"v_mfma_f32_16x16x32_bf16 v[204:207], a[52:55], a[164:167], v[204:207]\n"
"v_cvt_pk_bf16_f32 v135, v138, v139\n"
"v_sub_f32 v142, v142, v157\n"
"v_mfma_f32_16x16x32_bf16 v[204:207], a[56:59], a[168:171], v[204:207]\n"
"v_mul_f32_dpp v211, v254, v211 quad_perm:[3,3,3,3] row_mask:0xf bank_mask:0xf\n"
"v_mul_f32_dpp v212, v254, v212 quad_perm:[0,0,0,0] row_mask:0xf bank_mask:0xf\n"
"v_mfma_f32_16x16x32_bf16 v[204:207], a[60:63], a[172:175], v[204:207]\n"
"v_exp_f32 v141, v141\n"
"v_mfma_f32_16x16x32_bf16 v[208:211], a[60:63], a[188:191], v[208:211]\n"
"v_mul_f32_dpp v213, v254, v213 quad_perm:[1,1,1,1] row_mask:0xf bank_mask:0xf\n"
"v_sub_f32 v143, v143, v157\n"
"v_mfma_f32_16x16x32_bf16 v[208:211], a[56:59], a[184:187], v[208:211]\n"
"v_mul_f32_dpp v214, v254, v214 quad_perm:[2,2,2,2] row_mask:0xf bank_mask:0xf\n"
"v_mul_f32_dpp v215, v254, v215 quad_perm:[3,3,3,3] row_mask:0xf bank_mask:0xf\n"
"v_mfma_f32_16x16x32_bf16 v[208:211], a[52:55], a[180:183], v[208:211]\n"
"v_exp_f32 v142, v142\n"
"v_mfma_f32_16x16x32_bf16 v[208:211], a[48:51], a[176:179], v[208:211]\n"
"v_mul_f32_dpp v216, v254, v216 quad_perm:[0,0,0,0] row_mask:0xf bank_mask:0xf\n"
"v_mul_f32_dpp v217, v254, v217 quad_perm:[1,1,1,1] row_mask:0xf bank_mask:0xf\n"
"v_mfma_f32_16x16x32_bf16 v[212:215], a[48:51], a[192:195], v[212:215]\n"
"v_exp_f32 v143, v143\n"
"v_mfma_f32_16x16x32_bf16 v[212:215], a[52:55], a[196:199], v[212:215]\n"
"v_mul_f32_dpp v218, v254, v218 quad_perm:[2,2,2,2] row_mask:0xf bank_mask:0xf\n"
"v_sub_f32 v144, v144, v157\n"
"v_mfma_f32_16x16x32_bf16 v[212:215], a[56:59], a[200:203], v[212:215]\n"
"v_mul_f32_dpp v219, v254, v219 quad_perm:[3,3,3,3] row_mask:0xf bank_mask:0xf\n"
"v_sub_f32 v145, v145, v157\n"
"v_mfma_f32_16x16x32_bf16 v[212:215], a[60:63], a[204:207], v[212:215]\n"
"v_exp_f32 v144, v144\n"
"v_mfma_f32_16x16x32_bf16 v[216:219], a[60:63], a[220:223], v[216:219]\n"
"s_waitcnt vmcnt(17)\n"
"ds_read_b128 a[60:63], v154 offset:192\n"
"v_cvt_pk_bf16_f32 v140, v140, v141\n"
"v_mfma_f32_16x16x32_bf16 v[216:219], a[56:59], a[216:219], v[216:219]\n"
"ds_read_b128 a[56:59], v154 offset:128\n"
"v_cvt_pk_bf16_f32 v141, v142, v143\n"
"v_mfma_f32_16x16x32_bf16 v[216:219], a[52:55], a[212:215], v[216:219]\n"
"ds_read_b128 a[52:55], v154 offset:64\n"
"v_sub_f32 v146, v146, v157\n"
"v_mfma_f32_16x16x32_bf16 v[216:219], a[48:51], a[208:211], v[216:219]\n"
"ds_read_b128 a[48:51], v154 offset:0\n"
"v_sub_f32 v147, v147, v157\n"
"v_mfma_f32_16x16x32_bf16 v[252:255], a[252:255], v[124:127], v[148:151]\n"
"v_exp_f32 v145, v145\n"
"v_mfma_f32_16x16x32_bf16 v[252:255], a[252:255], v[132:135], v[252:255]\n"
"v_exp_f32 v146, v146\n"
"v_mfma_f32_16x16x32_bf16 v[220:223], v[132:135], v[52:55], v[220:223]\n"
"v_exp_f32 v147, v147\n"
"v_mfma_f32_16x16x32_bf16 v[228:231], v[132:135], v[76:79], v[228:231]\n"
"v_cvt_pk_bf16_f32 v142, v144, v145\n"
"v_cvt_pk_bf16_f32 v143, v146, v147\n"
"v_mfma_f32_16x16x32_bf16 v[192:195], v[236:239], a[28:31], 0\n"
"v_cvt_pk_bf16_f32 v0, v0, v1\n"
"v_cvt_pk_bf16_f32 v1, v2, v3\n"
"v_mfma_f32_16x16x32_bf16 v[192:195], a[224:227], a[24:27], v[192:195]\n"
"v_cvt_pk_bf16_f32 v9, v10, v11\n"
"v_cvt_pk_bf16_f32 v16, v16, v17\n"
"v_mfma_f32_16x16x32_bf16 v[192:195], v[248:251], a[20:23], v[192:195]\n"
"v_cvt_pk_bf16_f32 v17, v18, v19\n"
"v_cvt_pk_bf16_f32 v204, v204, v208\n"
"v_mfma_f32_16x16x32_bf16 v[192:195], v[244:247], a[16:19], v[192:195]\n"
"ds_read_b128 v[244:247], v153 offset:6144\n"
"v_cvt_pk_bf16_f32 v208, v205, v209\n"
"v_mfma_f32_16x16x32_bf16 v[252:255], a[252:255], v[140:143], v[252:255]\n"
"v_cvt_pk_bf16_f32 v205, v212, v216\n"
"v_cvt_pk_bf16_f32 v209, v213, v217\n"
"v_mfma_f32_16x16x32_bf16 v[220:223], v[140:143], v[56:59], v[220:223]\n"
"v_pk_max_i16 v204, v204, 0\n"
"v_pk_max_i16 v205, v205, 0\n"
"v_mfma_f32_16x16x32_bf16 v[224:227], v[140:143], v[68:71], v[224:227]\n"
"v_pk_max_i16 v208, v208, 0\n"
"v_pk_max_i16 v209, v209, 0\n"
"v_mfma_f32_16x16x32_bf16 v[224:227], v[132:135], v[64:67], v[224:227]\n"
"v_cvt_pk_bf16_f32 v206, v206, v210\n"
"v_cvt_pk_bf16_f32 v210, v207, v211\n"
"v_mfma_f32_16x16x32_bf16 v[224:227], v[124:127], v[60:63], v[224:227]\n"
"buffer_store_dwordx2 v[204:205], v162, s[84:87], s67 offen offset:0 sc0 sc1\n"
"v_mfma_f32_16x16x32_bf16 v[228:231], v[140:143], v[80:83], v[228:231]\n"
"buffer_store_dwordx2 v[208:209], v162, s[84:87], s67 offen offset:256 sc0 sc1\n"
"v_mfma_f32_16x16x32_bf16 v[232:235], v[140:143], v[92:95], v[232:235]\n"
"v_max3_f32 v157, v120, v121, v122\n"
"v_cvt_pk_bf16_f32 v207, v214, v218\n"
"v_mfma_f32_16x16x32_bf16 v[232:235], v[132:135], v[88:91], v[232:235]\n"
"s_add_u32 m0, s28, 0\n"
"v_max3_f32 v159, v123, v96, v97\n"
"v_mfma_f32_16x16x32_bf16 v[232:235], v[124:127], v[84:87], v[232:235]\n"
"s_waitcnt lgkmcnt(1)\n"
"v_cvt_pk_bf16_f32 v211, v215, v219\n"
"v_max3_f32 v157, v157, v98, v99\n"
"v_mfma_f32_16x16x32_bf16 v[24:27], a[96:99], a[48:51], 0\n"
"v_pk_max_i16 v206, v206, 0\n"
"v_max3_f32 v159, v159, v100, v101\n"
"v_mfma_f32_16x16x32_bf16 v[24:27], a[100:103], a[52:55], v[24:27]\n"
"v_pk_max_i16 v207, v207, 0\n"
"v_max3_f32 v157, v157, v102, v103\n"
"v_mfma_f32_16x16x32_bf16 v[24:27], a[104:107], a[56:59], v[24:27]\n"
"v_pk_max_i16 v210, v210, 0\n"
"v_max3_f32 v159, v159, v104, v105\n"
"v_mfma_f32_16x16x32_bf16 v[24:27], a[108:111], a[60:63], v[24:27]\n"
"buffer_store_dwordx2 v[206:207], v162, s[84:87], s67 offen offset:512 sc0 sc1\n"
"v_mfma_f32_16x16x32_bf16 v[176:179], a[112:115], a[48:51], 0\n"
"v_cvt_pk_bf16_f32 v2, v4, v5\n"
"v_cvt_pk_bf16_f32 v3, v6, v7\n"
"v_mfma_f32_16x16x32_bf16 v[176:179], a[116:119], a[52:55], v[176:179]\n"
"v_cvt_pk_bf16_f32 v10, v12, v13\n"
"v_cvt_pk_bf16_f32 v11, v14, v15\n"
"v_mfma_f32_16x16x32_bf16 v[176:179], a[120:123], a[56:59], v[176:179]\n"
"v_cvt_pk_bf16_f32 v18, v20, v21\n"
"v_cvt_pk_bf16_f32 v19, v22, v23\n"
"v_mfma_f32_16x16x32_bf16 v[176:179], a[124:127], a[60:63], v[176:179]\n"
"v_pk_max_i16 v211, v211, 0\n"
"v_max3_f32 v157, v157, v106, v107\n"
"v_mfma_f32_16x16x32_bf16 v[4:7], a[128:131], a[0:3], 0\n"
"v_max3_f32 v159, v159, v108, v109\n"
"v_max3_f32 v157, v157, v110, v111\n"
"v_mfma_f32_16x16x32_bf16 v[4:7], a[132:135], a[4:7], v[4:7]\n"
"v_max3_f32 v159, v159, v112, v113\n"
"v_max3_f32 v157, v157, v114, v115\n"
"v_mfma_f32_16x16x32_bf16 v[4:7], a[136:139], a[8:11], v[4:7]\n"
"v_max3_f32 v159, v159, v116, v117\n"
"v_max3_f32 v157, v157, v118, v119\n"
"v_mfma_f32_16x16x32_bf16 v[4:7], a[140:143], a[12:15], v[4:7]\n"
"buffer_store_dwordx2 v[210:211], v162, s[84:87], s67 offen offset:768 sc0 sc1\n"
"v_mfma_f32_16x16x32_bf16 v[12:15], a[140:143], a[28:31], 0\n"
"v_max_f32 v157, v157, v159\n"
"v_mov_b32 v161, v157\n"
"v_mfma_f32_16x16x32_bf16 v[12:15], a[136:139], a[24:27], v[12:15]\n"
"v_add_f32 v252, v252, v253\n"
"v_cvt_pk_bf16_f32 v24, v24, v25\n"
"v_mfma_f32_16x16x32_bf16 v[12:15], a[132:135], a[20:23], v[12:15]\n"
"v_permlane32_swap_b32 v157, v161\n"
"v_mfma_f32_16x16x32_bf16 v[12:15], a[128:131], a[16:19], v[12:15]\n"
"v_max_f32 v157, v157, v161\n"
"v_mov_b32 v161, v157\n"
"v_mfma_f32_16x16x32_bf16 v[20:23], a[128:131], a[32:35], 0\n"
"v_cvt_pk_bf16_f32 v4, v4, v5\n"
"v_cvt_pk_bf16_f32 v5, v6, v7\n"
"v_mfma_f32_16x16x32_bf16 v[20:23], a[132:135], a[36:39], v[20:23]\n"
"v_permlane16_swap_b32 v157, v161\n"
"v_mfma_f32_16x16x32_bf16 v[20:23], a[136:139], a[40:43], v[20:23]\n"
"v_max_f32 v157, v157, v161\n"
"v_sub_f32 v120, v120, v157\n"
"v_mfma_f32_16x16x32_bf16 v[20:23], a[140:143], a[44:47], v[20:23]\n"
"v_sub_f32 v121, v121, v157\n"
"v_sub_f32 v122, v122, v157\n"
"v_mfma_f32_16x16x32_bf16 v[28:31], a[128:131], a[48:51], 0\n"
"v_sub_f32 v123, v123, v157\n"
"v_cvt_pk_bf16_f32 v12, v12, v13\n"
"v_mfma_f32_16x16x32_bf16 v[28:31], a[132:135], a[52:55], v[28:31]\n"
"v_exp_f32 v161, v122\n"
"v_mfma_f32_16x16x32_bf16 v[28:31], a[136:139], a[56:59], v[28:31]\n"
"v_exp_f32 v120, v120\n"
"v_mfma_f32_16x16x32_bf16 v[28:31], a[140:143], a[60:63], v[28:31]\n"
"v_exp_f32 v121, v121\n"
"v_mfma_f32_16x16x32_bf16 v[200:203], v[248:251], a[36:39], v[200:203]\n"
"ds_read_b128 v[248:251], v153 offset:10240\n"
"v_cvt_pk_bf16_f32 v13, v14, v15\n"
"v_mfma_f32_16x16x32_bf16 v[200:203], a[224:227], a[40:43], v[200:203]\n"
"ds_read_b128 a[224:227], v153 offset:14336\n"
"v_cvt_pk_bf16_f32 v20, v20, v21\n"
"v_mfma_f32_16x16x32_bf16 v[200:203], v[236:239], a[44:47], v[200:203]\n"
"ds_read_b128 v[236:239], v153 offset:3072\n"
"s_add_u32 s24, s24, 13107200\n"
"s_addc_u32 s25, s25, 0\n"
"s_sub_u32 s26, s26, 13107200\n"
"s_cselect_b32 s26, 0, s26\n"
"v_exp_f32 v123, v123\n"
"v_cvt_pk_bf16_f32 v21, v22, v23\n"
"v_cvt_pk_bf16_f32 v122, v120, v121\n"
"v_add_f32 v120, v120, v121\n"
"v_add_f32 v121, v161, v123\n"
"v_cvt_pk_bf16_f32 v123, v161, v123\n"
"v_sub_f32 v96, v96, v157\n"
"v_sub_f32 v97, v97, v157\n"
"v_exp_f32 v96, v96\n"
"v_mfma_f32_16x16x4_4b_bf16 v[204:219], v[122:123], a[244:245], 0 cbsz:2 abid:0\n"
"buffer_load_dwordx4 v152, s[24:27], s40 offen offset:0 lds\n"
"v_sub_f32 v98, v98, v157\n"
"v_exp_f32 v97, v97\n"
"v_sub_f32 v99, v99, v157\n"
"v_exp_f32 v98, v98\n"
"v_exp_f32 v99, v99\n"
"v_sub_f32 v100, v100, v157\n"
"v_sub_f32 v101, v101, v157\n"
"v_exp_f32 v100, v100\n"
"v_cvt_pk_bf16_f32 v96, v96, v97\n"
"v_cvt_pk_bf16_f32 v97, v98, v99\n"
"v_sub_f32 v102, v102, v157\n"
"v_exp_f32 v101, v101\n"
"v_cvt_pk_bf16_f32 v6, v164, v165\n"
"v_cvt_pk_bf16_f32 v7, v166, v167\n"
"v_mfma_f32_16x16x32_bf16 v[164:167], a[144:147], a[48:51], 0\n"
"buffer_load_dwordx4 v152, s[24:27], s41 offen offset:1088 lds\n"
"ds_read_b64 a[244:245], v163 offset:0\n"
"v_mfma_f32_16x16x32_bf16 v[164:167], a[148:151], a[52:55], v[164:167]\n"
"buffer_load_dwordx4 v152, s[24:27], s42 offen offset:2176 lds\n"
"v_cvt_pk_bf16_f32 v14, v168, v169\n"
"v_mfma_f32_16x16x32_bf16 v[164:167], a[152:155], a[56:59], v[164:167]\n"
"buffer_load_dwordx4 v152, s[24:27], s43 offen offset:3264 lds\n"
"v_cvt_pk_bf16_f32 v15, v170, v171\n"
"v_mfma_f32_16x16x32_bf16 v[164:167], a[156:159], a[60:63], v[164:167]\n"
"v_cvt_pk_bf16_f32 v22, v172, v173\n"
"v_cvt_pk_bf16_f32 v23, v174, v175\n"
"v_sub_f32 v103, v103, v157\n"
"v_exp_f32 v102, v102\n"
"v_exp_f32 v103, v103\n"
"v_sub_f32 v104, v104, v157\n"
"v_sub_f32 v105, v105, v157\n"
"v_exp_f32 v104, v104\n"
"v_cvt_pk_bf16_f32 v98, v100, v101\n"
"v_cvt_pk_bf16_f32 v99, v102, v103\n"
"v_sub_f32 v106, v106, v157\n"
"v_exp_f32 v105, v105\n"
"v_mfma_f32_16x16x32_bf16 v[204:207], v[96:99], v[48:51], v[204:207]\n"
"v_sub_f32 v107, v107, v157\n"
"v_sub_f32 v108, v108, v157\n"
"v_mfma_f32_16x16x32_bf16 v[212:215], v[96:99], v[72:75], v[212:215]\n"
"v_exp_f32 v106, v106\n"
"v_exp_f32 v107, v107\n"
"v_sub_f32 v109, v109, v157\n"
"v_exp_f32 v108, v108\n"
"v_cvt_pk_bf16_f32 v104, v104, v105\n"
"v_cvt_pk_bf16_f32 v105, v106, v107\n"
"v_sub_f32 v110, v110, v157\n"
"v_exp_f32 v109, v109\n"
"v_sub_f32 v111, v111, v157\n"
"v_rcp_f32 v253, v252\n"
"v_exp_f32 v110, v110\n"
"v_exp_f32 v111, v111\n"
"v_sub_f32 v112, v112, v157\n"
"v_sub_f32 v113, v113, v157\n"
"v_exp_f32 v112, v112\n"
"v_cvt_pk_bf16_f32 v106, v108, v109\n"
"v_cvt_pk_bf16_f32 v107, v110, v111\n"
"v_sub_f32 v114, v114, v157\n"
"v_exp_f32 v113, v113\n"
"v_mfma_f32_16x16x32_bf16 v[204:207], v[104:107], v[52:55], v[204:207]\n"
"ds_bpermute_b32 v254, v160, v253 offset:0\n"
"v_mfma_f32_16x16x32_bf16 v[212:215], v[104:107], v[76:79], v[212:215]\n"
"v_sub_f32 v115, v115, v157\n"
"v_exp_f32 v114, v114\n"
"v_exp_f32 v115, v115\n"
"v_sub_f32 v116, v116, v157\n"
"v_sub_f32 v117, v117, v157\n"
"v_exp_f32 v116, v116\n"
"v_cvt_pk_bf16_f32 v112, v112, v113\n"
"v_cvt_pk_bf16_f32 v113, v114, v115\n"
"v_sub_f32 v118, v118, v157\n"
"v_exp_f32 v117, v117\n"
"v_sub_f32 v119, v119, v157\n"
"v_exp_f32 v118, v118\n"
"v_exp_f32 v119, v119\n"
"v_cvt_pk_bf16_f32 v114, v116, v117\n"
"v_cvt_pk_bf16_f32 v25, v26, v27\n"
"v_cvt_pk_bf16_f32 v115, v118, v119\n"
"v_cvt_pk_bf16_f32 v180, v180, v181\n"
"v_cvt_pk_bf16_f32 v26, v176, v177\n"
"v_cvt_pk_bf16_f32 v181, v182, v183\n"
"v_mfma_f32_16x16x32_bf16 v[204:207], v[112:115], v[56:59], v[204:207]\n"
"v_cvt_pk_bf16_f32 v27, v178, v179\n"
"v_cvt_pk_bf16_f32 v188, v188, v189\n"
"v_mfma_f32_16x16x32_bf16 v[208:211], v[112:115], v[68:71], v[208:211]\n"
"v_cvt_pk_bf16_f32 v189, v190, v191\n"
"v_cvt_pk_bf16_f32 v196, v196, v197\n"
"v_mfma_f32_16x16x32_bf16 v[208:211], v[104:107], v[64:67], v[208:211]\n"
"v_cvt_pk_bf16_f32 v197, v198, v199\n"
"v_cvt_pk_bf16_f32 v28, v28, v29\n"
"v_mfma_f32_16x16x32_bf16 v[208:211], v[96:99], v[60:63], v[208:211]\n"
"s_waitcnt lgkmcnt(0)\n"
"v_mul_f32_dpp v220, v254, v220 quad_perm:[0,0,0,0] row_mask:0xf bank_mask:0xf\n"
"v_mul_f32_dpp v221, v254, v221 quad_perm:[1,1,1,1] row_mask:0xf bank_mask:0xf\n"
"v_mfma_f32_16x16x32_bf16 v[212:215], v[112:115], v[80:83], v[212:215]\n"
"v_mul_f32_dpp v222, v254, v222 quad_perm:[2,2,2,2] row_mask:0xf bank_mask:0xf\n"
"v_mul_f32_dpp v223, v254, v223 quad_perm:[3,3,3,3] row_mask:0xf bank_mask:0xf\n"
"v_mfma_f32_16x16x32_bf16 v[216:219], v[112:115], v[92:95], v[216:219]\n"
"v_mul_f32_dpp v224, v254, v224 quad_perm:[0,0,0,0] row_mask:0xf bank_mask:0xf\n"
"v_mul_f32_dpp v225, v254, v225 quad_perm:[1,1,1,1] row_mask:0xf bank_mask:0xf\n"
"v_mfma_f32_16x16x32_bf16 v[216:219], v[104:107], v[88:91], v[216:219]\n"
"v_mul_f32_dpp v226, v254, v226 quad_perm:[2,2,2,2] row_mask:0xf bank_mask:0xf\n"
"v_mul_f32_dpp v227, v254, v227 quad_perm:[3,3,3,3] row_mask:0xf bank_mask:0xf\n"
"v_mfma_f32_16x16x32_bf16 v[216:219], v[96:99], v[84:87], v[216:219]\n"
"v_mul_f32_dpp v228, v254, v228 quad_perm:[0,0,0,0] row_mask:0xf bank_mask:0xf\n"
"v_mul_f32_dpp v229, v254, v229 quad_perm:[1,1,1,1] row_mask:0xf bank_mask:0xf\n"
"v_mfma_f32_16x16x32_bf16 v[220:223], a[64:67], a[160:163], v[220:223]\n"
"v_mul_f32_dpp v230, v254, v230 quad_perm:[2,2,2,2] row_mask:0xf bank_mask:0xf\n"
"v_mul_f32_dpp v231, v254, v231 quad_perm:[3,3,3,3] row_mask:0xf bank_mask:0xf\n"
"v_mfma_f32_16x16x32_bf16 v[220:223], a[68:71], a[164:167], v[220:223]\n"
"v_mul_f32_dpp v232, v254, v232 quad_perm:[0,0,0,0] row_mask:0xf bank_mask:0xf\n"
"v_mul_f32_dpp v233, v254, v233 quad_perm:[1,1,1,1] row_mask:0xf bank_mask:0xf\n"
"v_mfma_f32_16x16x32_bf16 v[220:223], a[72:75], a[168:171], v[220:223]\n"
"v_mul_f32_dpp v234, v254, v234 quad_perm:[2,2,2,2] row_mask:0xf bank_mask:0xf\n"
"v_mul_f32_dpp v235, v254, v235 quad_perm:[3,3,3,3] row_mask:0xf bank_mask:0xf\n"
"v_mfma_f32_16x16x32_bf16 v[220:223], a[76:79], a[172:175], v[220:223]\n"
"v_cvt_pk_bf16_f32 v182, v184, v185\n"
"v_cvt_pk_bf16_f32 v29, v30, v31\n"
"v_mfma_f32_16x16x32_bf16 v[224:227], a[76:79], a[188:191], v[224:227]\n"
"v_cvt_pk_bf16_f32 v183, v186, v187\n"
"v_cvt_pk_bf16_f32 v190, v192, v193\n"
"v_mfma_f32_16x16x32_bf16 v[224:227], a[72:75], a[184:187], v[224:227]\n"
"v_cvt_pk_bf16_f32 v191, v194, v195\n"
"v_cvt_pk_bf16_f32 v198, v200, v201\n"
"v_mfma_f32_16x16x32_bf16 v[224:227], a[68:71], a[180:183], v[224:227]\n"
"v_cvt_pk_bf16_f32 v199, v202, v203\n"
"v_cvt_pk_bf16_f32 v30, v164, v165\n"
"v_mfma_f32_16x16x32_bf16 v[224:227], a[64:67], a[176:179], v[224:227]\n"
"v_cvt_pk_bf16_f32 v31, v166, v167\n"
"v_add_u32 v156, 512, v156\n"
"v_mfma_f32_16x16x32_bf16 v[228:231], a[64:67], a[192:195], v[228:231]\n"
"v_mfma_f32_16x16x32_bf16 v[228:231], a[68:71], a[196:199], v[228:231]\n"
"v_mfma_f32_16x16x32_bf16 v[228:231], a[72:75], a[200:203], v[228:231]\n"
"v_cvt_pk_bf16_f32 v220, v220, v224\n"
"v_cvt_pk_bf16_f32 v224, v221, v225\n"
"v_mfma_f32_16x16x32_bf16 v[228:231], a[76:79], a[204:207], v[228:231]\n"
"v_pk_max_i16 v220, v220, 0\n"
"v_pk_max_i16 v224, v224, 0\n"
"v_mfma_f32_16x16x32_bf16 v[232:235], a[76:79], a[220:223], v[232:235]\n"
"s_waitcnt vmcnt(18)\n"
"ds_read_b128 a[76:79], v154 offset:4544\n"
"v_cvt_pk_bf16_f32 v222, v222, v226\n"
"v_mfma_f32_16x16x32_bf16 v[232:235], a[72:75], a[216:219], v[232:235]\n"
"ds_read_b128 a[72:75], v154 offset:4480\n"
"v_cvt_pk_bf16_f32 v226, v223, v227\n"
"v_mfma_f32_16x16x32_bf16 v[232:235], a[68:71], a[212:215], v[232:235]\n"
"ds_read_b128 a[68:71], v154 offset:4416\n"
"v_pk_max_i16 v222, v222, 0\n"
"v_mfma_f32_16x16x32_bf16 v[232:235], a[64:67], a[208:211], v[232:235]\n"
"ds_read_b128 a[64:67], v154 offset:4352\n"
"v_pk_max_i16 v226, v226, 0\n"
"v_mfma_f32_16x16x32_bf16 v[252:255], a[252:255], v[96:99], v[120:123]\n"
"v_mfma_f32_16x16x32_bf16 v[252:255], a[252:255], v[104:107], v[252:255]\n"
"v_mfma_f32_16x16x32_bf16 v[252:255], a[252:255], v[112:115], v[252:255]\n"
"v_cvt_pk_bf16_f32 v221, v228, v232\n"
"v_cvt_pk_bf16_f32 v225, v229, v233\n"
"v_mfma_f32_16x16x32_bf16 v[184:187], v[240:243], a[0:3], 0\n"
"v_pk_max_i16 v221, v221, 0\n"
"v_pk_max_i16 v225, v225, 0\n"
"v_mfma_f32_16x16x32_bf16 v[184:187], v[244:247], a[4:7], v[184:187]\n"
"v_cvt_pk_bf16_f32 v223, v230, v234\n"
"v_cvt_pk_bf16_f32 v227, v231, v235\n"
"v_mfma_f32_16x16x32_bf16 v[184:187], v[248:251], a[8:11], v[184:187]\n"
"v_pk_max_i16 v223, v223, 0\n"
"v_pk_max_i16 v227, v227, 0\n"
"v_mfma_f32_16x16x32_bf16 v[184:187], a[224:227], a[12:15], v[184:187]\n"
"s_waitcnt lgkmcnt(3)\n"
"buffer_store_dwordx2 v[220:221], v162, s[84:87], s68 offen offset:0 sc0 sc1\n"
"v_mfma_f32_16x16x32_bf16 v[32:35], a[108:111], a[76:79], 0\n"
"s_waitcnt lgkmcnt(2)\n"
"v_add_f32 v252, v252, v253\n"
"v_mfma_f32_16x16x32_bf16 v[32:35], a[104:107], a[72:75], v[32:35]\n"
"s_waitcnt lgkmcnt(1)\n"
"v_rcp_f32 v253, v252\n"
"v_mfma_f32_16x16x32_bf16 v[32:35], a[100:103], a[68:71], v[32:35]\n"
"s_waitcnt lgkmcnt(0)\n"
"v_cvt_pk_bf16_f32 v184, v184, v185\n"
"v_cvt_pk_bf16_f32 v185, v186, v187\n"
"v_mfma_f32_16x16x32_bf16 v[32:35], a[96:99], a[64:67], v[32:35]\n"
"buffer_store_dwordx2 v[224:225], v162, s[84:87], s68 offen offset:256 sc0 sc1\n"
"v_mfma_f32_16x16x32_bf16 v[172:175], a[124:127], a[76:79], 0\n"
"ds_bpermute_b32 v254, v160, v253 offset:0\n"
"v_mfma_f32_16x16x32_bf16 v[172:175], a[120:123], a[72:75], v[172:175]\n"
"v_mfma_f32_16x16x32_bf16 v[172:175], a[116:119], a[68:71], v[172:175]\n"
"v_cvt_pk_bf16_f32 v32, v32, v33\n"
"v_cvt_pk_bf16_f32 v33, v34, v35\n"
"v_mfma_f32_16x16x32_bf16 v[172:175], a[112:115], a[64:67], v[172:175]\n"
"buffer_store_dwordx2 v[222:223], v162, s[84:87], s68 offen offset:512 sc0 sc1\n"
"v_mfma_f32_16x16x32_bf16 v[36:39], a[140:143], a[76:79], 0\n"
"s_add_u32 m0, s28, 4352\n"
"v_mfma_f32_16x16x32_bf16 v[36:39], a[136:139], a[72:75], v[36:39]\n"
"buffer_load_dwordx4 v152, s[24:27], s44 offen offset:0 lds\n"
"v_mfma_f32_16x16x32_bf16 v[36:39], a[132:135], a[68:71], v[36:39]\n"
"buffer_load_dwordx4 v152, s[24:27], s45 offen offset:1088 lds\n"
"v_cvt_pk_bf16_f32 v34, v172, v173\n"
"v_mfma_f32_16x16x32_bf16 v[36:39], a[128:131], a[64:67], v[36:39]\n"
"buffer_load_dwordx4 v152, s[24:27], s46 offen offset:2176 lds\n"
"v_cvt_pk_bf16_f32 v35, v174, v175\n"
"v_mfma_f32_16x16x32_bf16 v[192:195], a[224:227], a[28:31], 0\n"
"buffer_load_dwordx4 v152, s[24:27], s47 offen offset:3264 lds\n"
"v_mfma_f32_16x16x32_bf16 v[192:195], v[248:251], a[24:27], v[192:195]\n"
"v_mfma_f32_16x16x32_bf16 v[192:195], v[244:247], a[20:23], v[192:195]\n"
"v_cvt_pk_bf16_f32 v36, v36, v37\n"
"v_cvt_pk_bf16_f32 v37, v38, v39\n"
"v_mfma_f32_16x16x32_bf16 v[192:195], v[240:243], a[16:19], v[192:195]\n"
"buffer_store_dwordx2 v[226:227], v162, s[84:87], s68 offen offset:768 sc0 sc1\n"
"v_mfma_f32_16x16x32_bf16 v[168:171], a[156:159], a[76:79], 0\n"
"s_waitcnt lgkmcnt(0)\n"
"v_mul_f32_dpp v204, v254, v204 quad_perm:[0,0,0,0] row_mask:0xf bank_mask:0xf\n"
"v_mul_f32_dpp v205, v254, v205 quad_perm:[1,1,1,1] row_mask:0xf bank_mask:0xf\n"
"v_mfma_f32_16x16x32_bf16 v[168:171], a[152:155], a[72:75], v[168:171]\n"
"v_mul_f32_dpp v206, v254, v206 quad_perm:[2,2,2,2] row_mask:0xf bank_mask:0xf\n"
"v_mul_f32_dpp v207, v254, v207 quad_perm:[3,3,3,3] row_mask:0xf bank_mask:0xf\n"
"v_mfma_f32_16x16x32_bf16 v[168:171], a[148:151], a[68:71], v[168:171]\n"
"v_cvt_pk_bf16_f32 v192, v192, v193\n"
"v_cvt_pk_bf16_f32 v193, v194, v195\n"
"v_mfma_f32_16x16x32_bf16 v[168:171], a[144:147], a[64:67], v[168:171]\n"
"v_mul_f32_dpp v208, v254, v208 quad_perm:[0,0,0,0] row_mask:0xf bank_mask:0xf\n"
"v_mul_f32_dpp v209, v254, v209 quad_perm:[1,1,1,1] row_mask:0xf bank_mask:0xf\n"
"v_mfma_f32_16x16x32_bf16 v[200:203], v[240:243], a[32:35], 0\n"
"ds_read_b128 v[240:243], v153 offset:7168\n"
"v_mul_f32_dpp v210, v254, v210 quad_perm:[2,2,2,2] row_mask:0xf bank_mask:0xf\n"
"v_mfma_f32_16x16x32_bf16 v[200:203], v[244:247], a[36:39], v[200:203]\n"
"ds_read_b128 v[244:247], v153 offset:11264\n"
"v_mul_f32_dpp v211, v254, v211 quad_perm:[3,3,3,3] row_mask:0xf bank_mask:0xf\n"
"v_mfma_f32_16x16x32_bf16 v[200:203], v[248:251], a[40:43], v[200:203]\n"
"ds_read_b128 v[248:251], v153 offset:15360\n"
"v_cvt_pk_bf16_f32 v38, v168, v169\n"
"v_mfma_f32_16x16x32_bf16 v[200:203], a[224:227], a[44:47], v[200:203]\n"
"ds_read_b128 a[224:227], v153 offset:18496\n"
"v_cvt_pk_bf16_f32 v39, v170, v171\n"
"v_mfma_f32_16x16x32_bf16 v[164:167], v[236:239], a[0:3], 0\n"
"v_mul_f32_dpp v212, v254, v212 quad_perm:[0,0,0,0] row_mask:0xf bank_mask:0xf\n"
"v_mul_f32_dpp v213, v254, v213 quad_perm:[1,1,1,1] row_mask:0xf bank_mask:0xf\n"
"v_mfma_f32_16x16x32_bf16 v[172:175], v[236:239], a[32:35], 0\n"
"v_mul_f32_dpp v214, v254, v214 quad_perm:[2,2,2,2] row_mask:0xf bank_mask:0xf\n"
"v_mul_f32_dpp v215, v254, v215 quad_perm:[3,3,3,3] row_mask:0xf bank_mask:0xf\n"
"v_mfma_f32_16x16x32_bf16 v[204:207], a[80:83], a[160:163], v[204:207]\n"
"v_cvt_pk_bf16_f32 v200, v200, v201\n"
"v_cvt_pk_bf16_f32 v201, v202, v203\n"
"v_mfma_f32_16x16x32_bf16 v[204:207], a[84:87], a[164:167], v[204:207]\n"
"v_mul_f32_dpp v216, v254, v216 quad_perm:[0,0,0,0] row_mask:0xf bank_mask:0xf\n"
"v_mul_f32_dpp v217, v254, v217 quad_perm:[1,1,1,1] row_mask:0xf bank_mask:0xf\n"
"v_mfma_f32_16x16x32_bf16 v[204:207], a[88:91], a[168:171], v[204:207]\n"
"v_mul_f32_dpp v218, v254, v218 quad_perm:[2,2,2,2] row_mask:0xf bank_mask:0xf\n"
"v_mul_f32_dpp v219, v254, v219 quad_perm:[3,3,3,3] row_mask:0xf bank_mask:0xf\n"
"v_mfma_f32_16x16x32_bf16 v[204:207], a[92:95], a[172:175], v[204:207]\n"
"v_mfma_f32_16x16x32_bf16 v[208:211], a[92:95], a[188:191], v[208:211]\n"
"v_mfma_f32_16x16x32_bf16 v[208:211], a[88:91], a[184:187], v[208:211]\n"
"v_mfma_f32_16x16x32_bf16 v[208:211], a[84:87], a[180:183], v[208:211]\n"
"v_mfma_f32_16x16x32_bf16 v[208:211], a[80:83], a[176:179], v[208:211]\n"
"s_waitcnt lgkmcnt(3)\n"
"v_mfma_f32_16x16x32_bf16 v[164:167], v[240:243], a[4:7], v[164:167]\n"
"s_waitcnt lgkmcnt(2)\n"
"v_mfma_f32_16x16x32_bf16 v[164:167], v[244:247], a[8:11], v[164:167]\n"
"s_waitcnt lgkmcnt(1)\n"
"v_mfma_f32_16x16x32_bf16 v[164:167], v[248:251], a[12:15], v[164:167]\n"
"v_cvt_pk_bf16_f32 v204, v204, v208\n"
"v_cvt_pk_bf16_f32 v208, v205, v209\n"
"v_mfma_f32_16x16x32_bf16 v[168:171], v[248:251], a[28:31], 0\n"
"v_pk_max_i16 v204, v204, 0\n"
"v_pk_max_i16 v208, v208, 0\n"
"v_mfma_f32_16x16x32_bf16 v[168:171], v[244:247], a[24:27], v[168:171]\n"
"v_cvt_pk_bf16_f32 v206, v206, v210\n"
"v_cvt_pk_bf16_f32 v210, v207, v211\n"
"v_mfma_f32_16x16x32_bf16 v[168:171], v[240:243], a[20:23], v[168:171]\n"
"v_cvt_pk_bf16_f32 v186, v164, v165\n"
"v_cvt_pk_bf16_f32 v187, v166, v167\n"
"v_mfma_f32_16x16x32_bf16 v[168:171], v[236:239], a[16:19], v[168:171]\n"
"ds_read_b128 v[236:239], v153 offset:16384\n"
"v_pk_max_i16 v206, v206, 0\n"
"v_mfma_f32_16x16x32_bf16 v[172:175], v[240:243], a[36:39], v[172:175]\n"
"ds_read_b128 v[240:243], v153 offset:20576\n"
"v_pk_max_i16 v210, v210, 0\n"
"v_mfma_f32_16x16x32_bf16 v[172:175], v[244:247], a[40:43], v[172:175]\n"
"ds_read_b128 v[244:247], v153 offset:24768\n"
"v_mfma_f32_16x16x32_bf16 v[172:175], v[248:251], a[44:47], v[172:175]\n"
"ds_read_b128 v[248:251], v153 offset:28960\n"
"v_cvt_pk_bf16_f32 v194, v168, v169\n"
"v_mfma_f32_16x16x32_bf16 v[212:215], a[80:83], a[192:195], v[212:215]\n"
"v_cvt_pk_bf16_f32 v195, v170, v171\n"
"v_mfma_f32_16x16x32_bf16 v[212:215], a[84:87], a[196:199], v[212:215]\n"
"v_mfma_f32_16x16x32_bf16 v[212:215], a[88:91], a[200:203], v[212:215]\n"
"v_cvt_pk_bf16_f32 v202, v172, v173\n"
"v_cvt_pk_bf16_f32 v203, v174, v175\n"
"v_mfma_f32_16x16x32_bf16 v[212:215], a[92:95], a[204:207], v[212:215]\n"
"v_mfma_f32_16x16x32_bf16 v[216:219], a[92:95], a[220:223], v[216:219]\n"
"s_waitcnt vmcnt(21)\n"
"ds_read_b128 a[92:95], v154 offset:8896\n"
"v_mfma_f32_16x16x32_bf16 v[216:219], a[88:91], a[216:219], v[216:219]\n"
"ds_read_b128 a[88:91], v154 offset:8832\n"
"v_mfma_f32_16x16x32_bf16 v[216:219], a[84:87], a[212:215], v[216:219]\n"
"ds_read_b128 a[84:87], v154 offset:8768\n"
"v_mfma_f32_16x16x32_bf16 v[216:219], a[80:83], a[208:211], v[216:219]\n"
"ds_read_b128 a[80:83], v154 offset:8704\n"
"v_mfma_f32_16x16x32_bf16 v[120:123], a[228:231], v[180:183], 0\n"
"ds_read_b128 a[228:231], v158 offset:0\n"
"v_mfma_f32_16x16x32_bf16 v[120:123], a[232:235], v[184:187], v[120:123]\n"
"ds_read_b128 a[232:235], v158 offset:2048\n"
"v_mfma_f32_16x16x32_bf16 v[96:99], v[0:3], v[180:183], 0\n"
"v_cvt_pk_bf16_f32 v205, v212, v216\n"
"v_cvt_pk_bf16_f32 v209, v213, v217\n"
"v_mfma_f32_16x16x32_bf16 v[96:99], v[4:7], v[184:187], v[96:99]\n"
"v_pk_max_i16 v205, v205, 0\n"
"v_pk_max_i16 v209, v209, 0\n"
"v_mfma_f32_16x16x32_bf16 v[100:103], v[12:15], v[184:187], 0\n"
"v_cvt_pk_bf16_f32 v207, v214, v218\n"
"v_cvt_pk_bf16_f32 v211, v215, v219\n"
"v_mfma_f32_16x16x32_bf16 v[100:103], v[8:11], v[180:183], v[100:103]\n"
"buffer_store_dwordx2 v[204:205], v162, s[84:87], s69 offen offset:0 sc0 sc1\n"
"v_mfma_f32_16x16x32_bf16 v[104:107], v[16:19], v[180:183], 0\n"
"v_pk_max_i16 v207, v207, 0\n"
"v_pk_max_i16 v211, v211, 0\n"
"v_mfma_f32_16x16x32_bf16 v[104:107], v[20:23], v[184:187], v[104:107]\n"
"buffer_store_dwordx2 v[208:209], v162, s[84:87], s69 offen offset:256 sc0 sc1\n"
"v_mfma_f32_16x16x32_bf16 v[108:111], v[28:31], v[184:187], 0\n"
"v_max3_f32 v157, v120, v121, v122\n"
"v_max3_f32 v159, v123, v96, v97\n"
"v_mfma_f32_16x16x32_bf16 v[108:111], v[24:27], v[180:183], v[108:111]\n"
"s_waitcnt lgkmcnt(2)\n"
"buffer_store_dwordx2 v[206:207], v162, s[84:87], s69 offen offset:512 sc0 sc1\n"
"v_mfma_f32_16x16x32_bf16 v[40:43], a[96:99], a[80:83], 0\n"
"v_max3_f32 v157, v157, v98, v99\n"
"v_max3_f32 v159, v159, v100, v101\n"
"v_mfma_f32_16x16x32_bf16 v[40:43], a[100:103], a[84:87], v[40:43]\n"
"v_max3_f32 v157, v157, v102, v103\n"
"v_max3_f32 v159, v159, v104, v105\n"
"v_mfma_f32_16x16x32_bf16 v[40:43], a[104:107], a[88:91], v[40:43]\n"
"v_max3_f32 v157, v157, v106, v107\n"
"v_max3_f32 v159, v159, v108, v109\n"
"v_mfma_f32_16x16x32_bf16 v[40:43], a[108:111], a[92:95], v[40:43]\n"
"buffer_store_dwordx2 v[210:211], v162, s[84:87], s69 offen offset:768 sc0 sc1\n"
"s_branch .Lp2s%=\n"
".Leps%=:\n"
"s_nop 0\n"
"v_mfma_f32_16x16x32_bf16 v[204:207], a[32:35], a[160:163], v[204:207]\n"
"v_sub_f32 v129, v129, v157\n"
"v_sub_f32 v130, v130, v157\n"
"v_mfma_f32_16x16x32_bf16 v[204:207], a[36:39], a[164:167], v[204:207]\n"
"v_sub_f32 v131, v131, v157\n"
"v_sub_f32 v132, v132, v157\n"
"v_mfma_f32_16x16x32_bf16 v[204:207], a[40:43], a[168:171], v[204:207]\n"
"v_exp_f32 v128, v128\n"
"v_mfma_f32_16x16x32_bf16 v[204:207], a[44:47], a[172:175], v[204:207]\n"
"v_exp_f32 v129, v129\n"
"v_exp_f32 v130, v130\n"
"v_exp_f32 v131, v131\n"
"v_sub_f32 v133, v133, v157\n"
"v_cvt_pk_bf16_f32 v124, v124, v125\n"
"v_exp_f32 v132, v132\n"
"v_sub_f32 v134, v134, v157\n"
"v_exp_f32 v133, v133\n"
"v_sub_f32 v135, v135, v157\n"
"v_cvt_pk_bf16_f32 v125, v126, v127\n"
"v_exp_f32 v134, v134\n"
"v_exp_f32 v135, v135\n"
"v_sub_f32 v136, v136, v157\n"
"v_sub_f32 v137, v137, v157\n"
"v_sub_f32 v138, v138, v157\n"
"v_sub_f32 v139, v139, v157\n"
"v_cvt_pk_bf16_f32 v126, v128, v129\n"
"v_cvt_pk_bf16_f32 v127, v130, v131\n"
"v_mfma_f32_16x16x32_bf16 v[128:131], v[12:15], v[192:195], 0\n"
"v_exp_f32 v136, v136\n"
"v_mfma_f32_16x16x32_bf16 v[128:131], v[8:11], v[188:191], v[128:131]\n"
"v_exp_f32 v137, v137\n"
"v_mfma_f32_16x16x32_bf16 v[164:167], v[124:127], v[48:51], v[164:167]\n"
"v_exp_f32 v138, v138\n"
"v_mfma_f32_16x16x32_bf16 v[172:175], v[124:127], v[72:75], v[172:175]\n"
"v_exp_f32 v139, v139\n"
"v_sub_f32 v140, v140, v157\n"
"v_sub_f32 v141, v141, v157\n"
"v_cvt_pk_bf16_f32 v132, v132, v133\n"
"v_exp_f32 v140, v140\n"
"v_sub_f32 v142, v142, v157\n"
"v_exp_f32 v141, v141\n"
"v_sub_f32 v143, v143, v157\n"
"v_mul_f32_dpp v208, v254, v208 quad_perm:[0,0,0,0] row_mask:0xf bank_mask:0xf\n"
"v_mul_f32_dpp v209, v254, v209 quad_perm:[1,1,1,1] row_mask:0xf bank_mask:0xf\n"
"v_mul_f32_dpp v210, v254, v210 quad_perm:[2,2,2,2] row_mask:0xf bank_mask:0xf\n"
"v_mul_f32_dpp v211, v254, v211 quad_perm:[3,3,3,3] row_mask:0xf bank_mask:0xf\n"
"v_mul_f32_dpp v212, v254, v212 quad_perm:[0,0,0,0] row_mask:0xf bank_mask:0xf\n"
"v_mul_f32_dpp v213, v254, v213 quad_perm:[1,1,1,1] row_mask:0xf bank_mask:0xf\n"
"v_mul_f32_dpp v214, v254, v214 quad_perm:[2,2,2,2] row_mask:0xf bank_mask:0xf\n"
"v_mfma_f32_16x16x32_bf16 v[208:211], a[44:47], a[188:191], v[208:211]\n"
"v_mul_f32_dpp v215, v254, v215 quad_perm:[3,3,3,3] row_mask:0xf bank_mask:0xf\n"
"v_mul_f32_dpp v216, v254, v216 quad_perm:[0,0,0,0] row_mask:0xf bank_mask:0xf\n"
"v_mfma_f32_16x16x32_bf16 v[208:211], a[40:43], a[184:187], v[208:211]\n"
"v_mul_f32_dpp v217, v254, v217 quad_perm:[1,1,1,1] row_mask:0xf bank_mask:0xf\n"
"v_mul_f32_dpp v218, v254, v218 quad_perm:[2,2,2,2] row_mask:0xf bank_mask:0xf\n"
"v_mfma_f32_16x16x32_bf16 v[208:211], a[36:39], a[180:183], v[208:211]\n"
"v_mul_f32_dpp v219, v254, v219 quad_perm:[3,3,3,3] row_mask:0xf bank_mask:0xf\n"
"v_cvt_pk_bf16_f32 v133, v134, v135\n"
"v_mfma_f32_16x16x32_bf16 v[208:211], a[32:35], a[176:179], v[208:211]\n"
"v_exp_f32 v142, v142\n"
"v_mfma_f32_16x16x32_bf16 v[252:255], a[252:255], v[124:127], v[148:151]\n"
"v_exp_f32 v143, v143\n"
"v_mfma_f32_16x16x32_bf16 v[212:215], a[32:35], a[192:195], v[212:215]\n"
"v_sub_f32 v144, v144, v157\n"
"v_sub_f32 v145, v145, v157\n"
"v_mfma_f32_16x16x32_bf16 v[212:215], a[36:39], a[196:199], v[212:215]\n"
"v_sub_f32 v146, v146, v157\n"
"v_sub_f32 v147, v147, v157\n"
"v_mfma_f32_16x16x32_bf16 v[212:215], a[40:43], a[200:203], v[212:215]\n"
"v_cvt_pk_bf16_f32 v134, v136, v137\n"
"v_cvt_pk_bf16_f32 v135, v138, v139\n"
"v_mfma_f32_16x16x32_bf16 v[212:215], a[44:47], a[204:207], v[212:215]\n"
"v_exp_f32 v144, v144\n"
"v_mfma_f32_16x16x32_bf16 v[252:255], a[252:255], v[132:135], v[252:255]\n"
"v_exp_f32 v145, v145\n"
"v_mfma_f32_16x16x32_bf16 v[164:167], v[132:135], v[52:55], v[164:167]\n"
"v_exp_f32 v146, v146\n"
"v_mfma_f32_16x16x32_bf16 v[172:175], v[132:135], v[76:79], v[172:175]\n"
"v_exp_f32 v147, v147\n"
"v_mfma_f32_16x16x32_bf16 v[216:219], a[44:47], a[220:223], v[216:219]\n"
"v_cvt_pk_bf16_f32 v140, v140, v141\n"
"v_cvt_pk_bf16_f32 v141, v142, v143\n"
"v_mfma_f32_16x16x32_bf16 v[216:219], a[40:43], a[216:219], v[216:219]\n"
"v_cvt_pk_bf16_f32 v142, v144, v145\n"
"v_cvt_pk_bf16_f32 v143, v146, v147\n"
"v_mfma_f32_16x16x32_bf16 v[216:219], a[36:39], a[212:215], v[216:219]\n"
"v_max3_f32 v157, v120, v121, v122\n"
"v_max3_f32 v159, v123, v96, v97\n"
"v_mfma_f32_16x16x32_bf16 v[216:219], a[32:35], a[208:211], v[216:219]\n"
"v_max3_f32 v157, v157, v98, v99\n"
"v_max3_f32 v159, v159, v100, v101\n"
"v_mfma_f32_16x16x32_bf16 v[252:255], a[252:255], v[140:143], v[252:255]\n"
"v_max3_f32 v157, v157, v102, v103\n"
"v_max3_f32 v159, v159, v104, v105\n"
"v_mfma_f32_16x16x32_bf16 v[168:171], v[140:143], v[68:71], v[168:171]\n"
"v_max3_f32 v157, v157, v106, v107\n"
"v_max3_f32 v159, v159, v108, v109\n"
"v_mfma_f32_16x16x32_bf16 v[168:171], v[132:135], v[64:67], v[168:171]\n"
"v_max3_f32 v157, v157, v110, v111\n"
"v_max3_f32 v159, v159, v112, v113\n"
"v_mfma_f32_16x16x32_bf16 v[168:171], v[124:127], v[60:63], v[168:171]\n"
"v_add_f32 v252, v252, v253\n"
"v_max3_f32 v157, v157, v114, v115\n"
"v_mfma_f32_16x16x32_bf16 v[176:179], v[140:143], v[92:95], v[176:179]\n"
"v_rcp_f32 v253, v252\n"
"v_mfma_f32_16x16x32_bf16 v[176:179], v[132:135], v[88:91], v[176:179]\n"
"ds_bpermute_b32 v254, v160, v253 offset:0\n"
"v_mfma_f32_16x16x32_bf16 v[176:179], v[124:127], v[84:87], v[176:179]\n"
"v_max3_f32 v159, v159, v116, v117\n"
"v_max3_f32 v157, v157, v118, v119\n"
"v_mfma_f32_16x16x32_bf16 v[164:167], v[140:143], v[56:59], v[164:167]\n"
"v_max_f32 v157, v157, v159\n"
"v_mov_b32 v161, v157\n"
"v_mfma_f32_16x16x32_bf16 v[172:175], v[140:143], v[80:83], v[172:175]\n"
"v_lshlrev_b32 v222, 16, v223\n"
"v_and_b32 v223, 0xffff0000, v223\n"
"v_mfma_f32_16x16x32_bf16 v[148:151], a[228:231], v[188:191], 0\n"
"s_waitcnt lgkmcnt(1)\n"
"ds_read_b128 a[228:231], v158 offset:0\n"
"v_cvt_pk_bf16_f32 v204, v204, v208\n"
"v_mfma_f32_16x16x32_bf16 v[148:151], a[232:235], v[192:195], v[148:151]\n"
"ds_read_b128 a[232:235], v158 offset:2048\n"
"v_cvt_pk_bf16_f32 v206, v206, v210\n"
"v_mfma_f32_16x16x32_bf16 v[124:127], v[0:3], v[188:191], 0\n"
"v_permlane32_swap_b32 v157, v161\n"
"v_mfma_f32_16x16x32_bf16 v[124:127], v[4:7], v[192:195], v[124:127]\n"
"v_max_f32 v157, v157, v161\n"
"v_mov_b32 v161, v157\n"
"v_mfma_f32_16x16x32_bf16 v[132:135], v[16:19], v[188:191], 0\n"
"v_cvt_pk_bf16_f32 v208, v205, v209\n"
"v_cvt_pk_bf16_f32 v210, v207, v211\n"
"v_mfma_f32_16x16x32_bf16 v[132:135], v[20:23], v[192:195], v[132:135]\n"
"v_permlane16_swap_b32 v157, v161\n"
"v_mfma_f32_16x16x32_bf16 v[136:139], v[28:31], v[192:195], 0\n"
"v_max_f32 v157, v157, v161\n"
"v_sub_f32 v120, v120, v157\n"
"v_mfma_f32_16x16x32_bf16 v[136:139], v[24:27], v[188:191], v[136:139]\n"
"v_sub_f32 v121, v121, v157\n"
"v_sub_f32 v96, v96, v157\n"
"v_mfma_f32_16x16x32_bf16 v[140:143], v[32:35], v[188:191], 0\n"
"v_exp_f32 v120, v120\n"
"v_mfma_f32_16x16x32_bf16 v[140:143], v[36:39], v[192:195], v[140:143]\n"
"v_exp_f32 v121, v121\n"
"v_mfma_f32_16x16x32_bf16 v[144:147], v[44:47], v[192:195], 0\n"
"v_sub_f32 v97, v97, v157\n"
"v_sub_f32 v122, v122, v157\n"
"v_mfma_f32_16x16x32_bf16 v[144:147], v[40:43], v[188:191], v[144:147]\n"
"v_sub_f32 v123, v123, v157\n"
"v_exp_f32 v96, v96\n"
"v_sub_f32 v98, v98, v157\n"
"v_exp_f32 v97, v97\n"
"v_sub_f32 v99, v99, v157\n"
"v_exp_f32 v161, v122\n"
"v_exp_f32 v123, v123\n"
"v_exp_f32 v98, v98\n"
"v_exp_f32 v99, v99\n"
"v_sub_f32 v100, v100, v157\n"
"v_sub_f32 v101, v101, v157\n"
"v_sub_f32 v102, v102, v157\n"
"v_sub_f32 v103, v103, v157\n"
"v_exp_f32 v100, v100\n"
"v_exp_f32 v101, v101\n"
"v_exp_f32 v102, v102\n"
"v_exp_f32 v103, v103\n"
"v_cvt_pk_bf16_f32 v122, v120, v121\n"
"v_sub_f32 v104, v104, v157\n"
"v_sub_f32 v105, v105, v157\n"
"v_add_f32 v120, v120, v121\n"
"v_cvt_pk_bf16_f32 v96, v96, v97\n"
"v_exp_f32 v104, v104\n"
"v_sub_f32 v106, v106, v157\n"
"v_exp_f32 v105, v105\n"
"v_sub_f32 v107, v107, v157\n"
"v_add_f32 v121, v161, v123\n"
"v_cvt_pk_bf16_f32 v97, v98, v99\n"
"v_exp_f32 v106, v106\n"
"v_exp_f32 v107, v107\n"
"v_sub_f32 v108, v108, v157\n"
"v_sub_f32 v109, v109, v157\n"
"v_sub_f32 v110, v110, v157\n"
"v_sub_f32 v111, v111, v157\n"
"v_cvt_pk_bf16_f32 v123, v161, v123\n"
"v_cvt_pk_bf16_f32 v98, v100, v101\n"
"v_cvt_pk_bf16_f32 v99, v102, v103\n"
"v_mfma_f32_16x16x32_bf16 v[100:103], v[12:15], v[200:203], 0\n"
"v_exp_f32 v108, v108\n"
"v_mfma_f32_16x16x32_bf16 v[100:103], v[8:11], v[196:199], v[100:103]\n"
"v_exp_f32 v109, v109\n"
"v_exp_f32 v110, v110\n"
"v_exp_f32 v111, v111\n"
"v_sub_f32 v112, v112, v157\n"
"v_sub_f32 v113, v113, v157\n"
"v_cvt_pk_bf16_f32 v104, v104, v105\n"
"v_exp_f32 v112, v112\n"
"v_sub_f32 v114, v114, v157\n"
"v_exp_f32 v113, v113\n"
"v_sub_f32 v115, v115, v157\n"
"s_waitcnt lgkmcnt(0)\n"
"v_fmac_f32_dpp v220, v254, v164 quad_perm:[0,0,0,0] row_mask:0xf bank_mask:0xf\n"
"v_fmac_f32_dpp v221, v254, v168 quad_perm:[0,0,0,0] row_mask:0xf bank_mask:0xf\n"
"v_fmac_f32_dpp v222, v254, v172 quad_perm:[0,0,0,0] row_mask:0xf bank_mask:0xf\n"
"v_fmac_f32_dpp v223, v254, v176 quad_perm:[0,0,0,0] row_mask:0xf bank_mask:0xf\n"
"v_mfma_f32_16x16x32_bf16 v[252:255], a[252:255], v[96:99], v[120:123]\n"
"v_cvt_pk_bf16_f32 v105, v106, v107\n"
"v_exp_f32 v114, v114\n"
"v_exp_f32 v115, v115\n"
"v_sub_f32 v116, v116, v157\n"
"v_sub_f32 v117, v117, v157\n"
"v_sub_f32 v118, v118, v157\n"
"v_sub_f32 v119, v119, v157\n"
"v_cvt_pk_bf16_f32 v106, v108, v109\n"
"v_cvt_pk_bf16_f32 v107, v110, v111\n"
"v_mfma_f32_16x16x32_bf16 v[108:111], v[28:31], v[200:203], 0\n"
"v_exp_f32 v116, v116\n"
"v_mfma_f32_16x16x32_bf16 v[108:111], v[24:27], v[196:199], v[108:111]\n"
"v_exp_f32 v117, v117\n"
"v_mfma_f32_16x16x32_bf16 v[252:255], a[252:255], v[104:107], v[252:255]\n"
"v_exp_f32 v118, v118\n"
"v_exp_f32 v119, v119\n"
"v_cvt_pk_bf16_f32 v112, v112, v113\n"
"v_cvt_pk_bf16_f32 v113, v114, v115\n"
"v_cvt_pk_bf16_f32 v114, v116, v117\n"
"v_cvt_pk_bf16_f32 v115, v118, v119\n"
"v_mfma_f32_16x16x32_bf16 v[116:119], v[44:47], v[200:203], 0\n"
"v_cvt_pk_bf16_f32 v205, v212, v216\n"
"v_cvt_pk_bf16_f32 v209, v213, v217\n"
"v_mfma_f32_16x16x32_bf16 v[116:119], v[40:43], v[196:199], v[116:119]\n"
"v_cvt_pk_bf16_f32 v207, v214, v218\n"
"v_cvt_pk_bf16_f32 v211, v215, v219\n"
"v_mfma_f32_16x16x32_bf16 v[252:255], a[252:255], v[112:115], v[252:255]\n"
"v_pk_max_i16 v204, v204, 0\n"
"v_pk_max_i16 v205, v205, 0\n"
"v_pk_max_i16 v208, v208, 0\n"
"v_pk_max_i16 v209, v209, 0\n"
"v_pk_max_i16 v206, v206, 0\n"
"v_pk_max_i16 v207, v207, 0\n"
"v_pk_max_i16 v210, v210, 0\n"
"v_pk_max_i16 v211, v211, 0\n"
"v_max3_f32 v157, v148, v149, v150\n"
"v_max3_f32 v159, v151, v124, v125\n"
"v_max3_f32 v157, v157, v126, v127\n"
"v_add_f32 v252, v252, v253\n"
"v_rcp_f32 v253, v252\n"
"v_max3_f32 v159, v159, v128, v129\n"
"v_max3_f32 v157, v157, v130, v131\n"
"v_max3_f32 v159, v159, v132, v133\n"
"v_max3_f32 v157, v157, v134, v135\n"
"v_max3_f32 v159, v159, v136, v137\n"
"v_max3_f32 v157, v157, v138, v139\n"
"v_max3_f32 v159, v159, v140, v141\n"
"v_max3_f32 v157, v157, v142, v143\n"
"v_max3_f32 v159, v159, v144, v145\n"
"v_max3_f32 v157, v157, v146, v147\n"
"v_max_f32 v157, v157, v159\n"
"v_mov_b32 v161, v157\n"
"v_cvt_pk_bf16_f32 v220, v220, v221\n"
"v_cvt_pk_bf16_f32 v221, v222, v223\n"
"v_pk_max_i16 v220, v220, 0\n"
"v_permlane32_swap_b32 v157, v161\n"
"v_max_f32 v157, v157, v161\n"
"v_mov_b32 v161, v157\n"
"v_pk_max_i16 v221, v221, 0\n"
"v_add_u32 v158, 64, v158\n"
"ds_bpermute_b32 v254, v160, v253 offset:0\n"
"buffer_store_dwordx2 v[204:205], v162, s[84:87], s66 offen offset:0 sc0 sc1\n"
"v_permlane16_swap_b32 v157, v161\n"
"v_max_f32 v157, v157, v161\n"
"v_sub_f32 v148, v148, v157\n"
"v_sub_f32 v149, v149, v157\n"
"v_exp_f32 v148, v148\n"
"v_exp_f32 v149, v149\n"
"v_sub_f32 v124, v124, v157\n"
"v_sub_f32 v125, v125, v157\n"
"v_sub_f32 v150, v150, v157\n"
"v_sub_f32 v151, v151, v157\n"
"v_exp_f32 v124, v124\n"
"v_sub_f32 v126, v126, v157\n"
"v_exp_f32 v125, v125\n"
"v_sub_f32 v127, v127, v157\n"
"v_exp_f32 v161, v150\n"
"v_exp_f32 v151, v151\n"
"v_exp_f32 v126, v126\n"
"v_exp_f32 v127, v127\n"
"v_sub_f32 v128, v128, v157\n"
"v_sub_f32 v129, v129, v157\n"
"v_sub_f32 v130, v130, v157\n"
"v_sub_f32 v131, v131, v157\n"
"v_exp_f32 v128, v128\n"
"v_exp_f32 v129, v129\n"
"v_exp_f32 v130, v130\n"
"v_exp_f32 v131, v131\n"
"v_cvt_pk_bf16_f32 v150, v148, v149\n"
"v_sub_f32 v132, v132, v157\n"
"v_sub_f32 v133, v133, v157\n"
"v_add_f32 v148, v148, v149\n"
"v_cvt_pk_bf16_f32 v124, v124, v125\n"
"v_exp_f32 v132, v132\n"
"v_sub_f32 v134, v134, v157\n"
"v_exp_f32 v133, v133\n"
"v_sub_f32 v135, v135, v157\n"
"v_add_f32 v149, v161, v151\n"
"v_cvt_pk_bf16_f32 v125, v126, v127\n"
"v_exp_f32 v134, v134\n"
"v_exp_f32 v135, v135\n"
"v_sub_f32 v136, v136, v157\n"
"v_sub_f32 v137, v137, v157\n"
"v_sub_f32 v138, v138, v157\n"
"v_sub_f32 v139, v139, v157\n"
"v_cvt_pk_bf16_f32 v151, v161, v151\n"
"v_cvt_pk_bf16_f32 v126, v128, v129\n"
"v_cvt_pk_bf16_f32 v127, v130, v131\n"
"v_exp_f32 v136, v136\n"
"v_exp_f32 v137, v137\n"
"v_exp_f32 v138, v138\n"
"v_exp_f32 v139, v139\n"
"v_sub_f32 v140, v140, v157\n"
"v_sub_f32 v141, v141, v157\n"
"v_cvt_pk_bf16_f32 v132, v132, v133\n"
"v_exp_f32 v140, v140\n"
"v_sub_f32 v142, v142, v157\n"
"v_exp_f32 v141, v141\n"
"v_sub_f32 v143, v143, v157\n"
"v_cvt_pk_bf16_f32 v133, v134, v135\n"
"v_exp_f32 v142, v142\n"
"v_exp_f32 v143, v143\n"
"v_sub_f32 v144, v144, v157\n"
"v_sub_f32 v145, v145, v157\n"
"v_sub_f32 v146, v146, v157\n"
"v_sub_f32 v147, v147, v157\n"
"v_cvt_pk_bf16_f32 v134, v136, v137\n"
"v_cvt_pk_bf16_f32 v135, v138, v139\n"
"v_exp_f32 v144, v144\n"
"v_exp_f32 v145, v145\n"
"v_exp_f32 v146, v146\n"
"v_exp_f32 v147, v147\n"
"v_cvt_pk_bf16_f32 v140, v140, v141\n"
"v_cvt_pk_bf16_f32 v141, v142, v143\n"
"v_cvt_pk_bf16_f32 v142, v144, v145\n"
"v_cvt_pk_bf16_f32 v143, v146, v147\n"
"buffer_store_dwordx2 v[208:209], v162, s[84:87], s66 offen offset:256 sc0 sc1\n"
"buffer_store_dwordx2 v[206:207], v162, s[84:87], s66 offen offset:512 sc0 sc1\n"
"buffer_store_dwordx2 v[210:211], v162, s[84:87], s66 offen offset:768 sc0 sc1\n"
"s_nop 1\n"
"v_mfma_f32_16x16x4_4b_bf16 v[204:219], v[122:123], a[244:245], 0 cbsz:2 abid:0\n"
"buffer_store_dwordx2 v[220:221], v155, s[84:87], s70 offen offset:0 sc0 sc1\n"
"v_mfma_f32_16x16x32_bf16 v[120:123], a[228:231], v[196:199], 0\n"
"v_mfma_f32_16x16x32_bf16 v[120:123], a[232:235], v[200:203], v[120:123]\n"
"v_mfma_f32_16x16x32_bf16 v[204:207], v[96:99], v[48:51], v[204:207]\n"
"v_mfma_f32_16x16x32_bf16 v[204:207], v[104:107], v[52:55], v[204:207]\n"
"v_mfma_f32_16x16x32_bf16 v[204:207], v[112:115], v[56:59], v[204:207]\n"
"v_max3_f32 v157, v120, v121, v122\n"
"v_mfma_f32_16x16x32_bf16 v[208:211], v[112:115], v[68:71], v[208:211]\n"
"v_mfma_f32_16x16x32_bf16 v[208:211], v[104:107], v[64:67], v[208:211]\n"
"v_mfma_f32_16x16x32_bf16 v[208:211], v[96:99], v[60:63], v[208:211]\n"
"s_waitcnt lgkmcnt(0)\n"
"v_mul_f32_dpp v204, v254, v204 quad_perm:[0,0,0,0] row_mask:0xf bank_mask:0xf\n"
"v_mul_f32_dpp v205, v254, v205 quad_perm:[1,1,1,1] row_mask:0xf bank_mask:0xf\n"
"v_mfma_f32_16x16x32_bf16 v[212:215], v[96:99], v[72:75], v[212:215]\n"
"v_mul_f32_dpp v206, v254, v206 quad_perm:[2,2,2,2] row_mask:0xf bank_mask:0xf\n"
"v_mul_f32_dpp v207, v254, v207 quad_perm:[3,3,3,3] row_mask:0xf bank_mask:0xf\n"
"v_mfma_f32_16x16x32_bf16 v[212:215], v[104:107], v[76:79], v[212:215]\n"
"v_mfma_f32_16x16x32_bf16 v[212:215], v[112:115], v[80:83], v[212:215]\n"
"v_mul_f32_dpp v208, v254, v208 quad_perm:[0,0,0,0] row_mask:0xf bank_mask:0xf\n"
"v_mul_f32_dpp v209, v254, v209 quad_perm:[1,1,1,1] row_mask:0xf bank_mask:0xf\n"
"v_mfma_f32_16x16x32_bf16 v[216:219], v[112:115], v[92:95], v[216:219]\n"
"v_mul_f32_dpp v210, v254, v210 quad_perm:[2,2,2,2] row_mask:0xf bank_mask:0xf\n"
"v_mul_f32_dpp v211, v254, v211 quad_perm:[3,3,3,3] row_mask:0xf bank_mask:0xf\n"
"v_mfma_f32_16x16x32_bf16 v[216:219], v[104:107], v[88:91], v[216:219]\n"
"v_mfma_f32_16x16x32_bf16 v[216:219], v[96:99], v[84:87], v[216:219]\n"
"v_mul_f32_dpp v212, v254, v212 quad_perm:[0,0,0,0] row_mask:0xf bank_mask:0xf\n"
"v_mul_f32_dpp v213, v254, v213 quad_perm:[1,1,1,1] row_mask:0xf bank_mask:0xf\n"
"v_mfma_f32_16x16x32_bf16 v[96:99], v[0:3], v[196:199], 0\n"
"v_mul_f32_dpp v214, v254, v214 quad_perm:[2,2,2,2] row_mask:0xf bank_mask:0xf\n"
"v_mul_f32_dpp v215, v254, v215 quad_perm:[3,3,3,3] row_mask:0xf bank_mask:0xf\n"
"v_mfma_f32_16x16x32_bf16 v[96:99], v[4:7], v[200:203], v[96:99]\n"
"v_mfma_f32_16x16x32_bf16 v[104:107], v[16:19], v[196:199], 0\n"
"v_mul_f32_dpp v216, v254, v216 quad_perm:[0,0,0,0] row_mask:0xf bank_mask:0xf\n"
"v_mul_f32_dpp v217, v254, v217 quad_perm:[1,1,1,1] row_mask:0xf bank_mask:0xf\n"
"v_mfma_f32_16x16x32_bf16 v[104:107], v[20:23], v[200:203], v[104:107]\n"
"v_mul_f32_dpp v218, v254, v218 quad_perm:[2,2,2,2] row_mask:0xf bank_mask:0xf\n"
"v_mul_f32_dpp v219, v254, v219 quad_perm:[3,3,3,3] row_mask:0xf bank_mask:0xf\n"
"v_mfma_f32_16x16x32_bf16 v[252:255], a[252:255], v[124:127], v[148:151]\n"
"v_max3_f32 v159, v123, v96, v97\n"
"v_max3_f32 v157, v157, v98, v99\n"
"v_mfma_f32_16x16x32_bf16 v[252:255], a[252:255], v[132:135], v[252:255]\n"
"v_max3_f32 v159, v159, v100, v101\n"
"v_max3_f32 v157, v157, v102, v103\n"
"v_mfma_f32_16x16x32_bf16 v[252:255], a[252:255], v[140:143], v[252:255]\n"
"v_max3_f32 v159, v159, v104, v105\n"
"v_max3_f32 v157, v157, v106, v107\n"
"v_mfma_f32_16x16x32_bf16 v[112:115], v[32:35], v[196:199], 0\n"
"v_max3_f32 v159, v159, v108, v109\n"
"v_max3_f32 v157, v157, v110, v111\n"
"v_mfma_f32_16x16x32_bf16 v[112:115], v[36:39], v[200:203], v[112:115]\n"
"v_mfma_f32_16x16x4_4b_bf16 v[220:235], v[150:151], a[244:245], 0 cbsz:2 abid:0\n"
"v_add_f32 v252, v252, v253\n"
"v_rcp_f32 v253, v252\n"
"s_nop 0\n"
"ds_bpermute_b32 v254, v160, v253 offset:0\n"
"v_mfma_f32_16x16x32_bf16 v[204:207], a[48:51], a[160:163], v[204:207]\n"
"v_max3_f32 v159, v159, v112, v113\n"
"v_max3_f32 v157, v157, v114, v115\n"
"v_mfma_f32_16x16x32_bf16 v[204:207], a[52:55], a[164:167], v[204:207]\n"
"v_max3_f32 v159, v159, v116, v117\n"
"v_max3_f32 v157, v157, v118, v119\n"
"v_mfma_f32_16x16x32_bf16 v[204:207], a[56:59], a[168:171], v[204:207]\n"
"v_max_f32 v157, v157, v159\n"
"v_mov_b32 v161, v157\n"
"v_mfma_f32_16x16x32_bf16 v[204:207], a[60:63], a[172:175], v[204:207]\n"
"s_nop 0\n"
"v_permlane32_swap_b32 v157, v161\n"
"v_mfma_f32_16x16x32_bf16 v[220:223], v[124:127], v[48:51], v[220:223]\n"
"v_max_f32 v157, v157, v161\n"
"v_mov_b32 v161, v157\n"
"v_mfma_f32_16x16x32_bf16 v[220:223], v[132:135], v[52:55], v[220:223]\n"
"v_mfma_f32_16x16x32_bf16 v[220:223], v[140:143], v[56:59], v[220:223]\n"
"v_permlane16_swap_b32 v157, v161\n"
"v_mfma_f32_16x16x32_bf16 v[224:227], v[140:143], v[68:71], v[224:227]\n"
"v_max_f32 v157, v157, v161\n"
"v_sub_f32 v120, v120, v157\n"
"v_mfma_f32_16x16x32_bf16 v[224:227], v[132:135], v[64:67], v[224:227]\n"
"v_sub_f32 v121, v121, v157\n"
"v_sub_f32 v96, v96, v157\n"
"v_mfma_f32_16x16x32_bf16 v[224:227], v[124:127], v[60:63], v[224:227]\n"
"v_exp_f32 v120, v120\n"
"v_mfma_f32_16x16x32_bf16 v[228:231], v[124:127], v[72:75], v[228:231]\n"
"v_exp_f32 v121, v121\n"
"v_mfma_f32_16x16x32_bf16 v[228:231], v[132:135], v[76:79], v[228:231]\n"
"v_sub_f32 v97, v97, v157\n"
"v_sub_f32 v122, v122, v157\n"
"v_mfma_f32_16x16x32_bf16 v[228:231], v[140:143], v[80:83], v[228:231]\n"
"v_sub_f32 v123, v123, v157\n"
"v_sub_f32 v98, v98, v157\n"
"v_mfma_f32_16x16x32_bf16 v[232:235], v[140:143], v[92:95], v[232:235]\n"
"v_exp_f32 v96, v96\n"
"v_mfma_f32_16x16x32_bf16 v[232:235], v[132:135], v[88:91], v[232:235]\n"
"v_exp_f32 v97, v97\n"
"v_mfma_f32_16x16x32_bf16 v[232:235], v[124:127], v[84:87], v[232:235]\n"
"v_sub_f32 v99, v99, v157\n"
"v_sub_f32 v100, v100, v157\n"
"v_mfma_f32_16x16x32_bf16 v[208:211], a[60:63], a[188:191], v[208:211]\n"
"v_exp_f32 v161, v122\n"
"v_mfma_f32_16x16x32_bf16 v[208:211], a[56:59], a[184:187], v[208:211]\n"
"v_exp_f32 v123, v123\n"
"v_mfma_f32_16x16x32_bf16 v[208:211], a[52:55], a[180:183], v[208:211]\n"
"v_exp_f32 v98, v98\n"
"v_mfma_f32_16x16x32_bf16 v[208:211], a[48:51], a[176:179], v[208:211]\n"
"v_exp_f32 v99, v99\n"
"v_mfma_f32_16x16x32_bf16 v[212:215], a[48:51], a[192:195], v[212:215]\n"
"v_sub_f32 v101, v101, v157\n"
"v_sub_f32 v102, v102, v157\n"
"v_mfma_f32_16x16x32_bf16 v[212:215], a[52:55], a[196:199], v[212:215]\n"
"v_sub_f32 v103, v103, v157\n"
"v_cvt_pk_bf16_f32 v122, v120, v121\n"
"v_mfma_f32_16x16x32_bf16 v[212:215], a[56:59], a[200:203], v[212:215]\n"
"v_exp_f32 v100, v100\n"
"v_mfma_f32_16x16x32_bf16 v[212:215], a[60:63], a[204:207], v[212:215]\n"
"v_exp_f32 v101, v101\n"
"v_mfma_f32_16x16x32_bf16 v[216:219], a[60:63], a[220:223], v[216:219]\n"
"v_exp_f32 v102, v102\n"
"v_mfma_f32_16x16x32_bf16 v[216:219], a[56:59], a[216:219], v[216:219]\n"
"v_exp_f32 v103, v103\n"
"v_mfma_f32_16x16x32_bf16 v[216:219], a[52:55], a[212:215], v[216:219]\n"
"v_sub_f32 v104, v104, v157\n"
"v_sub_f32 v105, v105, v157\n"
"v_mfma_f32_16x16x32_bf16 v[216:219], a[48:51], a[208:211], v[216:219]\n"
"v_add_f32 v120, v120, v121\n"
"v_cvt_pk_bf16_f32 v96, v96, v97\n"
"v_exp_f32 v104, v104\n"
"v_sub_f32 v106, v106, v157\n"
"v_exp_f32 v105, v105\n"
"v_sub_f32 v107, v107, v157\n"
"v_add_f32 v121, v161, v123\n"
"v_cvt_pk_bf16_f32 v97, v98, v99\n"
"v_exp_f32 v106, v106\n"
"v_exp_f32 v107, v107\n"
"v_sub_f32 v108, v108, v157\n"
"v_sub_f32 v109, v109, v157\n"
"v_sub_f32 v110, v110, v157\n"
"v_sub_f32 v111, v111, v157\n"
"v_cvt_pk_bf16_f32 v123, v161, v123\n"
"v_cvt_pk_bf16_f32 v98, v100, v101\n"
"v_cvt_pk_bf16_f32 v99, v102, v103\n"
"v_exp_f32 v108, v108\n"
"v_exp_f32 v109, v109\n"
"v_exp_f32 v110, v110\n"
"v_exp_f32 v111, v111\n"
"v_sub_f32 v112, v112, v157\n"
"v_sub_f32 v113, v113, v157\n"
"v_cvt_pk_bf16_f32 v104, v104, v105\n"
"v_exp_f32 v112, v112\n"
"v_sub_f32 v114, v114, v157\n"
"v_exp_f32 v113, v113\n"
"v_sub_f32 v115, v115, v157\n"
"v_cvt_pk_bf16_f32 v105, v106, v107\n"
"s_waitcnt lgkmcnt(0)\n"
"v_mul_f32_dpp v220, v254, v220 quad_perm:[0,0,0,0] row_mask:0xf bank_mask:0xf\n"
"v_mul_f32_dpp v221, v254, v221 quad_perm:[1,1,1,1] row_mask:0xf bank_mask:0xf\n"
"v_mul_f32_dpp v222, v254, v222 quad_perm:[2,2,2,2] row_mask:0xf bank_mask:0xf\n"
"v_mul_f32_dpp v223, v254, v223 quad_perm:[3,3,3,3] row_mask:0xf bank_mask:0xf\n"
"v_mul_f32_dpp v224, v254, v224 quad_perm:[0,0,0,0] row_mask:0xf bank_mask:0xf\n"
"v_mul_f32_dpp v225, v254, v225 quad_perm:[1,1,1,1] row_mask:0xf bank_mask:0xf\n"
"v_mul_f32_dpp v226, v254, v226 quad_perm:[2,2,2,2] row_mask:0xf bank_mask:0xf\n"
"v_mfma_f32_16x16x32_bf16 v[220:223], a[64:67], a[160:163], v[220:223]\n"
"v_mul_f32_dpp v227, v254, v227 quad_perm:[3,3,3,3] row_mask:0xf bank_mask:0xf\n"
"v_mul_f32_dpp v228, v254, v228 quad_perm:[0,0,0,0] row_mask:0xf bank_mask:0xf\n"
"v_mfma_f32_16x16x32_bf16 v[220:223], a[68:71], a[164:167], v[220:223]\n"
"v_mul_f32_dpp v229, v254, v229 quad_perm:[1,1,1,1] row_mask:0xf bank_mask:0xf\n"
"v_mul_f32_dpp v230, v254, v230 quad_perm:[2,2,2,2] row_mask:0xf bank_mask:0xf\n"
"v_mfma_f32_16x16x32_bf16 v[220:223], a[72:75], a[168:171], v[220:223]\n"
"v_mul_f32_dpp v231, v254, v231 quad_perm:[3,3,3,3] row_mask:0xf bank_mask:0xf\n"
"v_mul_f32_dpp v232, v254, v232 quad_perm:[0,0,0,0] row_mask:0xf bank_mask:0xf\n"
"v_mfma_f32_16x16x32_bf16 v[220:223], a[76:79], a[172:175], v[220:223]\n"
"v_mul_f32_dpp v233, v254, v233 quad_perm:[1,1,1,1] row_mask:0xf bank_mask:0xf\n"
"v_mul_f32_dpp v234, v254, v234 quad_perm:[2,2,2,2] row_mask:0xf bank_mask:0xf\n"
"v_mfma_f32_16x16x32_bf16 v[224:227], a[76:79], a[188:191], v[224:227]\n"
"v_mul_f32_dpp v235, v254, v235 quad_perm:[3,3,3,3] row_mask:0xf bank_mask:0xf\n"
"v_sub_f32 v116, v116, v157\n"
"v_mfma_f32_16x16x32_bf16 v[224:227], a[72:75], a[184:187], v[224:227]\n"
"v_exp_f32 v114, v114\n"
"v_mfma_f32_16x16x32_bf16 v[224:227], a[68:71], a[180:183], v[224:227]\n"
"v_exp_f32 v115, v115\n"
"v_mfma_f32_16x16x32_bf16 v[224:227], a[64:67], a[176:179], v[224:227]\n"
"v_sub_f32 v117, v117, v157\n"
"v_sub_f32 v118, v118, v157\n"
"v_mfma_f32_16x16x32_bf16 v[252:255], a[252:255], v[96:99], v[120:123]\n"
"v_sub_f32 v119, v119, v157\n"
"v_cvt_pk_bf16_f32 v106, v108, v109\n"
"v_mfma_f32_16x16x32_bf16 v[228:231], a[64:67], a[192:195], v[228:231]\n"
"v_cvt_pk_bf16_f32 v107, v110, v111\n"
"v_cvt_pk_bf16_f32 v112, v112, v113\n"
"v_mfma_f32_16x16x32_bf16 v[228:231], a[68:71], a[196:199], v[228:231]\n"
"v_exp_f32 v116, v116\n"
"v_mfma_f32_16x16x32_bf16 v[228:231], a[72:75], a[200:203], v[228:231]\n"
"v_exp_f32 v117, v117\n"
"v_mfma_f32_16x16x32_bf16 v[228:231], a[76:79], a[204:207], v[228:231]\n"
"v_exp_f32 v118, v118\n"
"v_mfma_f32_16x16x32_bf16 v[252:255], a[252:255], v[104:107], v[252:255]\n"
"v_exp_f32 v119, v119\n"
"v_mfma_f32_16x16x32_bf16 v[232:235], a[76:79], a[220:223], v[232:235]\n"
"v_cvt_pk_bf16_f32 v113, v114, v115\n"
"v_cvt_pk_bf16_f32 v114, v116, v117\n"
"v_mfma_f32_16x16x32_bf16 v[232:235], a[72:75], a[216:219], v[232:235]\n"
"v_cvt_pk_bf16_f32 v115, v118, v119\n"
"v_cvt_pk_bf16_f32 v204, v204, v208\n"
"v_mfma_f32_16x16x32_bf16 v[232:235], a[68:71], a[212:215], v[232:235]\n"
"v_cvt_pk_bf16_f32 v206, v206, v210\n"
"v_cvt_pk_bf16_f32 v208, v205, v209\n"
"v_mfma_f32_16x16x32_bf16 v[232:235], a[64:67], a[208:211], v[232:235]\n"
"v_cvt_pk_bf16_f32 v210, v207, v211\n"
"v_cvt_pk_bf16_f32 v205, v212, v216\n"
"v_mfma_f32_16x16x32_bf16 v[252:255], a[252:255], v[112:115], v[252:255]\n"
"v_cvt_pk_bf16_f32 v209, v213, v217\n"
"v_cvt_pk_bf16_f32 v207, v214, v218\n"
"v_cvt_pk_bf16_f32 v211, v215, v219\n"
"v_pk_max_i16 v204, v204, 0\n"
"v_pk_max_i16 v205, v205, 0\n"
"v_pk_max_i16 v208, v208, 0\n"
"v_pk_max_i16 v209, v209, 0\n"
"v_pk_max_i16 v206, v206, 0\n"
"v_pk_max_i16 v207, v207, 0\n"
"v_pk_max_i16 v210, v210, 0\n"
"v_pk_max_i16 v211, v211, 0\n"
"v_add_f32 v252, v252, v253\n"
"v_rcp_f32 v253, v252\n"
"v_cvt_pk_bf16_f32 v220, v220, v224\n"
"v_cvt_pk_bf16_f32 v222, v222, v226\n"
"v_cvt_pk_bf16_f32 v224, v221, v225\n"
"v_cvt_pk_bf16_f32 v226, v223, v227\n"
"v_cvt_pk_bf16_f32 v221, v228, v232\n"
"v_cvt_pk_bf16_f32 v225, v229, v233\n"
"v_cvt_pk_bf16_f32 v223, v230, v234\n"
"v_cvt_pk_bf16_f32 v227, v231, v235\n"
"v_pk_max_i16 v220, v220, 0\n"
"v_pk_max_i16 v221, v221, 0\n"
"v_pk_max_i16 v224, v224, 0\n"
"v_pk_max_i16 v225, v225, 0\n"
"v_pk_max_i16 v222, v222, 0\n"
"v_pk_max_i16 v223, v223, 0\n"
"v_pk_max_i16 v226, v226, 0\n"
"v_pk_max_i16 v227, v227, 0\n"
"ds_bpermute_b32 v254, v160, v253 offset:0\n"
"buffer_store_dwordx2 v[204:205], v162, s[84:87], s67 offen offset:0 sc0 sc1\n"
"buffer_store_dwordx2 v[208:209], v162, s[84:87], s67 offen offset:256 sc0 sc1\n"
"buffer_store_dwordx2 v[206:207], v162, s[84:87], s67 offen offset:512 sc0 sc1\n"
"buffer_store_dwordx2 v[210:211], v162, s[84:87], s67 offen offset:768 sc0 sc1\n"
"s_nop 1\n"
"v_mfma_f32_16x16x4_4b_bf16 v[204:219], v[122:123], a[244:245], 0 cbsz:2 abid:0\n"
"buffer_store_dwordx2 v[220:221], v162, s[84:87], s68 offen offset:0 sc0 sc1\n"
"buffer_store_dwordx2 v[224:225], v162, s[84:87], s68 offen offset:256 sc0 sc1\n"
"s_nop 5\n"
"v_mfma_f32_16x16x32_bf16 v[204:207], v[96:99], v[48:51], v[204:207]\n"
"s_nop 1\n"
"v_mfma_f32_16x16x32_bf16 v[204:207], v[104:107], v[52:55], v[204:207]\n"
"v_mfma_f32_16x16x32_bf16 v[204:207], v[112:115], v[56:59], v[204:207]\n"
"buffer_store_dwordx2 v[222:223], v162, s[84:87], s68 offen offset:512 sc0 sc1\n"
"v_mfma_f32_16x16x32_bf16 v[208:211], v[112:115], v[68:71], v[208:211]\n"
"v_mfma_f32_16x16x32_bf16 v[208:211], v[104:107], v[64:67], v[208:211]\n"
"v_mfma_f32_16x16x32_bf16 v[208:211], v[96:99], v[60:63], v[208:211]\n"
"buffer_store_dwordx2 v[226:227], v162, s[84:87], s68 offen offset:768 sc0 sc1\n"
"v_mfma_f32_16x16x32_bf16 v[212:215], v[96:99], v[72:75], v[212:215]\n"
"s_waitcnt lgkmcnt(0)\n"
"v_mul_f32_dpp v204, v254, v204 quad_perm:[0,0,0,0] row_mask:0xf bank_mask:0xf\n"
"v_mul_f32_dpp v205, v254, v205 quad_perm:[1,1,1,1] row_mask:0xf bank_mask:0xf\n"
"v_mfma_f32_16x16x32_bf16 v[212:215], v[104:107], v[76:79], v[212:215]\n"
"v_mul_f32_dpp v206, v254, v206 quad_perm:[2,2,2,2] row_mask:0xf bank_mask:0xf\n"
"v_mul_f32_dpp v207, v254, v207 quad_perm:[3,3,3,3] row_mask:0xf bank_mask:0xf\n"
"v_mfma_f32_16x16x32_bf16 v[212:215], v[112:115], v[80:83], v[212:215]\n"
"v_mul_f32_dpp v208, v254, v208 quad_perm:[0,0,0,0] row_mask:0xf bank_mask:0xf\n"
"v_mul_f32_dpp v209, v254, v209 quad_perm:[1,1,1,1] row_mask:0xf bank_mask:0xf\n"
"v_mfma_f32_16x16x32_bf16 v[216:219], v[112:115], v[92:95], v[216:219]\n"
"v_mul_f32_dpp v210, v254, v210 quad_perm:[2,2,2,2] row_mask:0xf bank_mask:0xf\n"
"v_mul_f32_dpp v211, v254, v211 quad_perm:[3,3,3,3] row_mask:0xf bank_mask:0xf\n"
"v_mfma_f32_16x16x32_bf16 v[216:219], v[104:107], v[88:91], v[216:219]\n"
"v_mfma_f32_16x16x32_bf16 v[216:219], v[96:99], v[84:87], v[216:219]\n"
"v_mul_f32_dpp v212, v254, v212 quad_perm:[0,0,0,0] row_mask:0xf bank_mask:0xf\n"
"v_mul_f32_dpp v213, v254, v213 quad_perm:[1,1,1,1] row_mask:0xf bank_mask:0xf\n"
"v_mfma_f32_16x16x32_bf16 v[204:207], a[80:83], a[160:163], v[204:207]\n"
"v_mul_f32_dpp v214, v254, v214 quad_perm:[2,2,2,2] row_mask:0xf bank_mask:0xf\n"
"v_mul_f32_dpp v215, v254, v215 quad_perm:[3,3,3,3] row_mask:0xf bank_mask:0xf\n"
"v_mfma_f32_16x16x32_bf16 v[204:207], a[84:87], a[164:167], v[204:207]\n"
"v_mfma_f32_16x16x32_bf16 v[204:207], a[88:91], a[168:171], v[204:207]\n"
"v_mul_f32_dpp v216, v254, v216 quad_perm:[0,0,0,0] row_mask:0xf bank_mask:0xf\n"
"v_mul_f32_dpp v217, v254, v217 quad_perm:[1,1,1,1] row_mask:0xf bank_mask:0xf\n"
"v_mfma_f32_16x16x32_bf16 v[204:207], a[92:95], a[172:175], v[204:207]\n"
"v_mul_f32_dpp v218, v254, v218 quad_perm:[2,2,2,2] row_mask:0xf bank_mask:0xf\n"
"v_mul_f32_dpp v219, v254, v219 quad_perm:[3,3,3,3] row_mask:0xf bank_mask:0xf\n"
"v_mfma_f32_16x16x32_bf16 v[208:211], a[92:95], a[188:191], v[208:211]\n"
"v_mfma_f32_16x16x32_bf16 v[208:211], a[88:91], a[184:187], v[208:211]\n"
"v_mfma_f32_16x16x32_bf16 v[208:211], a[84:87], a[180:183], v[208:211]\n"
"v_mfma_f32_16x16x32_bf16 v[208:211], a[80:83], a[176:179], v[208:211]\n"
"v_mfma_f32_16x16x32_bf16 v[212:215], a[80:83], a[192:195], v[212:215]\n"
"v_mfma_f32_16x16x32_bf16 v[212:215], a[84:87], a[196:199], v[212:215]\n"
"v_mfma_f32_16x16x32_bf16 v[212:215], a[88:91], a[200:203], v[212:215]\n"
"v_cvt_pk_bf16_f32 v204, v204, v208\n"
"v_cvt_pk_bf16_f32 v206, v206, v210\n"
"v_mfma_f32_16x16x32_bf16 v[212:215], a[92:95], a[204:207], v[212:215]\n"
"v_cvt_pk_bf16_f32 v208, v205, v209\n"
"v_cvt_pk_bf16_f32 v210, v207, v211\n"
"v_mfma_f32_16x16x32_bf16 v[216:219], a[92:95], a[220:223], v[216:219]\n"
"v_pk_max_i16 v204, v204, 0\n"
"v_pk_max_i16 v208, v208, 0\n"
"v_mfma_f32_16x16x32_bf16 v[216:219], a[88:91], a[216:219], v[216:219]\n"
"v_pk_max_i16 v206, v206, 0\n"
"v_pk_max_i16 v210, v210, 0\n"
"v_mfma_f32_16x16x32_bf16 v[216:219], a[84:87], a[212:215], v[216:219]\n"
"v_mfma_f32_16x16x32_bf16 v[216:219], a[80:83], a[208:211], v[216:219]\n"
"s_nop 7\n"
"v_cvt_pk_bf16_f32 v205, v212, v216\n"
"v_cvt_pk_bf16_f32 v209, v213, v217\n"
"v_cvt_pk_bf16_f32 v207, v214, v218\n"
"v_cvt_pk_bf16_f32 v211, v215, v219\n"
"v_pk_max_i16 v205, v205, 0\n"
"v_pk_max_i16 v209, v209, 0\n"
"v_pk_max_i16 v207, v207, 0\n"
"v_pk_max_i16 v211, v211, 0\n"
"buffer_store_dwordx2 v[204:205], v162, s[84:87], s69 offen offset:0 sc0 sc1\n"
"buffer_store_dwordx2 v[208:209], v162, s[84:87], s69 offen offset:256 sc0 sc1\n"
"buffer_store_dwordx2 v[206:207], v162, s[84:87], s69 offen offset:512 sc0 sc1\n"
"buffer_store_dwordx2 v[210:211], v162, s[84:87], s69 offen offset:768 sc0 sc1\n"
".Lend%=:\n"
    :
    : [wave] "s"(wave), [lds] "s"(lds), [ka] "s"(ka), [bid] "s"(bid)
    : "v0","v1","v2","v3","v4","v5","v6","v7","v8","v9","v10","v11","v12","v13","v14","v15","v16","v17","v18","v19","v20","v21","v22","v23","v24","v25","v26","v27","v28","v29","v30","v31","v32","v33","v34","v35","v36","v37","v38","v39","v40","v41","v42","v43","v44","v45","v46","v47","v48","v49","v50","v51","v52","v53","v54","v55","v56","v57","v58","v59","v60","v61","v62","v63","v64","v65","v66","v67","v68","v69","v70","v71","v72","v73","v74","v75","v76","v77","v78","v79","v80","v81","v82","v83","v84","v85","v86","v87","v88","v89","v90","v91","v92","v93","v94","v95","v96","v97","v98","v99","v100","v101","v102","v103","v104","v105","v106","v107","v108","v109","v110","v111","v112","v113","v114","v115","v116","v117","v118","v119","v120","v121","v122","v123","v124","v125","v126","v127","v128","v129","v130","v131","v132","v133","v134","v135","v136","v137","v138","v139","v140","v141","v142","v143","v144","v145","v146","v147","v148","v149","v150","v151","v152","v153","v154","v155","v156","v157","v158","v159","v160","v161","v162","v163","v164","v165","v166","v167","v168","v169","v170","v171","v172","v173","v174","v175","v176","v177","v178","v179","v180","v181","v182","v183","v184","v185","v186","v187","v188","v189","v190","v191","v192","v193","v194","v195","v196","v197","v198","v199","v200","v201","v202","v203","v204","v205","v206","v207","v208","v209","v210","v211","v212","v213","v214","v215","v216","v217","v218","v219","v220","v221","v222","v223","v224","v225","v226","v227","v228","v229","v230","v231","v232","v233","v234","v235","v236","v237","v238","v239","v240","v241","v242","v243","v244","v245","v246","v247","v248","v249","v250","v251","v252","v253","v254","v255","a0","a1","a2","a3","a4","a5","a6","a7","a8","a9","a10","a11","a12","a13","a14","a15","a16","a17","a18","a19","a20","a21","a22","a23","a24","a25","a26","a27","a28","a29","a30","a31","a32","a33","a34","a35","a36","a37","a38","a39","a40","a41","a42","a43","a44","a45","a46","a47","a48","a49","a50","a51","a52","a53","a54","a55","a56","a57","a58","a59","a60","a61","a62","a63","a64","a65","a66","a67","a68","a69","a70","a71","a72","a73","a74","a75","a76","a77","a78","a79","a80","a81","a82","a83","a84","a85","a86","a87","a88","a89","a90","a91","a92","a93","a94","a95","a96","a97","a98","a99","a100","a101","a102","a103","a104","a105","a106","a107","a108","a109","a110","a111","a112","a113","a114","a115","a116","a117","a118","a119","a120","a121","a122","a123","a124","a125","a126","a127","a128","a129","a130","a131","a132","a133","a134","a135","a136","a137","a138","a139","a140","a141","a142","a143","a144","a145","a146","a147","a148","a149","a150","a151","a152","a153","a154","a155","a156","a157","a158","a159","a160","a161","a162","a163","a164","a165","a166","a167","a168","a169","a170","a171","a172","a173","a174","a175","a176","a177","a178","a179","a180","a181","a182","a183","a184","a185","a186","a187","a188","a189","a190","a191","a192","a193","a194","a195","a196","a197","a198","a199","a200","a201","a202","a203","a204","a205","a206","a207","a208","a209","a210","a211","a212","a213","a214","a215","a216","a217","a218","a219","a220","a221","a222","a223","a224","a225","a226","a227","a228","a229","a230","a231","a232","a233","a234","a235","a236","a237","a238","a239","a240","a241","a242","a243","a244","a245","a246","a247","a248","a249","a250","a251","a252","a253","a254","a255","s8","s9","s10","s11","s12","s13","s14","s15","s16","s17","s18","s19","s20","s21","s22","s23","s24","s25","s26","s27","s28","s29","s40","s41","s42","s43","s44","s45","s46","s47","s48","s49","s50","s51","s52","s53","s54","s55","s56","s57","s58","s59","s60","s61","s62","s63","s64","s65","s66","s67","s68","s69","s70","s71","s72","s73","s74","s75","s76","s77","s78","s79","s80","s81","s82","s83","s84","s85","s86","s87","s88","s89","s90","s91","s92","s93","s94","s95","scc","vcc","memory");
  STAMP_END();
}

extern "C" void kernel_launch(void* const* d_in, const int* in_sizes, int n_in, void* d_out, int out_size, void* d_ws,
                              size_t ws_size, hipStream_t stream) {
  autoint_r_kernel<<<256, 256, 0, stream>>>((const char*)d_in[0], (const char*)d_in[1], (const char*)d_in[2],
                                            (const char*)d_in[3], (const char*)d_in[4], (char*)d_out, (char*)d_ws);
}
```

```cpp
#include <hip/hip_runtime.h>
#include <stdint.h>

#define STAMP_BEGIN()
#define STAMP_END()

__global__ __launch_bounds__(256, 1)
void autoint_r_kernel(const char* __restrict__ e, const char* __restrict__ wq, const char* __restrict__ wk,
                      const char* __restrict__ wv, const char* __restrict__ wres, char* __restrict__ out,
                      char* __restrict__ ws) {
  __shared__ __attribute__((aligned(1024))) uint8_t lds_raw[163840];
  STAMP_BEGIN();
  const uint32_t wave = __builtin_amdgcn_readfirstlane(threadIdx.x >> 6);
  const uint32_t lds = (uint32_t)(uintptr_t)(__attribute__((address_space(3))) uint8_t*)lds_raw;
  const uint64_t ka = (uint64_t)(uintptr_t)__builtin_amdgcn_kernarg_segment_ptr();
  const uint32_t bid = blockIdx.x;
  asm volatile(
"s_load_dwordx8 s[8:15], %[ka], 0x0\n"
"s_load_dwordx4 s[16:19], %[ka], 0x20\n"
"s_lshl_b32 s88, %[wave], 3\n"
"s_load_dwordx2 s[22:23], %[ka], s88 offset:0x8\n"
"v_mbcnt_lo_u32_b32 v239, -1, 0\n"
"v_mbcnt_hi_u32_b32 v239, -1, v239\n"
"s_getpc_b64 s[76:77]\n"
".Lpc0%=:\n"
"s_bfe_u32 s89, %[bid], 0x40003\n"
"s_lshl_b32 s89, s89, 2\n"
"s_add_u32 s89, s89, %[wave]\n"
"s_lshl_b32 s89, s89, 9\n"
"v_lshl_add_u32 v254, v239, 7, s89\n"
"s_mov_b64 exec, 0xf\n"
"global_load_dword v253, v254, s[76:77] offset:128\n"
"s_mov_b64 exec, -1\n"
"v_lshrrev_b32 v249, 4, v239\n"
"s_lshl_b32 s74, %[wave], 12\n"
"s_bfe_u32 s92, %[bid], 0x10003\n"
"s_lshl_b32 s93, s92, 14\n"
"s_add_u32 s93, s93, s74\n"
"v_lshlrev_b32 v240, 1, v249\n"
"v_xor_b32 v240, v240, v239\n"
"v_and_b32 v240, 7, v240\n"
"v_lshlrev_b32 v240, 4, v240\n"
"v_bfe_u32 v241, v239, 3, 2\n"
"v_lshrrev_b32 v242, 5, v239\n"
"v_lshl_add_u32 v241, v242, 3, v241\n"
"v_lshl_add_u32 v44, v241, 7, v240\n"
"v_add_u32 v44, s93, v44\n"
"s_bfe_u32 s88, %[bid], 0x50003\n"
"s_lshl_b32 s88, s88, 10\n"
"v_lshl_add_u32 v255, v239, 7, s88\n"
"s_and_b32 s90, %[bid], 7\n"
"s_lshl_b32 s90, s90, 4\n"
"s_lshr_b32 s91, %[bid], 4\n"
"s_add_u32 s90, s90, s91\n"
"s_lshl_b32 s90, s90, 2\n"
"s_add_u32 s90, s90, %[wave]\n"
"s_mul_i32 s91, s90, 25600\n"
"v_xor_b32 v152, v249, v239\n"
"v_lshlrev_b32 v152, 4, v152\n"
"s_mul_i32 s28, %[wave], 13056\n"
"s_add_u32 s28, s28, 66304\n"
"s_mul_i32 s29, %[wave], 10752\n"
"s_add_u32 s29, s29, 118528\n"
"s_mul_i32 s75, %[wave], 4192\n"
"s_mov_b32 s40, 192\n"
"s_mov_b32 s41, 128\n"
"s_mov_b32 s42, 64\n"
"s_mov_b32 s43, 0\n"
"s_mov_b32 s26, 104857600\n"
"s_mov_b32 s27, 0x20000\n"
"s_waitcnt lgkmcnt(0)\n"
"s_add_u32 m0, s74, 33152\n"
"s_nop 0\n"
"global_load_lds_dwordx4 v44, s[12:13] offset:0\n"
"global_load_lds_dwordx4 v44, s[12:13] offset:2048\n"
"s_add_u32 s8, s8, s91\n"
"s_addc_u32 s9, s9, 0\n"
"s_sub_u32 s24, s8, 192\n"
"s_subb_u32 s25, s9, 0\n"
"s_and_b32 s25, s25, 0xffff\n"
"s_mov_b32 s44, 4288\n"
"s_mov_b32 s45, 4224\n"
"s_mov_b32 s46, 4160\n"
"s_mov_b32 s47, 4096\n"
"s_mov_b32 s48, 8384\n"
"s_mov_b32 s49, 8320\n"
"s_mov_b32 s50, 8256\n"
"s_add_u32 m0, s74, 33664\n"
"s_nop 0\n"
"global_load_lds_dwordx4 v44, s[12:13] offset:512\n"
"s_mov_b32 s51, 8192\n"
"s_mov_b32 s52, 12480\n"
"s_mov_b32 s53, 12416\n"
"s_mov_b32 s54, 12352\n"
"s_mov_b32 s55, 12288\n"
"s_mov_b32 s56, 16576\n"
"s_mov_b32 s57, 16512\n"
"s_mov_b32 s58, 16448\n"
"s_mov_b32 s59, 16384\n"
"s_mov_b32 s60, 20672\n"
"s_mov_b32 s61, 20608\n"
"s_mov_b32 s62, 20544\n"
"global_load_lds_dwordx4 v44, s[12:13] offset:2560\n"
"s_mov_b32 s63, 20480\n"
"v_and_b32 v248, 15, v239\n"
"v_and_b32 v232, 3, v248\n"
"v_lshrrev_b32 v251, 2, v248\n"
"s_lshl_b32 s88, %[wave], 13\n"
"s_lshl_b32 s92, s92, 7\n"
"s_add_u32 s88, s88, s92\n"
"v_lshl_add_u32 v135, v241, 8, v240\n"
"v_add_u32 v135, s88, v135\n"
"v_add_u32 v252, 0x1000, v135\n"
"v_lshlrev_b32 v153, 4, v239\n"
"s_mov_b32 s65, 4096\n"
"s_mov_b64 exec, 0xff\n"
"global_load_dword v255, v255, s[22:23]\n"
"s_mov_b64 exec, -1\n"
"s_mov_b32 s66, 8192\n"
"s_mov_b32 s67, 12288\n"
"s_mov_b32 s68, 16384\n"
"s_mov_b32 s69, 20480\n"
"s_mov_b32 s70, 24576\n"
"v_bfe_u32 v240, v239, 2, 3\n"
"v_lshl_add_u32 v240, v242, 4, v240\n"
"v_lshlrev_b32 v240, 7, v240\n"
"v_lshl_add_u32 v240, v232, 3, v240\n"
"v_bfe_u32 v241, v239, 3, 2\n"
"v_xor_b32 v243, 0, v241\n"
"v_lshl_add_u32 v40, v243, 5, v240\n"
"s_add_u32 m0, s29, 0\n"
"s_mov_b32 s94, 24768\n"
"buffer_load_dwordx4 v152, s[24:27], s94 offen offset:0 lds\n"
"v_add_u32 v32, s74, v40\n"
"v_add_u32 v36, s75, v40\n"
"v_xor_b32 v243, 1, v241\n"
"v_lshl_add_u32 v41, v243, 5, v240\n"
"v_add_u32 v33, s74, v41\n"
"v_add_u32 v37, s75, v41\n"
"v_xor_b32 v243, 2, v241\n"
"v_lshl_add_u32 v42, v243, 5, v240\n"
"v_add_u32 v34, s74, v42\n"
"v_add_u32 v38, s75, v42\n"
"v_xor_b32 v243, 3, v241\n"
"v_lshl_add_u32 v43, v243, 5, v240\n"
"s_add_u32 m0, s74, 0\n"
"s_nop 0\n"
"global_load_lds_dwordx4 v44, s[10:11] offset:0\n"
"v_add_u32 v35, s74, v43\n"
"v_add_u32 v39, s75, v43\n"
"v_add_u32 v45, s74, v153\n"
"v_mul_u32_u24 v240, 1056, v232\n"
"v_lshl_add_u32 v240, v251, 4, v240\n"
"v_lshl_add_u32 v240, v249, 8, v240\n"
"v_add_u32 v46, s75, v240\n"
"v_mul_u32_u24 v240, 1088, v251\n"
"v_lshl_add_u32 v240, v232, 8, v240\n"
"v_xor_b32 v241, v249, v232\n"
"v_lshl_add_u32 v240, v241, 4, v240\n"
"v_add_u32 v154, s28, v240\n"
"s_add_u32 m0, s29, 0\n"
"s_mov_b32 s94, 13130880\n"
"buffer_load_dwordx4 v152, s[24:27], s94 offen offset:1088 lds\n"
"v_add_u32 v47, s29, v240\n"
"v_lshlrev_b32 v243, 6, v249\n"
"v_lshl_add_u32 v132, v249, 9, s29\n"
"v_lshl_add_u32 v132, v248, 4, v132\n"
"v_min_u32 v240, 1, v251\n"
"v_lshl_add_u32 v161, v240, 20, v132\n"
"v_lshl_add_u32 v158, v249, 10, s29\n"
"v_bfe_u32 v240, v239, 1, 2\n"
"v_lshl_add_u32 v158, v240, 4, v158\n"
"v_and_b32 v240, 1, v239\n"
"v_lshl_add_u32 v158, v240, 2, v158\n"
"v_bfe_u32 v240, v239, 3, 1\n"
"s_add_u32 m0, s74, 0\n"
"s_nop 0\n"
"global_load_lds_dwordx4 v44, s[10:11] offset:2048\n"
"v_lshl_add_u32 v158, v240, 20, v158\n"
"s_add_u32 s89, s29, 4096\n"
"v_lshl_add_u32 v133, v232, 4, v243\n"
"v_lshl_add_u32 v133, v251, 9, v133\n"
"v_add_u32 v133, s89, v133\n"
"v_lshl_add_u32 v156, v249, 7, s89\n"
"v_lshl_add_u32 v156, v251, 4, v156\n"
"v_and_b32 v240, 1, v239\n"
"v_lshl_add_u32 v156, v240, 3, v156\n"
"v_bfe_u32 v241, v239, 1, 1\n"
"v_lshl_add_u32 v156, v241, 20, v156\n"
"s_add_u32 s89, s74, 33152\n"
"s_add_u32 m0, s29, 0\n"
"s_mov_b32 s94, 26236992\n"
"buffer_load_dwordx4 v152, s[24:27], s94 offen offset:2176 lds\n"
"v_lshl_add_u32 v134, v249, 9, s89\n"
"v_lshl_add_u32 v134, v248, 3, v134\n"
"v_lshl_add_u32 v163, v239, 3, s89\n"
"s_add_u32 s89, s29, 8192\n"
"v_lshl_add_u32 v244, v240, 1, s89\n"
"v_lshl_add_u32 v244, v241, 4, v244\n"
"v_add_u32 v244, v243, v244\n"
"v_bfe_u32 v245, v239, 2, 1\n"
"v_lshl_add_u32 v244, v245, 3, v244\n"
"v_bfe_u32 v245, v239, 3, 1\n"
"v_lshl_add_u32 v244, v245, 8, v244\n"
"v_accvgpr_write_b32 a240, v244\n"
"s_add_u32 m0, s74, 512\n"
"s_nop 0\n"
"global_load_lds_dwordx4 v44, s[10:11] offset:512\n"
"v_add_u32 v244, s89, v153\n"
"v_accvgpr_write_b32 a241, v244\n"
"v_lshl_add_u32 v244, v248, 4, s89\n"
"v_add_u32 v244, 2048, v244\n"
"v_min_u32 v245, 1, v232\n"
"v_lshl_add_u32 v244, v245, 20, v244\n"
"v_accvgpr_write_b32 a242, v244\n"
"v_lshl_add_u32 v244, v239, 3, s89\n"
"v_mov_b32 v246, 0\n"
"v_mov_b32 v247, 0\n"
"ds_write_b64 v244, v[246:247] offset:1024\n"
"v_lshlrev_b32 v162, 3, v248\n"
"s_add_u32 m0, s29, 0\n"
"s_mov_b32 s94, 39343104\n"
"buffer_load_dwordx4 v152, s[24:27], s94 offen offset:3264 lds\n"
"v_lshl_add_u32 v155, v249, 8, v162\n"
"v_lshl_add_u32 v162, v249, 10, v162\n"
"v_lshl_add_u32 v160, v249, 2, v232\n"
"v_lshlrev_b32 v160, 2, v160\n"
"v_mul_u32_u24 v157, 0xaaaa, v240\n"
"v_add_u32 v157, 0x4444, v157\n"
"v_mov_b32 v159, 0\n"
"v_mov_b32 v243, 0x3f803f80\n"
"v_xor_b32 v242, v242, v241\n"
"v_subrev_u32 v242, 1, v242\n"
"v_and_b32 v242, v243, v242\n"
"v_accvgpr_write_b32 a248, v243\n"
"v_accvgpr_write_b32 a249, v243\n"
"v_accvgpr_write_b32 a250, v243\n"
"v_accvgpr_write_b32 a251, v243\n"
"v_accvgpr_write_b32 a252, v242\n"
"v_accvgpr_write_b32 a253, v242\n"
"v_accvgpr_write_b32 a254, v242\n"
"v_accvgpr_write_b32 a255, v242\n"
"s_mov_b32 s72, 0x3e38aa3b\n"
"s_getpc_b64 s[80:81]\n"
".Lpc3%=:\n"
"s_add_u32 s80, s80, .Lp2f%=-.Lpc3%=\n"
"s_addc_u32 s81, s81, 0\n"
"s_and_b32 s81, s81, 0xffff\n"
"s_mov_b32 s82, .Lcend%=-.Lp2f%=\n"
"s_mov_b32 s83, 0x20000\n"
"s_add_u32 s91, s91, s92\n"
"s_add_u32 s18, s18, s91\n"
"s_addc_u32 s19, s19, 0\n"
"s_sub_u32 s84, s18, 13107200\n"
"s_subb_u32 s85, s19, 0\n"
"s_and_b32 s85, s85, 0xffff\n"
"s_mov_b32 s86, 25472\n"
"s_mov_b32 s87, 0x20000\n"
"s_bfe_u32 s90, %[bid], 0x20004\n"
"s_lshl_b32 s90, s90, 12\n"
"s_bfe_u32 s91, %[bid], 0x10003\n"
"s_lshl_b32 s91, s91, 9\n"
"s_lshl_b32 s88, %[wave], 7\n"
"s_add_u32 s88, s88, s90\n"
"s_add_u32 s88, s88, s91\n"
"s_waitcnt lgkmcnt(0)\n"
".Lhead%=:\n"
"s_mov_b64 vcc, -1\n"
"s_cmp_eq_u32 %[wave], 0\n"
"s_cbranch_scc1 .Lpad0_%=\n"
"s_cmp_eq_u32 %[wave], 1\n"
"s_cbranch_scc1 .Lpad1_%=\n"
"s_cmp_eq_u32 %[wave], 2\n"
"s_cbranch_scc1 .Lpad3_%=\n"
"s_cmp_eq_u32 %[wave], 3\n"
"s_cbranch_scc1 .Lpad4_%=\n"
".Lback0_%=:\n"
"s_mov_b64 vcc, 0\n"
"s_add_u32 m0, s74, 512\n"
"s_nop 0\n"
"global_load_lds_dwordx4 v44, s[10:11] offset:2560\n"
"s_add_u32 m0, s75, 16384\n"
"s_nop 0\n"
"global_load_lds_dwordx4 v44, s[14:15] offset:0\n"
"s_waitcnt vmcnt(10)\n"
"s_barrier\n"
"ds_read_b64_tr_b16 a[96:97], v40 offset:33152\n"
"ds_read_b64_tr_b16 a[98:99], v40 offset:34176\n"
"ds_read_b64_tr_b16 a[100:101], v40 offset:37248\n"
"ds_read_b64_tr_b16 a[102:103], v40 offset:38272\n"
"ds_read_b64_tr_b16 a[104:105], v40 offset:41344\n"
"ds_read_b64_tr_b16 a[106:107], v40 offset:42368\n"
"ds_read_b64_tr_b16 a[108:109], v40 offset:45440\n"
"ds_read_b64_tr_b16 a[110:111], v40 offset:46464\n"
"global_load_lds_dwordx4 v44, s[14:15] offset:2048\n"
"ds_read_b64_tr_b16 a[112:113], v41 offset:33152\n"
"ds_read_b64_tr_b16 a[114:115], v41 offset:34176\n"
"ds_read_b64_tr_b16 a[116:117], v41 offset:37248\n"
"ds_read_b64_tr_b16 a[118:119], v41 offset:38272\n"
"ds_read_b64_tr_b16 a[120:121], v41 offset:41344\n"
"ds_read_b64_tr_b16 a[122:123], v41 offset:42368\n"
"ds_read_b64_tr_b16 a[124:125], v41 offset:45440\n"
"ds_read_b64_tr_b16 a[126:127], v41 offset:46464\n"
"ds_read_b64_tr_b16 a[128:129], v42 offset:33152\n"
"ds_read_b64_tr_b16 a[130:131], v42 offset:34176\n"
"ds_read_b64_tr_b16 a[132:133], v42 offset:37248\n"
"ds_read_b64_tr_b16 a[134:135], v42 offset:38272\n"
"ds_read_b64_tr_b16 a[136:137], v42 offset:41344\n"
"s_add_u32 m0, s75, 16896\n"
"s_nop 0\n"
"global_load_lds_dwordx4 v44, s[14:15] offset:512\n"
"ds_read_b64_tr_b16 a[138:139], v42 offset:42368\n"
"ds_read_b64_tr_b16 a[140:141], v42 offset:45440\n"
"ds_read_b64_tr_b16 a[142:143], v42 offset:46464\n"
"ds_read_b64_tr_b16 a[144:145], v43 offset:33152\n"
"ds_read_b64_tr_b16 a[146:147], v43 offset:34176\n"
"ds_read_b64_tr_b16 a[148:149], v43 offset:37248\n"
"ds_read_b64_tr_b16 a[150:151], v43 offset:38272\n"
"ds_read_b64_tr_b16 a[152:153], v43 offset:41344\n"
"ds_read_b64_tr_b16 a[154:155], v43 offset:42368\n"
"ds_read_b64_tr_b16 a[156:157], v43 offset:45440\n"
"ds_read_b64_tr_b16 a[158:159], v43 offset:46464\n"
"global_load_lds_dwordx4 v44, s[14:15] offset:2560\n"
"s_add_u32 m0, s28, 8704\n"
"s_mov_b32 s94, 52453568\n"
"buffer_load_dwordx4 v152, s[24:27], s94 offen offset:0 lds\n"
"s_mov_b32 s94, 65559680\n"
"buffer_load_dwordx4 v152, s[24:27], s94 offen offset:1088 lds\n"
"s_mov_b32 s94, 78665792\n"
"buffer_load_dwordx4 v152, s[24:27], s94 offen offset:2176 lds\n"
"s_mov_b32 s94, 91771904\n"
"buffer_load_dwordx4 v152, s[24:27], s94 offen offset:3264 lds\n"
"s_waitcnt vmcnt(9)\n"
"ds_read_b128 v[204:207], v47 offset:0\n"
"ds_read_b128 v[208:211], v47 offset:64\n"
"ds_read_b128 v[212:215], v47 offset:128\n"
"s_add_u32 m0, s28, 0\n"
"s_nop 0\n"
"buffer_load_dwordx4 v152, s[24:27], s40 offen offset:0 lds\n"
"ds_read_b128 v[216:219], v47 offset:192\n"
"s_waitcnt vmcnt(9)\n"
"ds_read_b64_tr_b16 v[112:113], v32 offset:0\n"
"ds_read_b64_tr_b16 v[114:115], v32 offset:1024\n"
"ds_read_b64_tr_b16 v[116:117], v33 offset:0\n"
"ds_read_b64_tr_b16 v[118:119], v33 offset:1024\n"
"ds_read_b64_tr_b16 v[120:121], v34 offset:0\n"
"ds_read_b64_tr_b16 v[122:123], v34 offset:1024\n"
"s_waitcnt lgkmcnt(10)\n"
"s_barrier\n"
"ds_read_b64_tr_b16 v[124:125], v35 offset:0\n"
"ds_read_b64_tr_b16 a[48:49], v40 offset:0\n"
"ds_read_b64_tr_b16 a[50:51], v40 offset:1024\n"
"ds_read_b64_tr_b16 a[52:53], v40 offset:4096\n"
"ds_read_b64_tr_b16 a[54:55], v40 offset:5120\n"
"ds_read_b64_tr_b16 a[56:57], v40 offset:8192\n"
"ds_read_b64_tr_b16 a[58:59], v40 offset:9216\n"
"s_waitcnt lgkmcnt(15)\n"
"v_mfma_f32_16x16x32_bf16 v[96:99], a[96:99], v[204:207], 0\n"
"ds_read_b64_tr_b16 a[60:61], v40 offset:12288\n"
"ds_read_b64_tr_b16 a[62:63], v40 offset:13312\n"
"v_mfma_f32_16x16x32_bf16 v[96:99], a[100:103], v[208:211], v[96:99]\n"
"s_waitcnt lgkmcnt(14)\n"
"ds_read_b64_tr_b16 a[64:65], v41 offset:0\n"
"v_mfma_f32_16x16x32_bf16 v[96:99], a[104:107], v[212:215], v[96:99]\n"
"ds_read_b64_tr_b16 a[66:67], v41 offset:1024\n"
"ds_read_b64_tr_b16 a[68:69], v41 offset:4096\n"
"v_mfma_f32_16x16x32_bf16 v[96:99], a[108:111], v[216:219], v[96:99]\n"
"ds_read_b64_tr_b16 a[70:71], v41 offset:5120\n"
"ds_read_b64_tr_b16 a[72:73], v41 offset:8192\n"
"v_mfma_f32_16x16x32_bf16 v[100:103], a[112:115], v[204:207], 0\n"
"ds_read_b64_tr_b16 a[74:75], v41 offset:9216\n"
"v_lshlrev_b32 v128, 16, v112\n"
"v_mfma_f32_16x16x32_bf16 v[100:103], a[116:119], v[208:211], v[100:103]\n"
"ds_read_b64_tr_b16 a[76:77], v41 offset:12288\n"
"ds_read_b64_tr_b16 a[78:79], v41 offset:13312\n"
"v_mfma_f32_16x16x32_bf16 v[100:103], a[120:123], v[212:215], v[100:103]\n"
"ds_read_b64_tr_b16 a[80:81], v42 offset:0\n"
"v_cvt_pk_bf16_f32 v96, v96, v97\n"
"v_mfma_f32_16x16x32_bf16 v[100:103], a[124:127], v[216:219], v[100:103]\n"
"ds_read_b64_tr_b16 a[82:83], v42 offset:1024\n"
"ds_read_b64_tr_b16 a[84:85], v42 offset:4096\n"
"v_mfma_f32_16x16x32_bf16 v[104:107], a[128:131], v[204:207], 0\n"
"ds_read_b64_tr_b16 a[86:87], v42 offset:5120\n"
"ds_read_b64_tr_b16 a[88:89], v42 offset:8192\n"
"v_mfma_f32_16x16x32_bf16 v[104:107], a[132:135], v[208:211], v[104:107]\n"
"ds_read_b64_tr_b16 a[90:91], v42 offset:9216\n"
"v_cvt_pk_bf16_f32 v97, v98, v99\n"
"v_mfma_f32_16x16x32_bf16 v[104:107], a[136:139], v[212:215], v[104:107]\n"
"ds_read_b64_tr_b16 a[92:93], v42 offset:12288\n"
"ds_read_b64_tr_b16 a[94:95], v42 offset:13312\n"
"v_mfma_f32_16x16x32_bf16 v[104:107], a[140:143], v[216:219], v[104:107]\n"
"ds_read_b64_tr_b16 a[224:225], v43 offset:0\n"
"v_cvt_pk_bf16_f32 v98, v100, v101\n"
"v_mfma_f32_16x16x32_bf16 v[108:111], a[144:147], v[204:207], 0\n"
"ds_read_b64_tr_b16 a[226:227], v43 offset:1024\n"
"ds_read_b64_tr_b16 a[228:229], v43 offset:4096\n"
"v_mfma_f32_16x16x32_bf16 v[108:111], a[148:151], v[208:211], v[108:111]\n"
"ds_read_b64_tr_b16 a[230:231], v43 offset:5120\n"
"ds_read_b64_tr_b16 a[232:233], v43 offset:8192\n"
"v_mfma_f32_16x16x32_bf16 v[108:111], a[152:155], v[212:215], v[108:111]\n"
"ds_read_b64_tr_b16 a[234:235], v43 offset:9216\n"
"v_cvt_pk_bf16_f32 v99, v102, v103\n"
"v_mfma_f32_16x16x32_bf16 v[108:111], a[156:159], v[216:219], v[108:111]\n"
"s_waitcnt lgkmcnt(14)\n"
"ds_write_b128 v132, v[96:99] offset:0\n"
"v_mfma_f32_16x16x32_bf16 v[136:139], a[48:51], v[204:207], 0\n"
"v_cvt_pk_bf16_f32 v104, v104, v105\n"
"v_cvt_pk_bf16_f32 v105, v106, v107\n"
"v_mfma_f32_16x16x32_bf16 v[136:139], a[52:55], v[208:211], v[136:139]\n"
"ds_read_b64_tr_b16 a[236:237], v43 offset:12288\n"
"v_cvt_pk_bf16_f32 v106, v108, v109\n"
"v_mfma_f32_16x16x32_bf16 v[136:139], a[56:59], v[212:215], v[136:139]\n"
"buffer_load_dwordx4 v152, s[24:27], s41 offen offset:1088 lds\n"
"ds_read_b64_tr_b16 a[238:239], v43 offset:13312\n"
"v_mfma_f32_16x16x32_bf16 v[136:139], a[60:63], v[216:219], v[136:139]\n"
"s_waitcnt vmcnt(6)\n"
"ds_read_b64_tr_b16 v[236:237], v36 offset:16384\n"
"ds_read_b64_tr_b16 v[238:239], v36 offset:17408\n"
"v_mfma_f32_16x16x32_bf16 v[140:143], a[64:67], v[204:207], 0\n"
"ds_read_b64_tr_b16 v[240:241], v37 offset:16384\n"
"ds_read_b64_tr_b16 v[242:243], v37 offset:17408\n"
"v_mfma_f32_16x16x32_bf16 v[140:143], a[68:71], v[208:211], v[140:143]\n"
"ds_read_b64_tr_b16 v[244:245], v38 offset:16384\n"
"v_cvt_pk_bf16_f32 v107, v110, v111\n"
"v_mfma_f32_16x16x32_bf16 v[140:143], a[72:75], v[212:215], v[140:143]\n"
"ds_read_b64_tr_b16 v[246:247], v38 offset:17408\n"
"ds_read_b64_tr_b16 v[248:249], v39 offset:16384\n"
"v_mfma_f32_16x16x32_bf16 v[140:143], a[76:79], v[216:219], v[140:143]\n"
"s_waitcnt lgkmcnt(12)\n"
"ds_write_b128 v132, v[104:107] offset:2048\n"
"v_mfma_f32_16x16x32_bf16 v[144:147], a[80:83], v[204:207], 0\n"
"v_mul_f32 v136, s72, v136\n"
"v_mul_f32 v137, s72, v137\n"
"v_mfma_f32_16x16x32_bf16 v[144:147], a[84:87], v[208:211], v[144:147]\n"
"v_mul_f32 v138, s72, v138\n"
"v_mul_f32 v139, s72, v139\n"
"v_mfma_f32_16x16x32_bf16 v[144:147], a[88:91], v[212:215], v[144:147]\n"
"ds_read_b64_tr_b16 v[250:251], v39 offset:17408\n"
"ds_read_b64_tr_b16 v[126:127], v35 offset:1024\n"
"v_mfma_f32_16x16x32_bf16 v[144:147], a[92:95], v[216:219], v[144:147]\n"
"v_mul_f32 v140, s72, v140\n"
"v_mul_f32 v141, s72, v141\n"
"v_mfma_f32_16x16x32_bf16 v[148:151], a[224:227], v[204:207], 0\n"
"buffer_load_dwordx4 v152, s[24:27], s42 offen offset:2176 lds\n"
"v_mul_f32 v142, s72, v142\n"
"v_mfma_f32_16x16x32_bf16 v[148:151], a[228:231], v[208:211], v[148:151]\n"
"s_waitcnt lgkmcnt(10)\n"
"v_mul_f32 v143, s72, v143\n"
"v_cvt_pk_bf16_f32 v136, v136, v137\n"
"v_mfma_f32_16x16x32_bf16 v[148:151], a[232:235], v[212:215], v[148:151]\n"
"v_cvt_pk_bf16_f32 v137, v140, v141\n"
"v_cvt_pk_bf16_f32 v138, v138, v139\n"
"v_mfma_f32_16x16x32_bf16 v[148:151], a[236:239], v[216:219], v[148:151]\n"
"s_waitcnt lgkmcnt(8)\n"
"ds_write_b128 v46, v[236:239] offset:16384\n"
"v_cvt_pk_bf16_f32 v139, v142, v143\n"
"ds_write_b128 v133, v[136:139] offset:0\n"
"v_mul_f32 v144, s72, v144\n"
"v_mul_f32 v145, s72, v145\n"
"v_mul_f32 v146, s72, v146\n"
"v_mul_f32 v147, s72, v147\n"
"v_mul_f32 v148, s72, v148\n"
"v_mul_f32 v149, s72, v149\n"
"v_mul_f32 v150, s72, v150\n"
"s_waitcnt lgkmcnt(4)\n"
"ds_write_b128 v46, v[240:243] offset:16448\n"
"v_mul_f32 v151, s72, v151\n"
"v_cvt_pk_bf16_f32 v144, v144, v145\n"
"v_cvt_pk_bf16_f32 v145, v148, v149\n"
"v_cvt_pk_bf16_f32 v146, v146, v147\n"
"buffer_load_dwordx4 v152, s[24:27], s43 offen offset:3264 lds\n"
"v_cvt_pk_bf16_f32 v147, v150, v151\n"
"ds_write_b128 v133, v[144:147] offset:256\n"
"v_and_b32 v112, 0xffff0000, v112\n"
"v_mul_f32 v128, s72, v128\n"
"v_mul_f32 v112, s72, v112\n"
"v_cvt_pk_bf16_f32 v112, v128, v112\n"
"v_lshlrev_b32 v129, 16, v113\n"
"v_and_b32 v113, 0xffff0000, v113\n"
"v_mul_f32 v129, s72, v129\n"
"ds_write_b128 v46, v[244:247] offset:16512\n"
"v_mul_f32 v113, s72, v113\n"
"v_cvt_pk_bf16_f32 v113, v129, v113\n"
"v_lshlrev_b32 v130, 16, v114\n"
"v_and_b32 v114, 0xffff0000, v114\n"
"v_mul_f32 v130, s72, v130\n"
"v_mul_f32 v114, s72, v114\n"
"v_cvt_pk_bf16_f32 v114, v130, v114\n"
"s_waitcnt lgkmcnt(4)\n"
"ds_write_b128 v46, v[248:251] offset:16576\n"
"s_add_u32 m0, s75, 49536\n"
"s_nop 0\n"
"global_load_lds_dwordx4 v135, s[16:17] offset:0\n"
"v_lshlrev_b32 v131, 16, v115\n"
"v_and_b32 v115, 0xffff0000, v115\n"
"v_mul_f32 v131, s72, v131\n"
"v_mul_f32 v115, s72, v115\n"
"v_cvt_pk_bf16_f32 v115, v131, v115\n"
"v_lshlrev_b32 v128, 16, v116\n"
"s_waitcnt vmcnt(5)\n"
"ds_read_b128 v[220:223], v154 offset:8704\n"
"v_and_b32 v116, 0xffff0000, v116\n"
"v_mul_f32 v128, s72, v128\n"
"ds_read_b128 v[224:227], v154 offset:8768\n"
"v_mul_f32 v116, s72, v116\n"
"ds_read_b128 v[228:231], v154 offset:8832\n"
"v_cvt_pk_bf16_f32 v116, v128, v116\n"
"ds_read_b128 v[232:235], v154 offset:8896\n"
"v_lshlrev_b32 v129, 16, v117\n"
"v_and_b32 v117, 0xffff0000, v117\n"
"v_mul_f32 v129, s72, v129\n"
"v_mul_f32 v117, s72, v117\n"
"v_cvt_pk_bf16_f32 v117, v129, v117\n"
"v_lshlrev_b32 v130, 16, v118\n"
"v_and_b32 v118, 0xffff0000, v118\n"
"v_mul_f32 v130, s72, v130\n"
"v_mul_f32 v118, s72, v118\n"
"v_cvt_pk_bf16_f32 v118, v130, v118\n"
"global_load_lds_dwordx4 v135, s[16:17] offset:1024\n"
"v_mov_b32 v135, 0\n"
"v_lshlrev_b32 v131, 16, v119\n"
"v_and_b32 v119, 0xffff0000, v119\n"
"v_mul_f32 v131, s72, v131\n"
"v_mul_f32 v119, s72, v119\n"
"v_cvt_pk_bf16_f32 v119, v131, v119\n"
"v_lshlrev_b32 v128, 16, v120\n"
"v_and_b32 v120, 0xffff0000, v120\n"
"v_mul_f32 v128, s72, v128\n"
"v_mul_f32 v120, s72, v120\n"
"v_cvt_pk_bf16_f32 v120, v128, v120\n"
"v_lshlrev_b32 v129, 16, v121\n"
"v_and_b32 v121, 0xffff0000, v121\n"
"v_mul_f32 v129, s72, v129\n"
"v_mul_f32 v121, s72, v121\n"
"v_cvt_pk_bf16_f32 v121, v129, v121\n"
"v_lshlrev_b32 v130, 16, v122\n"
"v_and_b32 v122, 0xffff0000, v122\n"
"v_mul_f32 v130, s72, v130\n"
"v_mul_f32 v122, s72, v122\n"
"v_cvt_pk_bf16_f32 v122, v130, v122\n"
"v_lshlrev_b32 v131, 16, v123\n"
"v_and_b32 v123, 0xffff0000, v123\n"
"v_mul_f32 v131, s72, v131\n"
"v_mul_f32 v123, s72, v123\n"
"s_waitcnt lgkmcnt(4)\n"
"s_barrier\n"
"s_waitcnt lgkmcnt(0)\n"
"v_mfma_f32_16x16x32_bf16 v[96:99], a[108:111], v[232:235], 0\n"
"s_add_u32 m0, s75, 51584\n"
"s_nop 0\n"
"global_load_lds_dwordx4 v252, s[16:17] offset:0\n"
"v_mfma_f32_16x16x32_bf16 v[96:99], a[104:107], v[228:231], v[96:99]\n"
"ds_read_b128 a[160:163], v153 offset:16384\n"
"ds_read_b128 a[164:167], v153 offset:20576\n"
"v_mfma_f32_16x16x32_bf16 v[96:99], a[100:103], v[224:227], v[96:99]\n"
"ds_read_b128 a[168:171], v153 offset:24768\n"
"ds_read_b128 a[172:175], v153 offset:28960\n"
"v_mfma_f32_16x16x32_bf16 v[96:99], a[96:99], v[220:223], v[96:99]\n"
"ds_read_b128 a[176:179], v153 offset:17440\n"
"v_cvt_pk_bf16_f32 v123, v131, v123\n"
"v_mfma_f32_16x16x32_bf16 v[100:103], a[124:127], v[232:235], 0\n"
"ds_read_b128 a[180:183], v153 offset:21632\n"
"v_lshlrev_b32 v128, 16, v124\n"
"v_mfma_f32_16x16x32_bf16 v[100:103], a[120:123], v[228:231], v[100:103]\n"
"ds_read_b128 a[184:187], v153 offset:25824\n"
"v_and_b32 v124, 0xffff0000, v124\n"
"v_mfma_f32_16x16x32_bf16 v[100:103], a[116:119], v[224:227], v[100:103]\n"
"ds_read_b128 a[188:191], v153 offset:30016\n"
"v_cvt_pk_bf16_f32 v96, v96, v97\n"
"v_mfma_f32_16x16x32_bf16 v[100:103], a[112:115], v[220:223], v[100:103]\n"
"ds_read_b128 a[192:195], v153 offset:18496\n"
"v_cvt_pk_bf16_f32 v97, v98, v99\n"
"v_mfma_f32_16x16x32_bf16 v[104:107], a[140:143], v[232:235], 0\n"
"ds_read_b128 a[196:199], v153 offset:22688\n"
"global_load_lds_dwordx4 v252, s[16:17] offset:1024\n"
"v_mfma_f32_16x16x32_bf16 v[104:107], a[136:139], v[228:231], v[104:107]\n"
"ds_read_b128 a[200:203], v153 offset:26880\n"
"v_mul_f32 v128, s72, v128\n"
"v_mfma_f32_16x16x32_bf16 v[104:107], a[132:135], v[224:227], v[104:107]\n"
"ds_read_b128 a[204:207], v153 offset:31072\n"
"v_cvt_pk_bf16_f32 v98, v100, v101\n"
"v_mfma_f32_16x16x32_bf16 v[104:107], a[128:131], v[220:223], v[104:107]\n"
"s_waitcnt lgkmcnt(11)\n"
"ds_read_b128 a[208:211], v153 offset:19552\n"
"v_cvt_pk_bf16_f32 v99, v102, v103\n"
"v_mfma_f32_16x16x32_bf16 v[48:51], v[204:207], a[160:163], 0\n"
"s_waitcnt lgkmcnt(11)\n"
"ds_read_b128 a[212:215], v153 offset:23744\n"
"v_mul_f32 v124, s72, v124\n"
"v_mfma_f32_16x16x32_bf16 v[48:51], v[208:211], a[164:167], v[48:51]\n"
"s_waitcnt lgkmcnt(11)\n"
"ds_read_b128 a[216:219], v153 offset:27936\n"
"v_cvt_pk_bf16_f32 v124, v128, v124\n"
"v_mfma_f32_16x16x32_bf16 v[48:51], v[212:215], a[168:171], v[48:51]\n"
"s_waitcnt lgkmcnt(11)\n"
"ds_read_b128 a[220:223], v153 offset:32128\n"
"v_mov_b32 v128, 0\n"
"v_mfma_f32_16x16x32_bf16 v[48:51], v[216:219], a[172:175], v[48:51]\n"
"s_waitcnt lgkmcnt(10)\n"
"ds_write_b128 v132, v[96:99] offset:256\n"
"v_mfma_f32_16x16x32_bf16 v[52:55], v[204:207], a[176:179], 0\n"
"s_add_u32 m0, s28, 4352\n"
"s_nop 0\n"
"buffer_load_dwordx4 v152, s[24:27], s44 offen offset:0 lds\n"
"v_mfma_f32_16x16x32_bf16 v[52:55], v[208:211], a[180:183], v[52:55]\n"
"s_nop 0\n"
"v_cvt_pk_bf16_f32 v48, v48, v49\n"
"v_cvt_pk_bf16_f32 v49, v50, v51\n"
"s_waitcnt lgkmcnt(8)\n"
"v_mfma_f32_16x16x32_bf16 v[52:55], v[212:215], a[184:187], v[52:55]\n"
"v_cvt_pk_bf16_f32 v104, v104, v105\n"
"v_cvt_pk_bf16_f32 v105, v106, v107\n"
"v_mfma_f32_16x16x32_bf16 v[52:55], v[216:219], a[188:191], v[52:55]\n"
"ds_write_b64 v134, v[48:49] offset:0\n"
"v_mfma_f32_16x16x32_bf16 v[60:63], v[204:207], a[192:195], 0\n"
"s_waitcnt lgkmcnt(6)\n"
"v_lshlrev_b32 v129, 16, v125\n"
"v_and_b32 v125, 0xffff0000, v125\n"
"v_mfma_f32_16x16x32_bf16 v[60:63], v[208:211], a[196:199], v[60:63]\n"
"v_mul_f32 v129, s72, v129\n"
"v_mul_f32 v125, s72, v125\n"
"v_mfma_f32_16x16x32_bf16 v[60:63], v[212:215], a[200:203], v[60:63]\n"
"v_cvt_pk_bf16_f32 v52, v52, v53\n"
"v_cvt_pk_bf16_f32 v53, v54, v55\n"
"v_mfma_f32_16x16x32_bf16 v[60:63], v[216:219], a[204:207], v[60:63]\n"
"s_waitcnt lgkmcnt(3)\n"
"ds_write_b64 v134, v[52:53] offset:128\n"
"v_mfma_f32_16x16x32_bf16 v[64:67], v[204:207], a[208:211], 0\n"
"buffer_load_dwordx4 v152, s[24:27], s45 offen offset:1088 lds\n"
"v_cvt_pk_bf16_f32 v125, v129, v125\n"
"v_mfma_f32_16x16x32_bf16 v[64:67], v[208:211], a[212:215], v[64:67]\n"
"v_mov_b32 v129, 0\n"
"v_lshlrev_b32 v130, 16, v126\n"
"v_mfma_f32_16x16x32_bf16 v[64:67], v[212:215], a[216:219], v[64:67]\n"
"s_waitcnt lgkmcnt(2)\n"
"v_cvt_pk_bf16_f32 v60, v60, v61\n"
".Lpad0_%=:\n"
"s_cbranch_vccnz .Lpad5_%=\n"
"v_cvt_pk_bf16_f32 v61, v62, v63\n"
"v_mfma_f32_16x16x32_bf16 v[64:67], v[216:219], a[220:223], v[64:67]\n"
"ds_write_b64 v134, v[60:61] offset:256\n"
"v_mfma_f32_16x16x32_bf16 v[108:111], a[156:159], v[232:235], 0\n"
"v_and_b32 v126, 0xffff0000, v126\n"
"v_mul_f32 v130, s72, v130\n"
"v_mfma_f32_16x16x32_bf16 v[108:111], a[152:155], v[228:231], v[108:111]\n"
"v_mul_f32 v126, s72, v126\n"
"v_cvt_pk_bf16_f32 v126, v130, v126\n"
"v_mfma_f32_16x16x32_bf16 v[108:111], a[148:151], v[224:227], v[108:111]\n"
"v_mov_b32 v130, 0\n"
"v_cvt_pk_bf16_f32 v64, v64, v65\n"
"v_mfma_f32_16x16x32_bf16 v[108:111], a[144:147], v[220:223], v[108:111]\n"
"ds_write_b128 v45, v[112:115] offset:0\n"
"v_mfma_f32_16x16x32_bf16 v[136:139], a[60:63], v[232:235], 0\n"
"buffer_load_dwordx4 v152, s[24:27], s46 offen offset:2176 lds\n"
"v_cvt_pk_bf16_f32 v65, v66, v67\n"
"v_mfma_f32_16x16x32_bf16 v[136:139], a[56:59], v[228:231], v[136:139]\n"
"s_waitcnt vmcnt(7)\n"
"ds_read_b128 a[0:3], v154 offset:0\n"
"ds_read_b128 a[4:7], v154 offset:64\n"
"v_mfma_f32_16x16x32_bf16 v[136:139], a[52:55], v[224:227], v[136:139]\n"
"ds_read_b128 a[8:11], v154 offset:128\n"
"v_cvt_pk_bf16_f32 v106, v108, v109\n"
"v_mfma_f32_16x16x32_bf16 v[136:139], a[48:51], v[220:223], v[136:139]\n"
"ds_read_b128 a[12:15], v154 offset:192\n"
"v_cvt_pk_bf16_f32 v107, v110, v111\n"
"v_mfma_f32_16x16x32_bf16 v[140:143], a[76:79], v[232:235], 0\n"
"v_lshlrev_b32 v131, 16, v127\n"
"v_and_b32 v127, 0xffff0000, v127\n"
"v_mfma_f32_16x16x32_bf16 v[140:143], a[72:75], v[228:231], v[140:143]\n"
"v_mul_f32 v131, s72, v131\n"
"v_mul_f32 v127, s72, v127\n"
"v_mfma_f32_16x16x32_bf16 v[140:143], a[68:71], v[224:227], v[140:143]\n"
"v_mul_f32 v136, s72, v136\n"
"v_mul_f32 v137, s72, v137\n"
"v_mfma_f32_16x16x32_bf16 v[140:143], a[64:67], v[220:223], v[140:143]\n"
"ds_write_b64 v134, v[64:65] offset:384\n"
"v_mfma_f32_16x16x32_bf16 v[144:147], a[92:95], v[232:235], 0\n"
"buffer_load_dwordx4 v152, s[24:27], s47 offen offset:3264 lds\n"
"v_mul_f32 v138, s72, v138\n"
"v_mfma_f32_16x16x32_bf16 v[144:147], a[88:91], v[228:231], v[144:147]\n"
"v_mul_f32 v139, s72, v139\n"
"v_cvt_pk_bf16_f32 v136, v136, v137\n"
"v_mfma_f32_16x16x32_bf16 v[144:147], a[84:87], v[224:227], v[144:147]\n"
"v_mul_f32 v140, s72, v140\n"
"v_mul_f32 v141, s72, v141\n"
"v_mfma_f32_16x16x32_bf16 v[144:147], a[80:83], v[220:223], v[144:147]\n"
"ds_write_b128 v132, v[104:107] offset:2304\n"
"v_mfma_f32_16x16x32_bf16 v[148:151], a[236:239], v[232:235], 0\n"
"v_mov_b32 v132, 0\n"
"v_mul_f32 v142, s72, v142\n"
"v_mfma_f32_16x16x32_bf16 v[148:151], a[232:235], v[228:231], v[148:151]\n"
"v_mul_f32 v143, s72, v143\n"
"v_cvt_pk_bf16_f32 v137, v140, v141\n"
"v_mfma_f32_16x16x32_bf16 v[148:151], a[228:231], v[224:227], v[148:151]\n"
"v_cvt_pk_bf16_f32 v138, v138, v139\n"
"v_cvt_pk_bf16_f32 v139, v142, v143\n"
"v_mfma_f32_16x16x32_bf16 v[148:151], a[224:227], v[220:223], v[148:151]\n"
"ds_write_b128 v133, v[136:139] offset:2048\n"
"v_mfma_f32_16x16x32_bf16 v[72:75], v[232:235], a[172:175], 0\n"
"s_add_u32 m0, s28, 8704\n"
"s_nop 0\n"
"buffer_load_dwordx4 v152, s[24:27], s48 offen offset:0 lds\n"
"v_mfma_f32_16x16x32_bf16 v[72:75], v[228:231], a[168:171], v[72:75]\n"
"s_waitcnt vmcnt(5)\n"
"ds_read_b64_tr_b16 v[236:237], v36 offset:49536\n"
"ds_read_b64_tr_b16 v[238:239], v36 offset:50560\n"
"v_mfma_f32_16x16x32_bf16 v[72:75], v[224:227], a[164:167], v[72:75]\n"
"ds_read_b64_tr_b16 v[240:241], v37 offset:49536\n"
"ds_read_b64_tr_b16 v[242:243], v37 offset:50560\n"
"v_mfma_f32_16x16x32_bf16 v[72:75], v[220:223], a[160:163], v[72:75]\n"
"ds_read_b64_tr_b16 v[244:245], v38 offset:49536\n"
"ds_read_b64_tr_b16 v[246:247], v38 offset:50560\n"
"v_mfma_f32_16x16x32_bf16 v[76:79], v[232:235], a[188:191], 0\n"
"ds_read_b64_tr_b16 v[248:249], v39 offset:49536\n"
"ds_read_b64_tr_b16 v[250:251], v39 offset:50560\n"
"v_mfma_f32_16x16x32_bf16 v[76:79], v[228:231], a[184:187], v[76:79]\n"
"ds_read_b128 v[136:139], v153 offset:18496\n"
"v_mul_f32 v144, s72, v144\n"
"v_mfma_f32_16x16x32_bf16 v[76:79], v[224:227], a[180:183], v[76:79]\n"
"ds_read_b128 v[140:143], v153 offset:22688\n"
"v_mul_f32 v145, s72, v145\n"
"v_mfma_f32_16x16x32_bf16 v[76:79], v[220:223], a[176:179], v[76:79]\n"
"ds_write_b128 v45, v[116:119] offset:1024\n"
"v_mfma_f32_16x16x32_bf16 v[84:87], v[232:235], a[204:207], 0\n"
"v_mul_f32 v146, s72, v146\n"
"v_mul_f32 v147, s72, v147\n"
"v_mfma_f32_16x16x32_bf16 v[84:87], v[228:231], a[200:203], v[84:87]\n"
"v_mul_f32 v148, s72, v148\n"
"v_mul_f32 v149, s72, v149\n"
"v_mfma_f32_16x16x32_bf16 v[84:87], v[224:227], a[196:199], v[84:87]\n"
"v_mul_f32 v150, s72, v150\n"
"v_mul_f32 v151, s72, v151\n"
"v_mfma_f32_16x16x32_bf16 v[84:87], v[220:223], a[192:195], v[84:87]\n"
"ds_write_b128 v45, v[120:123] offset:2048\n"
"v_mfma_f32_16x16x32_bf16 v[88:91], v[232:235], a[220:223], 0\n"
"v_cvt_pk_bf16_f32 v144, v144, v145\n"
"v_cvt_pk_bf16_f32 v145, v148, v149\n"
"v_mfma_f32_16x16x32_bf16 v[88:91], v[228:231], a[216:219], v[88:91]\n"
"v_cvt_pk_bf16_f32 v146, v146, v147\n"
"v_cvt_pk_bf16_f32 v147, v150, v151\n"
"v_mfma_f32_16x16x32_bf16 v[88:91], v[224:227], a[212:215], v[88:91]\n"
"ds_read_b128 v[148:151], v153 offset:31072\n"
"v_cvt_pk_bf16_f32 v72, v72, v73\n"
"v_mfma_f32_16x16x32_bf16 v[88:91], v[220:223], a[208:211], v[88:91]\n"
"s_waitcnt lgkmcnt(4)\n"
"ds_write_b128 v133, v[144:147] offset:2304\n"
"v_mfma_f32_16x16x32_bf16 v[0:3], a[96:99], a[0:3], 0\n"
"ds_read_b128 v[144:147], v153 offset:26880\n"
"v_mov_b32 v133, 0\n"
"v_mfma_f32_16x16x32_bf16 v[0:3], a[100:103], a[4:7], v[0:3]\n"
"v_cvt_pk_bf16_f32 v73, v74, v75\n"
"v_cvt_pk_bf16_f32 v76, v76, v77\n"
"v_mfma_f32_16x16x32_bf16 v[0:3], a[104:107], a[8:11], v[0:3]\n"
"v_cvt_pk_bf16_f32 v77, v78, v79\n"
"v_cvt_pk_bf16_f32 v84, v84, v85\n"
"v_mfma_f32_16x16x32_bf16 v[0:3], a[108:111], a[12:15], v[0:3]\n"
"ds_write_b64 v134, v[72:73] offset:2048\n"
"v_mfma_f32_16x16x32_bf16 v[176:179], a[112:115], a[0:3], 0\n"
"v_cvt_pk_bf16_f32 v85, v86, v87\n"
"v_cvt_pk_bf16_f32 v88, v88, v89\n"
"v_mfma_f32_16x16x32_bf16 v[176:179], a[116:119], a[4:7], v[176:179]\n"
"v_cvt_pk_bf16_f32 v89, v90, v91\n"
"v_cvt_pk_bf16_f32 v127, v131, v127\n"
"v_mfma_f32_16x16x32_bf16 v[176:179], a[120:123], a[8:11], v[176:179]\n"
"v_mov_b32 v131, 0\n"
"v_cvt_pk_bf16_f32 v0, v0, v1\n"
"v_mfma_f32_16x16x32_bf16 v[176:179], a[124:127], a[12:15], v[176:179]\n"
"ds_write_b64 v134, v[76:77] offset:2176\n"
"v_mfma_f32_16x16x32_bf16 v[4:7], a[128:131], a[0:3], 0\n"
"v_cvt_pk_bf16_f32 v1, v2, v3\n"
"v_mfma_f32_16x16x32_bf16 v[4:7], a[132:135], a[4:7], v[4:7]\n"
"buffer_load_dwordx4 v152, s[24:27], s49 offen offset:1088 lds\n"
"v_mfma_f32_16x16x32_bf16 v[4:7], a[136:139], a[8:11], v[4:7]\n"
"v_cvt_pk_bf16_f32 v2, v176, v177\n"
"v_cvt_pk_bf16_f32 v3, v178, v179\n"
"v_mfma_f32_16x16x32_bf16 v[4:7], a[140:143], a[12:15], v[4:7]\n"
"ds_write_b64 v134, v[84:85] offset:2304\n"
"v_mfma_f32_16x16x32_bf16 v[164:167], a[144:147], a[0:3], 0\n"
"v_mfma_f32_16x16x32_bf16 v[164:167], a[148:151], a[4:7], v[164:167]\n"
"v_mfma_f32_16x16x32_bf16 v[164:167], a[152:155], a[8:11], v[164:167]\n"
"v_cvt_pk_bf16_f32 v4, v4, v5\n"
"v_cvt_pk_bf16_f32 v5, v6, v7\n"
"v_mfma_f32_16x16x32_bf16 v[164:167], a[156:159], a[12:15], v[164:167]\n"
"ds_write_b64 v134, v[88:89] offset:2432\n"
"v_mfma_f32_16x16x32_bf16 v[180:183], a[48:51], a[0:3], 0\n"
"ds_read_b64 a[244:245], v163 offset:0\n"
"v_mfma_f32_16x16x32_bf16 v[180:183], a[52:55], a[4:7], v[180:183]\n"
"v_mfma_f32_16x16x32_bf16 v[180:183], a[56:59], a[8:11], v[180:183]\n"
"v_cvt_pk_bf16_f32 v6, v164, v165\n"
"v_cvt_pk_bf16_f32 v7, v166, v167\n"
"v_mfma_f32_16x16x32_bf16 v[180:183], a[60:63], a[12:15], v[180:183]\n"
"buffer_load_dwordx4 v152, s[24:27], s50 offen offset:2176 lds\n"
"ds_write_b128 v46, v[236:239] offset:49536\n"
"v_mfma_f32_16x16x32_bf16 v[176:179], a[64:67], a[0:3], 0\n"
"ds_read_b128 v[236:239], v153 offset:16384\n"
"v_mfma_f32_16x16x32_bf16 v[176:179], a[68:71], a[4:7], v[176:179]\n"
"s_nop 0\n"
"v_mul_f32 v180, s72, v180\n"
"v_mul_f32 v181, s72, v181\n"
"v_mfma_f32_16x16x32_bf16 v[176:179], a[72:75], a[8:11], v[176:179]\n"
"v_mul_f32 v182, s72, v182\n"
"v_mul_f32 v183, s72, v183\n"
"v_mfma_f32_16x16x32_bf16 v[176:179], a[76:79], a[12:15], v[176:179]\n"
"ds_write_b128 v46, v[240:243] offset:49600\n"
"v_mfma_f32_16x16x32_bf16 v[184:187], a[80:83], a[0:3], 0\n"
"ds_read_b128 v[240:243], v153 offset:20576\n"
"v_cvt_pk_bf16_f32 v180, v180, v181\n"
"v_mfma_f32_16x16x32_bf16 v[184:187], a[84:87], a[4:7], v[184:187]\n"
"v_cvt_pk_bf16_f32 v181, v182, v183\n"
"v_mul_f32 v176, s72, v176\n"
"v_mfma_f32_16x16x32_bf16 v[184:187], a[88:91], a[8:11], v[184:187]\n"
"v_mul_f32 v177, s72, v177\n"
"v_mul_f32 v178, s72, v178\n"
"v_mfma_f32_16x16x32_bf16 v[184:187], a[92:95], a[12:15], v[184:187]\n"
"ds_write_b128 v46, v[244:247] offset:49664\n"
"v_mfma_f32_16x16x32_bf16 v[168:171], a[224:227], a[0:3], 0\n"
"buffer_load_dwordx4 v152, s[24:27], s51 offen offset:3264 lds\n"
"ds_read_b128 v[244:247], v153 offset:24768\n"
"v_mfma_f32_16x16x32_bf16 v[168:171], a[228:231], a[4:7], v[168:171]\n"
"v_mul_f32 v179, s72, v179\n"
"v_cvt_pk_bf16_f32 v182, v176, v177\n"
"v_mfma_f32_16x16x32_bf16 v[168:171], a[232:235], a[8:11], v[168:171]\n"
"v_cvt_pk_bf16_f32 v183, v178, v179\n"
"v_mul_f32 v184, s72, v184\n"
"v_mfma_f32_16x16x32_bf16 v[168:171], a[236:239], a[12:15], v[168:171]\n"
"ds_write_b128 v46, v[248:251] offset:49728\n"
"v_mfma_f32_16x16x32_bf16 v[48:51], a[0:3], a[160:163], 0\n"
"ds_read_b128 v[248:251], v153 offset:28960\n"
"v_mul_f32 v185, s72, v185\n"
"v_mfma_f32_16x16x32_bf16 v[48:51], a[4:7], a[164:167], v[48:51]\n"
"v_mul_f32 v186, s72, v186\n"
"v_mul_f32 v187, s72, v187\n"
"v_mfma_f32_16x16x32_bf16 v[48:51], a[8:11], a[168:171], v[48:51]\n"
"v_cvt_pk_bf16_f32 v184, v184, v185\n"
"v_cvt_pk_bf16_f32 v185, v186, v187\n"
"v_mfma_f32_16x16x32_bf16 v[48:51], a[12:15], a[172:175], v[48:51]\n"
"ds_write_b128 v45, v[124:127] offset:3072\n"
"v_mfma_f32_16x16x32_bf16 v[60:63], a[0:3], a[176:179], 0\n"
"v_mul_f32 v168, s72, v168\n"
"v_mul_f32 v169, s72, v169\n"
"v_mfma_f32_16x16x32_bf16 v[60:63], a[4:7], a[180:183], v[60:63]\n"
"s_add_u32 m0, s28, 0\n"
"s_nop 0\n"
"buffer_load_dwordx4 v152, s[24:27], s52 offen offset:0 lds\n"
"v_mfma_f32_16x16x32_bf16 v[60:63], a[8:11], a[184:187], v[60:63]\n"
"s_waitcnt vmcnt(5)\n"
"ds_read_b128 a[16:19], v154 offset:4352\n"
"ds_read_b128 a[20:23], v154 offset:4416\n"
"v_mfma_f32_16x16x32_bf16 v[60:63], a[12:15], a[188:191], v[60:63]\n"
"ds_read_b128 a[24:27], v154 offset:4480\n"
"ds_read_b128 a[28:31], v154 offset:4544\n"
"v_mfma_f32_16x16x32_bf16 v[72:75], a[0:3], a[192:195], 0\n"
"v_mul_f32 v170, s72, v170\n"
"v_mul_f32 v171, s72, v171\n"
"v_mfma_f32_16x16x32_bf16 v[72:75], a[4:7], a[196:199], v[72:75]\n"
"v_cvt_pk_bf16_f32 v186, v168, v169\n"
"v_cvt_pk_bf16_f32 v187, v170, v171\n"
"v_mfma_f32_16x16x32_bf16 v[72:75], a[8:11], a[200:203], v[72:75]\n"
"v_cvt_pk_bf16_f32 v48, v48, v49\n"
"v_cvt_pk_bf16_f32 v49, v50, v51\n"
"v_mfma_f32_16x16x32_bf16 v[72:75], a[12:15], a[204:207], v[72:75]\n"
"v_cvt_pk_bf16_f32 v60, v60, v61\n"
"v_cvt_pk_bf16_f32 v61, v62, v63\n"
"v_mfma_f32_16x16x32_bf16 v[84:87], a[0:3], a[208:211], 0\n"
"v_mfma_f32_16x16x32_bf16 v[84:87], a[4:7], a[212:215], v[84:87]\n"
"v_mfma_f32_16x16x32_bf16 v[84:87], a[8:11], a[216:219], v[84:87]\n"
"v_cvt_pk_bf16_f32 v72, v72, v73\n"
"v_cvt_pk_bf16_f32 v73, v74, v75\n"
"v_mfma_f32_16x16x32_bf16 v[84:87], a[12:15], a[220:223], v[84:87]\n"
"s_waitcnt lgkmcnt(3)\n"
"buffer_load_dwordx4 v152, s[24:27], s53 offen offset:1088 lds\n"
"v_mfma_f32_16x16x32_bf16 v[8:11], a[96:99], a[16:19], 0\n"
"s_waitcnt lgkmcnt(2)\n"
"v_mfma_f32_16x16x32_bf16 v[8:11], a[100:103], a[20:23], v[8:11]\n"
"s_nop 0\n"
"v_cvt_pk_bf16_f32 v84, v84, v85\n"
"v_cvt_pk_bf16_f32 v85, v86, v87\n"
"s_waitcnt lgkmcnt(1)\n"
"v_mfma_f32_16x16x32_bf16 v[8:11], a[104:107], a[24:27], v[8:11]\n"
"s_waitcnt lgkmcnt(0)\n"
"v_mfma_f32_16x16x32_bf16 v[8:11], a[108:111], a[28:31], v[8:11]\n"
"s_barrier\n"
"v_mfma_f32_16x16x32_bf16 v[176:179], a[112:115], a[16:19], 0\n"
"s_nop 4\n"
"v_cvt_pk_bf16_f32 v8, v8, v9\n"
"v_cvt_pk_bf16_f32 v9, v10, v11\n"
"v_mfma_f32_16x16x32_bf16 v[176:179], a[116:119], a[20:23], v[176:179]\n"
"v_mfma_f32_16x16x32_bf16 v[176:179], a[120:123], a[24:27], v[176:179]\n"
"v_mfma_f32_16x16x32_bf16 v[176:179], a[124:127], a[28:31], v[176:179]\n"
"v_mfma_f32_16x16x32_bf16 v[12:15], a[128:131], a[16:19], 0\n"
"v_mfma_f32_16x16x32_bf16 v[12:15], a[132:135], a[20:23], v[12:15]\n"
"v_mfma_f32_16x16x32_bf16 v[12:15], a[136:139], a[24:27], v[12:15]\n"
"v_cvt_pk_bf16_f32 v10, v176, v177\n"
"v_cvt_pk_bf16_f32 v11, v178, v179\n"
"v_mfma_f32_16x16x32_bf16 v[12:15], a[140:143], a[28:31], v[12:15]\n"
"v_mfma_f32_16x16x32_bf16 v[168:171], a[144:147], a[16:19], 0\n"
"v_mfma_f32_16x16x32_bf16 v[168:171], a[148:151], a[20:23], v[168:171]\n"
"v_mfma_f32_16x16x32_bf16 v[168:171], a[152:155], a[24:27], v[168:171]\n"
"v_cvt_pk_bf16_f32 v12, v12, v13\n"
"v_cvt_pk_bf16_f32 v13, v14, v15\n"
"v_mfma_f32_16x16x32_bf16 v[168:171], a[156:159], a[28:31], v[168:171]\n"
"v_mfma_f32_16x16x32_bf16 v[188:191], a[48:51], a[16:19], 0\n"
"buffer_load_dwordx4 v152, s[24:27], s54 offen offset:2176 lds\n"
"v_mfma_f32_16x16x32_bf16 v[188:191], a[52:55], a[20:23], v[188:191]\n"
"v_mfma_f32_16x16x32_bf16 v[188:191], a[56:59], a[24:27], v[188:191]\n"
"v_cvt_pk_bf16_f32 v14, v168, v169\n"
"v_cvt_pk_bf16_f32 v15, v170, v171\n"
"v_mfma_f32_16x16x32_bf16 v[188:191], a[60:63], a[28:31], v[188:191]\n"
"v_mfma_f32_16x16x32_bf16 v[176:179], a[64:67], a[16:19], 0\n"
"v_mfma_f32_16x16x32_bf16 v[176:179], a[68:71], a[20:23], v[176:179]\n"
"v_mfma_f32_16x16x32_bf16 v[176:179], a[72:75], a[24:27], v[176:179]\n"
"v_mul_f32 v188, s72, v188\n"
"v_mul_f32 v189, s72, v189\n"
"v_mfma_f32_16x16x32_bf16 v[176:179], a[76:79], a[28:31], v[176:179]\n"
"v_mul_f32 v190, s72, v190\n"
"v_mul_f32 v191, s72, v191\n"
"v_mfma_f32_16x16x32_bf16 v[192:195], a[80:83], a[16:19], 0\n"
"v_cvt_pk_bf16_f32 v188, v188, v189\n"
"v_cvt_pk_bf16_f32 v189, v190, v191\n"
"v_mfma_f32_16x16x32_bf16 v[192:195], a[84:87], a[20:23], v[192:195]\n"
"v_mfma_f32_16x16x32_bf16 v[192:195], a[88:91], a[24:27], v[192:195]\n"
"v_mul_f32 v176, s72, v176\n"
"v_mul_f32 v177, s72, v177\n"
"v_mfma_f32_16x16x32_bf16 v[192:195], a[92:95], a[28:31], v[192:195]\n"
"buffer_load_dwordx4 v152, s[24:27], s55 offen offset:3264 lds\n"
"v_mul_f32 v178, s72, v178\n"
"v_mfma_f32_16x16x32_bf16 v[164:167], a[224:227], a[16:19], 0\n"
"v_mul_f32 v179, s72, v179\n"
"v_cvt_pk_bf16_f32 v190, v176, v177\n"
"v_mfma_f32_16x16x32_bf16 v[164:167], a[228:231], a[20:23], v[164:167]\n"
"v_cvt_pk_bf16_f32 v191, v178, v179\n"
"v_mfma_f32_16x16x32_bf16 v[164:167], a[232:235], a[24:27], v[164:167]\n"
"v_mul_f32 v192, s72, v192\n"
"v_mul_f32 v193, s72, v193\n"
"v_mfma_f32_16x16x32_bf16 v[164:167], a[236:239], a[28:31], v[164:167]\n"
"v_mul_f32 v194, s72, v194\n"
"v_mul_f32 v195, s72, v195\n"
"v_mfma_f32_16x16x32_bf16 v[172:175], a[28:31], a[172:175], 0\n"
"v_cvt_pk_bf16_f32 v192, v192, v193\n"
"v_cvt_pk_bf16_f32 v193, v194, v195\n"
"v_mfma_f32_16x16x32_bf16 v[172:175], a[24:27], a[168:171], v[172:175]\n"
"v_mfma_f32_16x16x32_bf16 v[172:175], a[20:23], a[164:167], v[172:175]\n"
"v_mul_f32 v164, s72, v164\n"
"v_mul_f32 v165, s72, v165\n"
"v_mfma_f32_16x16x32_bf16 v[172:175], a[16:19], a[160:163], v[172:175]\n"
"v_mul_f32 v166, s72, v166\n"
"v_mul_f32 v167, s72, v167\n"
"v_mfma_f32_16x16x32_bf16 v[176:179], a[28:31], a[188:191], 0\n"
"v_cvt_pk_bf16_f32 v194, v164, v165\n"
"v_cvt_pk_bf16_f32 v195, v166, v167\n"
"v_mfma_f32_16x16x32_bf16 v[176:179], a[24:27], a[184:187], v[176:179]\n"
"v_mfma_f32_16x16x32_bf16 v[176:179], a[20:23], a[180:183], v[176:179]\n"
"s_add_u32 m0, s28, 4352\n"
"s_nop 0\n"
"buffer_load_dwordx4 v152, s[24:27], s56 offen offset:0 lds\n"
"v_mfma_f32_16x16x32_bf16 v[176:179], a[16:19], a[176:179], v[176:179]\n"
"s_waitcnt vmcnt(5)\n"
"ds_read_b128 a[32:35], v154 offset:8704\n"
"ds_read_b128 a[36:39], v154 offset:8768\n"
"ds_read_b128 a[40:43], v154 offset:8832\n"
"v_cvt_pk_bf16_f32 v50, v172, v173\n"
"ds_read_b128 a[44:47], v154 offset:8896\n"
"v_cvt_pk_bf16_f32 v51, v174, v175\n"
"v_mfma_f32_16x16x32_bf16 v[172:175], a[28:31], a[204:207], 0\n"
"v_cvt_pk_bf16_f32 v62, v176, v177\n"
"v_mfma_f32_16x16x32_bf16 v[172:175], a[24:27], a[200:203], v[172:175]\n"
"v_cvt_pk_bf16_f32 v63, v178, v179\n"
"v_mfma_f32_16x16x32_bf16 v[172:175], a[20:23], a[196:199], v[172:175]\n"
"v_mfma_f32_16x16x32_bf16 v[172:175], a[16:19], a[192:195], v[172:175]\n"
"v_mfma_f32_16x16x32_bf16 v[176:179], a[28:31], a[220:223], 0\n"
"v_mfma_f32_16x16x32_bf16 v[176:179], a[24:27], a[216:219], v[176:179]\n"
"v_mfma_f32_16x16x32_bf16 v[176:179], a[20:23], a[212:215], v[176:179]\n"
"v_cvt_pk_bf16_f32 v74, v172, v173\n"
"v_cvt_pk_bf16_f32 v75, v174, v175\n"
"v_mfma_f32_16x16x32_bf16 v[176:179], a[16:19], a[208:211], v[176:179]\n"
"s_waitcnt lgkmcnt(3)\n"
"buffer_load_dwordx4 v152, s[24:27], s57 offen offset:1088 lds\n"
"v_mfma_f32_16x16x32_bf16 v[196:199], a[48:51], a[32:35], 0\n"
"s_waitcnt lgkmcnt(2)\n"
"v_mfma_f32_16x16x32_bf16 v[196:199], a[52:55], a[36:39], v[196:199]\n"
"s_nop 0\n"
"v_cvt_pk_bf16_f32 v86, v176, v177\n"
"s_waitcnt lgkmcnt(1)\n"
"v_mfma_f32_16x16x32_bf16 v[196:199], a[56:59], a[40:43], v[196:199]\n"
"s_waitcnt lgkmcnt(0)\n"
"v_cvt_pk_bf16_f32 v87, v178, v179\n"
"v_mfma_f32_16x16x32_bf16 v[196:199], a[60:63], a[44:47], v[196:199]\n"
"v_mfma_f32_16x16x32_bf16 v[176:179], a[64:67], a[32:35], 0\n"
"v_mfma_f32_16x16x32_bf16 v[176:179], a[68:71], a[36:39], v[176:179]\n"
"v_mfma_f32_16x16x32_bf16 v[176:179], a[72:75], a[40:43], v[176:179]\n"
"v_mul_f32 v196, s72, v196\n"
"v_mul_f32 v197, s72, v197\n"
"v_mfma_f32_16x16x32_bf16 v[176:179], a[76:79], a[44:47], v[176:179]\n"
"v_mul_f32 v198, s72, v198\n"
"v_mul_f32 v199, s72, v199\n"
"v_mfma_f32_16x16x32_bf16 v[200:203], a[80:83], a[32:35], 0\n"
"v_cvt_pk_bf16_f32 v196, v196, v197\n"
"v_cvt_pk_bf16_f32 v197, v198, v199\n"
"v_mfma_f32_16x16x32_bf16 v[200:203], a[84:87], a[36:39], v[200:203]\n"
"v_mfma_f32_16x16x32_bf16 v[200:203], a[88:91], a[40:43], v[200:203]\n"
"buffer_load_dwordx4 v152, s[24:27], s58 offen offset:2176 lds\n"
"v_mul_f32 v176, s72, v176\n"
"v_mfma_f32_16x16x32_bf16 v[200:203], a[92:95], a[44:47], v[200:203]\n"
"v_mul_f32 v177, s72, v177\n"
"v_mul_f32 v178, s72, v178\n"
"v_mfma_f32_16x16x32_bf16 v[168:171], a[224:227], a[32:35], 0\n"
"ds_read_b128 a[224:227], v153 offset:13312\n"
"v_mul_f32 v179, s72, v179\n"
"v_mfma_f32_16x16x32_bf16 v[168:171], a[228:231], a[36:39], v[168:171]\n"
"ds_read_b128 a[228:231], v161 offset:0\n"
"v_cvt_pk_bf16_f32 v198, v176, v177\n"
"v_mfma_f32_16x16x32_bf16 v[168:171], a[232:235], a[40:43], v[168:171]\n"
"ds_read_b128 a[232:235], v161 offset:2048\n"
"v_cvt_pk_bf16_f32 v199, v178, v179\n"
"v_mfma_f32_16x16x32_bf16 v[168:171], a[236:239], a[44:47], v[168:171]\n"
"ds_read_b64 a[236:237], v156 offset:0\n"
"ds_read_b64 a[238:239], v156 offset:64\n"
"v_mfma_f32_16x16x32_bf16 v[16:19], a[96:99], a[32:35], 0\n"
"v_mul_f32 v200, s72, v200\n"
"v_mul_f32 v201, s72, v201\n"
"v_mfma_f32_16x16x32_bf16 v[16:19], a[100:103], a[36:39], v[16:19]\n"
"v_mul_f32 v202, s72, v202\n"
"v_mul_f32 v203, s72, v203\n"
"v_mfma_f32_16x16x32_bf16 v[16:19], a[104:107], a[40:43], v[16:19]\n"
"v_cvt_pk_bf16_f32 v200, v200, v201\n"
"v_cvt_pk_bf16_f32 v201, v202, v203\n"
"v_mfma_f32_16x16x32_bf16 v[16:19], a[108:111], a[44:47], v[16:19]\n"
"v_mul_f32 v168, s72, v168\n"
"v_mul_f32 v169, s72, v169\n"
"v_mfma_f32_16x16x32_bf16 v[176:179], a[112:115], a[32:35], 0\n"
"v_mul_f32 v170, s72, v170\n"
"v_mul_f32 v171, s72, v171\n"
"v_mfma_f32_16x16x32_bf16 v[176:179], a[116:119], a[36:39], v[176:179]\n"
"v_cvt_pk_bf16_f32 v202, v168, v169\n"
"v_cvt_pk_bf16_f32 v203, v170, v171\n"
"v_mfma_f32_16x16x32_bf16 v[176:179], a[120:123], a[40:43], v[176:179]\n"
"v_cvt_pk_bf16_f32 v16, v16, v17\n"
"v_cvt_pk_bf16_f32 v17, v18, v19\n"
"v_mfma_f32_16x16x32_bf16 v[176:179], a[124:127], a[44:47], v[176:179]\n"
"v_add_u32 v156, 512, v156\n"
"v_add_u32 v161, 64, v161\n"
"v_mfma_f32_16x16x32_bf16 v[20:23], a[128:131], a[32:35], 0\n"
"v_mfma_f32_16x16x32_bf16 v[20:23], a[132:135], a[36:39], v[20:23]\n"
"v_mfma_f32_16x16x32_bf16 v[20:23], a[136:139], a[40:43], v[20:23]\n"
"v_cvt_pk_bf16_f32 v18, v176, v177\n"
"v_cvt_pk_bf16_f32 v19, v178, v179\n"
"v_mfma_f32_16x16x32_bf16 v[20:23], a[140:143], a[44:47], v[20:23]\n"
"v_mfma_f32_16x16x32_bf16 v[164:167], a[144:147], a[32:35], 0\n"
"v_mfma_f32_16x16x32_bf16 v[164:167], a[148:151], a[36:39], v[164:167]\n"
"buffer_load_dwordx4 v152, s[24:27], s59 offen offset:3264 lds\n"
"v_mfma_f32_16x16x32_bf16 v[164:167], a[152:155], a[40:43], v[164:167]\n"
"v_cvt_pk_bf16_f32 v20, v20, v21\n"
"v_cvt_pk_bf16_f32 v21, v22, v23\n"
"v_mfma_f32_16x16x32_bf16 v[164:167], a[156:159], a[44:47], v[164:167]\n"
"v_mfma_f32_16x16x32_bf16 v[52:55], a[32:35], a[160:163], 0\n"
"v_mfma_f32_16x16x32_bf16 v[52:55], a[36:39], a[164:167], v[52:55]\n"
"v_mfma_f32_16x16x32_bf16 v[52:55], a[40:43], a[168:171], v[52:55]\n"
"v_cvt_pk_bf16_f32 v22, v164, v165\n"
"v_cvt_pk_bf16_f32 v23, v166, v167\n"
"v_mfma_f32_16x16x32_bf16 v[52:55], a[44:47], a[172:175], v[52:55]\n"
"v_mfma_f32_16x16x32_bf16 v[64:67], a[32:35], a[176:179], 0\n"
"v_mfma_f32_16x16x32_bf16 v[64:67], a[36:39], a[180:183], v[64:67]\n"
"v_mfma_f32_16x16x32_bf16 v[64:67], a[40:43], a[184:187], v[64:67]\n"
"v_cvt_pk_bf16_f32 v52, v52, v53\n"
"v_cvt_pk_bf16_f32 v53, v54, v55\n"
"v_mfma_f32_16x16x32_bf16 v[64:67], a[44:47], a[188:191], v[64:67]\n"
"s_add_u32 m0, s28, 8704\n"
"s_nop 0\n"
"buffer_load_dwordx4 v152, s[24:27], s60 offen offset:0 lds\n"
"v_mfma_f32_16x16x32_bf16 v[76:79], a[32:35], a[192:195], 0\n"
"s_waitcnt vmcnt(5)\n"
"ds_read_b128 a[48:51], v154 offset:0\n"
"ds_read_b128 a[52:55], v154 offset:64\n"
"v_mfma_f32_16x16x32_bf16 v[76:79], a[36:39], a[196:199], v[76:79]\n"
"ds_read_b128 a[56:59], v154 offset:128\n"
"ds_read_b128 a[60:63], v154 offset:192\n"
"v_mfma_f32_16x16x32_bf16 v[76:79], a[40:43], a[200:203], v[76:79]\n"
"v_cvt_pk_bf16_f32 v64, v64, v65\n"
"v_cvt_pk_bf16_f32 v65, v66, v67\n"
"v_mfma_f32_16x16x32_bf16 v[76:79], a[44:47], a[204:207], v[76:79]\n"
"v_mfma_f32_16x16x32_bf16 v[88:91], a[32:35], a[208:211], 0\n"
"v_mfma_f32_16x16x32_bf16 v[88:91], a[36:39], a[212:215], v[88:91]\n"
"v_mfma_f32_16x16x32_bf16 v[88:91], a[40:43], a[216:219], v[88:91]\n"
"v_cvt_pk_bf16_f32 v76, v76, v77\n"
"v_cvt_pk_bf16_f32 v77, v78, v79\n"
"v_mfma_f32_16x16x32_bf16 v[88:91], a[44:47], a[220:223], v[88:91]\n"
"s_nop 7\n"
"v_cvt_pk_bf16_f32 v88, v88, v89\n"
"buffer_load_dwordx4 v152, s[24:27], s61 offen offset:1088 lds\n"
"s_waitcnt lgkmcnt(3)\n"
"v_mfma_f32_16x16x32_bf16 v[24:27], a[96:99], a[48:51], 0\n"
"s_waitcnt lgkmcnt(2)\n"
"v_cvt_pk_bf16_f32 v89, v90, v91\n"
"v_mfma_f32_16x16x32_bf16 v[24:27], a[100:103], a[52:55], v[24:27]\n"
"s_waitcnt lgkmcnt(1)\n"
"v_mfma_f32_16x16x32_bf16 v[24:27], a[104:107], a[56:59], v[24:27]\n"
"s_waitcnt lgkmcnt(0)\n"
"v_mfma_f32_16x16x32_bf16 v[24:27], a[108:111], a[60:63], v[24:27]\n"
"v_mfma_f32_16x16x32_bf16 v[172:175], a[60:63], a[172:175], 0\n"
"ds_read_b128 a[172:175], v153 offset:62112\n"
"v_mfma_f32_16x16x32_bf16 v[172:175], a[56:59], a[168:171], v[172:175]\n"
"ds_read_b128 a[168:171], v153 offset:57920\n"
"v_mfma_f32_16x16x32_bf16 v[172:175], a[52:55], a[164:167], v[172:175]\n"
"ds_read_b128 a[164:167], v153 offset:53728\n"
"v_cvt_pk_bf16_f32 v24, v24, v25\n"
"v_mfma_f32_16x16x32_bf16 v[172:175], a[48:51], a[160:163], v[172:175]\n"
"ds_read_b128 a[160:163], v153 offset:49536\n"
"v_cvt_pk_bf16_f32 v25, v26, v27\n"
"v_mfma_f32_16x16x32_bf16 v[176:179], a[60:63], a[188:191], 0\n"
"ds_read_b128 a[188:191], v153 offset:63168\n"
"v_mfma_f32_16x16x32_bf16 v[176:179], a[56:59], a[184:187], v[176:179]\n"
"ds_read_b128 a[184:187], v153 offset:58976\n"
"v_mfma_f32_16x16x32_bf16 v[176:179], a[52:55], a[180:183], v[176:179]\n"
"buffer_load_dwordx4 v152, s[24:27], s62 offen offset:2176 lds\n"
"ds_read_b128 a[180:183], v153 offset:54784\n"
"v_mfma_f32_16x16x32_bf16 v[176:179], a[48:51], a[176:179], v[176:179]\n"
"ds_read_b128 a[176:179], v153 offset:50592\n"
"v_cvt_pk_bf16_f32 v54, v172, v173\n"
"v_mfma_f32_16x16x32_bf16 v[28:31], a[128:131], a[48:51], 0\n"
"v_cvt_pk_bf16_f32 v55, v174, v175\n"
"v_mfma_f32_16x16x32_bf16 v[28:31], a[132:135], a[52:55], v[28:31]\n"
"v_mfma_f32_16x16x32_bf16 v[28:31], a[136:139], a[56:59], v[28:31]\n"
"v_cvt_pk_bf16_f32 v66, v176, v177\n"
"v_cvt_pk_bf16_f32 v67, v178, v179\n"
"v_mfma_f32_16x16x32_bf16 v[28:31], a[140:143], a[60:63], v[28:31]\n"
"v_mfma_f32_16x16x32_bf16 v[172:175], a[60:63], a[204:207], 0\n"
"ds_read_b128 a[204:207], v153 offset:64224\n"
"v_mfma_f32_16x16x32_bf16 v[172:175], a[56:59], a[200:203], v[172:175]\n"
"ds_read_b128 a[200:203], v153 offset:60032\n"
"v_mfma_f32_16x16x32_bf16 v[172:175], a[52:55], a[196:199], v[172:175]\n"
"ds_read_b128 a[196:199], v153 offset:55840\n"
"v_cvt_pk_bf16_f32 v28, v28, v29\n"
"v_mfma_f32_16x16x32_bf16 v[172:175], a[48:51], a[192:195], v[172:175]\n"
"ds_read_b128 a[192:195], v153 offset:51648\n"
"v_cvt_pk_bf16_f32 v29, v30, v31\n"
"v_mfma_f32_16x16x32_bf16 v[176:179], a[60:63], a[220:223], 0\n"
"ds_read_b128 a[220:223], v153 offset:65280\n"
"v_mfma_f32_16x16x32_bf16 v[176:179], a[56:59], a[216:219], v[176:179]\n"
"buffer_load_dwordx4 v152, s[24:27], s63 offen offset:3264 lds\n"
"ds_read_b128 a[216:219], v153 offset:61088\n"
"v_mfma_f32_16x16x32_bf16 v[176:179], a[52:55], a[212:215], v[176:179]\n"
"ds_read_b128 a[212:215], v153 offset:56896\n"
"v_cvt_pk_bf16_f32 v78, v172, v173\n"
"v_mfma_f32_16x16x32_bf16 v[176:179], a[48:51], a[208:211], v[176:179]\n"
"ds_read_b128 a[208:211], v153 offset:52704\n"
"s_add_u32 s24, s24, 13107200\n"
"s_addc_u32 s25, s25, 0\n"
"s_sub_u32 s26, s26, 13107200\n"
"s_cselect_b32 s26, 0, s26\n"
"v_mfma_f32_16x16x32_bf16 v[168:171], a[144:147], a[48:51], 0\n"
"v_cvt_pk_bf16_f32 v79, v174, v175\n"
"v_mfma_f32_16x16x32_bf16 v[168:171], a[148:151], a[52:55], v[168:171]\n"
"v_cvt_pk_bf16_f32 v90, v176, v177\n"
"v_cvt_pk_bf16_f32 v91, v178, v179\n"
"v_mfma_f32_16x16x32_bf16 v[168:171], a[152:155], a[56:59], v[168:171]\n"
"v_mfma_f32_16x16x32_bf16 v[168:171], a[156:159], a[60:63], v[168:171]\n"
"v_mfma_f32_16x16x32_bf16 v[176:179], a[112:115], a[48:51], 0\n"
"v_mfma_f32_16x16x32_bf16 v[176:179], a[116:119], a[52:55], v[176:179]\n"
"v_mfma_f32_16x16x32_bf16 v[176:179], a[120:123], a[56:59], v[176:179]\n"
"v_cvt_pk_bf16_f32 v30, v168, v169\n"
"v_cvt_pk_bf16_f32 v31, v170, v171\n"
"v_mfma_f32_16x16x32_bf16 v[176:179], a[124:127], a[60:63], v[176:179]\n"
"s_waitcnt lgkmcnt(4)\n"
"v_mfma_f32_16x16x32_bf16 v[96:99], v[204:207], a[160:163], 0\n"
"v_mfma_f32_16x16x32_bf16 v[96:99], v[208:211], a[164:167], v[96:99]\n"
"v_mfma_f32_16x16x32_bf16 v[96:99], v[212:215], a[168:171], v[96:99]\n"
"v_cvt_pk_bf16_f32 v26, v176, v177\n"
"v_cvt_pk_bf16_f32 v27, v178, v179\n"
"v_mfma_f32_16x16x32_bf16 v[96:99], v[216:219], a[172:175], v[96:99]\n"
"s_waitcnt lgkmcnt(0)\n"
"s_barrier\n"
"v_mfma_f32_16x16x32_bf16 v[100:103], v[204:207], a[176:179], 0\n"
"v_mfma_f32_16x16x32_bf16 v[100:103], v[208:211], a[180:183], v[100:103]\n"
"s_add_u32 m0, s28, 0\n"
"s_nop 0\n"
"buffer_load_dwordx4 v152, s[24:27], s40 offen offset:0 lds\n"
"v_mfma_f32_16x16x32_bf16 v[100:103], v[212:215], a[184:187], v[100:103]\n"
"s_waitcnt vmcnt(5)\n"
"ds_read_b128 a[64:67], v154 offset:4352\n"
"ds_read_b128 a[68:71], v154 offset:4416\n"
"v_mfma_f32_16x16x32_bf16 v[100:103], v[216:219], a[188:191], v[100:103]\n"
"ds_read_b128 a[72:75], v154 offset:4480\n"
"ds_read_b128 a[76:79], v154 offset:4544\n"
"v_mfma_f32_16x16x32_bf16 v[104:107], v[204:207], a[192:195], 0\n"
"v_mfma_f32_16x16x32_bf16 v[104:107], v[208:211], a[196:199], v[104:107]\n"
"v_mfma_f32_16x16x32_bf16 v[104:107], v[212:215], a[200:203], v[104:107]\n"
"v_cvt_pk_bf16_f32 v96, v96, v100\n"
"v_cvt_pk_bf16_f32 v100, v97, v101\n"
"v_mfma_f32_16x16x32_bf16 v[104:107], v[216:219], a[204:207], v[104:107]\n"
"v_cvt_pk_bf16_f32 v98, v98, v102\n"
"v_cvt_pk_bf16_f32 v102, v99, v103\n"
"v_mfma_f32_16x16x32_bf16 v[108:111], v[204:207], a[208:211], 0\n"
"v_mfma_f32_16x16x32_bf16 v[108:111], v[208:211], a[212:215], v[108:111]\n"
"v_mfma_f32_16x16x32_bf16 v[108:111], v[212:215], a[216:219], v[108:111]\n"
"v_mfma_f32_16x16x32_bf16 v[108:111], v[216:219], a[220:223], v[108:111]\n"
"v_accvgpr_read_b32 v219, a240\n"
"v_mfma_f32_16x16x32_bf16 v[112:115], v[232:235], a[172:175], 0\n"
"buffer_load_dwordx4 v152, s[24:27], s41 offen offset:1088 lds\n"
"v_mfma_f32_16x16x32_bf16 v[112:115], v[228:231], a[168:171], v[112:115]\n"
"v_mfma_f32_16x16x32_bf16 v[112:115], v[224:227], a[164:167], v[112:115]\n"
"v_cvt_pk_bf16_f32 v97, v104, v108\n"
"v_cvt_pk_bf16_f32 v101, v105, v109\n"
"v_mfma_f32_16x16x32_bf16 v[112:115], v[220:223], a[160:163], v[112:115]\n"
"ds_write_b64 v134, v[96:97] offset:16384\n"
"v_mfma_f32_16x16x32_bf16 v[116:119], v[232:235], a[188:191], 0\n"
".Lpad1_%=:\n"
"s_cbranch_vccnz .Lpad6_%=\n"
"v_cvt_pk_bf16_f32 v99, v106, v110\n"
"v_cvt_pk_bf16_f32 v103, v107, v111\n"
"v_mfma_f32_16x16x32_bf16 v[116:119], v[228:231], a[184:187], v[116:119]\n"
"v_mfma_f32_16x16x32_bf16 v[116:119], v[224:227], a[180:183], v[116:119]\n"
"v_mfma_f32_16x16x32_bf16 v[116:119], v[220:223], a[176:179], v[116:119]\n"
"ds_write_b64 v134, v[100:101] offset:16512\n"
"v_mfma_f32_16x16x32_bf16 v[108:111], v[0:3], v[188:191], 0\n"
"v_mfma_f32_16x16x32_bf16 v[108:111], v[4:7], v[192:195], v[108:111]\n"
"ds_write_b64 v134, v[98:99] offset:16640\n"
"v_mfma_f32_16x16x32_bf16 v[96:99], v[0:3], v[180:183], 0\n"
"buffer_load_dwordx4 v152, s[24:27], s42 offen offset:2176 lds\n"
"v_cvt_pk_bf16_f32 v112, v112, v116\n"
"v_mfma_f32_16x16x32_bf16 v[96:99], v[4:7], v[184:187], v[96:99]\n"
"ds_write_b64 v134, v[102:103] offset:16768\n"
"v_mfma_f32_16x16x32_bf16 v[100:103], v[16:19], v[180:183], 0\n"
"v_exp_f32 v108, v108\n"
"v_mfma_f32_16x16x32_bf16 v[100:103], v[20:23], v[184:187], v[100:103]\n"
"v_exp_f32 v109, v109\n"
"v_mfma_f32_16x16x32_bf16 v[120:123], v[232:235], a[204:207], 0\n"
"v_exp_f32 v96, v96\n"
"v_mfma_f32_16x16x32_bf16 v[120:123], v[228:231], a[200:203], v[120:123]\n"
"v_exp_f32 v97, v97\n"
"v_mfma_f32_16x16x32_bf16 v[120:123], v[224:227], a[196:199], v[120:123]\n"
"v_exp_f32 v98, v98\n"
"v_mfma_f32_16x16x32_bf16 v[120:123], v[220:223], a[192:195], v[120:123]\n"
"v_exp_f32 v99, v99\n"
"v_mfma_f32_16x16x32_bf16 v[124:127], v[232:235], a[220:223], 0\n"
"v_cvt_pk_bf16_f32 v96, v96, v97\n"
"v_cvt_pk_bf16_f32 v97, v98, v99\n"
"v_mfma_f32_16x16x32_bf16 v[124:127], v[228:231], a[216:219], v[124:127]\n"
"v_mov_b32 v228, 0\n"
"v_mov_b32 v229, 0\n"
"v_mfma_f32_16x16x32_bf16 v[124:127], v[224:227], a[212:215], v[124:127]\n"
"v_mov_b32 v224, 0\n"
"v_mov_b32 v225, 0\n"
"v_mfma_f32_16x16x32_bf16 v[124:127], v[220:223], a[208:211], v[124:127]\n"
"buffer_load_dwordx4 v152, s[24:27], s43 offen offset:3264 lds\n"
"v_mov_b32 v220, 0\n"
"v_smfmac_f32_16x16x64_bf16 v[224:227], a[236:239], v[8:15], v157\n"
"v_mov_b32 v221, 0\n"
"v_smfmac_f32_16x16x64_bf16 v[228:231], a[236:239], v[16:23], v157\n"
"v_exp_f32 v100, v100\n"
"v_smfmac_f32_16x16x64_bf16 v[220:223], a[236:239], v[0:7], v157\n"
"s_waitcnt lgkmcnt(0)\n"
"v_cvt_pk_bf16_f32 v108, v108, v109\n"
"v_mfma_f32_16x16x32_bf16 v[32:35], a[96:99], a[64:67], 0\n"
"v_add_f32 v209, v224, v225\n"
"v_mov_b32 v224, 0\n"
"v_mfma_f32_16x16x32_bf16 v[32:35], a[100:103], a[68:71], v[32:35]\n"
"v_exp_f32 v209, v209\n"
"v_mfma_f32_16x16x32_bf16 v[32:35], a[104:107], a[72:75], v[32:35]\n"
"v_add_f32 v208, v220, v221\n"
"v_add_f32 v210, v228, v229\n"
"v_mfma_f32_16x16x32_bf16 v[32:35], a[108:111], a[76:79], v[32:35]\n"
"v_exp_f32 v208, v208\n"
"v_mfma_f32_16x16x32_bf16 v[176:179], a[112:115], a[64:67], 0\n"
"v_cvt_pk_bf16_f32 v215, v208, v209\n"
"v_mov_b32 v220, 0\n"
"v_mfma_f32_16x16x32_bf16 v[176:179], a[116:119], a[68:71], v[176:179]\n"
"ds_write_b16 v219, v215 offset:0\n"
"v_mfma_f32_16x16x32_bf16 v[176:179], a[120:123], a[72:75], v[176:179]\n"
"ds_write_b16_d16_hi v219, v215 offset:4\n"
"v_mfma_f32_16x16x32_bf16 v[176:179], a[124:127], a[76:79], v[176:179]\n"
"s_add_u32 m0, s28, 4352\n"
"s_nop 0\n"
"buffer_load_dwordx4 v152, s[24:27], s44 offen offset:0 lds\n"
"v_mfma_f32_16x16x32_bf16 v[36:39], a[128:131], a[64:67], 0\n"
"s_waitcnt vmcnt(5)\n"
"ds_read_b128 a[80:83], v154 offset:8704\n"
"ds_read_b128 a[84:87], v154 offset:8768\n"
"v_mfma_f32_16x16x32_bf16 v[36:39], a[132:135], a[68:71], v[36:39]\n"
"ds_read_b128 a[88:91], v154 offset:8832\n"
"ds_read_b128 a[92:95], v154 offset:8896\n"
"v_mfma_f32_16x16x32_bf16 v[36:39], a[136:139], a[72:75], v[36:39]\n"
"v_exp_f32 v210, v210\n"
"v_mfma_f32_16x16x32_bf16 v[36:39], a[140:143], a[76:79], v[36:39]\n"
"v_mov_b32 v221, 0\n"
"v_mov_b32 v225, 0\n"
"v_mfma_f32_16x16x32_bf16 v[164:167], a[144:147], a[64:67], 0\n"
"v_mov_b32 v228, 0\n"
"v_mov_b32 v229, 0\n"
"v_mfma_f32_16x16x32_bf16 v[164:167], a[148:151], a[68:71], v[164:167]\n"
"v_add_f32 v208, v209, v208\n"
"v_add_f32 v208, v210, v208\n"
"v_mfma_f32_16x16x32_bf16 v[164:167], a[152:155], a[72:75], v[164:167]\n"
"v_exp_f32 v101, v101\n"
"v_mfma_f32_16x16x32_bf16 v[164:167], a[156:159], a[76:79], v[164:167]\n"
"v_exp_f32 v102, v102\n"
"v_smfmac_f32_16x16x64_bf16 v[220:223], a[236:239], v[24:31], v157\n"
"v_cvt_pk_bf16_f32 v100, v100, v101\n"
"v_mfma_f32_16x16x32_bf16 v[56:59], a[64:67], v[236:239], 0\n"
"v_exp_f32 v103, v103\n"
"v_mfma_f32_16x16x32_bf16 v[56:59], a[68:71], v[240:243], v[56:59]\n"
"v_cvt_pk_bf16_f32 v101, v102, v103\n"
"v_cvt_pk_bf16_f32 v116, v113, v117\n"
"v_mfma_f32_16x16x32_bf16 v[56:59], a[72:75], v[244:247], v[56:59]\n"
"v_add_f32 v211, v220, v221\n"
"v_mov_b32 v220, 0\n"
"v_mfma_f32_16x16x32_bf16 v[56:59], a[76:79], v[248:251], v[56:59]\n"
"s_waitcnt lgkmcnt(3)\n"
"v_exp_f32 v211, v211\n"
"v_mfma_f32_16x16x32_bf16 v[40:43], a[96:99], a[80:83], 0\n"
"s_waitcnt lgkmcnt(0)\n"
"v_cvt_pk_bf16_f32 v216, v210, v211\n"
"v_mov_b32 v221, 0\n"
"v_mfma_f32_16x16x32_bf16 v[40:43], a[100:103], a[84:87], v[40:43]\n"
"ds_write_b16 v219, v216 offset:512\n"
"v_mfma_f32_16x16x32_bf16 v[40:43], a[104:107], a[88:91], v[40:43]\n"
"ds_write_b16_d16_hi v219, v216 offset:516\n"
"v_mfma_f32_16x16x32_bf16 v[40:43], a[108:111], a[92:95], v[40:43]\n"
"v_add_f32 v208, v211, v208\n"
"v_exp_f32 v110, v110\n"
"v_smfmac_f32_16x16x64_bf16 v[220:223], a[236:239], a[228:235], v157\n"
"buffer_load_dwordx4 v152, s[24:27], s45 offen offset:1088 lds\n"
"v_mfma_f32_16x16x32_bf16 v[44:47], a[128:131], a[80:83], 0\n"
"ds_read_b128 a[228:231], v161 offset:0\n"
"ds_read_b128 a[232:235], v161 offset:2048\n"
"v_mfma_f32_16x16x32_bf16 v[44:47], a[132:135], a[84:87], v[44:47]\n"
"v_exp_f32 v111, v111\n"
"v_mfma_f32_16x16x32_bf16 v[44:47], a[136:139], a[88:91], v[44:47]\n"
"v_add_f32 v214, v220, v221\n"
"v_cvt_pk_bf16_f32 v109, v110, v111\n"
"v_mfma_f32_16x16x32_bf16 v[44:47], a[140:143], a[92:95], v[44:47]\n"
"v_exp_f32 v214, v214\n"
"v_mfma_f32_16x16x32_bf16 v[168:171], a[144:147], a[80:83], 0\n"
"v_cvt_pk_bf16_f32 v218, v214, v214\n"
"v_cvt_pk_bf16_f32 v113, v120, v124\n"
"v_mfma_f32_16x16x32_bf16 v[168:171], a[148:151], a[84:87], v[168:171]\n"
"ds_write_b16 v219, v218 offset:2048\n"
"v_mfma_f32_16x16x32_bf16 v[168:171], a[152:155], a[88:91], v[168:171]\n"
"v_cvt_pk_bf16_f32 v117, v121, v125\n"
"v_cvt_pk_bf16_f32 v114, v114, v118\n"
"v_mfma_f32_16x16x32_bf16 v[168:171], a[156:159], a[92:95], v[168:171]\n"
"ds_write_b64 v134, v[112:113] offset:18432\n"
"v_mfma_f32_16x16x32_bf16 v[68:71], a[92:95], v[248:251], 0\n"
"ds_read_b128 v[248:251], v153 offset:30016\n"
"v_cvt_pk_bf16_f32 v118, v115, v119\n"
"v_mfma_f32_16x16x32_bf16 v[68:71], a[88:91], v[244:247], v[68:71]\n"
"ds_read_b128 v[244:247], v153 offset:25824\n"
"v_cvt_pk_bf16_f32 v115, v122, v126\n"
"v_mfma_f32_16x16x32_bf16 v[68:71], a[84:87], v[240:243], v[68:71]\n"
"buffer_load_dwordx4 v152, s[24:27], s46 offen offset:2176 lds\n"
"ds_read_b128 v[240:243], v153 offset:21632\n"
"v_mfma_f32_16x16x32_bf16 v[68:71], a[80:83], v[236:239], v[68:71]\n"
"ds_write_b64 v134, v[116:117] offset:18560\n"
"v_mfma_f32_16x16x32_bf16 v[80:83], a[64:67], v[136:139], 0\n"
"ds_read_b128 v[236:239], v153 offset:17440\n"
"v_cvt_pk_bf16_f32 v119, v123, v127\n"
"v_mfma_f32_16x16x32_bf16 v[80:83], a[68:71], v[140:143], v[80:83]\n"
"v_cvt_pk_bf16_f32 v32, v32, v33\n"
"v_cvt_pk_bf16_f32 v33, v34, v35\n"
"v_mfma_f32_16x16x32_bf16 v[80:83], a[72:75], v[144:147], v[80:83]\n"
"v_cvt_pk_bf16_f32 v34, v176, v177\n"
"v_cvt_pk_bf16_f32 v35, v178, v179\n"
"v_mfma_f32_16x16x32_bf16 v[80:83], a[76:79], v[148:151], v[80:83]\n"
"ds_write_b64 v134, v[114:115] offset:18688\n"
"v_mfma_f32_16x16x32_bf16 v[120:123], v[12:15], v[184:187], 0\n"
"v_cvt_pk_bf16_f32 v36, v36, v37\n"
"v_cvt_pk_bf16_f32 v37, v38, v39\n"
"v_mfma_f32_16x16x32_bf16 v[120:123], v[8:11], v[180:183], v[120:123]\n"
"ds_write_b64 v134, v[118:119] offset:18816\n"
"v_mfma_f32_16x16x32_bf16 v[124:127], v[28:31], v[184:187], 0\n"
"ds_read_b64 v[206:207], v163 offset:16384\n"
"v_mov_b32 v134, 0\n"
"v_mfma_f32_16x16x32_bf16 v[124:127], v[24:27], v[180:183], v[124:127]\n"
"v_cvt_pk_bf16_f32 v38, v164, v165\n"
"v_cvt_pk_bf16_f32 v39, v166, v167\n"
"v_mfma_f32_16x16x32_bf16 v[104:107], v[32:35], v[180:183], 0\n"
"buffer_load_dwordx4 v152, s[24:27], s47 offen offset:3264 lds\n"
"v_cvt_pk_bf16_f32 v40, v40, v41\n"
"v_mfma_f32_16x16x32_bf16 v[104:107], v[36:39], v[184:187], v[104:107]\n"
"v_exp_f32 v120, v120\n"
"v_smfmac_f32_16x16x64_bf16 v[224:227], a[236:239], v[32:39], v157\n"
"v_exp_f32 v121, v121\n"
"v_mfma_f32_16x16x32_bf16 v[112:115], v[16:19], v[188:191], 0\n"
"v_exp_f32 v122, v122\n"
"v_mfma_f32_16x16x32_bf16 v[112:115], v[20:23], v[192:195], v[112:115]\n"
"v_exp_f32 v123, v123\n"
"v_mfma_f32_16x16x32_bf16 v[116:119], v[32:35], v[188:191], 0\n"
"v_add_f32 v212, v224, v225\n"
"v_cvt_pk_bf16_f32 v98, v120, v121\n"
"v_mfma_f32_16x16x32_bf16 v[116:119], v[36:39], v[192:195], v[116:119]\n"
"v_exp_f32 v212, v212\n"
"v_mfma_f32_16x16x32_bf16 v[176:179], a[112:115], a[80:83], 0\n"
"v_add_f32 v208, v212, v208\n"
"v_cvt_pk_bf16_f32 v99, v122, v123\n"
"v_mfma_f32_16x16x32_bf16 v[176:179], a[116:119], a[84:87], v[176:179]\n"
"v_exp_f32 v124, v124\n"
"v_mfma_f32_16x16x32_bf16 v[176:179], a[120:123], a[88:91], v[176:179]\n"
"v_exp_f32 v125, v125\n"
"v_mfma_f32_16x16x32_bf16 v[176:179], a[124:127], a[92:95], v[176:179]\n"
"v_exp_f32 v126, v126\n"
"v_mfma_f32_16x16x32_bf16 v[92:95], a[92:95], v[148:151], 0\n"
"s_add_u32 m0, s28, 8704\n"
"s_nop 0\n"
"buffer_load_dwordx4 v152, s[24:27], s48 offen offset:0 lds\n"
"v_mfma_f32_16x16x32_bf16 v[92:95], a[88:91], v[144:147], v[92:95]\n"
"ds_read_b128 v[144:147], v153 offset:27936\n"
"ds_read_b128 v[148:151], v153 offset:32128\n"
"v_mfma_f32_16x16x32_bf16 v[92:95], a[84:87], v[140:143], v[92:95]\n"
"ds_read_b128 v[140:143], v153 offset:23744\n"
"v_cvt_pk_bf16_f32 v102, v124, v125\n"
"v_mfma_f32_16x16x32_bf16 v[92:95], a[80:83], v[136:139], v[92:95]\n"
"s_waitcnt lgkmcnt(3)\n"
"ds_read_b128 v[136:139], v153 offset:19552\n"
"v_exp_f32 v127, v127\n"
"v_mfma_f32_16x16x32_bf16 v[172:175], a[92:95], v[248:251], 0\n"
"v_exp_f32 v104, v104\n"
"v_mfma_f32_16x16x32_bf16 v[172:175], a[88:91], v[244:247], v[172:175]\n"
"v_cvt_pk_bf16_f32 v103, v126, v127\n"
"v_cvt_pk_bf16_f32 v41, v42, v43\n"
"v_mfma_f32_16x16x32_bf16 v[172:175], a[84:87], v[240:243], v[172:175]\n"
"v_exp_f32 v105, v105\n"
"v_mfma_f32_16x16x32_bf16 v[172:175], a[80:83], v[236:239], v[172:175]\n"
"v_exp_f32 v106, v106\n"
"v_mfma_f32_16x16x32_bf16 v[124:127], v[12:15], v[192:195], 0\n"
"v_exp_f32 v107, v107\n"
"v_mfma_f32_16x16x32_bf16 v[124:127], v[8:11], v[188:191], v[124:127]\n"
"v_cvt_pk_bf16_f32 v104, v104, v105\n"
"v_cvt_pk_bf16_f32 v105, v106, v107\n"
"v_exp_f32 v112, v112\n"
"v_exp_f32 v113, v113\n"
"buffer_load_dwordx4 v152, s[24:27], s49 offen offset:1088 lds\n"
"v_exp_f32 v114, v114\n"
"v_exp_f32 v124, v124\n"
"v_exp_f32 v125, v125\n"
"v_exp_f32 v126, v126\n"
"v_exp_f32 v127, v127\n"
"v_cvt_pk_bf16_f32 v110, v124, v125\n"
"v_exp_f32 v115, v115\n"
"v_cvt_pk_bf16_f32 v111, v126, v127\n"
"v_cvt_pk_bf16_f32 v112, v112, v113\n"
"v_cvt_pk_bf16_f32 v113, v114, v115\n"
"v_cvt_pk_bf16_f32 v42, v176, v177\n"
"v_cvt_pk_bf16_f32 v43, v178, v179\n"
"s_waitcnt lgkmcnt(0)\n"
"v_mfma_f32_16x16x32_bf16 v[176:179], a[92:95], v[148:151], 0\n"
"v_cvt_pk_bf16_f32 v44, v44, v45\n"
"v_cvt_pk_bf16_f32 v45, v46, v47\n"
"v_mfma_f32_16x16x32_bf16 v[176:179], a[88:91], v[144:147], v[176:179]\n"
"v_cvt_pk_bf16_f32 v46, v168, v169\n"
"v_cvt_pk_bf16_f32 v47, v170, v171\n"
"v_mfma_f32_16x16x32_bf16 v[176:179], a[84:87], v[140:143], v[176:179]\n"
"v_cvt_pk_bf16_f32 v56, v56, v57\n"
"v_cvt_pk_bf16_f32 v57, v58, v59\n"
"v_mfma_f32_16x16x32_bf16 v[176:179], a[80:83], v[136:139], v[176:179]\n"
"v_cvt_pk_bf16_f32 v58, v68, v69\n"
"v_cvt_pk_bf16_f32 v59, v70, v71\n"
"v_smfmac_f32_16x16x64_bf16 v[228:231], a[236:239], v[40:47], v157\n"
"ds_read_b64 a[236:237], v156 offset:0\n"
"v_mfma_f32_16x16x32_bf16 v[120:123], v[44:47], v[184:187], 0\n"
"ds_read_b64 a[238:239], v156 offset:64\n"
"v_cvt_pk_bf16_f32 v80, v80, v81\n"
"v_mfma_f32_16x16x32_bf16 v[120:123], v[40:43], v[180:183], v[120:123]\n"
"v_cvt_pk_bf16_f32 v81, v82, v83\n"
"v_cvt_pk_bf16_f32 v82, v92, v93\n"
"v_mfma_f32_16x16x32_bf16 v[124:127], v[44:47], v[192:195], 0\n"
"v_add_f32 v213, v228, v229\n"
"v_cvt_pk_bf16_f32 v83, v94, v95\n"
"v_mfma_f32_16x16x32_bf16 v[124:127], v[40:43], v[188:191], v[124:127]\n"
"v_exp_f32 v213, v213\n"
"v_mfma_f32_16x16x32_bf16 v[68:71], a[64:67], v[236:239], 0\n"
"ds_read2_b32 v[236:237], v158 offset0:0 offset1:2\n"
"ds_read2_b32 v[238:239], v158 offset0:128 offset1:130\n"
"v_mfma_f32_16x16x32_bf16 v[68:71], a[68:71], v[240:243], v[68:71]\n"
"v_cvt_pk_bf16_f32 v217, v212, v213\n"
"v_add_f32 v208, v213, v208\n"
"v_mfma_f32_16x16x32_bf16 v[68:71], a[72:75], v[244:247], v[68:71]\n"
"ds_write_b16 v219, v217 offset:1536\n"
"v_mfma_f32_16x16x32_bf16 v[68:71], a[76:79], v[248:251], v[68:71]\n"
"ds_write_b16_d16_hi v219, v217 offset:1540\n"
"v_mfma_f32_16x16x32_bf16 v[92:95], a[64:67], v[136:139], 0\n"
"buffer_load_dwordx4 v152, s[24:27], s50 offen offset:2176 lds\n"
"ds_read_b128 v[244:247], v153 offset:5120\n"
"v_mfma_f32_16x16x32_bf16 v[92:95], a[68:71], v[140:143], v[92:95]\n"
"ds_read_b128 v[140:143], v153 offset:0\n"
"ds_read_b128 v[248:251], v153 offset:9216\n"
"v_mfma_f32_16x16x32_bf16 v[92:95], a[72:75], v[144:147], v[92:95]\n"
"ds_read_b128 v[144:147], v153 offset:4096\n"
"ds_read_b128 v[136:139], v153 offset:2048\n"
"v_mfma_f32_16x16x32_bf16 v[92:95], a[76:79], v[148:151], v[92:95]\n"
"v_accvgpr_read_b32 v219, a241\n"
"ds_read_b128 v[220:223], v219 offset:0\n"
"v_add_f32_dpp v208, v214, v208 quad_perm:[0,1,2,3] row_mask:0xf bank_mask:0x1\n"
"ds_read_b128 v[224:227], v219 offset:1024\n"
"v_accvgpr_read_b32 v219, a242\n"
"ds_read2_b32 v[228:229], v219 offset0:0 offset1:4\n"
"ds_read_b128 v[148:151], v153 offset:8192\n"
"v_add_f32_dpp v208, v208, v208 row_ror:8 row_mask:0xf bank_mask:0xf\n"
"s_waitcnt lgkmcnt(11)\n"
"v_smfmac_f32_16x16x64_bf16 v[128:131], v[236:239], v[180:187], v157\n"
"v_add_f32_dpp v208, v208, v208 row_ror:4 row_mask:0xf bank_mask:0xf\n"
"v_smfmac_f32_16x16x64_bf16 v[132:135], v[236:239], v[196:203], v157\n"
"ds_read_b128 v[236:239], v153 offset:12288\n"
"v_add_f32_dpp v208, v208, v208 row_ror:2 row_mask:0xf bank_mask:0xf\n"
"v_exp_f32 v120, v120\n"
"v_exp_f32 v121, v121\n"
"v_add_f32_dpp v208, v208, v208 row_ror:1 row_mask:0xf bank_mask:0xf\n"
"v_rcp_f32 v209, v208\n"
"buffer_load_dwordx4 v152, s[24:27], s51 offen offset:3264 lds\n"
"v_max3_f32 v159, v159, v208, v209\n"
"v_mov_b32_dpp v158, v158 row_ror:8 row_mask:0xf bank_mask:0xf\n"
"ds_read2_b32 v[240:241], v158 offset0:0 offset1:2\n"
"ds_read2_b32 v[242:243], v158 offset0:128 offset1:130\n"
"v_exp_f32 v122, v122\n"
"v_exp_f32 v123, v123\n"
"v_cvt_pk_bf16_f32 v106, v120, v121\n"
"v_cvt_pk_bf16_f32 v68, v68, v69\n"
"v_cvt_pk_bf16_f32 v107, v122, v123\n"
"v_mfma_f32_16x16x32_bf16 v[120:123], v[28:31], v[192:195], 0\n"
"v_cvt_pk_bf16_f32 v69, v70, v71\n"
"v_cvt_pk_bf16_f32 v70, v172, v173\n"
"v_mfma_f32_16x16x32_bf16 v[120:123], v[24:27], v[188:191], v[120:123]\n"
"v_cvt_pk_bf16_f32 v71, v174, v175\n"
"v_cvt_pk_bf16_f32 v92, v92, v93\n"
"v_cvt_pk_bf16_f32 v93, v94, v95\n"
"v_cvt_pk_bf16_f32 v94, v176, v177\n"
"v_cvt_pk_bf16_f32 v95, v178, v179\n"
"s_waitcnt lgkmcnt(4)\n"
"v_mfma_f32_16x16x4_4b_bf16 v[164:179], v[228:229], a[244:245], 0 cbsz:2 abid:0\n"
"v_add_u32 v163, 512, v163\n"
"s_add_u32 s84, s84, 13107200\n"
"s_addc_u32 s85, s85, 0\n"
"v_lshlrev_b32 v204, 16, v206\n"
"v_exp_f32 v120, v120\n"
"buffer_load_dword a243, v162, s[80:83], s70 offen\n"
"v_exp_f32 v121, v121\n"
"v_and_b32 v205, 0xffff0000, v206\n"
"v_lshlrev_b32 v206, 16, v207\n"
"v_and_b32 v207, 0xffff0000, v207\n"
"s_nop 0\n"
"v_smfmac_f32_16x16x64_bf16 v[164:167], v[220:223], v[48:55], v157\n"
"v_add_u32 v156, 512, v156\n"
"v_smfmac_f32_16x16x64_bf16 v[164:167], v[224:227], v[52:59], v157\n"
"s_waitcnt lgkmcnt(0)\n"
"v_smfmac_f32_16x16x64_bf16 v[128:131], v[240:243], v[188:195], v157\n"
"ds_read_b128 v[240:243], v153 offset:1024\n"
"v_smfmac_f32_16x16x64_bf16 v[168:171], v[224:227], v[64:71], v157\n"
"v_smfmac_f32_16x16x64_bf16 v[168:171], v[220:223], v[60:67], v157\n"
"v_fmac_f32 v204, v209, v164\n"
"v_smfmac_f32_16x16x64_bf16 v[172:175], v[220:223], v[72:79], v157\n"
"v_add_f32 v128, v128, v129\n"
"v_smfmac_f32_16x16x64_bf16 v[172:175], v[224:227], v[76:83], v157\n"
"buffer_load_dword a243, v162, s[80:83], s70 offen\n"
"v_smfmac_f32_16x16x64_bf16 v[176:179], v[224:227], v[88:95], v157\n"
"v_add_f32 v130, v130, v131\n"
"v_smfmac_f32_16x16x64_bf16 v[176:179], v[220:223], v[84:91], v157\n"
"v_exp_f32 v128, v128\n"
"v_exp_f32 v130, v130\n"
"v_fmac_f32 v205, v209, v168\n"
"v_fmac_f32 v204, v209, v165\n"
"v_cvt_pk_bf16_f32 v129, v128, v130\n"
"v_add_f32 v128, v128, v130\n"
"v_fmac_f32 v205, v209, v169\n"
"s_nop 0\n"
"v_permlane16_swap_b32 v128, v129\n"
"v_mov_b32 v252, v128\n"
"v_mov_b32 v253, v129\n"
"v_mfma_f32_16x16x4_4b_bf16 v[220:235], v[128:129], a[244:245], 0 cbsz:2 abid:1\n"
"s_nop 0\n"
"v_smfmac_f32_16x16x64_bf16 v[252:255], a[248:251], v[96:103], v157\n"
"buffer_load_dword a243, v162, s[80:83], s70 offen\n"
"buffer_load_dword a243, v162, s[80:83], s70 offen\n"
"s_waitcnt vmcnt(12) lgkmcnt(0)\n"
"s_mov_b32 s74, 7\n"
"s_nop 11\n"
"s_branch .Lp2b%=\n"
".p2align 12\n"
".Lp2f%=:\n"
".Lp2b%=:\n"
"v_exp_f32 v122, v122\n"
"v_mfma_f32_16x16x32_bf16 v[180:183], v[140:143], a[48:51], 0\n"
".Lpad2_%=:\n"
"s_cbranch_vccnz .Lback0_%=\n"
"v_mov_b32 v254, v128\n"
"v_mov_b32 v255, v129\n"
"v_mfma_f32_16x16x32_bf16 v[180:183], v[144:147], a[52:55], v[180:183]\n"
"v_exp_f32 v123, v123\n"
"v_mfma_f32_16x16x32_bf16 v[180:183], v[148:151], a[56:59], v[180:183]\n"
"v_fmac_f32 v206, v209, v172\n"
"v_fmac_f32 v207, v209, v176\n"
"v_mfma_f32_16x16x32_bf16 v[180:183], v[236:239], a[60:63], v[180:183]\n"
"v_fmac_f32 v206, v209, v173\n"
"v_fmac_f32 v207, v209, v177\n"
"v_mfma_f32_16x16x32_bf16 v[220:223], v[96:99], v[48:51], v[220:223]\n"
"v_cvt_pk_bf16_f32 v114, v120, v121\n"
"v_cvt_pk_bf16_f32 v115, v122, v123\n"
"v_mfma_f32_16x16x32_bf16 v[220:223], v[100:103], v[52:55], v[220:223]\n"
"v_exp_f32 v116, v116\n"
"v_mfma_f32_16x16x32_bf16 v[220:223], v[104:107], v[56:59], v[220:223]\n"
"v_cvt_pk_bf16_f32 v204, v204, v205\n"
"v_cvt_pk_bf16_f32 v205, v206, v207\n"
"v_mfma_f32_16x16x32_bf16 v[224:227], v[104:107], v[68:71], v[224:227]\n"
"v_pk_max_i16 v204, v204, 0\n"
"v_pk_max_i16 v205, v205, 0\n"
"v_mfma_f32_16x16x32_bf16 v[224:227], v[100:103], v[64:67], v[224:227]\n"
"v_exp_f32 v117, v117\n"
"v_mfma_f32_16x16x32_bf16 v[224:227], v[96:99], v[60:63], v[224:227]\n"
"buffer_store_dwordx2 v[204:205], v155, s[84:87], s70 offen offset:0 sc0 sc1\n"
"v_smfmac_f32_16x16x64_bf16 v[252:255], a[252:255], v[104:111], v157\n"
"v_cvt_pk_bf16_f32 v116, v116, v117\n"
"v_mfma_f32_16x16x32_bf16 v[228:231], v[96:99], v[72:75], v[228:231]\n"
"v_exp_f32 v118, v118\n"
"v_mfma_f32_16x16x32_bf16 v[228:231], v[100:103], v[76:79], v[228:231]\n"
"v_exp_f32 v119, v119\n"
"v_mfma_f32_16x16x32_bf16 v[228:231], v[104:107], v[80:83], v[228:231]\n"
"v_add_f32 v130, v252, v253\n"
"v_cvt_pk_bf16_f32 v117, v118, v119\n"
"v_mfma_f32_16x16x32_bf16 v[232:235], v[104:107], v[92:95], v[232:235]\n"
"v_rcp_f32 v131, v130\n"
"v_mfma_f32_16x16x32_bf16 v[232:235], v[100:103], v[88:91], v[232:235]\n"
"v_max3_f32 v159, v159, v130, v131\n"
"v_cvt_pk_bf16_f32 v180, v180, v181\n"
"v_mfma_f32_16x16x32_bf16 v[232:235], v[96:99], v[84:87], v[232:235]\n"
"ds_bpermute_b32 v130, v160, v131 offset:0\n"
"v_mfma_f32_16x16x32_bf16 v[96:99], v[0:3], v[196:199], 0\n"
"v_exp_f32 v124, v124\n"
"v_mfma_f32_16x16x32_bf16 v[96:99], v[4:7], v[200:203], v[96:99]\n"
"v_exp_f32 v125, v125\n"
"v_mfma_f32_16x16x32_bf16 v[120:123], v[12:15], v[200:203], 0\n"
"v_exp_f32 v126, v126\n"
"v_mfma_f32_16x16x32_bf16 v[120:123], v[8:11], v[196:199], v[120:123]\n"
"v_exp_f32 v127, v127\n"
"v_mfma_f32_16x16x32_bf16 v[100:103], v[16:19], v[196:199], 0\n"
"v_cvt_pk_bf16_f32 v118, v124, v125\n"
"v_cvt_pk_bf16_f32 v119, v126, v127\n"
"v_mfma_f32_16x16x32_bf16 v[100:103], v[20:23], v[200:203], v[100:103]\n"
"v_exp_f32 v96, v96\n"
"v_mfma_f32_16x16x32_bf16 v[124:127], v[28:31], v[200:203], 0\n"
"v_exp_f32 v97, v97\n"
"v_mfma_f32_16x16x32_bf16 v[124:127], v[24:27], v[196:199], v[124:127]\n"
"v_exp_f32 v98, v98\n"
"v_mfma_f32_16x16x32_bf16 v[104:107], v[32:35], v[196:199], 0\n"
"v_exp_f32 v99, v99\n"
"v_mfma_f32_16x16x32_bf16 v[104:107], v[36:39], v[200:203], v[104:107]\n"
"v_cvt_pk_bf16_f32 v96, v96, v97\n"
"v_exp_f32 v120, v120\n"
"v_mfma_f32_16x16x4_4b_bf16 v[204:219], v[128:129], a[244:245], 0 cbsz:2 abid:3\n"
"s_waitcnt lgkmcnt(0)\n"
"v_mul_f32_dpp v220, v130, v220 quad_perm:[0,0,0,0] row_mask:0xf bank_mask:0xf\n"
"v_mul_f32_dpp v221, v130, v221 quad_perm:[1,1,1,1] row_mask:0xf bank_mask:0xf\n"
"v_mul_f32_dpp v222, v130, v222 quad_perm:[2,2,2,2] row_mask:0xf bank_mask:0xf\n"
"v_mul_f32_dpp v223, v130, v223 quad_perm:[3,3,3,3] row_mask:0xf bank_mask:0xf\n"
"v_mul_f32_dpp v224, v130, v224 quad_perm:[0,0,0,0] row_mask:0xf bank_mask:0xf\n"
"v_mul_f32_dpp v225, v130, v225 quad_perm:[1,1,1,1] row_mask:0xf bank_mask:0xf\n"
"v_mul_f32_dpp v226, v130, v226 quad_perm:[2,2,2,2] row_mask:0xf bank_mask:0xf\n"
"v_mul_f32_dpp v227, v130, v227 quad_perm:[3,3,3,3] row_mask:0xf bank_mask:0xf\n"
"v_mfma_f32_16x16x32_bf16 v[220:223], a[0:3], a[160:163], v[220:223]\n"
"v_mul_f32_dpp v228, v130, v228 quad_perm:[0,0,0,0] row_mask:0xf bank_mask:0xf\n"
"v_mul_f32_dpp v229, v130, v229 quad_perm:[1,1,1,1] row_mask:0xf bank_mask:0xf\n"
"v_mfma_f32_16x16x32_bf16 v[220:223], a[4:7], a[164:167], v[220:223]\n"
"v_mul_f32_dpp v230, v130, v230 quad_perm:[2,2,2,2] row_mask:0xf bank_mask:0xf\n"
"v_mul_f32_dpp v231, v130, v231 quad_perm:[3,3,3,3] row_mask:0xf bank_mask:0xf\n"
"v_mfma_f32_16x16x32_bf16 v[220:223], a[8:11], a[168:171], v[220:223]\n"
"v_mul_f32_dpp v232, v130, v232 quad_perm:[0,0,0,0] row_mask:0xf bank_mask:0xf\n"
"v_mul_f32_dpp v233, v130, v233 quad_perm:[1,1,1,1] row_mask:0xf bank_mask:0xf\n"
"v_mfma_f32_16x16x32_bf16 v[220:223], a[12:15], a[172:175], v[220:223]\n"
"v_mul_f32_dpp v234, v130, v234 quad_perm:[2,2,2,2] row_mask:0xf bank_mask:0xf\n"
"v_mul_f32_dpp v235, v130, v235 quad_perm:[3,3,3,3] row_mask:0xf bank_mask:0xf\n"
"v_mfma_f32_16x16x32_bf16 v[224:227], a[12:15], a[188:191], v[224:227]\n"
"v_cvt_pk_bf16_f32 v97, v98, v99\n"
"v_mfma_f32_16x16x32_bf16 v[224:227], a[8:11], a[184:187], v[224:227]\n"
"v_exp_f32 v121, v121\n"
"v_mfma_f32_16x16x32_bf16 v[224:227], a[4:7], a[180:183], v[224:227]\n"
"v_exp_f32 v122, v122\n"
"v_mfma_f32_16x16x32_bf16 v[224:227], a[0:3], a[176:179], v[224:227]\n"
"v_exp_f32 v123, v123\n"
"v_mfma_f32_16x16x32_bf16 v[228:231], a[0:3], a[192:195], v[228:231]\n"
"v_cvt_pk_bf16_f32 v98, v120, v121\n"
"v_cvt_pk_bf16_f32 v99, v122, v123\n"
"v_mfma_f32_16x16x32_bf16 v[228:231], a[4:7], a[196:199], v[228:231]\n"
"v_exp_f32 v100, v100\n"
"v_mfma_f32_16x16x32_bf16 v[228:231], a[8:11], a[200:203], v[228:231]\n"
"buffer_load_dword a243, v162, s[80:83], s88 offen sc1\n"
"v_mfma_f32_16x16x32_bf16 v[228:231], a[12:15], a[204:207], v[228:231]\n"
"v_cvt_pk_bf16_f32 v220, v220, v224\n"
"v_cvt_pk_bf16_f32 v224, v221, v225\n"
"v_mfma_f32_16x16x32_bf16 v[232:235], a[12:15], a[220:223], v[232:235]\n"
"s_waitcnt vmcnt(17)\n"
"ds_read_b128 a[12:15], v154 offset:192\n"
"v_pk_max_i16 v220, v220, 0\n"
"v_mfma_f32_16x16x32_bf16 v[232:235], a[8:11], a[216:219], v[232:235]\n"
"ds_read_b128 a[8:11], v154 offset:128\n"
"v_pk_max_i16 v224, v224, 0\n"
"v_mfma_f32_16x16x32_bf16 v[232:235], a[4:7], a[212:215], v[232:235]\n"
"ds_read_b128 a[4:7], v154 offset:64\n"
"v_cvt_pk_bf16_f32 v222, v222, v226\n"
"v_mfma_f32_16x16x32_bf16 v[232:235], a[0:3], a[208:211], v[232:235]\n"
"ds_read_b128 a[0:3], v154 offset:0\n"
"v_cvt_pk_bf16_f32 v226, v223, v227\n"
"v_mfma_f32_16x16x32_bf16 v[120:123], v[44:47], v[200:203], 0\n"
"v_pk_max_i16 v222, v222, 0\n"
"v_pk_max_i16 v226, v226, 0\n"
"v_mfma_f32_16x16x32_bf16 v[120:123], v[40:43], v[196:199], v[120:123]\n"
"v_exp_f32 v101, v101\n"
"v_mfma_f32_16x16x32_bf16 v[204:207], v[108:111], v[48:51], v[204:207]\n"
"v_cvt_pk_bf16_f32 v221, v228, v232\n"
"v_cvt_pk_bf16_f32 v225, v229, v233\n"
"v_mfma_f32_16x16x32_bf16 v[204:207], v[112:115], v[52:55], v[204:207]\n"
"v_pk_max_i16 v221, v221, 0\n"
"v_pk_max_i16 v225, v225, 0\n"
"v_mfma_f32_16x16x32_bf16 v[204:207], v[116:119], v[56:59], v[204:207]\n"
"buffer_store_dwordx2 v[220:221], v162, s[84:87], 0 offen offset:0 sc0 sc1\n"
"v_mfma_f32_16x16x32_bf16 v[208:211], v[116:119], v[68:71], v[208:211]\n"
"v_cvt_pk_bf16_f32 v223, v230, v234\n"
"v_cvt_pk_bf16_f32 v227, v231, v235\n"
"v_mfma_f32_16x16x32_bf16 v[208:211], v[112:115], v[64:67], v[208:211]\n"
"v_pk_max_i16 v223, v223, 0\n"
"v_pk_max_i16 v227, v227, 0\n"
"v_mfma_f32_16x16x32_bf16 v[208:211], v[108:111], v[60:63], v[208:211]\n"
"v_exp_f32 v102, v102\n"
"v_smfmac_f32_16x16x64_bf16 v[252:255], a[248:251], v[112:119], v157\n"
"v_cvt_pk_bf16_f32 v100, v100, v101\n"
"v_mfma_f32_16x16x32_bf16 v[188:191], v[236:239], a[76:79], 0\n"
"s_add_u32 m0, s28, 0\n"
"v_cvt_pk_bf16_f32 v181, v182, v183\n"
"v_mfma_f32_16x16x32_bf16 v[188:191], v[148:151], a[72:75], v[188:191]\n"
"s_waitcnt lgkmcnt(0)\n"
"buffer_load_dwordx4 v152, s[24:27], s52 offen offset:0 lds\n"
"v_mfma_f32_16x16x32_bf16 v[188:191], v[144:147], a[68:71], v[188:191]\n"
"v_exp_f32 v103, v103\n"
"v_mfma_f32_16x16x32_bf16 v[188:191], v[140:143], a[64:67], v[188:191]\n"
"v_exp_f32 v124, v124\n"
"v_mfma_f32_16x16x32_bf16 v[196:199], v[140:143], a[80:83], 0\n"
"ds_read_b128 v[140:143], v153 offset:6144\n"
"v_cvt_pk_bf16_f32 v101, v102, v103\n"
"v_mfma_f32_16x16x32_bf16 v[196:199], v[144:147], a[84:87], v[196:199]\n"
"ds_read_b128 v[144:147], v153 offset:10240\n"
"v_add_f32 v130, v254, v255\n"
"v_mfma_f32_16x16x32_bf16 v[196:199], v[148:151], a[88:91], v[196:199]\n"
"ds_read_b128 v[148:151], v153 offset:14336\n"
"v_cvt_pk_bf16_f32 v188, v188, v189\n"
"v_mfma_f32_16x16x32_bf16 v[196:199], v[236:239], a[92:95], v[196:199]\n"
"ds_read_b128 v[236:239], v153 offset:3072\n"
"v_cvt_pk_bf16_f32 v189, v190, v191\n"
"v_mfma_f32_16x16x32_bf16 v[212:215], v[108:111], v[72:75], v[212:215]\n"
"v_exp_f32 v125, v125\n"
"v_mfma_f32_16x16x32_bf16 v[212:215], v[112:115], v[76:79], v[212:215]\n"
"v_exp_f32 v126, v126\n"
"v_mfma_f32_16x16x32_bf16 v[212:215], v[116:119], v[80:83], v[212:215]\n"
"buffer_store_dwordx2 v[224:225], v162, s[84:87], 0 offen offset:256 sc0 sc1\n"
"v_mfma_f32_16x16x32_bf16 v[216:219], v[116:119], v[92:95], v[216:219]\n"
"v_cvt_pk_bf16_f32 v196, v196, v197\n"
"v_cvt_pk_bf16_f32 v197, v198, v199\n"
"v_mfma_f32_16x16x32_bf16 v[216:219], v[112:115], v[88:91], v[216:219]\n"
"buffer_load_dwordx4 v152, s[24:27], s53 offen offset:1088 lds\n"
"v_mfma_f32_16x16x32_bf16 v[216:219], v[108:111], v[84:87], v[216:219]\n"
"v_exp_f32 v127, v127\n"
"v_mfma_f32_16x16x32_bf16 v[184:187], v[240:243], a[48:51], 0\n"
"v_rcp_f32 v131, v130\n"
"v_mfma_f32_16x16x32_bf16 v[184:187], v[244:247], a[52:55], v[184:187]\n"
"v_max3_f32 v159, v159, v130, v131\n"
"v_cvt_pk_bf16_f32 v102, v124, v125\n"
"v_mfma_f32_16x16x32_bf16 v[184:187], v[248:251], a[56:59], v[184:187]\n"
"ds_bpermute_b32 v130, v160, v131 offset:128\n"
"v_mfma_f32_16x16x32_bf16 v[184:187], a[224:227], a[60:63], v[184:187]\n"
"v_exp_f32 v104, v104\n"
"v_mfma_f32_16x16x32_bf16 v[192:195], a[224:227], a[76:79], 0\n"
"v_cvt_pk_bf16_f32 v103, v126, v127\n"
"v_mov_b32 v128, 0\n"
"v_mfma_f32_16x16x32_bf16 v[192:195], v[248:251], a[72:75], v[192:195]\n"
"v_exp_f32 v105, v105\n"
"v_mfma_f32_16x16x32_bf16 v[192:195], v[244:247], a[68:71], v[192:195]\n"
"v_cvt_pk_bf16_f32 v182, v184, v185\n"
"v_cvt_pk_bf16_f32 v183, v186, v187\n"
"v_mfma_f32_16x16x32_bf16 v[192:195], v[240:243], a[64:67], v[192:195]\n"
"v_exp_f32 v106, v106\n"
"v_mfma_f32_16x16x32_bf16 v[200:203], v[240:243], a[80:83], 0\n"
"ds_read_b128 v[240:243], v153 offset:7168\n"
"v_cvt_pk_bf16_f32 v104, v104, v105\n"
"v_mfma_f32_16x16x32_bf16 v[200:203], v[244:247], a[84:87], v[200:203]\n"
"ds_read_b128 v[244:247], v153 offset:11264\n"
"v_mov_b32 v129, 0\n"
"v_mfma_f32_16x16x32_bf16 v[200:203], v[248:251], a[88:91], v[200:203]\n"
"ds_read_b128 v[248:251], v153 offset:15360\n"
"v_cvt_pk_bf16_f32 v190, v192, v193\n"
"v_mfma_f32_16x16x32_bf16 v[200:203], a[224:227], a[92:95], v[200:203]\n"
"buffer_load_dwordx4 v152, s[24:27], s54 offen offset:2176 lds\n"
"v_mfma_f32_16x16x32_bf16 v[184:187], v[136:139], a[48:51], 0\n"
"s_waitcnt lgkmcnt(4)\n"
"ds_read_b128 a[224:227], v153 offset:9216\n"
"v_cvt_pk_bf16_f32 v191, v194, v195\n"
"v_mfma_f32_16x16x32_bf16 v[184:187], v[140:143], a[52:55], v[184:187]\n"
"v_exp_f32 v107, v107\n"
"v_mfma_f32_16x16x32_bf16 v[184:187], v[144:147], a[56:59], v[184:187]\n"
"v_cvt_pk_bf16_f32 v198, v200, v201\n"
"v_cvt_pk_bf16_f32 v199, v202, v203\n"
"v_mfma_f32_16x16x32_bf16 v[184:187], v[148:151], a[60:63], v[184:187]\n"
"buffer_store_dwordx2 v[222:223], v162, s[84:87], 0 offen offset:512 sc0 sc1\n"
"v_mfma_f32_16x16x32_bf16 v[192:195], v[148:151], a[76:79], 0\n"
"s_waitcnt lgkmcnt(4)\n"
"v_mul_f32_dpp v204, v130, v204 quad_perm:[0,0,0,0] row_mask:0xf bank_mask:0xf\n"
"v_mul_f32_dpp v205, v130, v205 quad_perm:[1,1,1,1] row_mask:0xf bank_mask:0xf\n"
"v_mfma_f32_16x16x32_bf16 v[192:195], v[144:147], a[72:75], v[192:195]\n"
"v_mul_f32_dpp v206, v130, v206 quad_perm:[2,2,2,2] row_mask:0xf bank_mask:0xf\n"
"v_mul_f32_dpp v207, v130, v207 quad_perm:[3,3,3,3] row_mask:0xf bank_mask:0xf\n"
"v_mfma_f32_16x16x32_bf16 v[192:195], v[140:143], a[68:71], v[192:195]\n"
"v_cvt_pk_bf16_f32 v184, v184, v185\n"
"v_cvt_pk_bf16_f32 v185, v186, v187\n"
"v_mfma_f32_16x16x32_bf16 v[192:195], v[136:139], a[64:67], v[192:195]\n"
"v_mul_f32_dpp v208, v130, v208 quad_perm:[0,0,0,0] row_mask:0xf bank_mask:0xf\n"
"v_mul_f32_dpp v209, v130, v209 quad_perm:[1,1,1,1] row_mask:0xf bank_mask:0xf\n"
"v_mfma_f32_16x16x32_bf16 v[200:203], v[136:139], a[80:83], 0\n"
"ds_read2_b32 v[136:137], v158 offset0:0 offset1:2\n"
"ds_read2_b32 v[138:139], v158 offset0:128 offset1:130\n"
"v_mfma_f32_16x16x32_bf16 v[200:203], v[140:143], a[84:87], v[200:203]\n"
"v_mul_f32_dpp v210, v130, v210 quad_perm:[2,2,2,2] row_mask:0xf bank_mask:0xf\n"
"v_mul_f32_dpp v211, v130, v211 quad_perm:[3,3,3,3] row_mask:0xf bank_mask:0xf\n"
"v_mfma_f32_16x16x32_bf16 v[200:203], v[144:147], a[88:91], v[200:203]\n"
"ds_read_b128 v[144:147], v153 offset:0\n"
"v_cvt_pk_bf16_f32 v192, v192, v193\n"
"v_mfma_f32_16x16x32_bf16 v[200:203], v[148:151], a[92:95], v[200:203]\n"
"buffer_load_dwordx4 v152, s[24:27], s55 offen offset:3264 lds\n"
"v_mfma_f32_16x16x32_bf16 v[164:167], v[236:239], a[48:51], 0\n"
"s_waitcnt lgkmcnt(3)\n"
"ds_read_b128 v[148:151], v153 offset:4096\n"
"v_cvt_pk_bf16_f32 v193, v194, v195\n"
"v_mfma_f32_16x16x32_bf16 v[164:167], v[240:243], a[52:55], v[164:167]\n"
"v_mul_f32_dpp v212, v130, v212 quad_perm:[0,0,0,0] row_mask:0xf bank_mask:0xf\n"
"v_mul_f32_dpp v213, v130, v213 quad_perm:[1,1,1,1] row_mask:0xf bank_mask:0xf\n"
"v_mfma_f32_16x16x32_bf16 v[164:167], v[244:247], a[56:59], v[164:167]\n"
"v_cvt_pk_bf16_f32 v200, v200, v201\n"
"v_cvt_pk_bf16_f32 v201, v202, v203\n"
"v_mfma_f32_16x16x32_bf16 v[164:167], v[248:251], a[60:63], v[164:167]\n"
"v_exp_f32 v120, v120\n"
"v_mfma_f32_16x16x32_bf16 v[168:171], v[248:251], a[76:79], 0\n"
"v_cvt_pk_bf16_f32 v105, v106, v107\n"
"v_mul_f32_dpp v214, v130, v214 quad_perm:[2,2,2,2] row_mask:0xf bank_mask:0xf\n"
"v_mfma_f32_16x16x32_bf16 v[168:171], v[244:247], a[72:75], v[168:171]\n"
"v_mul_f32_dpp v215, v130, v215 quad_perm:[3,3,3,3] row_mask:0xf bank_mask:0xf\n"
"v_mul_f32_dpp v216, v130, v216 quad_perm:[0,0,0,0] row_mask:0xf bank_mask:0xf\n"
"v_mfma_f32_16x16x32_bf16 v[168:171], v[240:243], a[68:71], v[168:171]\n"
"v_exp_f32 v121, v121\n"
"v_mfma_f32_16x16x32_bf16 v[168:171], v[236:239], a[64:67], v[168:171]\n"
"buffer_store_dwordx2 v[226:227], v162, s[84:87], 0 offen offset:768 sc0 sc1\n"
"v_mfma_f32_16x16x32_bf16 v[172:175], v[236:239], a[80:83], 0\n"
"ds_read_b128 v[236:239], v153 offset:8192\n"
"v_mul_f32_dpp v217, v130, v217 quad_perm:[1,1,1,1] row_mask:0xf bank_mask:0xf\n"
"v_mfma_f32_16x16x32_bf16 v[172:175], v[240:243], a[84:87], v[172:175]\n"
"ds_read_b128 v[240:243], v153 offset:12288\n"
"v_mul_f32_dpp v218, v130, v218 quad_perm:[2,2,2,2] row_mask:0xf bank_mask:0xf\n"
"v_mfma_f32_16x16x32_bf16 v[172:175], v[244:247], a[88:91], v[172:175]\n"
"ds_read_b128 v[244:247], v153 offset:1024\n"
"v_mul_f32_dpp v219, v130, v219 quad_perm:[3,3,3,3] row_mask:0xf bank_mask:0xf\n"
"v_mfma_f32_16x16x32_bf16 v[172:175], v[248:251], a[92:95], v[172:175]\n"
"ds_read_b128 v[248:251], v153 offset:5120\n"
"v_cvt_pk_bf16_f32 v186, v164, v165\n"
"v_mfma_f32_16x16x32_bf16 v[204:207], a[16:19], a[160:163], v[204:207]\n"
"v_exp_f32 v122, v122\n"
"v_mfma_f32_16x16x32_bf16 v[204:207], a[20:23], a[164:167], v[204:207]\n"
"v_exp_f32 v123, v123\n"
"v_mfma_f32_16x16x32_bf16 v[204:207], a[24:27], a[168:171], v[204:207]\n"
"v_cvt_pk_bf16_f32 v187, v166, v167\n"
"v_cvt_pk_bf16_f32 v194, v168, v169\n"
"v_mfma_f32_16x16x32_bf16 v[204:207], a[28:31], a[172:175], v[204:207]\n"
"s_waitcnt lgkmcnt(4)\n"
"v_cvt_pk_bf16_f32 v195, v170, v171\n"
"v_cvt_pk_bf16_f32 v202, v172, v173\n"
"v_smfmac_f32_16x16x64_bf16 v[132:135], v[136:139], v[180:187], v157\n"
"v_cvt_pk_bf16_f32 v203, v174, v175\n"
"v_mfma_f32_16x16x32_bf16 v[208:211], a[28:31], a[188:191], v[208:211]\n"
"v_cvt_pk_bf16_f32 v106, v120, v121\n"
"v_cvt_pk_bf16_f32 v107, v122, v123\n"
"v_mfma_f32_16x16x32_bf16 v[208:211], a[24:27], a[184:187], v[208:211]\n"
"v_mov_b32_dpp v158, v158 row_ror:8 row_mask:0xf bank_mask:0xf\n"
"v_mov_b32 v130, 0\n"
"v_mfma_f32_16x16x32_bf16 v[208:211], a[20:23], a[180:183], v[208:211]\n"
"ds_read2_b32 v[140:141], v158 offset0:0 offset1:2\n"
"ds_read2_b32 v[142:143], v158 offset0:128 offset1:130\n"
"v_mfma_f32_16x16x32_bf16 v[208:211], a[16:19], a[176:179], v[208:211]\n"
"v_add_f32 v132, v132, v133\n"
"v_add_f32 v134, v134, v135\n"
"v_mfma_f32_16x16x32_bf16 v[212:215], a[16:19], a[192:195], v[212:215]\n"
"v_exp_f32 v132, v132\n"
"v_mfma_f32_16x16x32_bf16 v[212:215], a[20:23], a[196:199], v[212:215]\n"
"v_exp_f32 v134, v134\n"
"v_mfma_f32_16x16x32_bf16 v[212:215], a[24:27], a[200:203], v[212:215]\n"
"v_cvt_pk_bf16_f32 v133, v132, v134\n"
"v_add_f32 v132, v132, v134\n"
"v_mfma_f32_16x16x32_bf16 v[212:215], a[28:31], a[204:207], v[212:215]\n"
"v_mov_b32 v131, 0\n"
"v_cvt_pk_bf16_f32 v204, v204, v208\n"
"v_mfma_f32_16x16x32_bf16 v[216:219], a[28:31], a[220:223], v[216:219]\n"
"s_waitcnt vmcnt(14)\n"
"ds_read_b128 a[28:31], v154 offset:4544\n"
"v_cvt_pk_bf16_f32 v208, v205, v209\n"
"v_mfma_f32_16x16x32_bf16 v[216:219], a[24:27], a[216:219], v[216:219]\n"
"ds_read_b128 a[24:27], v154 offset:4480\n"
"v_pk_max_i16 v204, v204, 0\n"
"v_mfma_f32_16x16x32_bf16 v[216:219], a[20:23], a[212:215], v[216:219]\n"
"ds_read_b128 a[20:23], v154 offset:4416\n"
"v_pk_max_i16 v208, v208, 0\n"
"v_mfma_f32_16x16x32_bf16 v[216:219], a[16:19], a[208:211], v[216:219]\n"
"ds_read_b128 a[16:19], v154 offset:4352\n"
"v_cvt_pk_bf16_f32 v206, v206, v210\n"
"v_mfma_f32_16x16x32_bf16 v[108:111], v[0:3], v[180:183], 0\n"
"v_permlane16_swap_b32 v132, v133\n"
"v_mfma_f32_16x16x32_bf16 v[108:111], v[4:7], v[184:187], v[108:111]\n"
"s_waitcnt lgkmcnt(4)\n"
"v_cvt_pk_bf16_f32 v210, v207, v211\n"
"v_pk_max_i16 v206, v206, 0\n"
"v_smfmac_f32_16x16x64_bf16 v[128:131], v[140:143], v[188:195], v157\n"
"ds_read_b128 v[140:143], v153 offset:16384\n"
"v_smfmac_f32_16x16x64_bf16 v[128:131], v[136:139], v[196:203], v157\n"
"ds_read_b128 v[136:139], v153 offset:13312\n"
"v_mfma_f32_16x16x32_bf16 v[124:127], v[12:15], v[184:187], 0\n"
"v_cvt_pk_bf16_f32 v205, v212, v216\n"
"v_cvt_pk_bf16_f32 v209, v213, v217\n"
"v_mfma_f32_16x16x32_bf16 v[124:127], v[8:11], v[180:183], v[124:127]\n"
"v_pk_max_i16 v205, v205, 0\n"
"v_pk_max_i16 v209, v209, 0\n"
"v_mfma_f32_16x16x32_bf16 v[112:115], v[16:19], v[180:183], 0\n"
"v_add_f32 v128, v128, v129\n"
"v_add_f32 v130, v130, v131\n"
"v_mfma_f32_16x16x32_bf16 v[112:115], v[20:23], v[184:187], v[112:115]\n"
"buffer_store_dwordx2 v[204:205], v162, s[84:87], s65 offen offset:0 sc0 sc1\n"
"v_mfma_f32_16x16x4_4b_bf16 v[220:235], v[132:133], a[244:245], 0 cbsz:2 abid:1\n"
"v_exp_f32 v128, v128\n"
"v_exp_f32 v130, v130\n"
"s_add_u32 m0, s28, 4352\n"
"v_cvt_pk_bf16_f32 v207, v214, v218\n"
"v_cvt_pk_bf16_f32 v129, v128, v130\n"
"v_add_f32 v128, v128, v130\n"
"v_mfma_f32_16x16x32_bf16 v[120:123], v[28:31], v[184:187], 0\n"
"s_waitcnt lgkmcnt(2)\n"
"buffer_load_dwordx4 v152, s[24:27], s56 offen offset:0 lds\n"
"v_mfma_f32_16x16x32_bf16 v[120:123], v[24:27], v[180:183], v[120:123]\n"
"v_permlane16_swap_b32 v128, v129\n"
"v_mfma_f32_16x16x32_bf16 v[220:223], v[96:99], v[48:51], v[220:223]\n"
"v_cvt_pk_bf16_f32 v211, v215, v219\n"
"v_pk_max_i16 v207, v207, 0\n"
"v_mfma_f32_16x16x32_bf16 v[220:223], v[100:103], v[52:55], v[220:223]\n"
"v_pk_max_i16 v210, v210, 0\n"
"v_pk_max_i16 v211, v211, 0\n"
"v_mfma_f32_16x16x32_bf16 v[220:223], v[104:107], v[56:59], v[220:223]\n"
"v_mov_b32 v252, v132\n"
"v_mov_b32 v253, v133\n"
"v_mfma_f32_16x16x32_bf16 v[224:227], v[104:107], v[68:71], v[224:227]\n"
"v_exp_f32 v108, v108\n"
"v_mfma_f32_16x16x32_bf16 v[224:227], v[100:103], v[64:67], v[224:227]\n"
"v_exp_f32 v109, v109\n"
"v_mfma_f32_16x16x32_bf16 v[224:227], v[96:99], v[60:63], v[224:227]\n"
"v_exp_f32 v110, v110\n"
"v_smfmac_f32_16x16x64_bf16 v[252:255], a[248:251], v[96:103], v157\n"
"v_add_u32 v158, 64, v158\n"
"v_mfma_f32_16x16x32_bf16 v[228:231], v[96:99], v[72:75], v[228:231]\n"
"v_exp_f32 v111, v111\n"
"v_mfma_f32_16x16x32_bf16 v[228:231], v[100:103], v[76:79], v[228:231]\n"
"v_cvt_pk_bf16_f32 v108, v108, v109\n"
"v_cvt_pk_bf16_f32 v109, v110, v111\n"
"v_mfma_f32_16x16x32_bf16 v[228:231], v[104:107], v[80:83], v[228:231]\n"
"buffer_load_dwordx4 v152, s[24:27], s57 offen offset:1088 lds\n"
"v_mfma_f32_16x16x32_bf16 v[232:235], v[104:107], v[92:95], v[232:235]\n"
"v_mov_b32 v254, v132\n"
"v_mov_b32 v255, v133\n"
"v_mfma_f32_16x16x32_bf16 v[232:235], v[100:103], v[88:91], v[232:235]\n"
"v_exp_f32 v124, v124\n"
"v_mfma_f32_16x16x32_bf16 v[232:235], v[96:99], v[84:87], v[232:235]\n"
"buffer_store_dwordx2 v[208:209], v162, s[84:87], s65 offen offset:256 sc0 sc1\n"
"v_mfma_f32_16x16x32_bf16 v[116:119], v[32:35], v[180:183], 0\n"
"v_exp_f32 v125, v125\n"
"v_mfma_f32_16x16x32_bf16 v[116:119], v[36:39], v[184:187], v[116:119]\n"
"v_exp_f32 v126, v126\n"
"v_mfma_f32_16x16x32_bf16 v[96:99], v[0:3], v[188:191], 0\n"
"v_exp_f32 v127, v127\n"
"v_mfma_f32_16x16x32_bf16 v[96:99], v[4:7], v[192:195], v[96:99]\n"
"v_cvt_pk_bf16_f32 v110, v124, v125\n"
"v_cvt_pk_bf16_f32 v111, v126, v127\n"
"v_mfma_f32_16x16x32_bf16 v[124:127], v[44:47], v[184:187], 0\n"
"v_exp_f32 v112, v112\n"
"v_mfma_f32_16x16x32_bf16 v[124:127], v[40:43], v[180:183], v[124:127]\n"
"v_exp_f32 v113, v113\n"
"v_smfmac_f32_16x16x64_bf16 v[252:255], a[252:255], v[104:111], v157\n"
"v_cvt_pk_bf16_f32 v112, v112, v113\n"
"v_mfma_f32_16x16x32_bf16 v[100:103], v[16:19], v[188:191], 0\n"
"v_exp_f32 v114, v114\n"
"v_mfma_f32_16x16x32_bf16 v[100:103], v[20:23], v[192:195], v[100:103]\n"
"buffer_load_dwordx4 v152, s[24:27], s58 offen offset:2176 lds\n"
"v_mfma_f32_16x16x32_bf16 v[104:107], v[32:35], v[188:191], 0\n"
"v_add_f32 v134, v252, v253\n"
"v_mfma_f32_16x16x32_bf16 v[104:107], v[36:39], v[192:195], v[104:107]\n"
"s_sub_u32 s74, s74, 1\n"
"v_rcp_f32 v135, v134\n"
"s_cbranch_scc1 .Lepf%=\n"
"v_mfma_f32_16x16x32_bf16 v[164:167], a[144:147], a[0:3], 0\n"
"v_max3_f32 v159, v159, v134, v135\n"
"v_mfma_f32_16x16x32_bf16 v[164:167], a[148:151], a[4:7], v[164:167]\n"
"ds_bpermute_b32 v134, v160, v135 offset:0\n"
"v_mfma_f32_16x16x32_bf16 v[164:167], a[152:155], a[8:11], v[164:167]\n"
"v_exp_f32 v115, v115\n"
"v_mfma_f32_16x16x32_bf16 v[164:167], a[156:159], a[12:15], v[164:167]\n"
"buffer_store_dwordx2 v[206:207], v162, s[84:87], s65 offen offset:512 sc0 sc1\n"
"v_mfma_f32_16x16x32_bf16 v[168:171], a[156:159], a[28:31], 0\n"
"v_exp_f32 v120, v120\n"
"v_mfma_f32_16x16x32_bf16 v[168:171], a[152:155], a[24:27], v[168:171]\n"
"v_cvt_pk_bf16_f32 v113, v114, v115\n"
"v_mfma_f32_16x16x32_bf16 v[168:171], a[148:151], a[20:23], v[168:171]\n"
"v_exp_f32 v121, v121\n"
"v_mfma_f32_16x16x32_bf16 v[168:171], a[144:147], a[16:19], v[168:171]\n"
"v_exp_f32 v122, v122\n"
"v_mfma_f32_16x16x32_bf16 v[180:183], v[144:147], a[0:3], 0\n"
"v_exp_f32 v123, v123\n"
"v_mfma_f32_16x16x32_bf16 v[180:183], v[148:151], a[4:7], v[180:183]\n"
"v_cvt_pk_bf16_f32 v114, v120, v121\n"
"v_cvt_pk_bf16_f32 v115, v122, v123\n"
"v_mfma_f32_16x16x32_bf16 v[180:183], v[236:239], a[8:11], v[180:183]\n"
"buffer_load_dwordx4 v152, s[24:27], s59 offen offset:3264 lds\n"
"v_mfma_f32_16x16x32_bf16 v[180:183], v[240:243], a[12:15], v[180:183]\n"
"v_exp_f32 v116, v116\n"
"v_mfma_f32_16x16x32_bf16 v[120:123], v[12:15], v[192:195], 0\n"
"s_waitcnt lgkmcnt(0)\n"
"v_mul_f32_dpp v220, v134, v220 quad_perm:[0,0,0,0] row_mask:0xf bank_mask:0xf\n"
"v_mul_f32_dpp v221, v134, v221 quad_perm:[1,1,1,1] row_mask:0xf bank_mask:0xf\n"
"v_mfma_f32_16x16x32_bf16 v[120:123], v[8:11], v[188:191], v[120:123]\n"
"v_mul_f32_dpp v222, v134, v222 quad_perm:[2,2,2,2] row_mask:0xf bank_mask:0xf\n"
"v_mul_f32_dpp v223, v134, v223 quad_perm:[3,3,3,3] row_mask:0xf bank_mask:0xf\n"
"v_mfma_f32_16x16x32_bf16 v[184:187], v[244:247], a[0:3], 0\n"
"v_mul_f32_dpp v224, v134, v224 quad_perm:[0,0,0,0] row_mask:0xf bank_mask:0xf\n"
"v_mul_f32_dpp v225, v134, v225 quad_perm:[1,1,1,1] row_mask:0xf bank_mask:0xf\n"
"v_mfma_f32_16x16x32_bf16 v[184:187], v[248:251], a[4:7], v[184:187]\n"
"v_mul_f32_dpp v226, v134, v226 quad_perm:[2,2,2,2] row_mask:0xf bank_mask:0xf\n"
"v_mul_f32_dpp v227, v134, v227 quad_perm:[3,3,3,3] row_mask:0xf bank_mask:0xf\n"
"v_mfma_f32_16x16x32_bf16 v[184:187], a[224:227], a[8:11], v[184:187]\n"
"v_mul_f32_dpp v228, v134, v228 quad_perm:[0,0,0,0] row_mask:0xf bank_mask:0xf\n"
"v_mul_f32_dpp v229, v134, v229 quad_perm:[1,1,1,1] row_mask:0xf bank_mask:0xf\n"
"v_mfma_f32_16x16x32_bf16 v[184:187], v[136:139], a[12:15], v[184:187]\n"
"buffer_store_dwordx2 v[210:211], v162, s[84:87], s65 offen offset:768 sc0 sc1\n"
"v_mfma_f32_16x16x32_bf16 v[220:223], a[32:35], a[160:163], v[220:223]\n"
"v_mul_f32_dpp v230, v134, v230 quad_perm:[2,2,2,2] row_mask:0xf bank_mask:0xf\n"
"v_mul_f32_dpp v231, v134, v231 quad_perm:[3,3,3,3] row_mask:0xf bank_mask:0xf\n"
"v_mfma_f32_16x16x32_bf16 v[220:223], a[36:39], a[164:167], v[220:223]\n"
"v_mul_f32_dpp v232, v134, v232 quad_perm:[0,0,0,0] row_mask:0xf bank_mask:0xf\n"
"v_mul_f32_dpp v233, v134, v233 quad_perm:[1,1,1,1] row_mask:0xf bank_mask:0xf\n"
"v_mfma_f32_16x16x32_bf16 v[220:223], a[40:43], a[168:171], v[220:223]\n"
"v_mul_f32_dpp v234, v134, v234 quad_perm:[2,2,2,2] row_mask:0xf bank_mask:0xf\n"
"v_mul_f32_dpp v235, v134, v235 quad_perm:[3,3,3,3] row_mask:0xf bank_mask:0xf\n"
"v_mfma_f32_16x16x32_bf16 v[220:223], a[44:47], a[172:175], v[220:223]\n"
"v_exp_f32 v117, v117\n"
"v_mfma_f32_16x16x32_bf16 v[224:227], a[44:47], a[188:191], v[224:227]\n"
"v_exp_f32 v118, v118\n"
"v_mfma_f32_16x16x32_bf16 v[224:227], a[40:43], a[184:187], v[224:227]\n"
"v_exp_f32 v119, v119\n"
"v_mfma_f32_16x16x32_bf16 v[224:227], a[36:39], a[180:183], v[224:227]\n"
"v_cvt_pk_bf16_f32 v116, v116, v117\n"
"v_cvt_pk_bf16_f32 v117, v118, v119\n"
"v_mfma_f32_16x16x32_bf16 v[224:227], a[32:35], a[176:179], v[224:227]\n"
"v_exp_f32 v124, v124\n"
"v_mfma_f32_16x16x32_bf16 v[228:231], a[32:35], a[192:195], v[228:231]\n"
"v_exp_f32 v125, v125\n"
"v_mfma_f32_16x16x32_bf16 v[228:231], a[36:39], a[196:199], v[228:231]\n"
"v_exp_f32 v126, v126\n"
"v_mfma_f32_16x16x32_bf16 v[228:231], a[40:43], a[200:203], v[228:231]\n"
"v_cvt_pk_bf16_f32 v220, v220, v224\n"
"v_cvt_pk_bf16_f32 v224, v221, v225\n"
"v_mfma_f32_16x16x32_bf16 v[228:231], a[44:47], a[204:207], v[228:231]\n"
"v_pk_max_i16 v220, v220, 0\n"
"v_pk_max_i16 v224, v224, 0\n"
"v_mfma_f32_16x16x32_bf16 v[232:235], a[44:47], a[220:223], v[232:235]\n"
"s_waitcnt vmcnt(18)\n"
"ds_read_b128 a[44:47], v154 offset:8896\n"
"v_cvt_pk_bf16_f32 v222, v222, v226\n"
"v_mfma_f32_16x16x32_bf16 v[232:235], a[40:43], a[216:219], v[232:235]\n"
"ds_read_b128 a[40:43], v154 offset:8832\n"
"v_cvt_pk_bf16_f32 v226, v223, v227\n"
"v_mfma_f32_16x16x32_bf16 v[232:235], a[36:39], a[212:215], v[232:235]\n"
"ds_read_b128 a[36:39], v154 offset:8768\n"
"v_pk_max_i16 v222, v222, 0\n"
"v_mfma_f32_16x16x32_bf16 v[232:235], a[32:35], a[208:211], v[232:235]\n"
"ds_read_b128 a[32:35], v154 offset:8704\n"
"v_exp_f32 v127, v127\n"
"v_mfma_f32_16x16x4_4b_bf16 v[204:219], v[132:133], a[244:245], 0 cbsz:2 abid:3\n"
"v_pk_max_i16 v226, v226, 0\n"
"v_cvt_pk_bf16_f32 v118, v124, v125\n"
"v_cvt_pk_bf16_f32 v119, v126, v127\n"
"v_exp_f32 v96, v96\n"
"v_exp_f32 v97, v97\n"
"v_cvt_pk_bf16_f32 v221, v228, v232\n"
"v_mfma_f32_16x16x32_bf16 v[124:127], v[28:31], v[192:195], 0\n"
"v_cvt_pk_bf16_f32 v225, v229, v233\n"
"v_pk_max_i16 v221, v221, 0\n"
"v_mfma_f32_16x16x32_bf16 v[124:127], v[24:27], v[188:191], v[124:127]\n"
"buffer_store_dwordx2 v[220:221], v162, s[84:87], s66 offen offset:0 sc0 sc1\n"
"v_mfma_f32_16x16x32_bf16 v[204:207], v[108:111], v[48:51], v[204:207]\n"
"v_pk_max_i16 v225, v225, 0\n"
"v_cvt_pk_bf16_f32 v223, v230, v234\n"
"v_mfma_f32_16x16x32_bf16 v[204:207], v[112:115], v[52:55], v[204:207]\n"
"v_cvt_pk_bf16_f32 v227, v231, v235\n"
"v_pk_max_i16 v223, v223, 0\n"
"v_mfma_f32_16x16x32_bf16 v[204:207], v[116:119], v[56:59], v[204:207]\n"
"v_pk_max_i16 v227, v227, 0\n"
"v_cvt_pk_bf16_f32 v96, v96, v97\n"
"v_mfma_f32_16x16x32_bf16 v[208:211], v[116:119], v[68:71], v[208:211]\n"
"v_exp_f32 v98, v98\n"
"v_mfma_f32_16x16x32_bf16 v[208:211], v[112:115], v[64:67], v[208:211]\n"
"s_add_u32 m0, s28, 8704\n"
"v_mfma_f32_16x16x32_bf16 v[208:211], v[108:111], v[60:63], v[208:211]\n"
"s_waitcnt lgkmcnt(0)\n"
"buffer_load_dwordx4 v152, s[24:27], s60 offen offset:0 lds\n"
"v_smfmac_f32_16x16x64_bf16 v[252:255], a[248:251], v[112:119], v157\n"
"v_exp_f32 v99, v99\n"
"v_mfma_f32_16x16x32_bf16 v[212:215], v[108:111], v[72:75], v[212:215]\n"
"v_exp_f32 v120, v120\n"
"v_mfma_f32_16x16x32_bf16 v[212:215], v[112:115], v[76:79], v[212:215]\n"
"v_cvt_pk_bf16_f32 v97, v98, v99\n"
"v_mfma_f32_16x16x32_bf16 v[212:215], v[116:119], v[80:83], v[212:215]\n"
"v_exp_f32 v121, v121\n"
"v_mfma_f32_16x16x32_bf16 v[216:219], v[116:119], v[92:95], v[216:219]\n"
"v_exp_f32 v122, v122\n"
"v_mfma_f32_16x16x32_bf16 v[216:219], v[112:115], v[88:91], v[216:219]\n"
"v_exp_f32 v123, v123\n"
"v_mfma_f32_16x16x32_bf16 v[216:219], v[108:111], v[84:87], v[216:219]\n"
"buffer_store_dwordx2 v[224:225], v162, s[84:87], s66 offen offset:256 sc0 sc1\n"
"v_mfma_f32_16x16x32_bf16 v[108:111], v[0:3], v[196:199], 0\n"
"v_cvt_pk_bf16_f32 v98, v120, v121\n"
"v_cvt_pk_bf16_f32 v99, v122, v123\n"
"v_mfma_f32_16x16x32_bf16 v[108:111], v[4:7], v[200:203], v[108:111]\n"
"v_exp_f32 v100, v100\n"
"v_mfma_f32_16x16x32_bf16 v[120:123], v[44:47], v[192:195], 0\n"
"v_exp_f32 v101, v101\n"
"v_mfma_f32_16x16x32_bf16 v[120:123], v[40:43], v[188:191], v[120:123]\n"
"buffer_load_dwordx4 v152, s[24:27], s61 offen offset:1088 lds\n"
"v_mfma_f32_16x16x32_bf16 v[112:115], v[16:19], v[196:199], 0\n"
"v_exp_f32 v102, v102\n"
"v_mfma_f32_16x16x32_bf16 v[112:115], v[20:23], v[200:203], v[112:115]\n"
"v_exp_f32 v103, v103\n"
"v_mfma_f32_16x16x32_bf16 v[116:119], v[32:35], v[196:199], 0\n"
"v_add_f32 v134, v254, v255\n"
"v_cvt_pk_bf16_f32 v100, v100, v101\n"
"v_mfma_f32_16x16x32_bf16 v[116:119], v[36:39], v[200:203], v[116:119]\n"
"v_rcp_f32 v135, v134\n"
"v_mfma_f32_16x16x32_bf16 v[0:3], a[96:99], a[0:3], 0\n"
"v_max3_f32 v159, v159, v134, v135\n"
"v_cvt_pk_bf16_f32 v101, v102, v103\n"
"v_mfma_f32_16x16x32_bf16 v[0:3], a[100:103], a[4:7], v[0:3]\n"
"ds_bpermute_b32 v134, v160, v135 offset:128\n"
"v_mfma_f32_16x16x32_bf16 v[0:3], a[104:107], a[8:11], v[0:3]\n"
"v_exp_f32 v124, v124\n"
"v_mfma_f32_16x16x32_bf16 v[0:3], a[108:111], a[12:15], v[0:3]\n"
"v_exp_f32 v125, v125\n"
"v_mfma_f32_16x16x32_bf16 v[16:19], a[96:99], a[32:35], 0\n"
"v_exp_f32 v126, v126\n"
"v_mfma_f32_16x16x32_bf16 v[16:19], a[100:103], a[36:39], v[16:19]\n"
"v_exp_f32 v127, v127\n"
"v_mfma_f32_16x16x32_bf16 v[16:19], a[104:107], a[40:43], v[16:19]\n"
"v_cvt_pk_bf16_f32 v102, v124, v125\n"
"v_cvt_pk_bf16_f32 v103, v126, v127\n"
"v_mfma_f32_16x16x32_bf16 v[16:19], a[108:111], a[44:47], v[16:19]\n"
"buffer_store_dwordx2 v[222:223], v162, s[84:87], s66 offen offset:512 sc0 sc1\n"
"v_mfma_f32_16x16x32_bf16 v[124:127], v[12:15], v[200:203], 0\n"
"buffer_load_dwordx4 v152, s[24:27], s62 offen offset:2176 lds\n"
"v_mfma_f32_16x16x32_bf16 v[124:127], v[8:11], v[196:199], v[124:127]\n"
".Lpad3_%=:\n"
"s_cbranch_vccnz .Lpad2_%=\n"
"v_exp_f32 v104, v104\n"
"v_mfma_f32_16x16x32_bf16 v[8:11], a[108:111], a[28:31], 0\n"
"v_exp_f32 v105, v105\n"
"v_mfma_f32_16x16x32_bf16 v[8:11], a[104:107], a[24:27], v[8:11]\n"
"v_exp_f32 v106, v106\n"
"v_mfma_f32_16x16x32_bf16 v[8:11], a[100:103], a[20:23], v[8:11]\n"
"s_waitcnt lgkmcnt(0)\n"
"v_mul_f32_dpp v204, v134, v204 quad_perm:[0,0,0,0] row_mask:0xf bank_mask:0xf\n"
"v_mul_f32_dpp v205, v134, v205 quad_perm:[1,1,1,1] row_mask:0xf bank_mask:0xf\n"
"v_mfma_f32_16x16x32_bf16 v[8:11], a[96:99], a[16:19], v[8:11]\n"
"v_mul_f32_dpp v206, v134, v206 quad_perm:[2,2,2,2] row_mask:0xf bank_mask:0xf\n"
"v_mul_f32_dpp v207, v134, v207 quad_perm:[3,3,3,3] row_mask:0xf bank_mask:0xf\n"
"v_mfma_f32_16x16x32_bf16 v[4:7], a[112:115], a[0:3], 0\n"
"v_mul_f32_dpp v208, v134, v208 quad_perm:[0,0,0,0] row_mask:0xf bank_mask:0xf\n"
"v_mul_f32_dpp v209, v134, v209 quad_perm:[1,1,1,1] row_mask:0xf bank_mask:0xf\n"
"v_mfma_f32_16x16x32_bf16 v[4:7], a[116:119], a[4:7], v[4:7]\n"
"v_mul_f32_dpp v210, v134, v210 quad_perm:[2,2,2,2] row_mask:0xf bank_mask:0xf\n"
"v_mul_f32_dpp v211, v134, v211 quad_perm:[3,3,3,3] row_mask:0xf bank_mask:0xf\n"
"v_mfma_f32_16x16x32_bf16 v[4:7], a[120:123], a[8:11], v[4:7]\n"
"v_mul_f32_dpp v212, v134, v212 quad_perm:[0,0,0,0] row_mask:0xf bank_mask:0xf\n"
"v_mul_f32_dpp v213, v134, v213 quad_perm:[1,1,1,1] row_mask:0xf bank_mask:0xf\n"
"v_mfma_f32_16x16x32_bf16 v[4:7], a[124:127], a[12:15], v[4:7]\n"
"v_mul_f32_dpp v214, v134, v214 quad_perm:[2,2,2,2] row_mask:0xf bank_mask:0xf\n"
"v_mul_f32_dpp v215, v134, v215 quad_perm:[3,3,3,3] row_mask:0xf bank_mask:0xf\n"
"v_mfma_f32_16x16x32_bf16 v[204:207], a[48:51], a[160:163], v[204:207]\n"
"v_mul_f32_dpp v216, v134, v216 quad_perm:[0,0,0,0] row_mask:0xf bank_mask:0xf\n"
"v_mul_f32_dpp v217, v134, v217 quad_perm:[1,1,1,1] row_mask:0xf bank_mask:0xf\n"
"v_mfma_f32_16x16x32_bf16 v[204:207], a[52:55], a[164:167], v[204:207]\n"
"v_mul_f32_dpp v218, v134, v218 quad_perm:[2,2,2,2] row_mask:0xf bank_mask:0xf\n"
"v_mul_f32_dpp v219, v134, v219 quad_perm:[3,3,3,3] row_mask:0xf bank_mask:0xf\n"
"v_mfma_f32_16x16x32_bf16 v[204:207], a[56:59], a[168:171], v[204:207]\n"
"buffer_load_dwordx4 v152, s[24:27], s63 offen offset:3264 lds\n"
"v_mfma_f32_16x16x32_bf16 v[204:207], a[60:63], a[172:175], v[204:207]\n"
"buffer_store_dwordx2 v[226:227], v162, s[84:87], s66 offen offset:768 sc0 sc1\n"
"v_mfma_f32_16x16x32_bf16 v[208:211], a[60:63], a[188:191], v[208:211]\n"
"v_exp_f32 v107, v107\n"
"v_mfma_f32_16x16x32_bf16 v[208:211], a[56:59], a[184:187], v[208:211]\n"
"v_cvt_pk_bf16_f32 v104, v104, v105\n"
"v_cvt_pk_bf16_f32 v105, v106, v107\n"
"v_mfma_f32_16x16x32_bf16 v[208:211], a[52:55], a[180:183], v[208:211]\n"
"v_exp_f32 v120, v120\n"
"v_mfma_f32_16x16x32_bf16 v[208:211], a[48:51], a[176:179], v[208:211]\n"
"v_exp_f32 v121, v121\n"
"v_mfma_f32_16x16x32_bf16 v[212:215], a[48:51], a[192:195], v[212:215]\n"
"v_exp_f32 v122, v122\n"
"v_mfma_f32_16x16x32_bf16 v[212:215], a[52:55], a[196:199], v[212:215]\n"
"v_exp_f32 v123, v123\n"
"v_mfma_f32_16x16x32_bf16 v[212:215], a[56:59], a[200:203], v[212:215]\n"
"v_exp_f32 v108, v108\n"
"v_mfma_f32_16x16x32_bf16 v[212:215], a[60:63], a[204:207], v[212:215]\n"
"v_cvt_pk_bf16_f32 v106, v120, v121\n"
"v_cvt_pk_bf16_f32 v107, v122, v123\n"
"v_mfma_f32_16x16x32_bf16 v[120:123], v[28:31], v[200:203], 0\n"
"v_exp_f32 v109, v109\n"
"v_mfma_f32_16x16x32_bf16 v[120:123], v[24:27], v[196:199], v[120:123]\n"
"v_exp_f32 v110, v110\n"
"v_mfma_f32_16x16x32_bf16 v[216:219], a[60:63], a[220:223], v[216:219]\n"
"s_waitcnt vmcnt(17)\n"
"ds_read_b128 a[60:63], v154 offset:192\n"
"v_mov_b32 v252, v128\n"
"v_mfma_f32_16x16x32_bf16 v[216:219], a[56:59], a[216:219], v[216:219]\n"
"ds_read_b128 a[56:59], v154 offset:128\n"
"v_mov_b32 v253, v129\n"
"v_mfma_f32_16x16x32_bf16 v[216:219], a[52:55], a[212:215], v[216:219]\n"
"ds_read_b128 a[52:55], v154 offset:64\n"
"v_cvt_pk_bf16_f32 v108, v108, v109\n"
"v_mfma_f32_16x16x32_bf16 v[216:219], a[48:51], a[208:211], v[216:219]\n"
"ds_read_b128 a[48:51], v154 offset:0\n"
"v_cvt_pk_bf16_f32 v204, v204, v208\n"
"v_smfmac_f32_16x16x64_bf16 v[252:255], a[248:251], v[96:103], v157\n"
"v_exp_f32 v111, v111\n"
"v_mfma_f32_16x16x4_4b_bf16 v[220:235], v[128:129], a[244:245], 0 cbsz:2 abid:1\n"
"v_exp_f32 v124, v124\n"
"v_cvt_pk_bf16_f32 v109, v110, v111\n"
"s_add_u32 s24, s24, 13107200\n"
"s_addc_u32 s25, s25, 0\n"
"s_sub_u32 s26, s26, 13107200\n"
"s_cselect_b32 s26, 0, s26\n"
"v_mov_b32 v254, v128\n"
"v_mfma_f32_16x16x32_bf16 v[12:15], a[124:127], a[28:31], 0\n"
"v_mov_b32 v255, v129\n"
"v_cvt_pk_bf16_f32 v208, v205, v209\n"
"v_mfma_f32_16x16x32_bf16 v[12:15], a[120:123], a[24:27], v[12:15]\n"
"v_exp_f32 v125, v125\n"
"v_mfma_f32_16x16x32_bf16 v[12:15], a[116:119], a[20:23], v[12:15]\n"
"v_cvt_pk_bf16_f32 v205, v212, v216\n"
"v_cvt_pk_bf16_f32 v209, v213, v217\n"
"v_mfma_f32_16x16x32_bf16 v[12:15], a[112:115], a[16:19], v[12:15]\n"
"v_pk_max_i16 v204, v204, 0\n"
"v_pk_max_i16 v205, v205, 0\n"
"v_mfma_f32_16x16x32_bf16 v[220:223], v[96:99], v[48:51], v[220:223]\n"
"v_exp_f32 v126, v126\n"
"v_mfma_f32_16x16x32_bf16 v[220:223], v[100:103], v[52:55], v[220:223]\n"
"v_pk_max_i16 v208, v208, 0\n"
"v_pk_max_i16 v209, v209, 0\n"
"v_mfma_f32_16x16x32_bf16 v[220:223], v[104:107], v[56:59], v[220:223]\n"
"buffer_store_dwordx2 v[204:205], v162, s[84:87], s67 offen offset:0 sc0 sc1\n"
"v_mfma_f32_16x16x32_bf16 v[224:227], v[104:107], v[68:71], v[224:227]\n"
"v_cvt_pk_bf16_f32 v206, v206, v210\n"
"v_cvt_pk_bf16_f32 v210, v207, v211\n"
"v_mfma_f32_16x16x32_bf16 v[224:227], v[100:103], v[64:67], v[224:227]\n"
"v_cvt_pk_bf16_f32 v207, v214, v218\n"
"v_cvt_pk_bf16_f32 v211, v215, v219\n"
"v_mfma_f32_16x16x32_bf16 v[224:227], v[96:99], v[60:63], v[224:227]\n"
"v_exp_f32 v127, v127\n"
"v_mfma_f32_16x16x32_bf16 v[228:231], v[96:99], v[72:75], v[228:231]\n"
"v_pk_max_i16 v206, v206, 0\n"
"v_pk_max_i16 v207, v207, 0\n"
"v_mfma_f32_16x16x32_bf16 v[228:231], v[100:103], v[76:79], v[228:231]\n"
"v_pk_max_i16 v210, v210, 0\n"
"v_pk_max_i16 v211, v211, 0\n"
"v_mfma_f32_16x16x32_bf16 v[228:231], v[104:107], v[80:83], v[228:231]\n"
"v_cvt_pk_bf16_f32 v110, v124, v125\n"
"v_cvt_pk_bf16_f32 v111, v126, v127\n"
"v_mfma_f32_16x16x32_bf16 v[232:235], v[104:107], v[92:95], v[232:235]\n"
"v_exp_f32 v112, v112\n"
"v_mfma_f32_16x16x32_bf16 v[232:235], v[100:103], v[88:91], v[232:235]\n"
"v_exp_f32 v113, v113\n"
"v_mfma_f32_16x16x32_bf16 v[232:235], v[96:99], v[84:87], v[232:235]\n"
"v_exp_f32 v114, v114\n"
"v_mfma_f32_16x16x32_bf16 v[124:127], v[44:47], v[200:203], 0\n"
"v_exp_f32 v115, v115\n"
"v_mfma_f32_16x16x32_bf16 v[124:127], v[40:43], v[196:199], v[124:127]\n"
"buffer_store_dwordx2 v[208:209], v162, s[84:87], s67 offen offset:256 sc0 sc1\n"
"v_smfmac_f32_16x16x64_bf16 v[252:255], a[252:255], v[104:111], v157\n"
"v_cvt_pk_bf16_f32 v112, v112, v113\n"
"v_mfma_f32_16x16x32_bf16 v[20:23], a[112:115], a[32:35], 0\n"
"v_exp_f32 v120, v120\n"
"v_mfma_f32_16x16x32_bf16 v[20:23], a[116:119], a[36:39], v[20:23]\n"
"v_cvt_pk_bf16_f32 v113, v114, v115\n"
"v_mfma_f32_16x16x32_bf16 v[20:23], a[120:123], a[40:43], v[20:23]\n"
"v_add_f32 v130, v252, v253\n"
"v_mfma_f32_16x16x32_bf16 v[20:23], a[124:127], a[44:47], v[20:23]\n"
"v_rcp_f32 v131, v130\n"
"v_mfma_f32_16x16x32_bf16 v[172:175], a[144:147], a[32:35], 0\n"
"v_max3_f32 v159, v159, v130, v131\n"
"v_mfma_f32_16x16x32_bf16 v[172:175], a[148:151], a[36:39], v[172:175]\n"
"ds_bpermute_b32 v130, v160, v131 offset:0\n"
"v_mfma_f32_16x16x32_bf16 v[172:175], a[152:155], a[40:43], v[172:175]\n"
"v_exp_f32 v121, v121\n"
"v_mfma_f32_16x16x32_bf16 v[172:175], a[156:159], a[44:47], v[172:175]\n"
"s_waitcnt lgkmcnt(1)\n"
"v_exp_f32 v122, v122\n"
"v_mfma_f32_16x16x32_bf16 v[24:27], a[96:99], a[48:51], 0\n"
"v_exp_f32 v123, v123\n"
"v_mfma_f32_16x16x32_bf16 v[24:27], a[100:103], a[52:55], v[24:27]\n"
"v_cvt_pk_bf16_f32 v114, v120, v121\n"
"v_cvt_pk_bf16_f32 v115, v122, v123\n"
"v_mfma_f32_16x16x32_bf16 v[24:27], a[104:107], a[56:59], v[24:27]\n"
"v_exp_f32 v116, v116\n"
"v_mfma_f32_16x16x32_bf16 v[24:27], a[108:111], a[60:63], v[24:27]\n"
"buffer_store_dwordx2 v[206:207], v162, s[84:87], s67 offen offset:512 sc0 sc1\n"
"v_mfma_f32_16x16x32_bf16 v[188:191], v[240:243], a[28:31], 0\n"
"s_add_u32 m0, s28, 0\n"
"v_cvt_pk_bf16_f32 v0, v0, v1\n"
"v_mfma_f32_16x16x32_bf16 v[188:191], v[236:239], a[24:27], v[188:191]\n"
"buffer_load_dwordx4 v152, s[24:27], s40 offen offset:0 lds\n"
"v_mfma_f32_16x16x32_bf16 v[188:191], v[148:151], a[20:23], v[188:191]\n"
"v_exp_f32 v117, v117\n"
"v_mfma_f32_16x16x32_bf16 v[188:191], v[144:147], a[16:19], v[188:191]\n"
"v_exp_f32 v118, v118\n"
"v_mfma_f32_16x16x32_bf16 v[196:199], v[144:147], a[32:35], 0\n"
"s_waitcnt lgkmcnt(0)\n"
"ds_read_b128 v[144:147], v153 offset:20576\n"
"v_mul_f32_dpp v220, v130, v220 quad_perm:[0,0,0,0] row_mask:0xf bank_mask:0xf\n"
"v_mfma_f32_16x16x32_bf16 v[196:199], v[148:151], a[36:39], v[196:199]\n"
"ds_read_b128 v[148:151], v153 offset:24768\n"
"v_mul_f32_dpp v221, v130, v221 quad_perm:[1,1,1,1] row_mask:0xf bank_mask:0xf\n"
"v_mfma_f32_16x16x32_bf16 v[196:199], v[236:239], a[40:43], v[196:199]\n"
"ds_read_b128 v[236:239], v153 offset:28960\n"
"v_mul_f32_dpp v222, v130, v222 quad_perm:[2,2,2,2] row_mask:0xf bank_mask:0xf\n"
"v_mfma_f32_16x16x32_bf16 v[196:199], v[240:243], a[44:47], v[196:199]\n"
"ds_read_b128 v[240:243], v153 offset:2048\n"
"v_mul_f32_dpp v223, v130, v223 quad_perm:[3,3,3,3] row_mask:0xf bank_mask:0xf\n"
"v_mfma_f32_16x16x32_bf16 v[176:179], a[112:115], a[48:51], 0\n"
"v_mul_f32_dpp v224, v130, v224 quad_perm:[0,0,0,0] row_mask:0xf bank_mask:0xf\n"
"v_mul_f32_dpp v225, v130, v225 quad_perm:[1,1,1,1] row_mask:0xf bank_mask:0xf\n"
"v_mfma_f32_16x16x32_bf16 v[176:179], a[116:119], a[52:55], v[176:179]\n"
"v_mul_f32_dpp v226, v130, v226 quad_perm:[2,2,2,2] row_mask:0xf bank_mask:0xf\n"
"v_mul_f32_dpp v227, v130, v227 quad_perm:[3,3,3,3] row_mask:0xf bank_mask:0xf\n"
"v_mfma_f32_16x16x32_bf16 v[176:179], a[120:123], a[56:59], v[176:179]\n"
"v_mul_f32_dpp v228, v130, v228 quad_perm:[0,0,0,0] row_mask:0xf bank_mask:0xf\n"
"v_mul_f32_dpp v229, v130, v229 quad_perm:[1,1,1,1] row_mask:0xf bank_mask:0xf\n"
"v_mfma_f32_16x16x32_bf16 v[176:179], a[124:127], a[60:63], v[176:179]\n"
"buffer_load_dwordx4 v152, s[24:27], s41 offen offset:1088 lds\n"
"v_mfma_f32_16x16x32_bf16 v[220:223], a[64:67], a[160:163], v[220:223]\n"
"v_mul_f32_dpp v230, v130, v230 quad_perm:[2,2,2,2] row_mask:0xf bank_mask:0xf\n"
"v_mul_f32_dpp v231, v130, v231 quad_perm:[3,3,3,3] row_mask:0xf bank_mask:0xf\n"
"v_mfma_f32_16x16x32_bf16 v[220:223], a[68:71], a[164:167], v[220:223]\n"
"v_mul_f32_dpp v232, v130, v232 quad_perm:[0,0,0,0] row_mask:0xf bank_mask:0xf\n"
"v_mul_f32_dpp v233, v130, v233 quad_perm:[1,1,1,1] row_mask:0xf bank_mask:0xf\n"
"v_mfma_f32_16x16x32_bf16 v[220:223], a[72:75], a[168:171], v[220:223]\n"
"v_mul_f32_dpp v234, v130, v234 quad_perm:[2,2,2,2] row_mask:0xf bank_mask:0xf\n"
"v_mul_f32_dpp v235, v130, v235 quad_perm:[3,3,3,3] row_mask:0xf bank_mask:0xf\n"
"v_mfma_f32_16x16x32_bf16 v[220:223], a[76:79], a[172:175], v[220:223]\n"
"buffer_store_dwordx2 v[210:211], v162, s[84:87], s67 offen offset:768 sc0 sc1\n"
"v_mfma_f32_16x16x32_bf16 v[224:227], a[76:79], a[188:191], v[224:227]\n"
"v_exp_f32 v119, v119\n"
"v_mfma_f32_16x16x32_bf16 v[224:227], a[72:75], a[184:187], v[224:227]\n"
"v_cvt_pk_bf16_f32 v116, v116, v117\n"
"v_cvt_pk_bf16_f32 v117, v118, v119\n"
"v_mfma_f32_16x16x32_bf16 v[224:227], a[68:71], a[180:183], v[224:227]\n"
"v_exp_f32 v124, v124\n"
"v_mfma_f32_16x16x32_bf16 v[224:227], a[64:67], a[176:179], v[224:227]\n"
"v_exp_f32 v125, v125\n"
"v_mfma_f32_16x16x32_bf16 v[228:231], a[64:67], a[192:195], v[228:231]\n"
"v_exp_f32 v126, v126\n"
"v_mfma_f32_16x16x32_bf16 v[228:231], a[68:71], a[196:199], v[228:231]\n"
"v_exp_f32 v127, v127\n"
"v_mfma_f32_16x16x32_bf16 v[228:231], a[72:75], a[200:203], v[228:231]\n"
"v_cvt_pk_bf16_f32 v118, v124, v125\n"
"v_cvt_pk_bf16_f32 v119, v126, v127\n"
"v_mfma_f32_16x16x32_bf16 v[228:231], a[76:79], a[204:207], v[228:231]\n"
"v_cvt_pk_bf16_f32 v220, v220, v224\n"
"v_cvt_pk_bf16_f32 v224, v221, v225\n"
"v_mfma_f32_16x16x32_bf16 v[232:235], a[76:79], a[220:223], v[232:235]\n"
"buffer_load_dwordx4 v152, s[24:27], s42 offen offset:2176 lds\n"
"v_mfma_f32_16x16x32_bf16 v[232:235], a[72:75], a[216:219], v[232:235]\n"
"s_waitcnt vmcnt(16)\n"
"ds_read_b128 a[72:75], v154 offset:4480\n"
"ds_read_b128 a[76:79], v154 offset:4544\n"
"v_mfma_f32_16x16x32_bf16 v[232:235], a[68:71], a[212:215], v[232:235]\n"
"ds_read_b128 a[68:71], v154 offset:4416\n"
"v_pk_max_i16 v220, v220, 0\n"
"v_mfma_f32_16x16x32_bf16 v[232:235], a[64:67], a[208:211], v[232:235]\n"
"ds_read_b128 a[64:67], v154 offset:4352\n"
"v_pk_max_i16 v224, v224, 0\n"
"v_cvt_pk_bf16_f32 v222, v222, v226\n"
"v_mfma_f32_16x16x4_4b_bf16 v[204:219], v[128:129], a[244:245], 0 cbsz:2 abid:3\n"
"ds_read_b64 a[244:245], v163 offset:0\n"
"v_cvt_pk_bf16_f32 v226, v223, v227\n"
"v_pk_max_i16 v222, v222, 0\n"
"v_pk_max_i16 v226, v226, 0\n"
"v_cvt_pk_bf16_f32 v1, v2, v3\n"
"v_cvt_pk_bf16_f32 v8, v8, v9\n"
"v_cvt_pk_bf16_f32 v9, v10, v11\n"
"v_cvt_pk_bf16_f32 v221, v228, v232\n"
"v_smfmac_f32_16x16x64_bf16 v[252:255], a[248:251], v[112:119], v157\n"
"v_cvt_pk_bf16_f32 v225, v229, v233\n"
"v_mfma_f32_16x16x32_bf16 v[192:195], v[136:139], a[28:31], 0\n"
"v_pk_max_i16 v221, v221, 0\n"
"v_pk_max_i16 v225, v225, 0\n"
"v_mfma_f32_16x16x32_bf16 v[192:195], a[224:227], a[24:27], v[192:195]\n"
"v_cvt_pk_bf16_f32 v223, v230, v234\n"
"v_cvt_pk_bf16_f32 v227, v231, v235\n"
"v_mfma_f32_16x16x32_bf16 v[192:195], v[248:251], a[20:23], v[192:195]\n"
"v_pk_max_i16 v223, v223, 0\n"
"v_pk_max_i16 v227, v227, 0\n"
"v_mfma_f32_16x16x32_bf16 v[192:195], v[244:247], a[16:19], v[192:195]\n"
"buffer_store_dwordx2 v[220:221], v162, s[84:87], s68 offen offset:0 sc0 sc1\n"
"v_mfma_f32_16x16x32_bf16 v[204:207], v[108:111], v[48:51], v[204:207]\n"
"buffer_load_dwordx4 v152, s[24:27], s43 offen offset:3264 lds\n"
"v_mfma_f32_16x16x32_bf16 v[204:207], v[112:115], v[52:55], v[204:207]\n"
"v_add_f32 v130, v254, v255\n"
"v_cvt_pk_bf16_f32 v16, v16, v17\n"
"v_mfma_f32_16x16x32_bf16 v[204:207], v[116:119], v[56:59], v[204:207]\n"
"v_rcp_f32 v131, v130\n"
"v_mfma_f32_16x16x32_bf16 v[208:211], v[116:119], v[68:71], v[208:211]\n"
"v_max3_f32 v159, v159, v130, v131\n"
"v_cvt_pk_bf16_f32 v17, v18, v19\n"
"v_mfma_f32_16x16x32_bf16 v[208:211], v[112:115], v[64:67], v[208:211]\n"
"ds_bpermute_b32 v130, v160, v131 offset:128\n"
"v_mfma_f32_16x16x32_bf16 v[208:211], v[108:111], v[60:63], v[208:211]\n"
"v_cvt_pk_bf16_f32 v2, v4, v5\n"
"v_cvt_pk_bf16_f32 v3, v6, v7\n"
"v_mfma_f32_16x16x32_bf16 v[212:215], v[108:111], v[72:75], v[212:215]\n"
"v_cvt_pk_bf16_f32 v10, v12, v13\n"
"v_cvt_pk_bf16_f32 v11, v14, v15\n"
"v_mfma_f32_16x16x32_bf16 v[212:215], v[112:115], v[76:79], v[212:215]\n"
"v_cvt_pk_bf16_f32 v18, v20, v21\n"
"v_cvt_pk_bf16_f32 v19, v22, v23\n"
"v_mfma_f32_16x16x32_bf16 v[212:215], v[116:119], v[80:83], v[212:215]\n"
"v_cvt_pk_bf16_f32 v24, v24, v25\n"
"v_cvt_pk_bf16_f32 v25, v26, v27\n"
"v_mfma_f32_16x16x32_bf16 v[216:219], v[116:119], v[92:95], v[216:219]\n"
"v_cvt_pk_bf16_f32 v180, v180, v181\n"
"v_cvt_pk_bf16_f32 v181, v182, v183\n"
"v_mfma_f32_16x16x32_bf16 v[216:219], v[112:115], v[88:91], v[216:219]\n"
"v_cvt_pk_bf16_f32 v188, v188, v189\n"
"v_cvt_pk_bf16_f32 v189, v190, v191\n"
"v_mfma_f32_16x16x32_bf16 v[216:219], v[108:111], v[84:87], v[216:219]\n"
"v_cvt_pk_bf16_f32 v196, v196, v197\n"
"v_cvt_pk_bf16_f32 v197, v198, v199\n"
"v_mfma_f32_16x16x32_bf16 v[4:7], a[128:131], a[0:3], 0\n"
"v_cvt_pk_bf16_f32 v26, v176, v177\n"
"v_cvt_pk_bf16_f32 v27, v178, v179\n"
"v_mfma_f32_16x16x32_bf16 v[4:7], a[132:135], a[4:7], v[4:7]\n"
"v_mov_b32 v220, 0\n"
"v_add_u32 v161, 64, v161\n"
"v_mfma_f32_16x16x32_bf16 v[4:7], a[136:139], a[8:11], v[4:7]\n"
"v_mov_b32 v221, 0\n"
"v_cvt_pk_bf16_f32 v182, v184, v185\n"
"v_mfma_f32_16x16x32_bf16 v[4:7], a[140:143], a[12:15], v[4:7]\n"
"buffer_store_dwordx2 v[224:225], v162, s[84:87], s68 offen offset:256 sc0 sc1\n"
"v_mfma_f32_16x16x32_bf16 v[12:15], a[140:143], a[28:31], 0\n"
"s_waitcnt lgkmcnt(0)\n"
"v_mul_f32_dpp v204, v130, v204 quad_perm:[0,0,0,0] row_mask:0xf bank_mask:0xf\n"
"v_mul_f32_dpp v205, v130, v205 quad_perm:[1,1,1,1] row_mask:0xf bank_mask:0xf\n"
"v_mfma_f32_16x16x32_bf16 v[12:15], a[136:139], a[24:27], v[12:15]\n"
"v_mul_f32_dpp v206, v130, v206 quad_perm:[2,2,2,2] row_mask:0xf bank_mask:0xf\n"
"v_mul_f32_dpp v207, v130, v207 quad_perm:[3,3,3,3] row_mask:0xf bank_mask:0xf\n"
"v_mfma_f32_16x16x32_bf16 v[12:15], a[132:135], a[20:23], v[12:15]\n"
"v_mul_f32_dpp v208, v130, v208 quad_perm:[0,0,0,0] row_mask:0xf bank_mask:0xf\n"
"v_mul_f32_dpp v209, v130, v209 quad_perm:[1,1,1,1] row_mask:0xf bank_mask:0xf\n"
"v_mfma_f32_16x16x32_bf16 v[12:15], a[128:131], a[16:19], v[12:15]\n"
"v_mul_f32_dpp v210, v130, v210 quad_perm:[2,2,2,2] row_mask:0xf bank_mask:0xf\n"
"v_mul_f32_dpp v211, v130, v211 quad_perm:[3,3,3,3] row_mask:0xf bank_mask:0xf\n"
"v_mfma_f32_16x16x32_bf16 v[20:23], a[128:131], a[32:35], 0\n"
"v_mul_f32_dpp v212, v130, v212 quad_perm:[0,0,0,0] row_mask:0xf bank_mask:0xf\n"
"v_mul_f32_dpp v213, v130, v213 quad_perm:[1,1,1,1] row_mask:0xf bank_mask:0xf\n"
"v_mfma_f32_16x16x32_bf16 v[20:23], a[132:135], a[36:39], v[20:23]\n"
"v_mul_f32_dpp v214, v130, v214 quad_perm:[2,2,2,2] row_mask:0xf bank_mask:0xf\n"
"v_mul_f32_dpp v215, v130, v215 quad_perm:[3,3,3,3] row_mask:0xf bank_mask:0xf\n"
"v_mfma_f32_16x16x32_bf16 v[20:23], a[136:139], a[40:43], v[20:23]\n"
"v_mul_f32_dpp v216, v130, v216 quad_perm:[0,0,0,0] row_mask:0xf bank_mask:0xf\n"
"v_mul_f32_dpp v217, v130, v217 quad_perm:[1,1,1,1] row_mask:0xf bank_mask:0xf\n"
"v_mfma_f32_16x16x32_bf16 v[20:23], a[140:143], a[44:47], v[20:23]\n"
"v_mul_f32_dpp v218, v130, v218 quad_perm:[2,2,2,2] row_mask:0xf bank_mask:0xf\n"
"v_mul_f32_dpp v219, v130, v219 quad_perm:[3,3,3,3] row_mask:0xf bank_mask:0xf\n"
"v_mfma_f32_16x16x32_bf16 v[204:207], a[80:83], a[160:163], v[204:207]\n"
"v_cvt_pk_bf16_f32 v4, v4, v5\n"
"v_cvt_pk_bf16_f32 v5, v6, v7\n"
"v_mfma_f32_16x16x32_bf16 v[204:207], a[84:87], a[164:167], v[204:207]\n"
"v_cvt_pk_bf16_f32 v12, v12, v13\n"
"v_cvt_pk_bf16_f32 v13, v14, v15\n"
"v_mfma_f32_16x16x32_bf16 v[204:207], a[88:91], a[168:171], v[204:207]\n"
"v_cvt_pk_bf16_f32 v20, v20, v21\n"
"v_cvt_pk_bf16_f32 v21, v22, v23\n"
"v_mfma_f32_16x16x32_bf16 v[204:207], a[92:95], a[172:175], v[204:207]\n"
"v_cvt_pk_bf16_f32 v6, v164, v165\n"
"v_cvt_pk_bf16_f32 v7, v166, v167\n"
"v_mfma_f32_16x16x32_bf16 v[208:211], a[92:95], a[188:191], v[208:211]\n"
"v_cvt_pk_bf16_f32 v14, v168, v169\n"
"v_cvt_pk_bf16_f32 v15, v170, v171\n"
"v_mfma_f32_16x16x32_bf16 v[208:211], a[88:91], a[184:187], v[208:211]\n"
"v_cvt_pk_bf16_f32 v22, v172, v173\n"
"v_cvt_pk_bf16_f32 v23, v174, v175\n"
"v_mfma_f32_16x16x32_bf16 v[208:211], a[84:87], a[180:183], v[208:211]\n"
"v_cvt_pk_bf16_f32 v183, v186, v187\n"
"v_cvt_pk_bf16_f32 v190, v192, v193\n"
"v_mfma_f32_16x16x32_bf16 v[208:211], a[80:83], a[176:179], v[208:211]\n"
"buffer_store_dwordx2 v[222:223], v162, s[84:87], s68 offen offset:512 sc0 sc1\n"
"v_mfma_f32_16x16x32_bf16 v[212:215], a[80:83], a[192:195], v[212:215]\n"
"v_cvt_pk_bf16_f32 v191, v194, v195\n"
"v_mov_b32 v224, 0\n"
"v_mfma_f32_16x16x32_bf16 v[212:215], a[84:87], a[196:199], v[212:215]\n"
"v_mov_b32 v225, 0\n"
"v_mov_b32 v228, 0\n"
"v_mfma_f32_16x16x32_bf16 v[212:215], a[88:91], a[200:203], v[212:215]\n"
"v_cvt_pk_bf16_f32 v204, v204, v208\n"
"v_cvt_pk_bf16_f32 v208, v205, v209\n"
"v_mfma_f32_16x16x32_bf16 v[212:215], a[92:95], a[204:207], v[212:215]\n"
"v_pk_max_i16 v204, v204, 0\n"
"v_pk_max_i16 v208, v208, 0\n"
"v_mfma_f32_16x16x32_bf16 v[216:219], a[92:95], a[220:223], v[216:219]\n"
"v_cvt_pk_bf16_f32 v206, v206, v210\n"
"v_cvt_pk_bf16_f32 v210, v207, v211\n"
"v_mfma_f32_16x16x32_bf16 v[216:219], a[88:91], a[216:219], v[216:219]\n"
"s_waitcnt vmcnt(12)\n"
"ds_read_b128 a[88:91], v154 offset:8832\n"
"ds_read_b128 a[92:95], v154 offset:8896\n"
"v_mfma_f32_16x16x32_bf16 v[216:219], a[84:87], a[212:215], v[216:219]\n"
"ds_read_b128 a[84:87], v154 offset:8768\n"
"v_pk_max_i16 v206, v206, 0\n"
"v_mfma_f32_16x16x32_bf16 v[216:219], a[80:83], a[208:211], v[216:219]\n"
"ds_read_b128 a[80:83], v154 offset:8704\n"
"v_pk_max_i16 v210, v210, 0\n"
"v_mfma_f32_16x16x32_bf16 v[32:35], a[108:111], a[76:79], 0\n"
"v_mov_b32 v229, 0\n"
"v_mov_b32 v128, 0\n"
"v_mfma_f32_16x16x32_bf16 v[32:35], a[104:107], a[72:75], v[32:35]\n"
"v_mov_b32 v129, 0\n"
"v_mov_b32 v130, 0\n"
"v_mfma_f32_16x16x32_bf16 v[32:35], a[100:103], a[68:71], v[32:35]\n"
"v_cvt_pk_bf16_f32 v205, v212, v216\n"
"v_cvt_pk_bf16_f32 v209, v213, v217\n"
"v_mfma_f32_16x16x32_bf16 v[32:35], a[96:99], a[64:67], v[32:35]\n"
"v_pk_max_i16 v205, v205, 0\n"
"v_pk_max_i16 v209, v209, 0\n"
"v_mfma_f32_16x16x32_bf16 v[172:175], a[124:127], a[76:79], 0\n"
"v_cvt_pk_bf16_f32 v207, v214, v218\n"
"v_cvt_pk_bf16_f32 v211, v215, v219\n"
"v_mfma_f32_16x16x32_bf16 v[172:175], a[120:123], a[72:75], v[172:175]\n"
"v_pk_max_i16 v207, v207, 0\n"
"v_pk_max_i16 v211, v211, 0\n"
"v_mfma_f32_16x16x32_bf16 v[172:175], a[116:119], a[68:71], v[172:175]\n"
"v_cvt_pk_bf16_f32 v32, v32, v33\n"
"v_cvt_pk_bf16_f32 v33, v34, v35\n"
"v_mfma_f32_16x16x32_bf16 v[172:175], a[112:115], a[64:67], v[172:175]\n"
"buffer_store_dwordx2 v[226:227], v162, s[84:87], s68 offen offset:768 sc0 sc1\n"
"v_smfmac_f32_16x16x64_bf16 v[220:223], a[236:239], v[0:7], v157\n"
"v_accvgpr_read_b32 v219, a240\n"
"v_mfma_f32_16x16x32_bf16 v[200:203], v[244:247], a[32:35], 0\n"
"ds_read_b128 v[244:247], v153 offset:6144\n"
"v_mov_b32 v131, 0\n"
"v_mfma_f32_16x16x32_bf16 v[200:203], v[248:251], a[36:39], v[200:203]\n"
"ds_read_b128 v[248:251], v153 offset:10240\n"
"v_cvt_pk_bf16_f32 v34, v172, v173\n"
"v_mfma_f32_16x16x32_bf16 v[200:203], a[224:227], a[40:43], v[200:203]\n"
"ds_read_b128 a[224:227], v153 offset:14336\n"
"v_cvt_pk_bf16_f32 v35, v174, v175\n"
"v_mfma_f32_16x16x32_bf16 v[200:203], v[136:139], a[44:47], v[200:203]\n"
"ds_read_b128 v[136:139], v153 offset:3072\n"
"v_mov_b32 v132, 0\n"
"v_mfma_f32_16x16x32_bf16 v[28:31], a[128:131], a[48:51], 0\n"
"v_mov_b32 v133, 0\n"
"v_mov_b32 v134, 0\n"
"v_mfma_f32_16x16x32_bf16 v[28:31], a[132:135], a[52:55], v[28:31]\n"
"v_mov_b32 v135, 0\n"
"v_mfma_f32_16x16x32_bf16 v[28:31], a[136:139], a[56:59], v[28:31]\n"
"v_cvt_pk_bf16_f32 v198, v200, v201\n"
"v_cvt_pk_bf16_f32 v199, v202, v203\n"
"v_mfma_f32_16x16x32_bf16 v[28:31], a[140:143], a[60:63], v[28:31]\n"
"v_mfma_f32_16x16x32_bf16 v[36:39], a[140:143], a[76:79], 0\n"
"v_mfma_f32_16x16x32_bf16 v[36:39], a[136:139], a[72:75], v[36:39]\n"
"v_mfma_f32_16x16x32_bf16 v[36:39], a[132:135], a[68:71], v[36:39]\n"
"v_cvt_pk_bf16_f32 v28, v28, v29\n"
"v_cvt_pk_bf16_f32 v29, v30, v31\n"
"v_mfma_f32_16x16x32_bf16 v[36:39], a[128:131], a[64:67], v[36:39]\n"
"buffer_store_dwordx2 v[204:205], v162, s[84:87], s69 offen offset:0 sc0 sc1\n"
"v_smfmac_f32_16x16x64_bf16 v[224:227], a[236:239], v[8:15], v157\n"
"v_mfma_f32_16x16x32_bf16 v[48:51], a[0:3], v[140:143], 0\n"
"v_mfma_f32_16x16x32_bf16 v[48:51], a[4:7], v[144:147], v[48:51]\n"
"v_cvt_pk_bf16_f32 v36, v36, v37\n"
"v_cvt_pk_bf16_f32 v37, v38, v39\n"
"v_mfma_f32_16x16x32_bf16 v[48:51], a[8:11], v[148:151], v[48:51]\n"
"v_mfma_f32_16x16x32_bf16 v[48:51], a[12:15], v[236:239], v[48:51]\n"
"v_mfma_f32_16x16x32_bf16 v[60:63], a[28:31], v[236:239], 0\n"
"v_mfma_f32_16x16x32_bf16 v[60:63], a[24:27], v[148:151], v[60:63]\n"
"v_mfma_f32_16x16x32_bf16 v[60:63], a[20:23], v[144:147], v[60:63]\n"
"v_cvt_pk_bf16_f32 v48, v48, v49\n"
"v_cvt_pk_bf16_f32 v49, v50, v51\n"
"v_mfma_f32_16x16x32_bf16 v[60:63], a[16:19], v[140:143], v[60:63]\n"
"buffer_store_dwordx2 v[208:209], v162, s[84:87], s69 offen offset:256 sc0 sc1\n"
"v_mfma_f32_16x16x32_bf16 v[52:55], a[32:35], v[140:143], 0\n"
"s_nop 0\n"
"v_add_f32 v208, v220, v221\n"
"v_add_f32 v209, v224, v225\n"
"v_mfma_f32_16x16x32_bf16 v[52:55], a[36:39], v[144:147], v[52:55]\n"
"v_exp_f32 v208, v208\n"
"v_mfma_f32_16x16x32_bf16 v[52:55], a[40:43], v[148:151], v[52:55]\n"
"v_cvt_pk_bf16_f32 v50, v60, v61\n"
"v_cvt_pk_bf16_f32 v51, v62, v63\n"
"v_mfma_f32_16x16x32_bf16 v[52:55], a[44:47], v[236:239], v[52:55]\n"
"v_exp_f32 v209, v209\n"
"v_mfma_f32_16x16x32_bf16 v[64:67], a[60:63], v[236:239], 0\n"
"v_cvt_pk_bf16_f32 v215, v208, v209\n"
"v_mov_b32 v220, 0\n"
"v_mfma_f32_16x16x32_bf16 v[64:67], a[56:59], v[148:151], v[64:67]\n"
"ds_write_b16 v219, v215 offset:0\n"
"v_mfma_f32_16x16x32_bf16 v[64:67], a[52:55], v[144:147], v[64:67]\n"
"ds_write_b16_d16_hi v219, v215 offset:4\n"
"v_mfma_f32_16x16x32_bf16 v[64:67], a[48:51], v[140:143], v[64:67]\n"
"buffer_store_dwordx2 v[206:207], v162, s[84:87], s69 offen offset:512 sc0 sc1\n"
"v_mfma_f32_16x16x32_bf16 v[56:59], a[64:67], v[140:143], 0\n"
"s_nop 0\n"
"ds_read_b64 v[206:207], v163 offset:16384\n"
"v_cvt_pk_bf16_f32 v52, v52, v53\n"
"v_mfma_f32_16x16x32_bf16 v[56:59], a[68:71], v[144:147], v[56:59]\n"
"v_cvt_pk_bf16_f32 v53, v54, v55\n"
"v_mov_b32 v221, 0\n"
"v_mfma_f32_16x16x32_bf16 v[56:59], a[72:75], v[148:151], v[56:59]\n"
"v_cvt_pk_bf16_f32 v54, v64, v65\n"
"v_cvt_pk_bf16_f32 v55, v66, v67\n"
"v_mfma_f32_16x16x32_bf16 v[56:59], a[76:79], v[236:239], v[56:59]\n"
"s_waitcnt lgkmcnt(3)\n"
"v_mov_b32 v224, 0\n"
"v_mov_b32 v225, 0\n"
"v_mfma_f32_16x16x32_bf16 v[68:71], a[92:95], v[236:239], 0\n"
"ds_read_b128 v[236:239], v153 offset:18496\n"
"v_add_f32 v208, v209, v208\n"
"v_mfma_f32_16x16x32_bf16 v[68:71], a[88:91], v[148:151], v[68:71]\n"
"ds_read_b128 v[148:151], v153 offset:15360\n"
"v_add_u32 v163, 512, v163\n"
"v_mfma_f32_16x16x32_bf16 v[68:71], a[84:87], v[144:147], v[68:71]\n"
"ds_read_b128 v[144:147], v153 offset:11264\n"
"v_cvt_pk_bf16_f32 v56, v56, v57\n"
"v_mfma_f32_16x16x32_bf16 v[68:71], a[80:83], v[140:143], v[68:71]\n"
"buffer_store_dwordx2 v[210:211], v162, s[84:87], s69 offen offset:768 sc0 sc1\n"
"v_mfma_f32_16x16x32_bf16 v[184:187], v[240:243], a[0:3], 0\n"
"ds_read_b128 v[140:143], v153 offset:7168\n"
"v_cvt_pk_bf16_f32 v57, v58, v59\n"
"v_mfma_f32_16x16x32_bf16 v[184:187], v[244:247], a[4:7], v[184:187]\n"
"v_mfma_f32_16x16x32_bf16 v[184:187], v[248:251], a[8:11], v[184:187]\n"
"v_cvt_pk_bf16_f32 v58, v68, v69\n"
"v_cvt_pk_bf16_f32 v59, v70, v71\n"
"v_mfma_f32_16x16x32_bf16 v[184:187], a[224:227], a[12:15], v[184:187]\n"
"s_waitcnt lgkmcnt(4)\n"
"v_lshlrev_b32 v204, 16, v206\n"
"v_and_b32 v205, 0xffff0000, v206\n"
"v_mfma_f32_16x16x32_bf16 v[192:195], a[224:227], a[28:31], 0\n"
"v_lshlrev_b32 v206, 16, v207\n"
"v_and_b32 v207, 0xffff0000, v207\n"
"v_mfma_f32_16x16x32_bf16 v[192:195], v[248:251], a[24:27], v[192:195]\n"
"s_add_u32 m0, s28, 4352\n"
"v_mfma_f32_16x16x32_bf16 v[192:195], v[244:247], a[20:23], v[192:195]\n"
"buffer_load_dwordx4 v152, s[24:27], s44 offen offset:0 lds\n"
"v_mfma_f32_16x16x32_bf16 v[192:195], v[240:243], a[16:19], v[192:195]\n"
"v_cvt_pk_bf16_f32 v184, v184, v185\n"
"v_cvt_pk_bf16_f32 v185, v186, v187\n"
"v_mfma_f32_16x16x32_bf16 v[200:203], v[240:243], a[32:35], 0\n"
"ds_read_b128 v[240:243], v153 offset:22688\n"
"v_mfma_f32_16x16x32_bf16 v[200:203], v[244:247], a[36:39], v[200:203]\n"
"ds_read_b128 v[244:247], v153 offset:26880\n"
"v_mfma_f32_16x16x32_bf16 v[200:203], v[248:251], a[40:43], v[200:203]\n"
"ds_read_b128 v[248:251], v153 offset:31072\n"
"v_cvt_pk_bf16_f32 v192, v192, v193\n"
"v_mfma_f32_16x16x32_bf16 v[200:203], a[224:227], a[44:47], v[200:203]\n"
"ds_read_b128 a[224:227], v153 offset:27936\n"
"v_cvt_pk_bf16_f32 v193, v194, v195\n"
"v_mfma_f32_16x16x32_bf16 v[40:43], a[96:99], a[80:83], 0\n"
"v_mfma_f32_16x16x32_bf16 v[40:43], a[100:103], a[84:87], v[40:43]\n"
"v_mfma_f32_16x16x32_bf16 v[40:43], a[104:107], a[88:91], v[40:43]\n"
"v_cvt_pk_bf16_f32 v200, v200, v201\n"
"v_cvt_pk_bf16_f32 v201, v202, v203\n"
"v_mfma_f32_16x16x32_bf16 v[40:43], a[108:111], a[92:95], v[40:43]\n"
"v_smfmac_f32_16x16x64_bf16 v[228:231], a[236:239], v[16:23], v157\n"
"v_mfma_f32_16x16x32_bf16 v[164:167], v[136:139], a[0:3], 0\n"
"s_waitcnt lgkmcnt(4)\n"
"v_mfma_f32_16x16x32_bf16 v[164:167], v[140:143], a[4:7], v[164:167]\n"
"v_cvt_pk_bf16_f32 v40, v40, v41\n"
"v_cvt_pk_bf16_f32 v41, v42, v43\n"
"v_mfma_f32_16x16x32_bf16 v[164:167], v[144:147], a[8:11], v[164:167]\n"
"buffer_load_dwordx4 v152, s[24:27], s45 offen offset:1088 lds\n"
"v_mfma_f32_16x16x32_bf16 v[164:167], v[148:151], a[12:15], v[164:167]\n"
"v_add_f32 v210, v228, v229\n"
"v_mov_b32 v228, 0\n"
"v_mfma_f32_16x16x32_bf16 v[168:171], v[148:151], a[28:31], 0\n"
"v_exp_f32 v210, v210\n"
"v_mfma_f32_16x16x32_bf16 v[168:171], v[144:147], a[24:27], v[168:171]\n"
"v_mov_b32 v229, 0\n"
"v_add_f32 v208, v210, v208\n"
"v_mfma_f32_16x16x32_bf16 v[168:171], v[140:143], a[20:23], v[168:171]\n"
"v_cvt_pk_bf16_f32 v186, v164, v165\n"
"v_cvt_pk_bf16_f32 v187, v166, v167\n"
"v_mfma_f32_16x16x32_bf16 v[168:171], v[136:139], a[16:19], v[168:171]\n"
"v_mfma_f32_16x16x32_bf16 v[172:175], v[136:139], a[32:35], 0\n"
"ds_read_b128 v[136:139], v153 offset:17440\n"
"v_mfma_f32_16x16x32_bf16 v[172:175], v[140:143], a[36:39], v[172:175]\n"
"ds_read_b128 v[140:143], v153 offset:21632\n"
"v_mfma_f32_16x16x32_bf16 v[172:175], v[144:147], a[40:43], v[172:175]\n"
"ds_read_b128 v[144:147], v153 offset:25824\n"
"v_cvt_pk_bf16_f32 v194, v168, v169\n"
"v_mfma_f32_16x16x32_bf16 v[172:175], v[148:151], a[44:47], v[172:175]\n"
"ds_read_b128 v[148:151], v153 offset:30016\n"
"v_cvt_pk_bf16_f32 v195, v170, v171\n"
"v_mfma_f32_16x16x32_bf16 v[164:167], a[144:147], a[48:51], 0\n"
"v_mfma_f32_16x16x32_bf16 v[164:167], a[148:151], a[52:55], v[164:167]\n"
"v_mfma_f32_16x16x32_bf16 v[164:167], a[152:155], a[56:59], v[164:167]\n"
"v_cvt_pk_bf16_f32 v202, v172, v173\n"
"v_cvt_pk_bf16_f32 v203, v174, v175\n"
"v_mfma_f32_16x16x32_bf16 v[164:167], a[156:159], a[60:63], v[164:167]\n"
"v_mfma_f32_16x16x32_bf16 v[168:171], a[156:159], a[76:79], 0\n"
"buffer_load_dwordx4 v152, s[24:27], s46 offen offset:2176 lds\n"
"v_mfma_f32_16x16x32_bf16 v[168:171], a[152:155], a[72:75], v[168:171]\n"
"v_mfma_f32_16x16x32_bf16 v[168:171], a[148:151], a[68:71], v[168:171]\n"
".Lpad4_%=:\n"
"s_cbranch_vccnz .Lback0_%=\n"
"v_cvt_pk_bf16_f32 v30, v164, v165\n"
"v_cvt_pk_bf16_f32 v31, v166, v167\n"
"v_mfma_f32_16x16x32_bf16 v[168:171], a[144:147], a[64:67], v[168:171]\n"
"v_mfma_f32_16x16x32_bf16 v[176:179], a[112:115], a[80:83], 0\n"
"v_mfma_f32_16x16x32_bf16 v[176:179], a[116:119], a[84:87], v[176:179]\n"
"v_mfma_f32_16x16x32_bf16 v[176:179], a[120:123], a[88:91], v[176:179]\n"
"v_cvt_pk_bf16_f32 v38, v168, v169\n"
"v_cvt_pk_bf16_f32 v39, v170, v171\n"
"v_mfma_f32_16x16x32_bf16 v[176:179], a[124:127], a[92:95], v[176:179]\n"
"v_smfmac_f32_16x16x64_bf16 v[220:223], a[236:239], v[24:31], v157\n"
"v_mfma_f32_16x16x32_bf16 v[96:99], v[0:3], v[180:183], 0\n"
"v_mfma_f32_16x16x32_bf16 v[96:99], v[4:7], v[184:187], v[96:99]\n"
"v_cvt_pk_bf16_f32 v42, v176, v177\n"
"v_cvt_pk_bf16_f32 v43, v178, v179\n"
"v_mfma_f32_16x16x32_bf16 v[44:47], a[128:131], a[80:83], 0\n"
"v_add_f32 v211, v220, v221\n"
"v_mov_b32 v220, 0\n"
"v_mfma_f32_16x16x32_bf16 v[44:47], a[132:135], a[84:87], v[44:47]\n"
"v_exp_f32 v211, v211\n"
"v_mfma_f32_16x16x32_bf16 v[44:47], a[136:139], a[88:91], v[44:47]\n"
"v_cvt_pk_bf16_f32 v216, v210, v211\n"
"v_mov_b32 v221, 0\n"
"v_mfma_f32_16x16x32_bf16 v[44:47], a[140:143], a[92:95], v[44:47]\n"
"buffer_load_dwordx4 v152, s[24:27], s47 offen offset:3264 lds\n"
"v_smfmac_f32_16x16x64_bf16 v[224:227], a[236:239], v[32:39], v157\n"
"ds_write_b16 v219, v216 offset:512\n"
"v_mfma_f32_16x16x32_bf16 v[120:123], v[12:15], v[184:187], 0\n"
"ds_write_b16_d16_hi v219, v216 offset:516\n"
"v_mfma_f32_16x16x32_bf16 v[120:123], v[8:11], v[180:183], v[120:123]\n"
"v_cvt_pk_bf16_f32 v44, v44, v45\n"
"v_cvt_pk_bf16_f32 v45, v46, v47\n"
"v_mfma_f32_16x16x32_bf16 v[164:167], a[144:147], a[80:83], 0\n"
"v_add_f32 v212, v224, v225\n"
"v_add_f32 v208, v211, v208\n"
"v_mfma_f32_16x16x32_bf16 v[164:167], a[148:151], a[84:87], v[164:167]\n"
"v_exp_f32 v212, v212\n"
"v_mfma_f32_16x16x32_bf16 v[164:167], a[152:155], a[88:91], v[164:167]\n"
"v_exp_f32 v96, v96\n"
"v_mfma_f32_16x16x32_bf16 v[164:167], a[156:159], a[92:95], v[164:167]\n"
"v_exp_f32 v97, v97\n"
"v_mfma_f32_16x16x32_bf16 v[100:103], v[16:19], v[180:183], 0\n"
"v_exp_f32 v98, v98\n"
"v_mfma_f32_16x16x32_bf16 v[100:103], v[20:23], v[184:187], v[100:103]\n"
"v_exp_f32 v99, v99\n"
"v_mfma_f32_16x16x32_bf16 v[72:75], a[0:3], v[236:239], 0\n"
"s_waitcnt lgkmcnt(2)\n"
"v_cvt_pk_bf16_f32 v46, v164, v165\n"
"v_cvt_pk_bf16_f32 v47, v166, v167\n"
"v_mfma_f32_16x16x32_bf16 v[72:75], a[4:7], v[240:243], v[72:75]\n"
"v_cvt_pk_bf16_f32 v96, v96, v97\n"
"v_cvt_pk_bf16_f32 v97, v98, v99\n"
"v_mfma_f32_16x16x32_bf16 v[72:75], a[8:11], v[244:247], v[72:75]\n"
"v_exp_f32 v120, v120\n"
"v_mfma_f32_16x16x32_bf16 v[72:75], a[12:15], v[248:251], v[72:75]\n"
"v_exp_f32 v121, v121\n"
"v_smfmac_f32_16x16x64_bf16 v[228:231], a[236:239], v[40:47], v157\n"
"s_add_u32 m0, s28, 8704\n"
"v_mfma_f32_16x16x32_bf16 v[84:87], a[28:31], v[248:251], 0\n"
"buffer_load_dwordx4 v152, s[24:27], s48 offen offset:0 lds\n"
"v_mfma_f32_16x16x32_bf16 v[84:87], a[24:27], v[244:247], v[84:87]\n"
"v_cvt_pk_bf16_f32 v72, v72, v73\n"
"v_cvt_pk_bf16_f32 v73, v74, v75\n"
"v_mfma_f32_16x16x32_bf16 v[84:87], a[20:23], v[240:243], v[84:87]\n"
"v_add_f32 v213, v228, v229\n"
"v_add_f32 v208, v212, v208\n"
"v_mfma_f32_16x16x32_bf16 v[84:87], a[16:19], v[236:239], v[84:87]\n"
"v_exp_f32 v213, v213\n"
"v_mfma_f32_16x16x32_bf16 v[76:79], a[32:35], v[236:239], 0\n"
"v_cvt_pk_bf16_f32 v217, v212, v213\n"
"v_add_f32 v208, v213, v208\n"
"v_mfma_f32_16x16x32_bf16 v[76:79], a[36:39], v[240:243], v[76:79]\n"
"ds_write_b16 v219, v217 offset:1536\n"
"v_mfma_f32_16x16x32_bf16 v[76:79], a[40:43], v[244:247], v[76:79]\n"
"ds_write_b16_d16_hi v219, v217 offset:1540\n"
"v_mfma_f32_16x16x32_bf16 v[76:79], a[44:47], v[248:251], v[76:79]\n"
"v_cvt_pk_bf16_f32 v74, v84, v85\n"
"v_cvt_pk_bf16_f32 v75, v86, v87\n"
"v_mfma_f32_16x16x32_bf16 v[88:91], a[60:63], v[248:251], 0\n"
"v_exp_f32 v122, v122\n"
"v_mfma_f32_16x16x32_bf16 v[88:91], a[56:59], v[244:247], v[88:91]\n"
"v_exp_f32 v123, v123\n"
"v_mfma_f32_16x16x32_bf16 v[88:91], a[52:55], v[240:243], v[88:91]\n"
"v_cvt_pk_bf16_f32 v76, v76, v77\n"
"v_cvt_pk_bf16_f32 v77, v78, v79\n"
"v_mfma_f32_16x16x32_bf16 v[88:91], a[48:51], v[236:239], v[88:91]\n"
"v_cvt_pk_bf16_f32 v98, v120, v121\n"
"v_cvt_pk_bf16_f32 v99, v122, v123\n"
"v_mfma_f32_16x16x32_bf16 v[80:83], a[64:67], v[236:239], 0\n"
"v_exp_f32 v100, v100\n"
"v_mfma_f32_16x16x32_bf16 v[80:83], a[68:71], v[240:243], v[80:83]\n"
"buffer_load_dwordx4 v152, s[24:27], s49 offen offset:1088 lds\n"
"v_mfma_f32_16x16x32_bf16 v[80:83], a[72:75], v[244:247], v[80:83]\n"
"v_cvt_pk_bf16_f32 v78, v88, v89\n"
"v_cvt_pk_bf16_f32 v79, v90, v91\n"
"v_mfma_f32_16x16x32_bf16 v[80:83], a[76:79], v[248:251], v[80:83]\n"
"v_exp_f32 v101, v101\n"
"v_mfma_f32_16x16x32_bf16 v[92:95], a[92:95], v[248:251], 0\n"
"ds_read_b128 v[248:251], v153 offset:23744\n"
"v_cvt_pk_bf16_f32 v100, v100, v101\n"
"v_mfma_f32_16x16x32_bf16 v[92:95], a[88:91], v[244:247], v[92:95]\n"
"ds_read_b128 v[244:247], v153 offset:19552\n"
"v_mfma_f32_16x16x32_bf16 v[92:95], a[84:87], v[240:243], v[92:95]\n"
"v_cvt_pk_bf16_f32 v80, v80, v81\n"
"v_cvt_pk_bf16_f32 v81, v82, v83\n"
"v_mfma_f32_16x16x32_bf16 v[92:95], a[80:83], v[236:239], v[92:95]\n"
"ds_read2_b32 v[236:237], v158 offset0:0 offset1:2\n"
"ds_read2_b32 v[238:239], v158 offset0:128 offset1:130\n"
"v_mfma_f32_16x16x32_bf16 v[124:127], v[28:31], v[184:187], 0\n"
"v_exp_f32 v102, v102\n"
"v_mfma_f32_16x16x32_bf16 v[124:127], v[24:27], v[180:183], v[124:127]\n"
"v_exp_f32 v103, v103\n"
"v_smfmac_f32_16x16x64_bf16 v[220:223], a[236:239], a[228:235], v157\n"
"ds_read_b64 a[236:237], v156 offset:0\n"
"v_mfma_f32_16x16x32_bf16 v[104:107], v[32:35], v[180:183], 0\n"
"ds_read_b64 a[238:239], v156 offset:64\n"
"ds_read_b128 a[228:231], v161 offset:0\n"
"v_mfma_f32_16x16x32_bf16 v[104:107], v[36:39], v[184:187], v[104:107]\n"
"ds_read_b128 a[232:235], v161 offset:2048\n"
"v_cvt_pk_bf16_f32 v82, v92, v93\n"
"v_mfma_f32_16x16x32_bf16 v[120:123], v[44:47], v[184:187], 0\n"
"v_cvt_pk_bf16_f32 v83, v94, v95\n"
"v_add_f32 v214, v220, v221\n"
"v_mfma_f32_16x16x32_bf16 v[120:123], v[40:43], v[180:183], v[120:123]\n"
"v_exp_f32 v214, v214\n"
"v_mfma_f32_16x16x32_bf16 v[60:63], a[0:3], v[136:139], 0\n"
"buffer_load_dwordx4 v152, s[24:27], s50 offen offset:2176 lds\n"
"v_mfma_f32_16x16x32_bf16 v[60:63], a[4:7], v[140:143], v[60:63]\n"
"v_cvt_pk_bf16_f32 v218, v214, v214\n"
"v_add_f32_dpp v208, v214, v208 quad_perm:[0,1,2,3] row_mask:0xf bank_mask:0x1\n"
"v_mfma_f32_16x16x32_bf16 v[60:63], a[8:11], v[144:147], v[60:63]\n"
"ds_write_b16 v219, v218 offset:2048\n"
"v_mfma_f32_16x16x32_bf16 v[60:63], a[12:15], v[148:151], v[60:63]\n"
"v_accvgpr_read_b32 v219, a241\n"
"v_add_f32_dpp v208, v208, v208 row_ror:8 row_mask:0xf bank_mask:0xf\n"
"v_mfma_f32_16x16x32_bf16 v[164:167], a[28:31], v[148:151], 0\n"
"ds_read_b128 v[220:223], v219 offset:0\n"
"ds_read_b128 v[224:227], v219 offset:1024\n"
"v_mfma_f32_16x16x32_bf16 v[164:167], a[24:27], v[144:147], v[164:167]\n"
"v_accvgpr_read_b32 v219, a242\n"
"v_add_f32_dpp v208, v208, v208 row_ror:4 row_mask:0xf bank_mask:0xf\n"
"v_mfma_f32_16x16x32_bf16 v[164:167], a[20:23], v[140:143], v[164:167]\n"
"ds_read2_b32 v[228:229], v219 offset0:0 offset1:4\n"
"v_add_f32_dpp v208, v208, v208 row_ror:2 row_mask:0xf bank_mask:0xf\n"
"v_mfma_f32_16x16x32_bf16 v[164:167], a[16:19], v[136:139], v[164:167]\n"
"v_exp_f32 v124, v124\n"
"v_mfma_f32_16x16x32_bf16 v[64:67], a[32:35], v[136:139], 0\n"
"v_add_f32_dpp v208, v208, v208 row_ror:1 row_mask:0xf bank_mask:0xf\n"
"v_cvt_pk_bf16_f32 v101, v102, v103\n"
"v_mfma_f32_16x16x32_bf16 v[64:67], a[36:39], v[140:143], v[64:67]\n"
"v_rcp_f32 v209, v208\n"
"v_mfma_f32_16x16x32_bf16 v[64:67], a[40:43], v[144:147], v[64:67]\n"
"v_max3_f32 v159, v159, v208, v209\n"
"v_cvt_pk_bf16_f32 v60, v60, v61\n"
"v_mfma_f32_16x16x32_bf16 v[64:67], a[44:47], v[148:151], v[64:67]\n"
"v_exp_f32 v125, v125\n"
"v_mfma_f32_16x16x32_bf16 v[168:171], a[60:63], v[148:151], 0\n"
"v_exp_f32 v126, v126\n"
"v_mfma_f32_16x16x32_bf16 v[168:171], a[56:59], v[144:147], v[168:171]\n"
"v_exp_f32 v127, v127\n"
"v_mfma_f32_16x16x32_bf16 v[168:171], a[52:55], v[140:143], v[168:171]\n"
"buffer_load_dwordx4 v152, s[24:27], s51 offen offset:3264 lds\n"
"v_mfma_f32_16x16x32_bf16 v[168:171], a[48:51], v[136:139], v[168:171]\n"
"v_cvt_pk_bf16_f32 v102, v124, v125\n"
"v_cvt_pk_bf16_f32 v103, v126, v127\n"
"v_mfma_f32_16x16x32_bf16 v[68:71], a[64:67], v[136:139], 0\n"
"v_exp_f32 v104, v104\n"
"v_mfma_f32_16x16x32_bf16 v[68:71], a[68:71], v[140:143], v[68:71]\n"
"v_exp_f32 v105, v105\n"
"v_mfma_f32_16x16x32_bf16 v[68:71], a[72:75], v[144:147], v[68:71]\n"
"v_cvt_pk_bf16_f32 v61, v62, v63\n"
"v_cvt_pk_bf16_f32 v62, v164, v165\n"
"v_mfma_f32_16x16x32_bf16 v[68:71], a[76:79], v[148:151], v[68:71]\n"
"v_cvt_pk_bf16_f32 v63, v166, v167\n"
"v_cvt_pk_bf16_f32 v64, v64, v65\n"
"v_mfma_f32_16x16x32_bf16 v[172:175], a[92:95], v[148:151], 0\n"
"ds_read_b128 v[148:151], v153 offset:8192\n"
"v_cvt_pk_bf16_f32 v65, v66, v67\n"
"v_mfma_f32_16x16x32_bf16 v[172:175], a[88:91], v[144:147], v[172:175]\n"
"ds_read_b128 v[144:147], v153 offset:4096\n"
"v_cvt_pk_bf16_f32 v66, v168, v169\n"
"v_mfma_f32_16x16x32_bf16 v[172:175], a[84:87], v[140:143], v[172:175]\n"
"ds_read_b128 v[140:143], v153 offset:0\n"
"v_cvt_pk_bf16_f32 v67, v170, v171\n"
"v_mfma_f32_16x16x32_bf16 v[172:175], a[80:83], v[136:139], v[172:175]\n"
"s_waitcnt lgkmcnt(3)\n"
"ds_read_b128 v[136:139], v153 offset:32128\n"
"v_cvt_pk_bf16_f32 v68, v68, v69\n"
"v_smfmac_f32_16x16x64_bf16 v[128:131], v[236:239], v[180:187], v157\n"
"v_cvt_pk_bf16_f32 v69, v70, v71\n"
"v_smfmac_f32_16x16x64_bf16 v[132:135], v[236:239], v[196:203], v157\n"
"ds_read_b128 v[236:239], v153 offset:12288\n"
"v_mfma_f32_16x16x32_bf16 v[108:111], v[0:3], v[188:191], 0\n"
"v_cvt_pk_bf16_f32 v70, v172, v173\n"
"v_cvt_pk_bf16_f32 v71, v174, v175\n"
"v_mfma_f32_16x16x32_bf16 v[108:111], v[4:7], v[192:195], v[108:111]\n"
"v_exp_f32 v106, v106\n"
"v_mfma_f32_16x16x32_bf16 v[124:127], v[12:15], v[192:195], 0\n"
"v_exp_f32 v107, v107\n"
"v_mfma_f32_16x16x32_bf16 v[124:127], v[8:11], v[188:191], v[124:127]\n"
"v_cvt_pk_bf16_f32 v104, v104, v105\n"
"v_cvt_pk_bf16_f32 v105, v106, v107\n"
"v_mfma_f32_16x16x32_bf16 v[112:115], v[16:19], v[188:191], 0\n"
"v_exp_f32 v120, v120\n"
"v_mfma_f32_16x16x32_bf16 v[112:115], v[20:23], v[192:195], v[112:115]\n"
"v_exp_f32 v121, v121\n"
"v_mfma_f32_16x16x32_bf16 v[84:87], a[0:3], v[244:247], 0\n"
"v_mov_b32_dpp v158, v158 row_ror:8 row_mask:0xf bank_mask:0xf\n"
"v_cvt_pk_bf16_f32 v106, v120, v121\n"
"v_mfma_f32_16x16x32_bf16 v[84:87], a[4:7], v[248:251], v[84:87]\n"
"ds_read2_b32 v[240:241], v158 offset0:0 offset1:2\n"
"ds_read2_b32 v[242:243], v158 offset0:128 offset1:130\n"
"v_mfma_f32_16x16x32_bf16 v[84:87], a[8:11], a[224:227], v[84:87]\n"
"s_waitcnt lgkmcnt(3)\n"
"v_exp_f32 v122, v122\n"
"v_mfma_f32_16x16x32_bf16 v[84:87], a[12:15], v[136:139], v[84:87]\n"
"v_exp_f32 v123, v123\n"
"v_mfma_f32_16x16x32_bf16 v[176:179], a[28:31], v[136:139], 0\n"
"v_cvt_pk_bf16_f32 v107, v122, v123\n"
"v_mfma_f32_16x16x32_bf16 v[176:179], a[24:27], a[224:227], v[176:179]\n"
"v_exp_f32 v108, v108\n"
"v_mfma_f32_16x16x32_bf16 v[176:179], a[20:23], v[248:251], v[176:179]\n"
"v_cvt_pk_bf16_f32 v84, v84, v85\n"
"v_cvt_pk_bf16_f32 v85, v86, v87\n"
"v_mfma_f32_16x16x32_bf16 v[176:179], a[16:19], v[244:247], v[176:179]\n"
"v_exp_f32 v109, v109\n"
"v_mfma_f32_16x16x32_bf16 v[88:91], a[32:35], v[244:247], 0\n"
"s_add_u32 s84, s84, 13107200\n"
"s_addc_u32 s85, s85, 0\n"
"v_mfma_f32_16x16x32_bf16 v[88:91], a[36:39], v[248:251], v[88:91]\n"
"v_exp_f32 v110, v110\n"
"v_mfma_f32_16x16x32_bf16 v[88:91], a[40:43], a[224:227], v[88:91]\n"
"v_cvt_pk_bf16_f32 v86, v176, v177\n"
"v_cvt_pk_bf16_f32 v87, v178, v179\n"
"v_mfma_f32_16x16x32_bf16 v[88:91], a[44:47], v[136:139], v[88:91]\n"
"v_exp_f32 v111, v111\n"
"v_mfma_f32_16x16x32_bf16 v[164:167], a[60:63], v[136:139], 0\n"
"v_cvt_pk_bf16_f32 v108, v108, v109\n"
"v_cvt_pk_bf16_f32 v109, v110, v111\n"
"v_mfma_f32_16x16x32_bf16 v[164:167], a[56:59], a[224:227], v[164:167]\n"
"v_exp_f32 v124, v124\n"
"v_mfma_f32_16x16x32_bf16 v[164:167], a[52:55], v[248:251], v[164:167]\n"
"v_cvt_pk_bf16_f32 v88, v88, v89\n"
"v_cvt_pk_bf16_f32 v89, v90, v91\n"
"v_mfma_f32_16x16x32_bf16 v[164:167], a[48:51], v[244:247], v[164:167]\n"
"s_waitcnt lgkmcnt(0)\n"
"v_exp_f32 v125, v125\n"
"v_smfmac_f32_16x16x64_bf16 v[128:131], v[240:243], v[188:195], v157\n"
"ds_read_b128 v[240:243], v153 offset:1024\n"
"v_mfma_f32_16x16x32_bf16 v[92:95], a[64:67], v[244:247], 0\n"
"v_exp_f32 v126, v126\n"
"v_mfma_f32_16x16x32_bf16 v[92:95], a[68:71], v[248:251], v[92:95]\n"
"v_cvt_pk_bf16_f32 v90, v164, v165\n"
"v_cvt_pk_bf16_f32 v91, v166, v167\n"
"v_mfma_f32_16x16x32_bf16 v[92:95], a[72:75], a[224:227], v[92:95]\n"
"v_add_f32 v128, v128, v129\n"
"v_add_f32 v130, v130, v131\n"
"v_mfma_f32_16x16x32_bf16 v[92:95], a[76:79], v[136:139], v[92:95]\n"
"v_exp_f32 v128, v128\n"
"v_mfma_f32_16x16x32_bf16 v[168:171], a[92:95], v[136:139], 0\n"
"ds_read_b128 v[136:139], v153 offset:2048\n"
"v_cvt_pk_bf16_f32 v110, v124, v125\n"
"v_mfma_f32_16x16x32_bf16 v[168:171], a[88:91], a[224:227], v[168:171]\n"
"ds_read_b128 a[224:227], v153 offset:13312\n"
"v_add_u32 v156, 512, v156\n"
"v_mfma_f32_16x16x32_bf16 v[168:171], a[84:87], v[248:251], v[168:171]\n"
"ds_read_b128 v[248:251], v153 offset:9216\n"
"v_cvt_pk_bf16_f32 v92, v92, v93\n"
"v_mfma_f32_16x16x32_bf16 v[168:171], a[80:83], v[244:247], v[168:171]\n"
"ds_read_b128 v[244:247], v153 offset:5120\n"
"v_cvt_pk_bf16_f32 v93, v94, v95\n"
"v_mfma_f32_16x16x32_bf16 v[120:123], v[28:31], v[192:195], 0\n"
"v_exp_f32 v130, v130\n"
"v_mfma_f32_16x16x32_bf16 v[120:123], v[24:27], v[188:191], v[120:123]\n"
"v_cvt_pk_bf16_f32 v129, v128, v130\n"
"v_add_f32 v128, v128, v130\n"
"v_mfma_f32_16x16x32_bf16 v[116:119], v[32:35], v[188:191], 0\n"
"v_cvt_pk_bf16_f32 v94, v168, v169\n"
"v_cvt_pk_bf16_f32 v95, v170, v171\n"
"v_mfma_f32_16x16x32_bf16 v[116:119], v[36:39], v[192:195], v[116:119]\n"
"v_permlane16_swap_b32 v128, v129\n"
"v_mov_b32 v252, v128\n"
"v_mfma_f32_16x16x4_4b_bf16 v[164:179], v[228:229], a[244:245], 0 cbsz:2 abid:0\n"
"v_exp_f32 v127, v127\n"
"v_exp_f32 v112, v112\n"
"v_cvt_pk_bf16_f32 v111, v126, v127\n"
"v_exp_f32 v113, v113\n"
"v_mov_b32 v253, v129\n"
"v_mfma_f32_16x16x32_bf16 v[124:127], v[44:47], v[192:195], 0\n"
"v_exp_f32 v114, v114\n"
"v_mfma_f32_16x16x32_bf16 v[124:127], v[40:43], v[188:191], v[124:127]\n"
"v_exp_f32 v115, v115\n"
"v_smfmac_f32_16x16x64_bf16 v[164:167], v[220:223], v[48:55], v157\n"
"v_cvt_pk_bf16_f32 v112, v112, v113\n"
"v_smfmac_f32_16x16x64_bf16 v[164:167], v[224:227], v[52:59], v157\n"
"v_cvt_pk_bf16_f32 v113, v114, v115\n"
"v_smfmac_f32_16x16x64_bf16 v[168:171], v[224:227], v[64:71], v157\n"
"v_smfmac_f32_16x16x64_bf16 v[168:171], v[220:223], v[60:67], v157\n"
"v_exp_f32 v120, v120\n"
"v_smfmac_f32_16x16x64_bf16 v[172:175], v[220:223], v[72:79], v157\n"
"v_fmac_f32 v204, v209, v164\n"
"v_smfmac_f32_16x16x64_bf16 v[172:175], v[224:227], v[76:83], v157\n"
"v_fmac_f32 v204, v209, v165\n"
"v_smfmac_f32_16x16x64_bf16 v[176:179], v[224:227], v[88:95], v157\n"
"v_fmac_f32 v205, v209, v168\n"
"v_smfmac_f32_16x16x64_bf16 v[176:179], v[220:223], v[84:91], v157\n"
"v_fmac_f32 v205, v209, v169\n"
"v_smfmac_f32_16x16x64_bf16 v[252:255], a[248:251], v[96:103], v157\n"
"v_exp_f32 v121, v121\n"
"v_mfma_f32_16x16x4_4b_bf16 v[220:235], v[128:129], a[244:245], 0 cbsz:2 abid:1\n"
"s_branch .Lp2f%=\n"
".Lepf%=:\n"
"v_mfma_f32_16x16x32_bf16 v[164:167], a[80:83], a[160:163], 0\n"
".Lpad5_%=:\n"
"s_cbranch_vccnz .Lback0_%=\n"
"v_max3_f32 v159, v159, v134, v135\n"
"v_mfma_f32_16x16x32_bf16 v[164:167], a[84:87], a[164:167], v[164:167]\n"
"ds_bpermute_b32 v134, v160, v135 offset:0\n"
"v_mfma_f32_16x16x32_bf16 v[164:167], a[88:91], a[168:171], v[164:167]\n"
"v_exp_f32 v116, v116\n"
"v_mfma_f32_16x16x32_bf16 v[164:167], a[92:95], a[172:175], v[164:167]\n"
"buffer_store_dwordx2 v[206:207], v162, s[84:87], s65 offen offset:512 sc0 sc1\n"
"v_exp_f32 v117, v117\n"
"v_mfma_f32_16x16x32_bf16 v[168:171], a[92:95], a[188:191], 0\n"
"v_exp_f32 v115, v115\n"
"v_mfma_f32_16x16x32_bf16 v[168:171], a[88:91], a[184:187], v[168:171]\n"
"v_exp_f32 v118, v118\n"
"v_mfma_f32_16x16x32_bf16 v[168:171], a[84:87], a[180:183], v[168:171]\n"
"v_exp_f32 v119, v119\n"
"v_mfma_f32_16x16x32_bf16 v[168:171], a[80:83], a[176:179], v[168:171]\n"
"buffer_store_dwordx2 v[210:211], v162, s[84:87], s65 offen offset:768 sc0 sc1\n"
"v_exp_f32 v120, v120\n"
"v_mfma_f32_16x16x32_bf16 v[172:175], a[80:83], a[192:195], 0\n"
"v_exp_f32 v121, v121\n"
"v_mfma_f32_16x16x32_bf16 v[172:175], a[84:87], a[196:199], v[172:175]\n"
"v_exp_f32 v122, v122\n"
"v_mfma_f32_16x16x32_bf16 v[172:175], a[88:91], a[200:203], v[172:175]\n"
"v_exp_f32 v123, v123\n"
"v_mfma_f32_16x16x32_bf16 v[172:175], a[92:95], a[204:207], v[172:175]\n"
"v_exp_f32 v124, v124\n"
"v_cvt_pk_bf16_f32 v116, v116, v117\n"
"v_mfma_f32_16x16x4_4b_bf16 v[204:219], v[132:133], a[244:245], 0 cbsz:2 abid:3\n"
"v_exp_f32 v125, v125\n"
"v_exp_f32 v126, v126\n"
"v_exp_f32 v127, v127\n"
"v_cvt_pk_bf16_f32 v113, v114, v115\n"
"v_cvt_pk_bf16_f32 v117, v118, v119\n"
"v_mfma_f32_16x16x32_bf16 v[176:179], a[92:95], a[220:223], 0\n"
"v_cvt_pk_bf16_f32 v114, v120, v121\n"
"v_cvt_pk_bf16_f32 v115, v122, v123\n"
"v_mfma_f32_16x16x32_bf16 v[176:179], a[88:91], a[216:219], v[176:179]\n"
"v_cvt_pk_bf16_f32 v118, v124, v125\n"
"v_cvt_pk_bf16_f32 v119, v126, v127\n"
"v_mfma_f32_16x16x32_bf16 v[176:179], a[84:87], a[212:215], v[176:179]\n"
"v_exp_f32 v96, v96\n"
"v_mfma_f32_16x16x32_bf16 v[176:179], a[80:83], a[208:211], v[176:179]\n"
"v_exp_f32 v97, v97\n"
"v_mfma_f32_16x16x32_bf16 v[120:123], v[12:15], v[192:195], 0\n"
"v_exp_f32 v100, v100\n"
"v_mfma_f32_16x16x32_bf16 v[120:123], v[8:11], v[188:191], v[120:123]\n"
"v_exp_f32 v101, v101\n"
"v_mfma_f32_16x16x32_bf16 v[124:127], v[28:31], v[192:195], 0\n"
"v_exp_f32 v98, v98\n"
"v_mfma_f32_16x16x32_bf16 v[124:127], v[24:27], v[188:191], v[124:127]\n"
"v_exp_f32 v99, v99\n"
"v_mfma_f32_16x16x32_bf16 v[208:211], v[116:119], v[68:71], v[208:211]\n"
"v_exp_f32 v102, v102\n"
"v_mfma_f32_16x16x32_bf16 v[208:211], v[112:115], v[64:67], v[208:211]\n"
"v_exp_f32 v103, v103\n"
"v_mfma_f32_16x16x32_bf16 v[208:211], v[108:111], v[60:63], v[208:211]\n"
"v_exp_f32 v120, v120\n"
"v_mfma_f32_16x16x32_bf16 v[216:219], v[116:119], v[92:95], v[216:219]\n"
"v_exp_f32 v121, v121\n"
"v_mfma_f32_16x16x32_bf16 v[216:219], v[112:115], v[88:91], v[216:219]\n"
"v_exp_f32 v122, v122\n"
"v_mfma_f32_16x16x32_bf16 v[216:219], v[108:111], v[84:87], v[216:219]\n"
"v_exp_f32 v123, v123\n"
"v_smfmac_f32_16x16x64_bf16 v[252:255], a[248:251], v[112:119], v157\n"
"v_cvt_pk_bf16_f32 v96, v96, v97\n"
"v_mfma_f32_16x16x32_bf16 v[204:207], v[108:111], v[48:51], v[204:207]\n"
"v_exp_f32 v124, v124\n"
"v_mfma_f32_16x16x32_bf16 v[204:207], v[112:115], v[52:55], v[204:207]\n"
"v_exp_f32 v125, v125\n"
"v_mfma_f32_16x16x32_bf16 v[204:207], v[116:119], v[56:59], v[204:207]\n"
"v_exp_f32 v126, v126\n"
"v_mfma_f32_16x16x32_bf16 v[212:215], v[108:111], v[72:75], v[212:215]\n"
"v_exp_f32 v127, v127\n"
"v_mfma_f32_16x16x32_bf16 v[212:215], v[112:115], v[76:79], v[212:215]\n"
"v_cvt_pk_bf16_f32 v100, v100, v101\n"
"v_cvt_pk_bf16_f32 v97, v98, v99\n"
"v_mfma_f32_16x16x32_bf16 v[212:215], v[116:119], v[80:83], v[212:215]\n"
"v_cvt_pk_bf16_f32 v101, v102, v103\n"
"v_cvt_pk_bf16_f32 v98, v120, v121\n"
"v_mfma_f32_16x16x32_bf16 v[108:111], v[0:3], v[196:199], 0\n"
"v_cvt_pk_bf16_f32 v99, v122, v123\n"
"v_cvt_pk_bf16_f32 v102, v124, v125\n"
"v_mfma_f32_16x16x32_bf16 v[108:111], v[4:7], v[200:203], v[108:111]\n"
"v_cvt_pk_bf16_f32 v103, v126, v127\n"
"v_mov_b32 v252, v128\n"
"v_mfma_f32_16x16x32_bf16 v[120:123], v[44:47], v[192:195], 0\n"
"s_waitcnt lgkmcnt(0)\n"
"v_mov_b32 v253, v129\n"
"v_mul_f32_dpp v220, v134, v220 quad_perm:[0,0,0,0] row_mask:0xf bank_mask:0xf\n"
"v_mfma_f32_16x16x32_bf16 v[120:123], v[40:43], v[188:191], v[120:123]\n"
"v_mul_f32_dpp v221, v134, v221 quad_perm:[1,1,1,1] row_mask:0xf bank_mask:0xf\n"
"v_mul_f32_dpp v222, v134, v222 quad_perm:[2,2,2,2] row_mask:0xf bank_mask:0xf\n"
"v_mfma_f32_16x16x32_bf16 v[124:127], v[12:15], v[200:203], 0\n"
"v_mul_f32_dpp v223, v134, v223 quad_perm:[3,3,3,3] row_mask:0xf bank_mask:0xf\n"
"v_mul_f32_dpp v224, v134, v224 quad_perm:[0,0,0,0] row_mask:0xf bank_mask:0xf\n"
"v_mfma_f32_16x16x32_bf16 v[124:127], v[8:11], v[196:199], v[124:127]\n"
"v_mul_f32_dpp v225, v134, v225 quad_perm:[1,1,1,1] row_mask:0xf bank_mask:0xf\n"
"v_mul_f32_dpp v226, v134, v226 quad_perm:[2,2,2,2] row_mask:0xf bank_mask:0xf\n"
"v_mfma_f32_16x16x32_bf16 v[220:223], a[32:35], a[160:163], v[220:223]\n"
"v_mul_f32_dpp v227, v134, v227 quad_perm:[3,3,3,3] row_mask:0xf bank_mask:0xf\n"
"v_mul_f32_dpp v228, v134, v228 quad_perm:[0,0,0,0] row_mask:0xf bank_mask:0xf\n"
"v_mfma_f32_16x16x32_bf16 v[220:223], a[36:39], a[164:167], v[220:223]\n"
"v_mul_f32_dpp v229, v134, v229 quad_perm:[1,1,1,1] row_mask:0xf bank_mask:0xf\n"
"v_mul_f32_dpp v230, v134, v230 quad_perm:[2,2,2,2] row_mask:0xf bank_mask:0xf\n"
"v_mfma_f32_16x16x32_bf16 v[220:223], a[40:43], a[168:171], v[220:223]\n"
"v_mul_f32_dpp v231, v134, v231 quad_perm:[3,3,3,3] row_mask:0xf bank_mask:0xf\n"
"v_mul_f32_dpp v232, v134, v232 quad_perm:[0,0,0,0] row_mask:0xf bank_mask:0xf\n"
"v_mfma_f32_16x16x32_bf16 v[220:223], a[44:47], a[172:175], v[220:223]\n"
"v_mul_f32_dpp v233, v134, v233 quad_perm:[1,1,1,1] row_mask:0xf bank_mask:0xf\n"
"v_mul_f32_dpp v234, v134, v234 quad_perm:[2,2,2,2] row_mask:0xf bank_mask:0xf\n"
"v_mfma_f32_16x16x32_bf16 v[224:227], a[44:47], a[188:191], v[224:227]\n"
"v_mul_f32_dpp v235, v134, v235 quad_perm:[3,3,3,3] row_mask:0xf bank_mask:0xf\n"
"v_add_f32 v134, v254, v255\n"
"v_mfma_f32_16x16x32_bf16 v[224:227], a[40:43], a[184:187], v[224:227]\n"
"v_rcp_f32 v135, v134\n"
"v_mfma_f32_16x16x32_bf16 v[224:227], a[36:39], a[180:183], v[224:227]\n"
"v_exp_f32 v104, v104\n"
"v_mfma_f32_16x16x32_bf16 v[224:227], a[32:35], a[176:179], v[224:227]\n"
"v_exp_f32 v105, v105\n"
"v_smfmac_f32_16x16x64_bf16 v[252:255], a[248:251], v[96:103], v157\n"
"v_exp_f32 v108, v108\n"
"v_mfma_f32_16x16x32_bf16 v[228:231], a[32:35], a[192:195], v[228:231]\n"
"v_exp_f32 v109, v109\n"
"v_mfma_f32_16x16x32_bf16 v[228:231], a[36:39], a[196:199], v[228:231]\n"
"v_exp_f32 v106, v106\n"
"v_mfma_f32_16x16x32_bf16 v[228:231], a[40:43], a[200:203], v[228:231]\n"
"v_exp_f32 v107, v107\n"
"v_mfma_f32_16x16x32_bf16 v[228:231], a[44:47], a[204:207], v[228:231]\n"
"v_exp_f32 v110, v110\n"
"v_mfma_f32_16x16x32_bf16 v[232:235], a[44:47], a[220:223], v[232:235]\n"
"v_exp_f32 v111, v111\n"
"v_mfma_f32_16x16x32_bf16 v[232:235], a[40:43], a[216:219], v[232:235]\n"
"v_max3_f32 v159, v159, v134, v135\n"
"v_mfma_f32_16x16x32_bf16 v[232:235], a[36:39], a[212:215], v[232:235]\n"
"ds_bpermute_b32 v134, v160, v135 offset:128\n"
"v_mfma_f32_16x16x32_bf16 v[232:235], a[32:35], a[208:211], v[232:235]\n"
"v_exp_f32 v120, v120\n"
"v_mfma_f32_16x16x32_bf16 v[112:115], v[16:19], v[196:199], 0\n"
"v_exp_f32 v121, v121\n"
"v_mfma_f32_16x16x32_bf16 v[112:115], v[20:23], v[200:203], v[112:115]\n"
"v_exp_f32 v122, v122\n"
"v_mfma_f32_16x16x32_bf16 v[116:119], v[32:35], v[196:199], 0\n"
"v_exp_f32 v123, v123\n"
"v_mfma_f32_16x16x32_bf16 v[116:119], v[36:39], v[200:203], v[116:119]\n"
"v_exp_f32 v124, v124\n"
"v_exp_f32 v125, v125\n"
"v_exp_f32 v126, v126\n"
"v_exp_f32 v127, v127\n"
"v_cvt_pk_bf16_f32 v104, v104, v105\n"
"v_cvt_pk_bf16_f32 v108, v108, v109\n"
"v_cvt_pk_bf16_f32 v105, v106, v107\n"
"v_cvt_pk_bf16_f32 v109, v110, v111\n"
"v_cvt_pk_bf16_f32 v106, v120, v121\n"
"v_cvt_pk_bf16_f32 v107, v122, v123\n"
"v_mfma_f32_16x16x32_bf16 v[120:123], v[28:31], v[200:203], 0\n"
"v_mov_b32 v254, v128\n"
"v_mov_b32 v255, v129\n"
"v_mfma_f32_16x16x32_bf16 v[120:123], v[24:27], v[196:199], v[120:123]\n"
"v_cvt_pk_bf16_f32 v110, v124, v125\n"
"v_cvt_pk_bf16_f32 v111, v126, v127\n"
"v_mfma_f32_16x16x32_bf16 v[124:127], v[44:47], v[200:203], 0\n"
"v_cvt_pk_bf16_f32 v220, v220, v224\n"
"v_cvt_pk_bf16_f32 v222, v222, v226\n"
"v_mfma_f32_16x16x32_bf16 v[124:127], v[40:43], v[196:199], v[124:127]\n"
"v_cvt_pk_bf16_f32 v224, v221, v225\n"
"v_cvt_pk_bf16_f32 v226, v223, v227\n"
"v_smfmac_f32_16x16x64_bf16 v[252:255], a[252:255], v[104:111], v157\n"
"v_cvt_pk_bf16_f32 v221, v228, v232\n"
"v_cvt_pk_bf16_f32 v225, v229, v233\n"
"v_cvt_pk_bf16_f32 v223, v230, v234\n"
"v_cvt_pk_bf16_f32 v227, v231, v235\n"
"v_pk_max_i16 v220, v220, 0\n"
"v_pk_max_i16 v221, v221, 0\n"
"v_pk_max_i16 v224, v224, 0\n"
"v_pk_max_i16 v225, v225, 0\n"
"v_pk_max_i16 v222, v222, 0\n"
"v_pk_max_i16 v223, v223, 0\n"
"v_add_f32 v130, v252, v253\n"
"v_rcp_f32 v131, v130\n"
"v_pk_max_i16 v226, v226, 0\n"
"v_pk_max_i16 v227, v227, 0\n"
"v_max3_f32 v159, v159, v130, v131\n"
"s_waitcnt lgkmcnt(0)\n"
"v_mul_f32_dpp v204, v134, v204 quad_perm:[0,0,0,0] row_mask:0xf bank_mask:0xf\n"
"v_mul_f32_dpp v205, v134, v205 quad_perm:[1,1,1,1] row_mask:0xf bank_mask:0xf\n"
"v_mul_f32_dpp v206, v134, v206 quad_perm:[2,2,2,2] row_mask:0xf bank_mask:0xf\n"
"v_mul_f32_dpp v207, v134, v207 quad_perm:[3,3,3,3] row_mask:0xf bank_mask:0xf\n"
"v_mul_f32_dpp v208, v134, v208 quad_perm:[0,0,0,0] row_mask:0xf bank_mask:0xf\n"
"v_mul_f32_dpp v209, v134, v209 quad_perm:[1,1,1,1] row_mask:0xf bank_mask:0xf\n"
"v_mul_f32_dpp v210, v134, v210 quad_perm:[2,2,2,2] row_mask:0xf bank_mask:0xf\n"
"v_mfma_f32_16x16x32_bf16 v[204:207], a[48:51], a[160:163], v[204:207]\n"
"ds_bpermute_b32 v130, v160, v131 offset:0\n"
"v_mfma_f32_16x16x32_bf16 v[204:207], a[52:55], a[164:167], v[204:207]\n"
"v_mul_f32_dpp v211, v134, v211 quad_perm:[3,3,3,3] row_mask:0xf bank_mask:0xf\n"
"v_mul_f32_dpp v212, v134, v212 quad_perm:[0,0,0,0] row_mask:0xf bank_mask:0xf\n"
"v_mfma_f32_16x16x32_bf16 v[204:207], a[56:59], a[168:171], v[204:207]\n"
"v_mul_f32_dpp v213, v134, v213 quad_perm:[1,1,1,1] row_mask:0xf bank_mask:0xf\n"
"v_mul_f32_dpp v214, v134, v214 quad_perm:[2,2,2,2] row_mask:0xf bank_mask:0xf\n"
"v_mfma_f32_16x16x32_bf16 v[204:207], a[60:63], a[172:175], v[204:207]\n"
"buffer_store_dwordx2 v[220:221], v162, s[84:87], s66 offen offset:0 sc0 sc1\n"
"v_mul_f32_dpp v215, v134, v215 quad_perm:[3,3,3,3] row_mask:0xf bank_mask:0xf\n"
"v_mul_f32_dpp v216, v134, v216 quad_perm:[0,0,0,0] row_mask:0xf bank_mask:0xf\n"
"v_mfma_f32_16x16x32_bf16 v[208:211], a[60:63], a[188:191], v[208:211]\n"
"v_mul_f32_dpp v217, v134, v217 quad_perm:[1,1,1,1] row_mask:0xf bank_mask:0xf\n"
"v_mul_f32_dpp v218, v134, v218 quad_perm:[2,2,2,2] row_mask:0xf bank_mask:0xf\n"
"v_mfma_f32_16x16x32_bf16 v[208:211], a[56:59], a[184:187], v[208:211]\n"
"v_mul_f32_dpp v219, v134, v219 quad_perm:[3,3,3,3] row_mask:0xf bank_mask:0xf\n"
"v_mfma_f32_16x16x32_bf16 v[208:211], a[52:55], a[180:183], v[208:211]\n"
"v_exp_f32 v112, v112\n"
"v_mfma_f32_16x16x32_bf16 v[208:211], a[48:51], a[176:179], v[208:211]\n"
"buffer_store_dwordx2 v[224:225], v162, s[84:87], s66 offen offset:256 sc0 sc1\n"
"v_exp_f32 v113, v113\n"
"v_mfma_f32_16x16x32_bf16 v[212:215], a[48:51], a[192:195], v[212:215]\n"
"v_exp_f32 v116, v116\n"
"v_mfma_f32_16x16x32_bf16 v[212:215], a[52:55], a[196:199], v[212:215]\n"
"v_exp_f32 v117, v117\n"
"v_mfma_f32_16x16x32_bf16 v[212:215], a[56:59], a[200:203], v[212:215]\n"
"v_exp_f32 v114, v114\n"
"v_mfma_f32_16x16x32_bf16 v[212:215], a[60:63], a[204:207], v[212:215]\n"
"buffer_store_dwordx2 v[222:223], v162, s[84:87], s66 offen offset:512 sc0 sc1\n"
"v_exp_f32 v115, v115\n"
"v_mfma_f32_16x16x32_bf16 v[216:219], a[60:63], a[220:223], v[216:219]\n"
"v_exp_f32 v118, v118\n"
"v_mfma_f32_16x16x32_bf16 v[216:219], a[56:59], a[216:219], v[216:219]\n"
"v_exp_f32 v119, v119\n"
"v_mfma_f32_16x16x32_bf16 v[216:219], a[52:55], a[212:215], v[216:219]\n"
"v_exp_f32 v120, v120\n"
"v_mfma_f32_16x16x32_bf16 v[216:219], a[48:51], a[208:211], v[216:219]\n"
"buffer_store_dwordx2 v[226:227], v162, s[84:87], s66 offen offset:768 sc0 sc1\n"
"v_exp_f32 v121, v121\n"
"v_exp_f32 v122, v122\n"
"v_mfma_f32_16x16x4_4b_bf16 v[220:235], v[128:129], a[244:245], 0 cbsz:2 abid:1\n"
"v_exp_f32 v123, v123\n"
"v_exp_f32 v124, v124\n"
"v_exp_f32 v125, v125\n"
"v_exp_f32 v126, v126\n"
"v_exp_f32 v127, v127\n"
"v_cvt_pk_bf16_f32 v112, v112, v113\n"
"v_cvt_pk_bf16_f32 v116, v116, v117\n"
"v_cvt_pk_bf16_f32 v113, v114, v115\n"
"v_cvt_pk_bf16_f32 v117, v118, v119\n"
"v_mfma_f32_16x16x32_bf16 v[220:223], v[96:99], v[48:51], v[220:223]\n"
"v_cvt_pk_bf16_f32 v114, v120, v121\n"
"v_cvt_pk_bf16_f32 v115, v122, v123\n"
"v_mfma_f32_16x16x32_bf16 v[220:223], v[100:103], v[52:55], v[220:223]\n"
"v_cvt_pk_bf16_f32 v118, v124, v125\n"
"v_cvt_pk_bf16_f32 v119, v126, v127\n"
"v_mfma_f32_16x16x32_bf16 v[220:223], v[104:107], v[56:59], v[220:223]\n"
"v_cvt_pk_bf16_f32 v204, v204, v208\n"
"v_cvt_pk_bf16_f32 v206, v206, v210\n"
"v_mfma_f32_16x16x32_bf16 v[224:227], v[104:107], v[68:71], v[224:227]\n"
"v_cvt_pk_bf16_f32 v208, v205, v209\n"
"v_cvt_pk_bf16_f32 v210, v207, v211\n"
"v_mfma_f32_16x16x32_bf16 v[224:227], v[100:103], v[64:67], v[224:227]\n"
"v_cvt_pk_bf16_f32 v205, v212, v216\n"
"v_cvt_pk_bf16_f32 v209, v213, v217\n"
"v_mfma_f32_16x16x32_bf16 v[224:227], v[96:99], v[60:63], v[224:227]\n"
"s_waitcnt lgkmcnt(0)\n"
"v_mul_f32_dpp v220, v130, v220 quad_perm:[0,0,0,0] row_mask:0xf bank_mask:0xf\n"
"v_mul_f32_dpp v221, v130, v221 quad_perm:[1,1,1,1] row_mask:0xf bank_mask:0xf\n"
"v_mfma_f32_16x16x32_bf16 v[228:231], v[96:99], v[72:75], v[228:231]\n"
"v_mul_f32_dpp v222, v130, v222 quad_perm:[2,2,2,2] row_mask:0xf bank_mask:0xf\n"
"v_mul_f32_dpp v223, v130, v223 quad_perm:[3,3,3,3] row_mask:0xf bank_mask:0xf\n"
"v_mfma_f32_16x16x32_bf16 v[228:231], v[100:103], v[76:79], v[228:231]\n"
"v_cvt_pk_bf16_f32 v207, v214, v218\n"
"v_cvt_pk_bf16_f32 v211, v215, v219\n"
"v_mfma_f32_16x16x32_bf16 v[228:231], v[104:107], v[80:83], v[228:231]\n"
".Lpad6_%=:\n"
"s_cbranch_vccnz .Lback0_%=\n"
"v_mul_f32_dpp v224, v130, v224 quad_perm:[0,0,0,0] row_mask:0xf bank_mask:0xf\n"
"v_mul_f32_dpp v225, v130, v225 quad_perm:[1,1,1,1] row_mask:0xf bank_mask:0xf\n"
"v_mfma_f32_16x16x32_bf16 v[232:235], v[104:107], v[92:95], v[232:235]\n"
"v_mul_f32_dpp v226, v130, v226 quad_perm:[2,2,2,2] row_mask:0xf bank_mask:0xf\n"
"v_mul_f32_dpp v227, v130, v227 quad_perm:[3,3,3,3] row_mask:0xf bank_mask:0xf\n"
"v_mfma_f32_16x16x32_bf16 v[232:235], v[100:103], v[88:91], v[232:235]\n"
"v_pk_max_i16 v204, v204, 0\n"
"v_pk_max_i16 v205, v205, 0\n"
"v_mfma_f32_16x16x32_bf16 v[232:235], v[96:99], v[84:87], v[232:235]\n"
"buffer_store_dwordx2 v[204:205], v162, s[84:87], s67 offen offset:0 sc0 sc1\n"
"v_mul_f32_dpp v228, v130, v228 quad_perm:[0,0,0,0] row_mask:0xf bank_mask:0xf\n"
"v_mul_f32_dpp v229, v130, v229 quad_perm:[1,1,1,1] row_mask:0xf bank_mask:0xf\n"
"v_smfmac_f32_16x16x64_bf16 v[252:255], a[248:251], v[112:119], v157\n"
"v_mul_f32_dpp v230, v130, v230 quad_perm:[2,2,2,2] row_mask:0xf bank_mask:0xf\n"
"v_mfma_f32_16x16x32_bf16 v[220:223], a[64:67], a[160:163], v[220:223]\n"
"v_mul_f32_dpp v231, v130, v231 quad_perm:[3,3,3,3] row_mask:0xf bank_mask:0xf\n"
"v_pk_max_i16 v208, v208, 0\n"
"v_mfma_f32_16x16x32_bf16 v[220:223], a[68:71], a[164:167], v[220:223]\n"
"v_mul_f32_dpp v232, v130, v232 quad_perm:[0,0,0,0] row_mask:0xf bank_mask:0xf\n"
"v_mul_f32_dpp v233, v130, v233 quad_perm:[1,1,1,1] row_mask:0xf bank_mask:0xf\n"
"v_mfma_f32_16x16x32_bf16 v[220:223], a[72:75], a[168:171], v[220:223]\n"
"v_mul_f32_dpp v234, v130, v234 quad_perm:[2,2,2,2] row_mask:0xf bank_mask:0xf\n"
"v_mul_f32_dpp v235, v130, v235 quad_perm:[3,3,3,3] row_mask:0xf bank_mask:0xf\n"
"v_mfma_f32_16x16x32_bf16 v[220:223], a[76:79], a[172:175], v[220:223]\n"
"v_add_f32 v130, v254, v255\n"
"v_pk_max_i16 v209, v209, 0\n"
"v_mfma_f32_16x16x32_bf16 v[224:227], a[76:79], a[188:191], v[224:227]\n"
"v_rcp_f32 v131, v130\n"
"v_mfma_f32_16x16x32_bf16 v[224:227], a[72:75], a[184:187], v[224:227]\n"
"v_pk_max_i16 v206, v206, 0\n"
"v_pk_max_i16 v207, v207, 0\n"
"v_mfma_f32_16x16x32_bf16 v[224:227], a[68:71], a[180:183], v[224:227]\n"
"v_pk_max_i16 v210, v210, 0\n"
"v_pk_max_i16 v211, v211, 0\n"
"v_mfma_f32_16x16x32_bf16 v[224:227], a[64:67], a[176:179], v[224:227]\n"
"buffer_store_dwordx2 v[208:209], v162, s[84:87], s67 offen offset:256 sc0 sc1\n"
"v_max3_f32 v159, v159, v130, v131\n"
"buffer_store_dwordx2 v[206:207], v162, s[84:87], s67 offen offset:512 sc0 sc1\n"
"ds_bpermute_b32 v130, v160, v131 offset:128\n"
"v_mfma_f32_16x16x32_bf16 v[228:231], a[64:67], a[192:195], v[228:231]\n"
"v_mfma_f32_16x16x32_bf16 v[228:231], a[68:71], a[196:199], v[228:231]\n"
"v_cvt_pk_bf16_f32 v220, v220, v224\n"
"v_cvt_pk_bf16_f32 v222, v222, v226\n"
"v_mfma_f32_16x16x32_bf16 v[228:231], a[72:75], a[200:203], v[228:231]\n"
"v_cvt_pk_bf16_f32 v224, v221, v225\n"
"v_cvt_pk_bf16_f32 v226, v223, v227\n"
"v_mfma_f32_16x16x32_bf16 v[228:231], a[76:79], a[204:207], v[228:231]\n"
"buffer_store_dwordx2 v[210:211], v162, s[84:87], s67 offen offset:768 sc0 sc1\n"
"v_pk_max_i16 v220, v220, 0\n"
"v_pk_max_i16 v224, v224, 0\n"
"v_mfma_f32_16x16x32_bf16 v[232:235], a[76:79], a[220:223], v[232:235]\n"
"v_pk_max_i16 v222, v222, 0\n"
"v_pk_max_i16 v226, v226, 0\n"
"v_mfma_f32_16x16x32_bf16 v[232:235], a[72:75], a[216:219], v[232:235]\n"
"v_mfma_f32_16x16x32_bf16 v[232:235], a[68:71], a[212:215], v[232:235]\n"
"v_mfma_f32_16x16x32_bf16 v[232:235], a[64:67], a[208:211], v[232:235]\n"
"v_mfma_f32_16x16x4_4b_bf16 v[204:219], v[128:129], a[244:245], 0 cbsz:2 abid:3\n"
"s_nop 5\n"
"v_cvt_pk_bf16_f32 v221, v228, v232\n"
"v_cvt_pk_bf16_f32 v225, v229, v233\n"
"v_cvt_pk_bf16_f32 v223, v230, v234\n"
"v_cvt_pk_bf16_f32 v227, v231, v235\n"
"v_pk_max_i16 v221, v221, 0\n"
"v_pk_max_i16 v225, v225, 0\n"
"v_pk_max_i16 v223, v223, 0\n"
"v_mfma_f32_16x16x32_bf16 v[204:207], v[108:111], v[48:51], v[204:207]\n"
"v_pk_max_i16 v227, v227, 0\n"
"v_mfma_f32_16x16x32_bf16 v[204:207], v[112:115], v[52:55], v[204:207]\n"
"v_mfma_f32_16x16x32_bf16 v[204:207], v[116:119], v[56:59], v[204:207]\n"
"buffer_store_dwordx2 v[220:221], v162, s[84:87], s68 offen offset:0 sc0 sc1\n"
"v_mfma_f32_16x16x32_bf16 v[208:211], v[116:119], v[68:71], v[208:211]\n"
"v_mfma_f32_16x16x32_bf16 v[208:211], v[112:115], v[64:67], v[208:211]\n"
"v_mfma_f32_16x16x32_bf16 v[208:211], v[108:111], v[60:63], v[208:211]\n"
"s_waitcnt lgkmcnt(0)\n"
"buffer_store_dwordx2 v[224:225], v162, s[84:87], s68 offen offset:256 sc0 sc1\n"
"v_fmac_f32_dpp v164, v130, v204 quad_perm:[0,0,0,0] row_mask:0xf bank_mask:0xf\n"
"v_fmac_f32_dpp v166, v130, v206 quad_perm:[2,2,2,2] row_mask:0xf bank_mask:0xf\n"
"v_mfma_f32_16x16x32_bf16 v[212:215], v[108:111], v[72:75], v[212:215]\n"
"v_fmac_f32_dpp v165, v130, v205 quad_perm:[1,1,1,1] row_mask:0xf bank_mask:0xf\n"
"v_fmac_f32_dpp v167, v130, v207 quad_perm:[3,3,3,3] row_mask:0xf bank_mask:0xf\n"
"v_mfma_f32_16x16x32_bf16 v[212:215], v[112:115], v[76:79], v[212:215]\n"
"v_mfma_f32_16x16x32_bf16 v[212:215], v[116:119], v[80:83], v[212:215]\n"
"buffer_store_dwordx2 v[222:223], v162, s[84:87], s68 offen offset:512 sc0 sc1\n"
"v_fmac_f32_dpp v168, v130, v208 quad_perm:[0,0,0,0] row_mask:0xf bank_mask:0xf\n"
"v_fmac_f32_dpp v170, v130, v210 quad_perm:[2,2,2,2] row_mask:0xf bank_mask:0xf\n"
"v_mfma_f32_16x16x32_bf16 v[216:219], v[116:119], v[92:95], v[216:219]\n"
"v_fmac_f32_dpp v169, v130, v209 quad_perm:[1,1,1,1] row_mask:0xf bank_mask:0xf\n"
"v_fmac_f32_dpp v171, v130, v211 quad_perm:[3,3,3,3] row_mask:0xf bank_mask:0xf\n"
"v_mfma_f32_16x16x32_bf16 v[216:219], v[112:115], v[88:91], v[216:219]\n"
"v_cvt_pk_bf16_f32 v164, v164, v168\n"
"v_cvt_pk_bf16_f32 v166, v166, v170\n"
"v_mfma_f32_16x16x32_bf16 v[216:219], v[108:111], v[84:87], v[216:219]\n"
"buffer_store_dwordx2 v[226:227], v162, s[84:87], s68 offen offset:768 sc0 sc1\n"
"v_fmac_f32_dpp v172, v130, v212 quad_perm:[0,0,0,0] row_mask:0xf bank_mask:0xf\n"
"v_fmac_f32_dpp v173, v130, v213 quad_perm:[1,1,1,1] row_mask:0xf bank_mask:0xf\n"
"v_fmac_f32_dpp v174, v130, v214 quad_perm:[2,2,2,2] row_mask:0xf bank_mask:0xf\n"
"v_fmac_f32_dpp v175, v130, v215 quad_perm:[3,3,3,3] row_mask:0xf bank_mask:0xf\n"
"v_cvt_pk_bf16_f32 v168, v165, v169\n"
"v_cvt_pk_bf16_f32 v170, v167, v171\n"
"v_pk_max_i16 v164, v164, 0\n"
"v_pk_max_i16 v168, v168, 0\n"
"v_pk_max_i16 v166, v166, 0\n"
"v_pk_max_i16 v170, v170, 0\n"
"v_fmac_f32_dpp v176, v130, v216 quad_perm:[0,0,0,0] row_mask:0xf bank_mask:0xf\n"
"v_fmac_f32_dpp v177, v130, v217 quad_perm:[1,1,1,1] row_mask:0xf bank_mask:0xf\n"
"v_fmac_f32_dpp v178, v130, v218 quad_perm:[2,2,2,2] row_mask:0xf bank_mask:0xf\n"
"v_fmac_f32_dpp v179, v130, v219 quad_perm:[3,3,3,3] row_mask:0xf bank_mask:0xf\n"
"v_cvt_pk_bf16_f32 v165, v172, v176\n"
"v_cvt_pk_bf16_f32 v169, v173, v177\n"
"v_cvt_pk_bf16_f32 v167, v174, v178\n"
"v_cvt_pk_bf16_f32 v171, v175, v179\n"
"v_pk_max_i16 v165, v165, 0\n"
"v_pk_max_i16 v169, v169, 0\n"
"v_pk_max_i16 v167, v167, 0\n"
"v_pk_max_i16 v171, v171, 0\n"
"buffer_store_dwordx2 v[164:165], v162, s[84:87], s69 offen offset:0 sc0 sc1\n"
"buffer_store_dwordx2 v[168:169], v162, s[84:87], s69 offen offset:256 sc0 sc1\n"
"buffer_store_dwordx2 v[166:167], v162, s[84:87], s69 offen offset:512 sc0 sc1\n"
"buffer_store_dwordx2 v[170:171], v162, s[84:87], s69 offen offset:768 sc0 sc1\n"
"v_mov_b32 v0, 0x4e800000\n"
"v_cmp_nle_f32 vcc, v159, v0\n"
"s_nop 4\n"
"s_cmp_eq_u64 vcc, 0\n"
"s_cbranch_scc0 .Lhot%=\n"
"s_endpgm\n"
".Lcend%=:\n"
".Lhot%=:\n"
"s_waitcnt vmcnt(0) lgkmcnt(0)\n"
"s_sub_u32 s24, s8, 192\n"
"s_subb_u32 s25, s9, 0\n"
"s_and_b32 s25, s25, 0xffff\n"
"s_mov_b32 s26, 104857600\n"
"s_mov_b32 s27, 0x20000\n"
"s_sub_u32 s84, s18, 13107200\n"
"s_subb_u32 s85, s19, 0\n"
"s_and_b32 s85, s85, 0xffff\n"
"s_mov_b32 s86, 25472\n"
"s_mov_b32 s87, 0x20000\n"
"v_mov_b32 v0, 0x3f003f00\n"
"v_accvgpr_write_b32 a252, v0\n"
"v_accvgpr_write_b32 a253, v0\n"
"v_accvgpr_write_b32 a254, v0\n"
"v_accvgpr_write_b32 a255, v0\n"
"v_lshrrev_b32 v0, 8, v153\n"
"v_lshl_add_u32 v158, v0, 9, s29\n"
"v_and_b32 v0, 0x30, v153\n"
"v_add_u32 v158, v0, v158\n"
"s_add_u32 s89, s29, 4096\n"
"v_lshrrev_b32 v156, 2, v153\n"
"v_and_b32 v0, 12, v156\n"
"v_and_b32 v156, 0xf0, v156\n"
"v_lshl_add_u32 v156, v0, 20, v156\n"
"v_add_u32 v156, s89, v156\n"
"s_lshl_b32 s89, %[wave], 12\n"
"s_add_u32 s89, s89, 33152\n"
"v_lshrrev_b32 v163, 1, v153\n"
"v_add_u32 v163, s89, v163\n"
"s_nop 11\n"
"ds_read_b128 v[248:251], v153 offset:0\n"
"ds_read_b128 a[224:227], v153 offset:4096\n"
"ds_read_b128 v[236:239], v153 offset:8192\n"
"ds_read_b128 v[240:243], v153 offset:12288\n"
"ds_read_b128 v[244:247], v153 offset:1024\n"
"ds_read_b128 a[228:231], v158 offset:0\n"
"ds_read_b128 a[232:235], v158 offset:2048\n"
"ds_read_b64 a[244:245], v163 offset:0\n"
"ds_read_b32 a236, v156 offset:0\n"
"ds_read_b32 a237, v156 offset:8\n"
"ds_read_b32 a238, v156 offset:4\n"
"ds_read_b32 a239, v156 offset:12\n"
"ds_read_b32 a240, v156 offset:256\n"
"ds_read_b32 a241, v156 offset:264\n"
"ds_read_b32 a242, v156 offset:260\n"
"ds_read_b32 a243, v156 offset:268\n"
"s_add_u32 m0, s28, 0\n"
"v_add_u32 v156, 512, v156\n"
"buffer_load_dwordx4 v152, s[24:27], s40 offen offset:0 lds\n"
"buffer_load_dwordx4 v152, s[24:27], s41 offen offset:1088 lds\n"
"buffer_load_dwordx4 v152, s[24:27], s42 offen offset:2176 lds\n"
"buffer_load_dwordx4 v152, s[24:27], s43 offen offset:3264 lds\n"
"s_add_u32 m0, s28, 4352\n"
"s_nop 0\n"
"buffer_load_dwordx4 v152, s[24:27], s44 offen offset:0 lds\n"
"buffer_load_dwordx4 v152, s[24:27], s45 offen offset:1088 lds\n"
"s_waitcnt vmcnt(2)\n"
"ds_read_b128 a[0:3], v154 offset:0\n"
"ds_read_b128 a[4:7], v154 offset:64\n"
"buffer_load_dwordx4 v152, s[24:27], s46 offen offset:2176 lds\n"
"ds_read_b128 a[8:11], v154 offset:128\n"
"ds_read_b128 a[12:15], v154 offset:192\n"
"s_waitcnt lgkmcnt(3)\n"
"v_mfma_f32_16x16x32_bf16 v[0:3], a[96:99], a[0:3], 0\n"
"s_waitcnt lgkmcnt(2)\n"
"buffer_load_dwordx4 v152, s[24:27], s47 offen offset:3264 lds\n"
"s_add_u32 m0, s28, 8704\n"
"v_mfma_f32_16x16x32_bf16 v[0:3], a[100:103], a[4:7], v[0:3]\n"
"s_waitcnt lgkmcnt(1)\n"
"buffer_load_dwordx4 v152, s[24:27], s48 offen offset:0 lds\n"
"v_mfma_f32_16x16x32_bf16 v[0:3], a[104:107], a[8:11], v[0:3]\n"
"s_waitcnt lgkmcnt(0)\n"
"buffer_load_dwordx4 v152, s[24:27], s49 offen offset:1088 lds\n"
"v_mfma_f32_16x16x32_bf16 v[0:3], a[108:111], a[12:15], v[0:3]\n"
"buffer_load_dwordx4 v152, s[24:27], s50 offen offset:2176 lds\n"
"v_mfma_f32_16x16x32_bf16 v[4:7], a[112:115], a[0:3], 0\n"
"buffer_load_dwordx4 v152, s[24:27], s51 offen offset:3264 lds\n"
"v_mfma_f32_16x16x32_bf16 v[4:7], a[116:119], a[4:7], v[4:7]\n"
"v_mfma_f32_16x16x32_bf16 v[4:7], a[120:123], a[8:11], v[4:7]\n"
"v_cvt_pk_bf16_f32 v0, v0, v1\n"
"v_cvt_pk_bf16_f32 v1, v2, v3\n"
"v_mfma_f32_16x16x32_bf16 v[4:7], a[124:127], a[12:15], v[4:7]\n"
"v_mfma_f32_16x16x32_bf16 v[164:167], a[144:147], a[0:3], 0\n"
"v_mfma_f32_16x16x32_bf16 v[164:167], a[148:151], a[4:7], v[164:167]\n"
"s_add_u32 m0, s28, 0\n"
"v_mfma_f32_16x16x32_bf16 v[164:167], a[152:155], a[8:11], v[164:167]\n"
"buffer_load_dwordx4 v152, s[24:27], s52 offen offset:0 lds\n"
"v_cvt_pk_bf16_f32 v2, v4, v5\n"
"v_mfma_f32_16x16x32_bf16 v[164:167], a[156:159], a[12:15], v[164:167]\n"
"buffer_load_dwordx4 v152, s[24:27], s53 offen offset:1088 lds\n"
"v_cvt_pk_bf16_f32 v3, v6, v7\n"
"v_mfma_f32_16x16x32_bf16 v[4:7], a[128:131], a[0:3], 0\n"
"buffer_load_dwordx4 v152, s[24:27], s54 offen offset:2176 lds\n"
"v_mfma_f32_16x16x32_bf16 v[4:7], a[132:135], a[4:7], v[4:7]\n"
"buffer_load_dwordx4 v152, s[24:27], s55 offen offset:3264 lds\n"
"v_mfma_f32_16x16x32_bf16 v[4:7], a[136:139], a[8:11], v[4:7]\n"
"v_mfma_f32_16x16x32_bf16 v[4:7], a[140:143], a[12:15], v[4:7]\n"
"v_mfma_f32_16x16x32_bf16 v[180:183], v[248:251], a[0:3], 0\n"
"v_mfma_f32_16x16x32_bf16 v[180:183], a[224:227], a[4:7], v[180:183]\n"
"v_mfma_f32_16x16x32_bf16 v[180:183], v[236:239], a[8:11], v[180:183]\n"
"v_cvt_pk_bf16_f32 v4, v4, v5\n"
"v_cvt_pk_bf16_f32 v5, v6, v7\n"
"v_mfma_f32_16x16x32_bf16 v[180:183], v[240:243], a[12:15], v[180:183]\n"
"v_cvt_pk_bf16_f32 v6, v164, v165\n"
"v_cvt_pk_bf16_f32 v7, v166, v167\n"
"v_mfma_f32_16x16x32_bf16 v[184:187], v[244:247], a[0:3], 0\n"
"s_nop 4\n"
"v_cvt_pk_bf16_f32 v180, v180, v181\n"
"v_cvt_pk_bf16_f32 v181, v182, v183\n"
"s_waitcnt vmcnt(8)\n"
"ds_read_b128 a[16:19], v154 offset:4352\n"
"ds_read_b128 a[20:23], v154 offset:4416\n"
"ds_read_b128 a[24:27], v154 offset:4480\n"
"ds_read_b128 a[28:31], v154 offset:4544\n"
"s_waitcnt vmcnt(4)\n"
"ds_read_b128 a[32:35], v154 offset:8704\n"
"ds_read_b128 a[36:39], v154 offset:8768\n"
"ds_read_b128 a[40:43], v154 offset:8832\n"
"ds_read_b128 a[44:47], v154 offset:8896\n"
"s_waitcnt lgkmcnt(4)\n"
"v_mfma_f32_16x16x32_bf16 v[8:11], a[108:111], a[28:31], 0\n"
"v_mfma_f32_16x16x32_bf16 v[8:11], a[104:107], a[24:27], v[8:11]\n"
"s_waitcnt vmcnt(0)\n"
"ds_read_b128 a[48:51], v154 offset:0\n"
"ds_read_b128 a[52:55], v154 offset:64\n"
"v_mfma_f32_16x16x32_bf16 v[8:11], a[100:103], a[20:23], v[8:11]\n"
"ds_read_b128 a[56:59], v154 offset:128\n"
"ds_read_b128 a[60:63], v154 offset:192\n"
"v_mfma_f32_16x16x32_bf16 v[8:11], a[96:99], a[16:19], v[8:11]\n"
"s_waitcnt lgkmcnt(7)\n"
"v_mfma_f32_16x16x32_bf16 v[16:19], a[96:99], a[32:35], 0\n"
"s_waitcnt lgkmcnt(6)\n"
"v_mfma_f32_16x16x32_bf16 v[16:19], a[100:103], a[36:39], v[16:19]\n"
"s_waitcnt lgkmcnt(5)\n"
"v_mfma_f32_16x16x32_bf16 v[16:19], a[104:107], a[40:43], v[16:19]\n"
"s_waitcnt lgkmcnt(4)\n"
"v_cvt_pk_bf16_f32 v8, v8, v9\n"
"v_cvt_pk_bf16_f32 v9, v10, v11\n"
"v_mfma_f32_16x16x32_bf16 v[16:19], a[108:111], a[44:47], v[16:19]\n"
"v_mfma_f32_16x16x32_bf16 v[12:15], a[124:127], a[28:31], 0\n"
"s_add_u32 m0, s28, 4352\n"
"v_mfma_f32_16x16x32_bf16 v[12:15], a[120:123], a[24:27], v[12:15]\n"
"buffer_load_dwordx4 v152, s[24:27], s56 offen offset:0 lds\n"
"v_mfma_f32_16x16x32_bf16 v[12:15], a[116:119], a[20:23], v[12:15]\n"
"buffer_load_dwordx4 v152, s[24:27], s57 offen offset:1088 lds\n"
"v_cvt_pk_bf16_f32 v16, v16, v17\n"
"v_mfma_f32_16x16x32_bf16 v[12:15], a[112:115], a[16:19], v[12:15]\n"
"buffer_load_dwordx4 v152, s[24:27], s58 offen offset:2176 lds\n"
"v_cvt_pk_bf16_f32 v17, v18, v19\n"
"v_mfma_f32_16x16x32_bf16 v[20:23], a[112:115], a[32:35], 0\n"
"buffer_load_dwordx4 v152, s[24:27], s59 offen offset:3264 lds\n"
"s_add_u32 m0, s28, 8704\n"
"v_mfma_f32_16x16x32_bf16 v[20:23], a[116:119], a[36:39], v[20:23]\n"
"buffer_load_dwordx4 v152, s[24:27], s60 offen offset:0 lds\n"
"v_mfma_f32_16x16x32_bf16 v[20:23], a[120:123], a[40:43], v[20:23]\n"
"buffer_load_dwordx4 v152, s[24:27], s61 offen offset:1088 lds\n"
"v_cvt_pk_bf16_f32 v10, v12, v13\n"
"v_mfma_f32_16x16x32_bf16 v[20:23], a[124:127], a[44:47], v[20:23]\n"
"buffer_load_dwordx4 v152, s[24:27], s62 offen offset:2176 lds\n"
"v_cvt_pk_bf16_f32 v11, v14, v15\n"
"v_mfma_f32_16x16x32_bf16 v[12:15], a[140:143], a[28:31], 0\n"
"buffer_load_dwordx4 v152, s[24:27], s63 offen offset:3264 lds\n"
"v_mfma_f32_16x16x32_bf16 v[12:15], a[136:139], a[24:27], v[12:15]\n"
"v_mfma_f32_16x16x32_bf16 v[12:15], a[132:135], a[20:23], v[12:15]\n"
"v_cvt_pk_bf16_f32 v18, v20, v21\n"
"v_cvt_pk_bf16_f32 v19, v22, v23\n"
"v_mfma_f32_16x16x32_bf16 v[12:15], a[128:131], a[16:19], v[12:15]\n"
"s_add_u32 s24, s24, 13107200\n"
"s_addc_u32 s25, s25, 0\n"
"s_sub_u32 s26, s26, 13107200\n"
"s_cselect_b32 s26, 0, s26\n"
"v_mfma_f32_16x16x32_bf16 v[20:23], a[128:131], a[32:35], 0\n"
"v_mfma_f32_16x16x32_bf16 v[20:23], a[132:135], a[36:39], v[20:23]\n"
"s_add_u32 m0, s28, 0\n"
"v_cvt_pk_bf16_f32 v12, v12, v13\n"
"v_mfma_f32_16x16x32_bf16 v[20:23], a[136:139], a[40:43], v[20:23]\n"
"s_waitcnt lgkmcnt(0)\n"
"buffer_load_dwordx4 v152, s[24:27], s40 offen offset:0 lds\n"
"v_cvt_pk_bf16_f32 v13, v14, v15\n"
"v_mfma_f32_16x16x32_bf16 v[20:23], a[140:143], a[44:47], v[20:23]\n"
"buffer_load_dwordx4 v152, s[24:27], s41 offen offset:1088 lds\n"
"v_mfma_f32_16x16x32_bf16 v[168:171], a[156:159], a[28:31], 0\n"
"buffer_load_dwordx4 v152, s[24:27], s42 offen offset:2176 lds\n"
"v_mfma_f32_16x16x32_bf16 v[168:171], a[152:155], a[24:27], v[168:171]\n"
"buffer_load_dwordx4 v152, s[24:27], s43 offen offset:3264 lds\n"
"v_mfma_f32_16x16x32_bf16 v[168:171], a[148:151], a[20:23], v[168:171]\n"
"v_cvt_pk_bf16_f32 v20, v20, v21\n"
"v_cvt_pk_bf16_f32 v21, v22, v23\n"
"v_mfma_f32_16x16x32_bf16 v[168:171], a[144:147], a[16:19], v[168:171]\n"
"v_mfma_f32_16x16x32_bf16 v[172:175], a[144:147], a[32:35], 0\n"
"v_mfma_f32_16x16x32_bf16 v[172:175], a[148:151], a[36:39], v[172:175]\n"
"v_mfma_f32_16x16x32_bf16 v[172:175], a[152:155], a[40:43], v[172:175]\n"
"v_cvt_pk_bf16_f32 v14, v168, v169\n"
"v_cvt_pk_bf16_f32 v15, v170, v171\n"
"v_mfma_f32_16x16x32_bf16 v[172:175], a[156:159], a[44:47], v[172:175]\n"
"v_mfma_f32_16x16x32_bf16 v[24:27], a[96:99], a[48:51], 0\n"
"v_mfma_f32_16x16x32_bf16 v[24:27], a[100:103], a[52:55], v[24:27]\n"
"v_mfma_f32_16x16x32_bf16 v[24:27], a[104:107], a[56:59], v[24:27]\n"
"v_cvt_pk_bf16_f32 v22, v172, v173\n"
"v_cvt_pk_bf16_f32 v23, v174, v175\n"
"v_mfma_f32_16x16x32_bf16 v[24:27], a[108:111], a[60:63], v[24:27]\n"
"v_mfma_f32_16x16x32_bf16 v[176:179], a[112:115], a[48:51], 0\n"
"v_mfma_f32_16x16x32_bf16 v[176:179], a[116:119], a[52:55], v[176:179]\n"
"v_mfma_f32_16x16x32_bf16 v[176:179], a[120:123], a[56:59], v[176:179]\n"
"v_cvt_pk_bf16_f32 v24, v24, v25\n"
"v_cvt_pk_bf16_f32 v25, v26, v27\n"
"v_mfma_f32_16x16x32_bf16 v[176:179], a[124:127], a[60:63], v[176:179]\n"
"v_mfma_f32_16x16x32_bf16 v[188:191], v[240:243], a[28:31], 0\n"
"v_mfma_f32_16x16x32_bf16 v[188:191], v[236:239], a[24:27], v[188:191]\n"
"v_mfma_f32_16x16x32_bf16 v[188:191], a[224:227], a[20:23], v[188:191]\n"
"v_cvt_pk_bf16_f32 v26, v176, v177\n"
"v_cvt_pk_bf16_f32 v27, v178, v179\n"
"v_mfma_f32_16x16x32_bf16 v[188:191], v[248:251], a[16:19], v[188:191]\n"
"v_mfma_f32_16x16x32_bf16 v[196:199], v[248:251], a[32:35], 0\n"
"ds_read_b128 v[248:251], v153 offset:5120\n"
"v_mfma_f32_16x16x32_bf16 v[196:199], a[224:227], a[36:39], v[196:199]\n"
"ds_read_b128 a[224:227], v153 offset:9216\n"
"v_mfma_f32_16x16x32_bf16 v[196:199], v[236:239], a[40:43], v[196:199]\n"
"ds_read_b128 v[236:239], v153 offset:13312\n"
"v_cvt_pk_bf16_f32 v188, v188, v189\n"
"v_mfma_f32_16x16x32_bf16 v[196:199], v[240:243], a[44:47], v[196:199]\n"
"ds_read_b128 v[240:243], v153 offset:2048\n"
"v_cvt_pk_bf16_f32 v189, v190, v191\n"
"v_mfma_f32_16x16x32_bf16 v[28:31], a[128:131], a[48:51], 0\n"
"v_mfma_f32_16x16x32_bf16 v[28:31], a[132:135], a[52:55], v[28:31]\n"
"v_mfma_f32_16x16x32_bf16 v[28:31], a[136:139], a[56:59], v[28:31]\n"
"v_cvt_pk_bf16_f32 v196, v196, v197\n"
"v_cvt_pk_bf16_f32 v197, v198, v199\n"
"v_mfma_f32_16x16x32_bf16 v[28:31], a[140:143], a[60:63], v[28:31]\n"
"v_mfma_f32_16x16x32_bf16 v[200:203], v[244:247], a[32:35], 0\n"
"v_mfma_f32_16x16x32_bf16 v[164:167], a[144:147], a[48:51], 0\n"
"v_mfma_f32_16x16x32_bf16 v[164:167], a[148:151], a[52:55], v[164:167]\n"
"v_cvt_pk_bf16_f32 v28, v28, v29\n"
"v_cvt_pk_bf16_f32 v29, v30, v31\n"
"v_mfma_f32_16x16x32_bf16 v[164:167], a[152:155], a[56:59], v[164:167]\n"
"v_mfma_f32_16x16x32_bf16 v[164:167], a[156:159], a[60:63], v[164:167]\n"
"s_waitcnt lgkmcnt(3)\n"
"v_mfma_f32_16x16x32_bf16 v[184:187], v[248:251], a[4:7], v[184:187]\n"
"s_waitcnt lgkmcnt(2)\n"
"v_mfma_f32_16x16x32_bf16 v[184:187], a[224:227], a[8:11], v[184:187]\n"
"s_waitcnt lgkmcnt(1)\n"
"v_mfma_f32_16x16x32_bf16 v[184:187], v[236:239], a[12:15], v[184:187]\n"
"v_cvt_pk_bf16_f32 v30, v164, v165\n"
"v_cvt_pk_bf16_f32 v31, v166, v167\n"
"v_mfma_f32_16x16x32_bf16 v[192:195], v[236:239], a[28:31], 0\n"
"v_mfma_f32_16x16x32_bf16 v[192:195], a[224:227], a[24:27], v[192:195]\n"
"v_mfma_f32_16x16x32_bf16 v[192:195], v[248:251], a[20:23], v[192:195]\n"
"v_cvt_pk_bf16_f32 v182, v184, v185\n"
"v_cvt_pk_bf16_f32 v183, v186, v187\n"
"v_mfma_f32_16x16x32_bf16 v[192:195], v[244:247], a[16:19], v[192:195]\n"
"ds_read_b128 v[244:247], v153 offset:6144\n"
"v_mfma_f32_16x16x32_bf16 v[200:203], v[248:251], a[36:39], v[200:203]\n"
"ds_read_b128 v[248:251], v153 offset:10240\n"
"v_mfma_f32_16x16x32_bf16 v[200:203], a[224:227], a[40:43], v[200:203]\n"
"ds_read_b128 a[224:227], v153 offset:14336\n"
"v_mfma_f32_16x16x32_bf16 v[200:203], v[236:239], a[44:47], v[200:203]\n"
"s_waitcnt lgkmcnt(3)\n"
"ds_read_b128 v[236:239], v153 offset:3072\n"
"v_cvt_pk_bf16_f32 v190, v192, v193\n"
"v_mfma_f32_16x16x32_bf16 v[184:187], v[240:243], a[0:3], 0\n"
"v_cvt_pk_bf16_f32 v191, v194, v195\n"
"v_mfma_f32_16x16x32_bf16 v[120:123], a[228:231], v[180:183], 0\n"
"ds_read_b128 a[228:231], v158 offset:0\n"
"v_mfma_f32_16x16x32_bf16 v[96:99], v[0:3], v[180:183], 0\n"
"v_cvt_pk_bf16_f32 v198, v200, v201\n"
"v_cvt_pk_bf16_f32 v199, v202, v203\n"
"v_mfma_f32_16x16x32_bf16 v[200:203], v[240:243], a[32:35], 0\n"
"v_mfma_f32_16x16x32_bf16 v[104:107], v[16:19], v[180:183], 0\n"
"s_waitcnt lgkmcnt(4)\n"
"v_mfma_f32_16x16x32_bf16 v[184:187], v[244:247], a[4:7], v[184:187]\n"
"s_waitcnt lgkmcnt(3)\n"
"v_mfma_f32_16x16x32_bf16 v[184:187], v[248:251], a[8:11], v[184:187]\n"
"s_waitcnt lgkmcnt(2)\n"
"v_mfma_f32_16x16x32_bf16 v[184:187], a[224:227], a[12:15], v[184:187]\n"
"v_mfma_f32_16x16x32_bf16 v[192:195], a[224:227], a[28:31], 0\n"
"v_mfma_f32_16x16x32_bf16 v[192:195], v[248:251], a[24:27], v[192:195]\n"
"v_mfma_f32_16x16x32_bf16 v[192:195], v[244:247], a[20:23], v[192:195]\n"
"v_cvt_pk_bf16_f32 v184, v184, v185\n"
"v_cvt_pk_bf16_f32 v185, v186, v187\n"
"v_mfma_f32_16x16x32_bf16 v[192:195], v[240:243], a[16:19], v[192:195]\n"
"ds_read_b128 v[240:243], v153 offset:7168\n"
"v_mfma_f32_16x16x32_bf16 v[200:203], v[244:247], a[36:39], v[200:203]\n"
"ds_read_b128 v[244:247], v153 offset:11264\n"
"v_mfma_f32_16x16x32_bf16 v[200:203], v[248:251], a[40:43], v[200:203]\n"
"ds_read_b128 v[248:251], v153 offset:15360\n"
"v_mfma_f32_16x16x32_bf16 v[200:203], a[224:227], a[44:47], v[200:203]\n"
"s_waitcnt lgkmcnt(3)\n"
"ds_read_b128 a[224:227], v153 offset:18496\n"
"v_cvt_pk_bf16_f32 v192, v192, v193\n"
"v_mfma_f32_16x16x32_bf16 v[164:167], v[236:239], a[0:3], 0\n"
"v_cvt_pk_bf16_f32 v193, v194, v195\n"
"s_nop 2\n"
"v_cvt_pk_bf16_f32 v200, v200, v201\n"
"v_cvt_pk_bf16_f32 v201, v202, v203\n"
"s_waitcnt lgkmcnt(3)\n"
"v_mfma_f32_16x16x32_bf16 v[164:167], v[240:243], a[4:7], v[164:167]\n"
"s_waitcnt lgkmcnt(2)\n"
"v_mfma_f32_16x16x32_bf16 v[164:167], v[244:247], a[8:11], v[164:167]\n"
"s_waitcnt lgkmcnt(1)\n"
"v_mfma_f32_16x16x32_bf16 v[164:167], v[248:251], a[12:15], v[164:167]\n"
"s_nop 7\n"
"v_cvt_pk_bf16_f32 v186, v164, v165\n"
"v_cvt_pk_bf16_f32 v187, v166, v167\n"
"s_nop 1\n"
"v_mfma_f32_16x16x32_bf16 v[120:123], a[232:235], v[184:187], v[120:123]\n"
"ds_read_b128 a[232:235], v158 offset:2048\n"
"v_mfma_f32_16x16x32_bf16 v[96:99], v[4:7], v[184:187], v[96:99]\n"
"v_mfma_f32_16x16x32_bf16 v[100:103], v[12:15], v[184:187], 0\n"
"v_mfma_f32_16x16x32_bf16 v[100:103], v[8:11], v[180:183], v[100:103]\n"
"v_max3_f32 v157, v120, v121, v122\n"
"v_mfma_f32_16x16x32_bf16 v[104:107], v[20:23], v[184:187], v[104:107]\n"
"v_max3_f32 v159, v123, v96, v97\n"
"v_max3_f32 v157, v157, v98, v99\n"
"v_mfma_f32_16x16x32_bf16 v[108:111], v[28:31], v[184:187], 0\n"
"v_mfma_f32_16x16x32_bf16 v[108:111], v[24:27], v[180:183], v[108:111]\n"
"v_max3_f32 v159, v159, v100, v101\n"
"v_max3_f32 v157, v157, v102, v103\n"
"v_max3_f32 v159, v159, v104, v105\n"
"v_max3_f32 v157, v157, v106, v107\n"
"s_nop 3\n"
"v_max3_f32 v159, v159, v108, v109\n"
"s_waitcnt vmcnt(8)\n"
"ds_read_b128 a[64:67], v154 offset:4352\n"
"ds_read_b128 a[68:71], v154 offset:4416\n"
"ds_read_b128 a[72:75], v154 offset:4480\n"
"ds_read_b128 a[76:79], v154 offset:4544\n"
"s_waitcnt vmcnt(4)\n"
"ds_read_b128 a[80:83], v154 offset:8704\n"
"ds_read_b128 a[84:87], v154 offset:8768\n"
"ds_read_b128 a[88:91], v154 offset:8832\n"
"ds_read_b128 a[92:95], v154 offset:8896\n"
"s_waitcnt lgkmcnt(4)\n"
"v_mfma_f32_16x16x32_bf16 v[32:35], a[108:111], a[76:79], 0\n"
"v_mfma_f32_16x16x32_bf16 v[32:35], a[104:107], a[72:75], v[32:35]\n"
"v_mfma_f32_16x16x32_bf16 v[32:35], a[100:103], a[68:71], v[32:35]\n"
"v_mfma_f32_16x16x32_bf16 v[32:35], a[96:99], a[64:67], v[32:35]\n"
"v_mfma_f32_16x16x32_bf16 v[172:175], a[124:127], a[76:79], 0\n"
"v_mfma_f32_16x16x32_bf16 v[172:175], a[120:123], a[72:75], v[172:175]\n"
"v_mfma_f32_16x16x32_bf16 v[172:175], a[116:119], a[68:71], v[172:175]\n"
"v_cvt_pk_bf16_f32 v32, v32, v33\n"
"v_cvt_pk_bf16_f32 v33, v34, v35\n"
"v_mfma_f32_16x16x32_bf16 v[172:175], a[112:115], a[64:67], v[172:175]\n"
"v_mfma_f32_16x16x32_bf16 v[36:39], a[140:143], a[76:79], 0\n"
"s_add_u32 m0, s28, 4352\n"
"v_mfma_f32_16x16x32_bf16 v[36:39], a[136:139], a[72:75], v[36:39]\n"
"buffer_load_dwordx4 v152, s[24:27], s44 offen offset:0 lds\n"
"v_mfma_f32_16x16x32_bf16 v[36:39], a[132:135], a[68:71], v[36:39]\n"
"buffer_load_dwordx4 v152, s[24:27], s45 offen offset:1088 lds\n"
"v_cvt_pk_bf16_f32 v34, v172, v173\n"
"v_mfma_f32_16x16x32_bf16 v[36:39], a[128:131], a[64:67], v[36:39]\n"
"buffer_load_dwordx4 v152, s[24:27], s46 offen offset:2176 lds\n"
"v_cvt_pk_bf16_f32 v35, v174, v175\n"
"v_mfma_f32_16x16x32_bf16 v[168:171], a[156:159], a[76:79], 0\n"
"buffer_load_dwordx4 v152, s[24:27], s47 offen offset:3264 lds\n"
"v_mfma_f32_16x16x32_bf16 v[168:171], a[152:155], a[72:75], v[168:171]\n"
"v_mfma_f32_16x16x32_bf16 v[168:171], a[148:151], a[68:71], v[168:171]\n"
"v_cvt_pk_bf16_f32 v36, v36, v37\n"
"v_cvt_pk_bf16_f32 v37, v38, v39\n"
"v_mfma_f32_16x16x32_bf16 v[168:171], a[144:147], a[64:67], v[168:171]\n"
"v_mfma_f32_16x16x32_bf16 v[172:175], v[236:239], a[32:35], 0\n"
"v_mfma_f32_16x16x32_bf16 v[172:175], v[240:243], a[36:39], v[172:175]\n"
"v_mfma_f32_16x16x32_bf16 v[172:175], v[244:247], a[40:43], v[172:175]\n"
"v_cvt_pk_bf16_f32 v38, v168, v169\n"
"v_cvt_pk_bf16_f32 v39, v170, v171\n"
"v_mfma_f32_16x16x32_bf16 v[172:175], v[248:251], a[44:47], v[172:175]\n"
"v_mfma_f32_16x16x32_bf16 v[168:171], v[248:251], a[28:31], 0\n"
"ds_read_b128 v[248:251], v153 offset:28960\n"
"v_mfma_f32_16x16x32_bf16 v[168:171], v[244:247], a[24:27], v[168:171]\n"
"ds_read_b128 v[244:247], v153 offset:24768\n"
"v_mfma_f32_16x16x32_bf16 v[168:171], v[240:243], a[20:23], v[168:171]\n"
"ds_read_b128 v[240:243], v153 offset:20576\n"
"v_cvt_pk_bf16_f32 v202, v172, v173\n"
"v_mfma_f32_16x16x32_bf16 v[168:171], v[236:239], a[16:19], v[168:171]\n"
"s_waitcnt lgkmcnt(3)\n"
"ds_read_b128 v[236:239], v153 offset:16384\n"
"v_cvt_pk_bf16_f32 v203, v174, v175\n"
"v_mfma_f32_16x16x32_bf16 v[40:43], a[96:99], a[80:83], 0\n"
"v_mfma_f32_16x16x32_bf16 v[40:43], a[100:103], a[84:87], v[40:43]\n"
"v_mfma_f32_16x16x32_bf16 v[40:43], a[104:107], a[88:91], v[40:43]\n"
"v_cvt_pk_bf16_f32 v194, v168, v169\n"
"v_cvt_pk_bf16_f32 v195, v170, v171\n"
"v_mfma_f32_16x16x32_bf16 v[40:43], a[108:111], a[92:95], v[40:43]\n"
"s_waitcnt vmcnt(0) lgkmcnt(0)\n"
"s_mov_b32 s74, 7\n"
"s_nop 11\n"
".Lp2s%=:\n"
"v_mfma_f32_16x16x32_bf16 v[176:179], a[112:115], a[80:83], 0\n"
"s_add_u32 m0, s28, 8704\n"
"v_max3_f32 v157, v157, v110, v111\n"
"v_mfma_f32_16x16x32_bf16 v[176:179], a[116:119], a[84:87], v[176:179]\n"
"buffer_load_dwordx4 v152, s[24:27], s48 offen offset:0 lds\n"
"v_mfma_f32_16x16x32_bf16 v[176:179], a[120:123], a[88:91], v[176:179]\n"
"buffer_load_dwordx4 v152, s[24:27], s49 offen offset:1088 lds\n"
"v_cvt_pk_bf16_f32 v40, v40, v41\n"
"v_mfma_f32_16x16x32_bf16 v[176:179], a[124:127], a[92:95], v[176:179]\n"
"buffer_load_dwordx4 v152, s[24:27], s50 offen offset:2176 lds\n"
"v_cvt_pk_bf16_f32 v41, v42, v43\n"
"v_mfma_f32_16x16x32_bf16 v[44:47], a[128:131], a[80:83], 0\n"
"buffer_load_dwordx4 v152, s[24:27], s51 offen offset:3264 lds\n"
"v_mfma_f32_16x16x32_bf16 v[44:47], a[132:135], a[84:87], v[44:47]\n"
"v_mfma_f32_16x16x32_bf16 v[44:47], a[136:139], a[88:91], v[44:47]\n"
"v_cvt_pk_bf16_f32 v42, v176, v177\n"
"v_cvt_pk_bf16_f32 v43, v178, v179\n"
"v_mfma_f32_16x16x32_bf16 v[44:47], a[140:143], a[92:95], v[44:47]\n"
"v_mfma_f32_16x16x32_bf16 v[164:167], a[144:147], a[80:83], 0\n"
"v_mfma_f32_16x16x32_bf16 v[164:167], a[148:151], a[84:87], v[164:167]\n"
"v_mfma_f32_16x16x32_bf16 v[164:167], a[152:155], a[88:91], v[164:167]\n"
"v_cvt_pk_bf16_f32 v44, v44, v45\n"
"v_cvt_pk_bf16_f32 v45, v46, v47\n"
"v_mfma_f32_16x16x32_bf16 v[164:167], a[156:159], a[92:95], v[164:167]\n"
"v_mfma_f32_16x16x32_bf16 v[112:115], v[32:35], v[180:183], 0\n"
"v_mfma_f32_16x16x32_bf16 v[112:115], v[36:39], v[184:187], v[112:115]\n"
"v_mfma_f32_16x16x32_bf16 v[48:51], a[0:3], v[236:239], 0\n"
"v_cvt_pk_bf16_f32 v46, v164, v165\n"
"v_cvt_pk_bf16_f32 v47, v166, v167\n"
"v_mfma_f32_16x16x32_bf16 v[48:51], a[4:7], v[240:243], v[48:51]\n"
"v_mfma_f32_16x16x32_bf16 v[48:51], a[8:11], v[244:247], v[48:51]\n"
"v_max3_f32 v159, v159, v112, v113\n"
"v_max3_f32 v157, v157, v114, v115\n"
"v_mfma_f32_16x16x32_bf16 v[48:51], a[12:15], v[248:251], v[48:51]\n"
"v_mfma_f32_16x16x32_bf16 v[116:119], v[44:47], v[184:187], 0\n"
"v_mfma_f32_16x16x32_bf16 v[116:119], v[40:43], v[180:183], v[116:119]\n"
"v_mfma_f32_16x16x32_bf16 v[60:63], a[28:31], v[248:251], 0\n"
"v_cvt_pk_bf16_f32 v48, v48, v49\n"
"v_cvt_pk_bf16_f32 v49, v50, v51\n"
"v_mfma_f32_16x16x32_bf16 v[60:63], a[24:27], v[244:247], v[60:63]\n"
"v_mfma_f32_16x16x32_bf16 v[60:63], a[20:23], v[240:243], v[60:63]\n"
"v_max3_f32 v159, v159, v116, v117\n"
"v_max3_f32 v157, v157, v118, v119\n"
"v_mfma_f32_16x16x32_bf16 v[60:63], a[16:19], v[236:239], v[60:63]\n"
"v_max_f32 v157, v157, v159\n"
"v_mov_b32 v161, v157\n"
"v_mfma_f32_16x16x32_bf16 v[52:55], a[32:35], v[236:239], 0\n"
"v_mfma_f32_16x16x32_bf16 v[52:55], a[36:39], v[240:243], v[52:55]\n"
"v_permlane32_swap_b32 v157, v161\n"
"v_mfma_f32_16x16x32_bf16 v[52:55], a[40:43], v[244:247], v[52:55]\n"
"v_cvt_pk_bf16_f32 v50, v60, v61\n"
"v_cvt_pk_bf16_f32 v51, v62, v63\n"
"v_mfma_f32_16x16x32_bf16 v[52:55], a[44:47], v[248:251], v[52:55]\n"
"v_max_f32 v157, v157, v161\n"
"v_mov_b32 v161, v157\n"
"v_mfma_f32_16x16x32_bf16 v[64:67], a[60:63], v[248:251], 0\n"
"v_mfma_f32_16x16x32_bf16 v[64:67], a[56:59], v[244:247], v[64:67]\n"
"v_permlane16_swap_b32 v157, v161\n"
"v_mfma_f32_16x16x32_bf16 v[64:67], a[52:55], v[240:243], v[64:67]\n"
"v_cvt_pk_bf16_f32 v52, v52, v53\n"
"v_cvt_pk_bf16_f32 v53, v54, v55\n"
"v_mfma_f32_16x16x32_bf16 v[64:67], a[48:51], v[236:239], v[64:67]\n"
"v_max_f32 v157, v157, v161\n"
"v_sub_f32 v120, v120, v157\n"
"v_mfma_f32_16x16x32_bf16 v[56:59], a[64:67], v[236:239], 0\n"
"v_sub_f32 v121, v121, v157\n"
"v_sub_f32 v122, v122, v157\n"
"v_mfma_f32_16x16x32_bf16 v[56:59], a[68:71], v[240:243], v[56:59]\n"
"v_sub_f32 v123, v123, v157\n"
"v_sub_f32 v96, v96, v157\n"
"v_mfma_f32_16x16x32_bf16 v[56:59], a[72:75], v[244:247], v[56:59]\n"
"v_cvt_pk_bf16_f32 v54, v64, v65\n"
"v_cvt_pk_bf16_f32 v55, v66, v67\n"
"v_mfma_f32_16x16x32_bf16 v[56:59], a[76:79], v[248:251], v[56:59]\n"
"v_exp_f32 v161, v122\n"
"v_mfma_f32_16x16x32_bf16 v[68:71], a[92:95], v[248:251], 0\n"
"ds_read_b128 v[248:251], v153 offset:17440\n"
"v_sub_f32 v97, v97, v157\n"
"v_mfma_f32_16x16x32_bf16 v[68:71], a[88:91], v[244:247], v[68:71]\n"
"ds_read_b128 v[244:247], v153 offset:31072\n"
"v_sub_f32 v98, v98, v157\n"
"v_mfma_f32_16x16x32_bf16 v[68:71], a[84:87], v[240:243], v[68:71]\n"
"ds_read_b128 v[240:243], v153 offset:26880\n"
"v_cvt_pk_bf16_f32 v56, v56, v57\n"
"v_mfma_f32_16x16x32_bf16 v[68:71], a[80:83], v[236:239], v[68:71]\n"
"s_waitcnt lgkmcnt(3)\n"
"ds_read_b128 v[236:239], v153 offset:22688\n"
"v_cvt_pk_bf16_f32 v57, v58, v59\n"
"v_mfma_f32_16x16x32_bf16 v[148:151], a[228:231], v[188:191], 0\n"
"ds_read_b128 a[228:231], v158 offset:0\n"
"v_sub_f32 v99, v99, v157\n"
"v_mfma_f32_16x16x32_bf16 v[148:151], a[232:235], v[192:195], v[148:151]\n"
"ds_read_b128 a[232:235], v158 offset:2048\n"
"v_exp_f32 v120, v120\n"
"v_mfma_f32_16x16x32_bf16 v[124:127], v[0:3], v[188:191], 0\n"
"v_cvt_pk_bf16_f32 v58, v68, v69\n"
"v_cvt_pk_bf16_f32 v59, v70, v71\n"
"v_mfma_f32_16x16x32_bf16 v[124:127], v[4:7], v[192:195], v[124:127]\n"
"v_exp_f32 v121, v121\n"
"v_mfma_f32_16x16x32_bf16 v[128:131], v[12:15], v[192:195], 0\n"
"v_exp_f32 v123, v123\n"
"v_mfma_f32_16x16x32_bf16 v[128:131], v[8:11], v[188:191], v[128:131]\n"
"v_cvt_pk_bf16_f32 v122, v120, v121\n"
"v_add_f32 v120, v120, v121\n"
"v_mfma_f32_16x16x32_bf16 v[132:135], v[16:19], v[188:191], 0\n"
"v_add_f32 v121, v161, v123\n"
"v_cvt_pk_bf16_f32 v123, v161, v123\n"
"v_mfma_f32_16x16x32_bf16 v[132:135], v[20:23], v[192:195], v[132:135]\n"
"v_exp_f32 v96, v96\n"
"v_mfma_f32_16x16x32_bf16 v[136:139], v[28:31], v[192:195], 0\n"
"v_exp_f32 v97, v97\n"
"v_mfma_f32_16x16x32_bf16 v[136:139], v[24:27], v[188:191], v[136:139]\n"
"v_exp_f32 v98, v98\n"
"v_mfma_f32_16x16x32_bf16 v[140:143], v[32:35], v[188:191], 0\n"
"v_exp_f32 v99, v99\n"
"v_mfma_f32_16x16x32_bf16 v[140:143], v[36:39], v[192:195], v[140:143]\n"
"v_sub_f32 v100, v100, v157\n"
"v_sub_f32 v101, v101, v157\n"
"v_mfma_f32_16x16x32_bf16 v[144:147], v[44:47], v[192:195], 0\n"
"v_exp_f32 v100, v100\n"
"v_mfma_f32_16x16x32_bf16 v[144:147], v[40:43], v[188:191], v[144:147]\n"
"v_cvt_pk_bf16_f32 v96, v96, v97\n"
"v_cvt_pk_bf16_f32 v97, v98, v99\n"
"v_mfma_f32_16x16x32_bf16 v[72:75], a[0:3], a[224:227], 0\n"
"s_waitcnt lgkmcnt(0)\n"
"v_sub_f32 v102, v102, v157\n"
"v_sub_f32 v103, v103, v157\n"
"v_mfma_f32_16x16x32_bf16 v[72:75], a[4:7], v[236:239], v[72:75]\n"
"v_exp_f32 v101, v101\n"
"v_mfma_f32_16x16x32_bf16 v[72:75], a[8:11], v[240:243], v[72:75]\n"
"v_exp_f32 v102, v102\n"
"v_mfma_f32_16x16x32_bf16 v[72:75], a[12:15], v[244:247], v[72:75]\n"
"v_exp_f32 v103, v103\n"
"v_mfma_f32_16x16x32_bf16 v[84:87], a[28:31], v[244:247], 0\n"
"v_sub_f32 v104, v104, v157\n"
"v_sub_f32 v105, v105, v157\n"
"v_mfma_f32_16x16x32_bf16 v[84:87], a[24:27], v[240:243], v[84:87]\n"
"v_exp_f32 v104, v104\n"
"v_mfma_f32_16x16x32_bf16 v[84:87], a[20:23], v[236:239], v[84:87]\n"
"v_cvt_pk_bf16_f32 v72, v72, v73\n"
"v_cvt_pk_bf16_f32 v73, v74, v75\n"
"v_mfma_f32_16x16x32_bf16 v[84:87], a[16:19], a[224:227], v[84:87]\n"
"v_cvt_pk_bf16_f32 v98, v100, v101\n"
"v_cvt_pk_bf16_f32 v99, v102, v103\n"
"v_mfma_f32_16x16x32_bf16 v[76:79], a[32:35], a[224:227], 0\n"
"v_sub_f32 v106, v106, v157\n"
"v_sub_f32 v107, v107, v157\n"
"v_mfma_f32_16x16x32_bf16 v[76:79], a[36:39], v[236:239], v[76:79]\n"
"v_exp_f32 v105, v105\n"
"v_mfma_f32_16x16x32_bf16 v[76:79], a[40:43], v[240:243], v[76:79]\n"
"v_cvt_pk_bf16_f32 v74, v84, v85\n"
"v_cvt_pk_bf16_f32 v75, v86, v87\n"
"v_mfma_f32_16x16x32_bf16 v[76:79], a[44:47], v[244:247], v[76:79]\n"
"v_exp_f32 v106, v106\n"
"v_mfma_f32_16x16x32_bf16 v[88:91], a[60:63], v[244:247], 0\n"
"v_exp_f32 v107, v107\n"
"v_mfma_f32_16x16x32_bf16 v[88:91], a[56:59], v[240:243], v[88:91]\n"
"v_sub_f32 v108, v108, v157\n"
"v_sub_f32 v109, v109, v157\n"
"v_mfma_f32_16x16x32_bf16 v[88:91], a[52:55], v[236:239], v[88:91]\n"
"v_cvt_pk_bf16_f32 v76, v76, v77\n"
"v_cvt_pk_bf16_f32 v77, v78, v79\n"
"v_mfma_f32_16x16x32_bf16 v[88:91], a[48:51], a[224:227], v[88:91]\n"
"v_exp_f32 v108, v108\n"
"v_mfma_f32_16x16x32_bf16 v[80:83], a[64:67], a[224:227], 0\n"
"v_cvt_pk_bf16_f32 v104, v104, v105\n"
"v_cvt_pk_bf16_f32 v105, v106, v107\n"
"v_mfma_f32_16x16x32_bf16 v[80:83], a[68:71], v[236:239], v[80:83]\n"
"v_sub_f32 v110, v110, v157\n"
"v_sub_f32 v111, v111, v157\n"
"v_mfma_f32_16x16x32_bf16 v[80:83], a[72:75], v[240:243], v[80:83]\n"
"v_cvt_pk_bf16_f32 v78, v88, v89\n"
"v_cvt_pk_bf16_f32 v79, v90, v91\n"
"v_mfma_f32_16x16x32_bf16 v[80:83], a[76:79], v[244:247], v[80:83]\n"
"v_exp_f32 v109, v109\n"
"v_mfma_f32_16x16x32_bf16 v[92:95], a[92:95], v[244:247], 0\n"
"ds_read_b128 v[244:247], v153 offset:19552\n"
"v_sub_f32 v112, v112, v157\n"
"v_mfma_f32_16x16x32_bf16 v[92:95], a[88:91], v[240:243], v[92:95]\n"
"ds_read_b128 v[240:243], v153 offset:30016\n"
"v_sub_f32 v113, v113, v157\n"
"v_mfma_f32_16x16x32_bf16 v[92:95], a[84:87], v[236:239], v[92:95]\n"
"ds_read_b128 v[236:239], v153 offset:25824\n"
"v_cvt_pk_bf16_f32 v80, v80, v81\n"
"v_mfma_f32_16x16x32_bf16 v[92:95], a[80:83], a[224:227], v[92:95]\n"
"ds_read_b128 a[224:227], v153 offset:21632\n"
"v_cvt_pk_bf16_f32 v81, v82, v83\n"
"v_mfma_f32_16x16x32_bf16 v[60:63], a[0:3], v[248:251], 0\n"
"v_exp_f32 v110, v110\n"
"v_mfma_f32_16x16x32_bf16 v[64:67], a[32:35], v[248:251], 0\n"
"v_exp_f32 v111, v111\n"
"v_mfma_f32_16x16x32_bf16 v[68:71], a[64:67], v[248:251], 0\n"
"v_cvt_pk_bf16_f32 v82, v92, v93\n"
"v_cvt_pk_bf16_f32 v83, v94, v95\n"
"v_cvt_pk_bf16_f32 v106, v108, v109\n"
"v_mfma_f32_16x16x4_4b_bf16 v[204:219], v[122:123], a[244:245], 0 cbsz:2 abid:0\n"
"v_exp_f32 v112, v112\n"
"v_cvt_pk_bf16_f32 v107, v110, v111\n"
"v_sub_f32 v114, v114, v157\n"
"v_exp_f32 v113, v113\n"
"v_sub_f32 v115, v115, v157\n"
"v_sub_f32 v116, v116, v157\n"
"v_mfma_f32_16x16x32_bf16 v[252:255], a[252:255], v[96:99], v[120:123]\n"
"v_exp_f32 v114, v114\n"
"v_mfma_f32_16x16x32_bf16 v[252:255], a[252:255], v[104:107], v[252:255]\n"
"s_waitcnt lgkmcnt(3)\n"
"v_exp_f32 v115, v115\n"
"v_mfma_f32_16x16x32_bf16 v[84:87], a[0:3], v[244:247], 0\n"
"s_waitcnt lgkmcnt(2)\n"
"v_sub_f32 v117, v117, v157\n"
"v_cvt_pk_bf16_f32 v112, v112, v113\n"
"v_mfma_f32_16x16x32_bf16 v[164:167], a[28:31], v[240:243], 0\n"
"s_waitcnt lgkmcnt(1)\n"
"v_exp_f32 v116, v116\n"
"v_mfma_f32_16x16x32_bf16 v[164:167], a[24:27], v[236:239], v[164:167]\n"
"s_waitcnt lgkmcnt(0)\n"
"v_cvt_pk_bf16_f32 v113, v114, v115\n"
"v_sub_f32 v118, v118, v157\n"
"v_mfma_f32_16x16x32_bf16 v[164:167], a[20:23], a[224:227], v[164:167]\n"
"v_exp_f32 v117, v117\n"
"v_mfma_f32_16x16x32_bf16 v[164:167], a[16:19], v[248:251], v[164:167]\n"
"v_sub_f32 v119, v119, v157\n"
"v_cvt_pk_bf16_f32 v114, v116, v117\n"
"v_mfma_f32_16x16x32_bf16 v[60:63], a[4:7], a[224:227], v[60:63]\n"
"v_exp_f32 v118, v118\n"
"v_mfma_f32_16x16x32_bf16 v[60:63], a[8:11], v[236:239], v[60:63]\n"
"v_exp_f32 v119, v119\n"
"v_mfma_f32_16x16x32_bf16 v[60:63], a[12:15], v[240:243], v[60:63]\n"
"v_cvt_pk_bf16_f32 v115, v118, v119\n"
"v_max3_f32 v157, v148, v149, v150\n"
"v_mfma_f32_16x16x32_bf16 v[64:67], a[36:39], a[224:227], v[64:67]\n"
"s_add_u32 s84, s84, 13107200\n"
"s_addc_u32 s85, s85, 0\n"
"v_mfma_f32_16x16x32_bf16 v[64:67], a[40:43], v[236:239], v[64:67]\n"
"v_max3_f32 v159, v151, v124, v125\n"
"v_max3_f32 v157, v157, v126, v127\n"
"v_mfma_f32_16x16x32_bf16 v[64:67], a[44:47], v[240:243], v[64:67]\n"
"v_cvt_pk_bf16_f32 v60, v60, v61\n"
"v_cvt_pk_bf16_f32 v61, v62, v63\n"
"v_mfma_f32_16x16x32_bf16 v[168:171], a[60:63], v[240:243], 0\n"
"v_cvt_pk_bf16_f32 v62, v164, v165\n"
"v_cvt_pk_bf16_f32 v63, v166, v167\n"
"v_mfma_f32_16x16x32_bf16 v[168:171], a[56:59], v[236:239], v[168:171]\n"
"v_max3_f32 v159, v159, v128, v129\n"
"v_max3_f32 v157, v157, v130, v131\n"
"v_mfma_f32_16x16x32_bf16 v[168:171], a[52:55], a[224:227], v[168:171]\n"
"v_cvt_pk_bf16_f32 v64, v64, v65\n"
"v_cvt_pk_bf16_f32 v65, v66, v67\n"
"v_mfma_f32_16x16x32_bf16 v[168:171], a[48:51], v[248:251], v[168:171]\n"
"v_max3_f32 v159, v159, v132, v133\n"
"v_max3_f32 v157, v157, v134, v135\n"
"v_mfma_f32_16x16x32_bf16 v[68:71], a[68:71], a[224:227], v[68:71]\n"
"v_max3_f32 v159, v159, v136, v137\n"
"v_max3_f32 v157, v157, v138, v139\n"
"v_mfma_f32_16x16x32_bf16 v[68:71], a[72:75], v[236:239], v[68:71]\n"
"v_max3_f32 v159, v159, v140, v141\n"
"v_max3_f32 v157, v157, v142, v143\n"
"v_mfma_f32_16x16x32_bf16 v[68:71], a[76:79], v[240:243], v[68:71]\n"
"v_cvt_pk_bf16_f32 v66, v168, v169\n"
"v_cvt_pk_bf16_f32 v67, v170, v171\n"
"v_mfma_f32_16x16x32_bf16 v[172:175], a[92:95], v[240:243], 0\n"
"ds_read_b128 v[240:243], v153 offset:0\n"
"v_max3_f32 v159, v159, v144, v145\n"
"v_mfma_f32_16x16x32_bf16 v[172:175], a[88:91], v[236:239], v[172:175]\n"
"ds_read_b128 v[236:239], v153 offset:32128\n"
"v_max3_f32 v157, v157, v146, v147\n"
"v_mfma_f32_16x16x32_bf16 v[172:175], a[84:87], a[224:227], v[172:175]\n"
"ds_read_b128 a[224:227], v153 offset:27936\n"
"v_cvt_pk_bf16_f32 v68, v68, v69\n"
"v_mfma_f32_16x16x32_bf16 v[172:175], a[80:83], v[248:251], v[172:175]\n"
"ds_read_b128 v[248:251], v153 offset:23744\n"
"v_cvt_pk_bf16_f32 v69, v70, v71\n"
"v_mfma_f32_16x16x32_bf16 v[88:91], a[32:35], v[244:247], 0\n"
"v_max_f32 v157, v157, v159\n"
"v_mov_b32 v161, v157\n"
"v_mfma_f32_16x16x32_bf16 v[92:95], a[64:67], v[244:247], 0\n"
"s_nop 0\n"
"v_permlane32_swap_b32 v157, v161\n"
"v_mfma_f32_16x16x32_bf16 v[252:255], a[252:255], v[112:115], v[252:255]\n"
"v_cvt_pk_bf16_f32 v70, v172, v173\n"
"v_cvt_pk_bf16_f32 v71, v174, v175\n"
"v_mfma_f32_16x16x32_bf16 v[204:207], v[96:99], v[48:51], v[204:207]\n"
"v_max_f32 v157, v157, v161\n"
"v_mov_b32 v161, v157\n"
"v_mfma_f32_16x16x32_bf16 v[204:207], v[104:107], v[52:55], v[204:207]\n"
"v_mfma_f32_16x16x32_bf16 v[204:207], v[112:115], v[56:59], v[204:207]\n"
"v_permlane16_swap_b32 v157, v161\n"
"v_mfma_f32_16x16x32_bf16 v[208:211], v[112:115], v[68:71], v[208:211]\n"
"v_max_f32 v157, v157, v161\n"
"v_sub_f32 v148, v148, v157\n"
"v_mfma_f32_16x16x32_bf16 v[208:211], v[104:107], v[64:67], v[208:211]\n"
"v_sub_f32 v149, v149, v157\n"
"v_sub_f32 v150, v150, v157\n"
"v_mfma_f32_16x16x32_bf16 v[208:211], v[96:99], v[60:63], v[208:211]\n"
"s_waitcnt lgkmcnt(2)\n"
"v_sub_f32 v151, v151, v157\n"
"v_sub_f32 v124, v124, v157\n"
"v_mfma_f32_16x16x32_bf16 v[176:179], a[28:31], v[236:239], 0\n"
"s_waitcnt lgkmcnt(1)\n"
"v_exp_f32 v161, v150\n"
"v_mfma_f32_16x16x32_bf16 v[176:179], a[24:27], a[224:227], v[176:179]\n"
"s_waitcnt lgkmcnt(0)\n"
"v_exp_f32 v148, v148\n"
"v_mfma_f32_16x16x32_bf16 v[176:179], a[20:23], v[248:251], v[176:179]\n"
"v_exp_f32 v149, v149\n"
"v_mfma_f32_16x16x32_bf16 v[176:179], a[16:19], v[244:247], v[176:179]\n"
"v_exp_f32 v151, v151\n"
"v_mfma_f32_16x16x32_bf16 v[84:87], a[4:7], v[248:251], v[84:87]\n"
"v_cvt_pk_bf16_f32 v150, v148, v149\n"
"v_add_f32 v148, v148, v149\n"
"v_mfma_f32_16x16x32_bf16 v[84:87], a[8:11], a[224:227], v[84:87]\n"
"v_add_f32 v149, v161, v151\n"
"v_cvt_pk_bf16_f32 v151, v161, v151\n"
"v_mfma_f32_16x16x32_bf16 v[84:87], a[12:15], v[236:239], v[84:87]\n"
"v_sub_f32 v125, v125, v157\n"
"v_sub_f32 v126, v126, v157\n"
"v_mfma_f32_16x16x32_bf16 v[88:91], a[36:39], v[248:251], v[88:91]\n"
"v_exp_f32 v124, v124\n"
"v_mfma_f32_16x16x32_bf16 v[88:91], a[40:43], a[224:227], v[88:91]\n"
"v_exp_f32 v125, v125\n"
"v_mfma_f32_16x16x32_bf16 v[88:91], a[44:47], v[236:239], v[88:91]\n"
"v_cvt_pk_bf16_f32 v84, v84, v85\n"
"v_cvt_pk_bf16_f32 v85, v86, v87\n"
"v_mfma_f32_16x16x32_bf16 v[164:167], a[60:63], v[236:239], 0\n"
"v_cvt_pk_bf16_f32 v86, v176, v177\n"
"v_cvt_pk_bf16_f32 v87, v178, v179\n"
"v_mfma_f32_16x16x32_bf16 v[164:167], a[56:59], a[224:227], v[164:167]\n"
"v_sub_f32 v127, v127, v157\n"
"v_sub_f32 v128, v128, v157\n"
"v_mfma_f32_16x16x32_bf16 v[164:167], a[52:55], v[248:251], v[164:167]\n"
"v_cvt_pk_bf16_f32 v88, v88, v89\n"
"v_cvt_pk_bf16_f32 v89, v90, v91\n"
"v_mfma_f32_16x16x32_bf16 v[164:167], a[48:51], v[244:247], v[164:167]\n"
"v_exp_f32 v126, v126\n"
"v_mfma_f32_16x16x32_bf16 v[92:95], a[68:71], v[248:251], v[92:95]\n"
"v_exp_f32 v127, v127\n"
"v_mfma_f32_16x16x32_bf16 v[92:95], a[72:75], a[224:227], v[92:95]\n"
"v_sub_f32 v129, v129, v157\n"
"v_cvt_pk_bf16_f32 v124, v124, v125\n"
"v_mfma_f32_16x16x32_bf16 v[92:95], a[76:79], v[236:239], v[92:95]\n"
"v_cvt_pk_bf16_f32 v90, v164, v165\n"
"v_cvt_pk_bf16_f32 v91, v166, v167\n"
"v_mfma_f32_16x16x32_bf16 v[168:171], a[92:95], v[236:239], 0\n"
"ds_read_b128 v[236:239], v153 offset:1024\n"
"v_cvt_pk_bf16_f32 v125, v126, v127\n"
"v_mfma_f32_16x16x32_bf16 v[168:171], a[88:91], a[224:227], v[168:171]\n"
"ds_read_b128 a[224:227], v153 offset:12288\n"
"v_sub_f32 v130, v130, v157\n"
"v_mfma_f32_16x16x32_bf16 v[168:171], a[84:87], v[248:251], v[168:171]\n"
"ds_read_b128 v[248:251], v153 offset:8192\n"
"v_cvt_pk_bf16_f32 v92, v92, v93\n"
"v_mfma_f32_16x16x32_bf16 v[168:171], a[80:83], v[244:247], v[168:171]\n"
"ds_read_b128 v[244:247], v153 offset:4096\n"
"v_cvt_pk_bf16_f32 v93, v94, v95\n"
"v_mfma_f32_16x16x32_bf16 v[212:215], v[96:99], v[72:75], v[212:215]\n"
"v_exp_f32 v128, v128\n"
"v_mfma_f32_16x16x32_bf16 v[212:215], v[104:107], v[76:79], v[212:215]\n"
"v_exp_f32 v129, v129\n"
"v_mfma_f32_16x16x32_bf16 v[212:215], v[112:115], v[80:83], v[212:215]\n"
"v_cvt_pk_bf16_f32 v94, v168, v169\n"
"v_cvt_pk_bf16_f32 v95, v170, v171\n"
"v_mfma_f32_16x16x32_bf16 v[120:123], a[228:231], v[196:199], 0\n"
"ds_read_b128 a[228:231], v158 offset:0\n"
"v_sub_f32 v131, v131, v157\n"
"v_mfma_f32_16x16x32_bf16 v[120:123], a[232:235], v[200:203], v[120:123]\n"
"ds_read_b128 a[232:235], v158 offset:2048\n"
"v_sub_f32 v132, v132, v157\n"
"v_mfma_f32_16x16x32_bf16 v[216:219], v[112:115], v[92:95], v[216:219]\n"
"v_exp_f32 v130, v130\n"
"v_mfma_f32_16x16x32_bf16 v[216:219], v[104:107], v[88:91], v[216:219]\n"
"v_exp_f32 v131, v131\n"
"v_mfma_f32_16x16x32_bf16 v[216:219], v[96:99], v[84:87], v[216:219]\n"
"v_sub_f32 v133, v133, v157\n"
"v_cvt_pk_bf16_f32 v126, v128, v129\n"
"v_mfma_f32_16x16x32_bf16 v[96:99], v[0:3], v[196:199], 0\n"
"v_exp_f32 v132, v132\n"
"v_mfma_f32_16x16x32_bf16 v[96:99], v[4:7], v[200:203], v[96:99]\n"
"v_cvt_pk_bf16_f32 v127, v130, v131\n"
"v_sub_f32 v134, v134, v157\n"
"v_mfma_f32_16x16x32_bf16 v[100:103], v[12:15], v[200:203], 0\n"
"v_exp_f32 v133, v133\n"
"v_mfma_f32_16x16x32_bf16 v[100:103], v[8:11], v[196:199], v[100:103]\n"
"v_sub_f32 v135, v135, v157\n"
"v_sub_f32 v136, v136, v157\n"
"v_mfma_f32_16x16x32_bf16 v[104:107], v[16:19], v[196:199], 0\n"
"v_exp_f32 v134, v134\n"
"v_mfma_f32_16x16x32_bf16 v[104:107], v[20:23], v[200:203], v[104:107]\n"
"v_exp_f32 v135, v135\n"
"v_mfma_f32_16x16x32_bf16 v[108:111], v[28:31], v[200:203], 0\n"
"v_sub_f32 v137, v137, v157\n"
"v_cvt_pk_bf16_f32 v132, v132, v133\n"
"v_mfma_f32_16x16x32_bf16 v[108:111], v[24:27], v[196:199], v[108:111]\n"
"v_exp_f32 v136, v136\n"
"v_mfma_f32_16x16x32_bf16 v[112:115], v[32:35], v[196:199], 0\n"
"v_cvt_pk_bf16_f32 v133, v134, v135\n"
"v_sub_f32 v138, v138, v157\n"
"v_mfma_f32_16x16x32_bf16 v[112:115], v[36:39], v[200:203], v[112:115]\n"
"v_exp_f32 v137, v137\n"
"v_mfma_f32_16x16x32_bf16 v[116:119], v[44:47], v[200:203], 0\n"
"v_sub_f32 v139, v139, v157\n"
"v_sub_f32 v140, v140, v157\n"
"v_mfma_f32_16x16x32_bf16 v[116:119], v[40:43], v[196:199], v[116:119]\n"
"v_exp_f32 v138, v138\n"
"v_sub_f32 v141, v141, v157\n"
"v_mfma_f32_16x16x4_4b_bf16 v[220:235], v[150:151], a[244:245], 0 cbsz:2 abid:0\n"
"v_exp_f32 v139, v139\n"
"v_exp_f32 v140, v140\n"
"v_cvt_pk_bf16_f32 v134, v136, v137\n"
"v_cvt_pk_bf16_f32 v135, v138, v139\n"
"v_sub_f32 v142, v142, v157\n"
"v_sub_f32 v143, v143, v157\n"
"v_mfma_f32_16x16x32_bf16 v[128:131], v[12:15], a[240:243], 0\n"
"v_exp_f32 v141, v141\n"
"v_mfma_f32_16x16x32_bf16 v[128:131], v[8:11], a[236:239], v[128:131]\n"
"v_exp_f32 v142, v142\n"
"v_mfma_f32_16x16x32_bf16 v[220:223], v[124:127], v[48:51], v[220:223]\n"
"v_exp_f32 v143, v143\n"
"v_mfma_f32_16x16x32_bf16 v[220:223], v[132:135], v[52:55], v[220:223]\n"
"v_sub_f32 v144, v144, v157\n"
"v_sub_f32 v145, v145, v157\n"
"v_mfma_f32_16x16x32_bf16 v[228:231], v[124:127], v[72:75], v[228:231]\n"
"v_exp_f32 v144, v144\n"
"v_mfma_f32_16x16x32_bf16 v[228:231], v[132:135], v[76:79], v[228:231]\n"
"v_cvt_pk_bf16_f32 v140, v140, v141\n"
"v_cvt_pk_bf16_f32 v141, v142, v143\n"
"v_mfma_f32_16x16x32_bf16 v[136:139], v[28:31], a[240:243], 0\n"
"v_sub_f32 v146, v146, v157\n"
"v_sub_f32 v147, v147, v157\n"
"v_mfma_f32_16x16x32_bf16 v[136:139], v[24:27], a[236:239], v[136:139]\n"
"v_exp_f32 v145, v145\n"
"v_mfma_f32_16x16x32_bf16 v[180:183], v[240:243], a[48:51], 0\n"
"s_waitcnt lgkmcnt(0)\n"
"v_exp_f32 v146, v146\n"
"v_mfma_f32_16x16x32_bf16 v[180:183], v[244:247], a[52:55], v[180:183]\n"
"v_exp_f32 v147, v147\n"
"v_mfma_f32_16x16x32_bf16 v[180:183], v[248:251], a[56:59], v[180:183]\n"
"v_cvt_pk_bf16_f32 v142, v144, v145\n"
"v_cvt_pk_bf16_f32 v143, v146, v147\n"
"v_mfma_f32_16x16x32_bf16 v[180:183], a[224:227], a[60:63], v[180:183]\n"
"v_add_f32 v252, v252, v253\n"
"v_max3_f32 v157, v120, v121, v122\n"
"v_mfma_f32_16x16x32_bf16 v[220:223], v[140:143], v[56:59], v[220:223]\n"
"v_rcp_f32 v253, v252\n"
"v_mfma_f32_16x16x32_bf16 v[224:227], v[140:143], v[68:71], v[224:227]\n"
"ds_bpermute_b32 v254, v160, v253 offset:0\n"
"v_mfma_f32_16x16x32_bf16 v[224:227], v[132:135], v[64:67], v[224:227]\n"
"v_max3_f32 v159, v123, v96, v97\n"
"v_max3_f32 v157, v157, v98, v99\n"
"v_mfma_f32_16x16x32_bf16 v[224:227], v[124:127], v[60:63], v[224:227]\n"
"v_max3_f32 v159, v159, v100, v101\n"
"v_max3_f32 v157, v157, v102, v103\n"
"v_mfma_f32_16x16x32_bf16 v[228:231], v[140:143], v[80:83], v[228:231]\n"
"v_max3_f32 v159, v159, v104, v105\n"
"v_max3_f32 v157, v157, v106, v107\n"
"v_mfma_f32_16x16x32_bf16 v[232:235], v[140:143], v[92:95], v[232:235]\n"
"v_max3_f32 v159, v159, v108, v109\n"
"v_max3_f32 v157, v157, v110, v111\n"
"v_mfma_f32_16x16x32_bf16 v[232:235], v[132:135], v[88:91], v[232:235]\n"
"v_max3_f32 v159, v159, v112, v113\n"
"v_max3_f32 v157, v157, v114, v115\n"
"v_mfma_f32_16x16x32_bf16 v[232:235], v[124:127], v[84:87], v[232:235]\n"
"v_max3_f32 v159, v159, v116, v117\n"
"v_max3_f32 v157, v157, v118, v119\n"
"v_mfma_f32_16x16x32_bf16 v[144:147], v[44:47], a[240:243], 0\n"
"v_max_f32 v157, v157, v159\n"
"v_mov_b32 v161, v157\n"
"v_mfma_f32_16x16x32_bf16 v[144:147], v[40:43], a[236:239], v[144:147]\n"
"v_cvt_pk_bf16_f32 v180, v180, v181\n"
"v_cvt_pk_bf16_f32 v181, v182, v183\n"
"v_mfma_f32_16x16x32_bf16 v[188:191], a[224:227], a[76:79], 0\n"
"v_permlane32_swap_b32 v157, v161\n"
"v_mfma_f32_16x16x32_bf16 v[188:191], v[248:251], a[72:75], v[188:191]\n"
"v_max_f32 v157, v157, v161\n"
"v_mov_b32 v161, v157\n"
"v_mfma_f32_16x16x32_bf16 v[188:191], v[244:247], a[68:71], v[188:191]\n"
"s_waitcnt lgkmcnt(0)\n"
"v_mul_f32_dpp v204, v254, v204 quad_perm:[0,0,0,0] row_mask:0xf bank_mask:0xf\n"
"v_mul_f32_dpp v205, v254, v205 quad_perm:[1,1,1,1] row_mask:0xf bank_mask:0xf\n"
"v_mfma_f32_16x16x32_bf16 v[188:191], v[240:243], a[64:67], v[188:191]\n"
"v_mul_f32_dpp v206, v254, v206 quad_perm:[2,2,2,2] row_mask:0xf bank_mask:0xf\n"
"v_mul_f32_dpp v207, v254, v207 quad_perm:[3,3,3,3] row_mask:0xf bank_mask:0xf\n"
"v_mfma_f32_16x16x32_bf16 v[196:199], v[240:243], a[80:83], 0\n"
"ds_read_b128 v[240:243], v153 offset:5120\n"
"v_mul_f32_dpp v208, v254, v208 quad_perm:[0,0,0,0] row_mask:0xf bank_mask:0xf\n"
"v_mfma_f32_16x16x32_bf16 v[196:199], v[244:247], a[84:87], v[196:199]\n"
"ds_read_b128 v[244:247], v153 offset:9216\n"
"v_mul_f32_dpp v209, v254, v209 quad_perm:[1,1,1,1] row_mask:0xf bank_mask:0xf\n"
"v_mfma_f32_16x16x32_bf16 v[196:199], v[248:251], a[88:91], v[196:199]\n"
"ds_read_b128 v[248:251], v153 offset:13312\n"
"v_mul_f32_dpp v210, v254, v210 quad_perm:[2,2,2,2] row_mask:0xf bank_mask:0xf\n"
"v_mfma_f32_16x16x32_bf16 v[196:199], a[224:227], a[92:95], v[196:199]\n"
"ds_read_b128 a[224:227], v153 offset:3072\n"
"v_mul_f32_dpp v211, v254, v211 quad_perm:[3,3,3,3] row_mask:0xf bank_mask:0xf\n"
"v_mfma_f32_16x16x32_bf16 v[204:207], a[0:3], a[160:163], v[204:207]\n"
"v_mul_f32_dpp v212, v254, v212 quad_perm:[0,0,0,0] row_mask:0xf bank_mask:0xf\n"
"v_mul_f32_dpp v213, v254, v213 quad_perm:[1,1,1,1] row_mask:0xf bank_mask:0xf\n"
"v_mfma_f32_16x16x32_bf16 v[204:207], a[4:7], a[164:167], v[204:207]\n"
"v_mul_f32_dpp v214, v254, v214 quad_perm:[2,2,2,2] row_mask:0xf bank_mask:0xf\n"
"v_mul_f32_dpp v215, v254, v215 quad_perm:[3,3,3,3] row_mask:0xf bank_mask:0xf\n"
"v_mfma_f32_16x16x32_bf16 v[204:207], a[8:11], a[168:171], v[204:207]\n"
"v_mul_f32_dpp v216, v254, v216 quad_perm:[0,0,0,0] row_mask:0xf bank_mask:0xf\n"
"v_mul_f32_dpp v217, v254, v217 quad_perm:[1,1,1,1] row_mask:0xf bank_mask:0xf\n"
"v_mfma_f32_16x16x32_bf16 v[204:207], a[12:15], a[172:175], v[204:207]\n"
"v_mul_f32_dpp v218, v254, v218 quad_perm:[2,2,2,2] row_mask:0xf bank_mask:0xf\n"
"v_mul_f32_dpp v219, v254, v219 quad_perm:[3,3,3,3] row_mask:0xf bank_mask:0xf\n"
"v_mfma_f32_16x16x32_bf16 v[208:211], a[12:15], a[188:191], v[208:211]\n"
"v_permlane16_swap_b32 v157, v161\n"
"v_mfma_f32_16x16x32_bf16 v[208:211], a[8:11], a[184:187], v[208:211]\n"
"v_max_f32 v157, v157, v161\n"
"v_sub_f32 v120, v120, v157\n"
"v_mfma_f32_16x16x32_bf16 v[208:211], a[4:7], a[180:183], v[208:211]\n"
"v_sub_f32 v121, v121, v157\n"
"v_sub_f32 v122, v122, v157\n"
"v_mfma_f32_16x16x32_bf16 v[208:211], a[0:3], a[176:179], v[208:211]\n"
"v_sub_f32 v123, v123, v157\n"
"v_sub_f32 v96, v96, v157\n"
"v_mfma_f32_16x16x32_bf16 v[212:215], a[0:3], a[192:195], v[212:215]\n"
"v_exp_f32 v161, v122\n"
"v_mfma_f32_16x16x32_bf16 v[212:215], a[4:7], a[196:199], v[212:215]\n"
"v_exp_f32 v120, v120\n"
"v_mfma_f32_16x16x32_bf16 v[212:215], a[8:11], a[200:203], v[212:215]\n"
"v_cvt_pk_bf16_f32 v204, v204, v208\n"
"v_cvt_pk_bf16_f32 v208, v205, v209\n"
"v_mfma_f32_16x16x32_bf16 v[212:215], a[12:15], a[204:207], v[212:215]\n"
"v_pk_max_i16 v204, v204, 0\n"
"v_pk_max_i16 v208, v208, 0\n"
"v_mfma_f32_16x16x32_bf16 v[216:219], a[12:15], a[220:223], v[216:219]\n"
"s_waitcnt vmcnt(16)\n"
"ds_read_b128 a[12:15], v154 offset:192\n"
"v_cvt_pk_bf16_f32 v206, v206, v210\n"
"v_mfma_f32_16x16x32_bf16 v[216:219], a[8:11], a[216:219], v[216:219]\n"
"ds_read_b128 a[8:11], v154 offset:128\n"
"v_cvt_pk_bf16_f32 v210, v207, v211\n"
"v_mfma_f32_16x16x32_bf16 v[216:219], a[4:7], a[212:215], v[216:219]\n"
"ds_read_b128 a[4:7], v154 offset:64\n"
"v_pk_max_i16 v206, v206, 0\n"
"v_mfma_f32_16x16x32_bf16 v[216:219], a[0:3], a[208:211], v[216:219]\n"
"ds_read_b128 a[0:3], v154 offset:0\n"
"v_pk_max_i16 v210, v210, 0\n"
"v_mfma_f32_16x16x32_bf16 v[252:255], a[252:255], v[124:127], v[148:151]\n"
"v_exp_f32 v121, v121\n"
"v_mfma_f32_16x16x32_bf16 v[252:255], a[252:255], v[132:135], v[252:255]\n"
"v_exp_f32 v123, v123\n"
"v_mfma_f32_16x16x32_bf16 v[252:255], a[252:255], v[140:143], v[252:255]\n"
"v_cvt_pk_bf16_f32 v205, v212, v216\n"
"v_cvt_pk_bf16_f32 v209, v213, v217\n"
"v_mfma_f32_16x16x32_bf16 v[148:151], a[228:231], a[236:239], 0\n"
"ds_read_b128 a[228:231], v158 offset:0\n"
"v_pk_max_i16 v205, v205, 0\n"
"v_mfma_f32_16x16x32_bf16 v[148:151], a[232:235], a[240:243], v[148:151]\n"
"buffer_store_dwordx2 v[204:205], v162, s[84:87], 0 offen offset:0 sc0 sc1\n"
"v_mfma_f32_16x16x32_bf16 v[124:127], v[0:3], a[236:239], 0\n"
"ds_read_b128 a[232:235], v158 offset:2048\n"
"v_pk_max_i16 v209, v209, 0\n"
"v_mfma_f32_16x16x32_bf16 v[124:127], v[4:7], a[240:243], v[124:127]\n"
"buffer_store_dwordx2 v[208:209], v162, s[84:87], 0 offen offset:256 sc0 sc1\n"
"v_mfma_f32_16x16x32_bf16 v[132:135], v[16:19], a[236:239], 0\n"
"v_cvt_pk_bf16_f32 v207, v214, v218\n"
"v_cvt_pk_bf16_f32 v211, v215, v219\n"
"v_mfma_f32_16x16x32_bf16 v[132:135], v[20:23], a[240:243], v[132:135]\n"
"v_pk_max_i16 v207, v207, 0\n"
"v_pk_max_i16 v211, v211, 0\n"
"v_mfma_f32_16x16x32_bf16 v[140:143], v[32:35], a[236:239], 0\n"
"ds_read_b32 a236, v156 offset:0\n"
"ds_read_b32 a237, v156 offset:8\n"
"v_mfma_f32_16x16x32_bf16 v[140:143], v[36:39], a[240:243], v[140:143]\n"
"buffer_store_dwordx2 v[206:207], v162, s[84:87], 0 offen offset:512 sc0 sc1\n"
"v_mfma_f32_16x16x32_bf16 v[184:187], v[236:239], a[48:51], 0\n"
"s_waitcnt lgkmcnt(4)\n"
"ds_read_b32 a238, v156 offset:4\n"
"ds_read_b32 a239, v156 offset:12\n"
"v_mfma_f32_16x16x32_bf16 v[184:187], v[240:243], a[52:55], v[184:187]\n"
"ds_read_b32 a240, v156 offset:256\n"
"ds_read_b32 a241, v156 offset:264\n"
"v_mfma_f32_16x16x32_bf16 v[184:187], v[244:247], a[56:59], v[184:187]\n"
"ds_read_b32 a242, v156 offset:260\n"
"ds_read_b32 a243, v156 offset:268\n"
"v_mfma_f32_16x16x32_bf16 v[184:187], v[248:251], a[60:63], v[184:187]\n"
"buffer_store_dwordx2 v[210:211], v162, s[84:87], 0 offen offset:768 sc0 sc1\n"
"v_mfma_f32_16x16x32_bf16 v[192:195], v[248:251], a[76:79], 0\n"
"s_add_u32 m0, s28, 0\n"
"v_cvt_pk_bf16_f32 v122, v120, v121\n"
"v_mfma_f32_16x16x32_bf16 v[192:195], v[244:247], a[72:75], v[192:195]\n"
"buffer_load_dwordx4 v152, s[24:27], s52 offen offset:0 lds\n"
"v_add_f32 v120, v120, v121\n"
"v_mfma_f32_16x16x32_bf16 v[192:195], v[240:243], a[68:71], v[192:195]\n"
"buffer_load_dwordx4 v152, s[24:27], s53 offen offset:1088 lds\n"
"v_add_f32 v121, v161, v123\n"
"v_mfma_f32_16x16x32_bf16 v[192:195], v[236:239], a[64:67], v[192:195]\n"
"buffer_load_dwordx4 v152, s[24:27], s54 offen offset:2176 lds\n"
"v_cvt_pk_bf16_f32 v123, v161, v123\n"
"v_mfma_f32_16x16x32_bf16 v[200:203], v[236:239], a[80:83], 0\n"
"buffer_load_dwordx4 v152, s[24:27], s55 offen offset:3264 lds\n"
"ds_read_b128 v[236:239], v153 offset:2048\n"
"v_mfma_f32_16x16x32_bf16 v[200:203], v[240:243], a[84:87], v[200:203]\n"
"ds_read_b128 v[240:243], v153 offset:6144\n"
"v_sub_f32 v97, v97, v157\n"
"v_mfma_f32_16x16x32_bf16 v[200:203], v[244:247], a[88:91], v[200:203]\n"
"ds_read_b128 v[244:247], v153 offset:10240\n"
"v_add_f32 v252, v252, v253\n"
"v_mfma_f32_16x16x32_bf16 v[200:203], v[248:251], a[92:95], v[200:203]\n"
"ds_read_b128 v[248:251], v153 offset:14336\n"
"v_exp_f32 v96, v96\n"
"v_mfma_f32_16x16x4_4b_bf16 v[204:219], v[122:123], a[244:245], 0 cbsz:2 abid:0\n"
"v_sub_f32 v98, v98, v157\n"
"v_exp_f32 v97, v97\n"
"v_rcp_f32 v253, v252\n"
"v_sub_f32 v99, v99, v157\n"
"v_exp_f32 v98, v98\n"
"v_mfma_f32_16x16x32_bf16 v[164:167], a[224:227], a[48:51], 0\n"
"ds_bpermute_b32 v254, v160, v253 offset:0\n"
"v_mfma_f32_16x16x32_bf16 v[172:175], a[224:227], a[80:83], 0\n"
"v_exp_f32 v99, v99\n"
"v_sub_f32 v100, v100, v157\n"
"v_sub_f32 v101, v101, v157\n"
"v_exp_f32 v100, v100\n"
"v_cvt_pk_bf16_f32 v96, v96, v97\n"
"v_cvt_pk_bf16_f32 v97, v98, v99\n"
"v_sub_f32 v102, v102, v157\n"
"v_exp_f32 v101, v101\n"
"v_sub_f32 v103, v103, v157\n"
"v_exp_f32 v102, v102\n"
"v_exp_f32 v103, v103\n"
"v_sub_f32 v104, v104, v157\n"
"v_sub_f32 v105, v105, v157\n"
"v_exp_f32 v104, v104\n"
"v_cvt_pk_bf16_f32 v98, v100, v101\n"
"v_cvt_pk_bf16_f32 v99, v102, v103\n"
"v_sub_f32 v106, v106, v157\n"
"v_exp_f32 v105, v105\n"
"v_mfma_f32_16x16x32_bf16 v[204:207], v[96:99], v[48:51], v[204:207]\n"
"v_sub_f32 v107, v107, v157\n"
"v_sub_f32 v108, v108, v157\n"
"v_mfma_f32_16x16x32_bf16 v[212:215], v[96:99], v[72:75], v[212:215]\n"
"v_exp_f32 v106, v106\n"
"v_exp_f32 v107, v107\n"
"v_sub_f32 v109, v109, v157\n"
"v_exp_f32 v108, v108\n"
"v_cvt_pk_bf16_f32 v104, v104, v105\n"
"s_waitcnt lgkmcnt(0)\n"
"v_mul_f32_dpp v220, v254, v220 quad_perm:[0,0,0,0] row_mask:0xf bank_mask:0xf\n"
"v_mul_f32_dpp v221, v254, v221 quad_perm:[1,1,1,1] row_mask:0xf bank_mask:0xf\n"
"v_mul_f32_dpp v222, v254, v222 quad_perm:[2,2,2,2] row_mask:0xf bank_mask:0xf\n"
"v_mul_f32_dpp v223, v254, v223 quad_perm:[3,3,3,3] row_mask:0xf bank_mask:0xf\n"
"v_mul_f32_dpp v224, v254, v224 quad_perm:[0,0,0,0] row_mask:0xf bank_mask:0xf\n"
"v_mul_f32_dpp v225, v254, v225 quad_perm:[1,1,1,1] row_mask:0xf bank_mask:0xf\n"
"v_mul_f32_dpp v226, v254, v226 quad_perm:[2,2,2,2] row_mask:0xf bank_mask:0xf\n"
"v_mfma_f32_16x16x32_bf16 v[220:223], a[16:19], a[160:163], v[220:223]\n"
"v_mul_f32_dpp v227, v254, v227 quad_perm:[3,3,3,3] row_mask:0xf bank_mask:0xf\n"
"v_mul_f32_dpp v228, v254, v228 quad_perm:[0,0,0,0] row_mask:0xf bank_mask:0xf\n"
"v_mfma_f32_16x16x32_bf16 v[220:223], a[20:23], a[164:167], v[220:223]\n"
"v_mul_f32_dpp v229, v254, v229 quad_perm:[1,1,1,1] row_mask:0xf bank_mask:0xf\n"
"v_mul_f32_dpp v230, v254, v230 quad_perm:[2,2,2,2] row_mask:0xf bank_mask:0xf\n"
"v_mfma_f32_16x16x32_bf16 v[220:223], a[24:27], a[168:171], v[220:223]\n"
"v_mul_f32_dpp v231, v254, v231 quad_perm:[3,3,3,3] row_mask:0xf bank_mask:0xf\n"
"v_mul_f32_dpp v232, v254, v232 quad_perm:[0,0,0,0] row_mask:0xf bank_mask:0xf\n"
"v_mfma_f32_16x16x32_bf16 v[220:223], a[28:31], a[172:175], v[220:223]\n"
"v_mul_f32_dpp v233, v254, v233 quad_perm:[1,1,1,1] row_mask:0xf bank_mask:0xf\n"
"v_cvt_pk_bf16_f32 v105, v106, v107\n"
"v_mfma_f32_16x16x32_bf16 v[224:227], a[28:31], a[188:191], v[224:227]\n"
"v_mul_f32_dpp v234, v254, v234 quad_perm:[2,2,2,2] row_mask:0xf bank_mask:0xf\n"
"v_sub_f32 v110, v110, v157\n"
"v_mfma_f32_16x16x32_bf16 v[224:227], a[24:27], a[184:187], v[224:227]\n"
"v_exp_f32 v109, v109\n"
"v_mfma_f32_16x16x32_bf16 v[224:227], a[20:23], a[180:183], v[224:227]\n"
"v_mul_f32_dpp v235, v254, v235 quad_perm:[3,3,3,3] row_mask:0xf bank_mask:0xf\n"
"v_sub_f32 v111, v111, v157\n"
"v_mfma_f32_16x16x32_bf16 v[224:227], a[16:19], a[176:179], v[224:227]\n"
"v_exp_f32 v110, v110\n"
"v_mfma_f32_16x16x32_bf16 v[228:231], a[16:19], a[192:195], v[228:231]\n"
"v_exp_f32 v111, v111\n"
"v_mfma_f32_16x16x32_bf16 v[228:231], a[20:23], a[196:199], v[228:231]\n"
"v_sub_f32 v112, v112, v157\n"
"v_sub_f32 v113, v113, v157\n"
"v_mfma_f32_16x16x32_bf16 v[228:231], a[24:27], a[200:203], v[228:231]\n"
"v_exp_f32 v112, v112\n"
"v_mfma_f32_16x16x32_bf16 v[228:231], a[28:31], a[204:207], v[228:231]\n"
"v_cvt_pk_bf16_f32 v106, v108, v109\n"
"v_cvt_pk_bf16_f32 v107, v110, v111\n"
"v_mfma_f32_16x16x32_bf16 v[232:235], a[28:31], a[220:223], v[232:235]\n"
"s_waitcnt vmcnt(17)\n"
"ds_read_b128 a[28:31], v154 offset:4544\n"
"v_sub_f32 v114, v114, v157\n"
"v_mfma_f32_16x16x32_bf16 v[232:235], a[24:27], a[216:219], v[232:235]\n"
"ds_read_b128 a[24:27], v154 offset:4480\n"
"v_sub_f32 v115, v115, v157\n"
"v_mfma_f32_16x16x32_bf16 v[232:235], a[20:23], a[212:215], v[232:235]\n"
"ds_read_b128 a[20:23], v154 offset:4416\n"
"v_sub_f32 v116, v116, v157\n"
"v_mfma_f32_16x16x32_bf16 v[232:235], a[16:19], a[208:211], v[232:235]\n"
"ds_read_b128 a[16:19], v154 offset:4352\n"
"v_sub_f32 v117, v117, v157\n"
"v_mfma_f32_16x16x32_bf16 v[252:255], a[252:255], v[96:99], v[120:123]\n"
"v_exp_f32 v113, v113\n"
"v_mfma_f32_16x16x32_bf16 v[252:255], a[252:255], v[104:107], v[252:255]\n"
"v_exp_f32 v114, v114\n"
"v_mfma_f32_16x16x32_bf16 v[204:207], v[104:107], v[52:55], v[204:207]\n"
"v_exp_f32 v115, v115\n"
"v_mfma_f32_16x16x32_bf16 v[212:215], v[104:107], v[76:79], v[212:215]\n"
"v_exp_f32 v116, v116\n"
"v_cvt_pk_bf16_f32 v112, v112, v113\n"
"v_cvt_pk_bf16_f32 v113, v114, v115\n"
"v_sub_f32 v118, v118, v157\n"
"v_exp_f32 v117, v117\n"
"v_sub_f32 v119, v119, v157\n"
"v_exp_f32 v118, v118\n"
"v_exp_f32 v119, v119\n"
"v_cvt_pk_bf16_f32 v220, v220, v224\n"
"v_cvt_pk_bf16_f32 v224, v221, v225\n"
"v_cvt_pk_bf16_f32 v221, v228, v232\n"
"v_cvt_pk_bf16_f32 v225, v229, v233\n"
"v_pk_max_i16 v220, v220, 0\n"
"v_pk_max_i16 v221, v221, 0\n"
"v_cvt_pk_bf16_f32 v114, v116, v117\n"
"v_cvt_pk_bf16_f32 v115, v118, v119\n"
"v_pk_max_i16 v224, v224, 0\n"
"v_pk_max_i16 v225, v225, 0\n"
"v_cvt_pk_bf16_f32 v222, v222, v226\n"
"v_mfma_f32_16x16x32_bf16 v[252:255], a[252:255], v[112:115], v[252:255]\n"
"buffer_store_dwordx2 v[220:221], v162, s[84:87], s65 offen offset:0 sc0 sc1\n"
"v_mfma_f32_16x16x32_bf16 v[204:207], v[112:115], v[56:59], v[204:207]\n"
"buffer_store_dwordx2 v[224:225], v162, s[84:87], s65 offen offset:256 sc0 sc1\n"
"v_mfma_f32_16x16x32_bf16 v[208:211], v[112:115], v[68:71], v[208:211]\n"
"v_cvt_pk_bf16_f32 v226, v223, v227\n"
"v_cvt_pk_bf16_f32 v223, v230, v234\n"
"v_mfma_f32_16x16x32_bf16 v[208:211], v[104:107], v[64:67], v[208:211]\n"
"v_cvt_pk_bf16_f32 v227, v231, v235\n"
"v_pk_max_i16 v222, v222, 0\n"
"v_mfma_f32_16x16x32_bf16 v[208:211], v[96:99], v[60:63], v[208:211]\n"
"v_pk_max_i16 v223, v223, 0\n"
"v_pk_max_i16 v226, v226, 0\n"
"v_mfma_f32_16x16x32_bf16 v[212:215], v[112:115], v[80:83], v[212:215]\n"
"buffer_store_dwordx2 v[222:223], v162, s[84:87], s65 offen offset:512 sc0 sc1\n"
"v_mfma_f32_16x16x32_bf16 v[216:219], v[112:115], v[92:95], v[216:219]\n"
"s_nop 0\n"
"ds_read_b64 v[222:223], v163 offset:16384\n"
"s_add_u32 m0, s28, 4352\n"
"v_mfma_f32_16x16x32_bf16 v[216:219], v[104:107], v[88:91], v[216:219]\n"
"s_waitcnt lgkmcnt(1)\n"
"buffer_load_dwordx4 v152, s[24:27], s56 offen offset:0 lds\n"
"v_pk_max_i16 v227, v227, 0\n"
"v_mfma_f32_16x16x32_bf16 v[216:219], v[96:99], v[84:87], v[216:219]\n"
"buffer_load_dwordx4 v152, s[24:27], s57 offen offset:1088 lds\n"
"v_cvt_pk_bf16_f32 v188, v188, v189\n"
"v_cvt_pk_bf16_f32 v189, v190, v191\n"
"v_cvt_pk_bf16_f32 v196, v196, v197\n"
"v_cvt_pk_bf16_f32 v197, v198, v199\n"
"v_cvt_pk_bf16_f32 v182, v184, v185\n"
"v_cvt_pk_bf16_f32 v183, v186, v187\n"
"v_mfma_f32_16x16x32_bf16 v[184:187], v[236:239], a[48:51], 0\n"
"buffer_load_dwordx4 v152, s[24:27], s58 offen offset:2176 lds\n"
"v_cvt_pk_bf16_f32 v190, v192, v193\n"
"v_mfma_f32_16x16x32_bf16 v[184:187], v[240:243], a[52:55], v[184:187]\n"
"buffer_load_dwordx4 v152, s[24:27], s59 offen offset:3264 lds\n"
"v_cvt_pk_bf16_f32 v191, v194, v195\n"
"v_mfma_f32_16x16x32_bf16 v[184:187], v[244:247], a[56:59], v[184:187]\n"
"v_cvt_pk_bf16_f32 v198, v200, v201\n"
"v_cvt_pk_bf16_f32 v199, v202, v203\n"
"v_mfma_f32_16x16x32_bf16 v[184:187], v[248:251], a[60:63], v[184:187]\n"
"buffer_store_dwordx2 v[226:227], v162, s[84:87], s65 offen offset:768 sc0 sc1\n"
"v_mfma_f32_16x16x32_bf16 v[192:195], v[248:251], a[76:79], 0\n"
"v_max3_f32 v157, v148, v149, v150\n"
"v_max3_f32 v159, v151, v124, v125\n"
"v_mfma_f32_16x16x32_bf16 v[192:195], v[244:247], a[72:75], v[192:195]\n"
"v_max3_f32 v157, v157, v126, v127\n"
"v_max3_f32 v159, v159, v128, v129\n"
"v_mfma_f32_16x16x32_bf16 v[192:195], v[240:243], a[68:71], v[192:195]\n"
"v_max3_f32 v157, v157, v130, v131\n"
"v_add_u32 v163, 512, v163\n"
"v_mfma_f32_16x16x32_bf16 v[192:195], v[236:239], a[64:67], v[192:195]\n"
"v_max3_f32 v159, v159, v132, v133\n"
"v_max3_f32 v157, v157, v134, v135\n"
"v_mfma_f32_16x16x32_bf16 v[200:203], v[236:239], a[80:83], 0\n"
"ds_read_b128 v[236:239], v153 offset:7168\n"
"v_max3_f32 v159, v159, v136, v137\n"
"v_mfma_f32_16x16x32_bf16 v[200:203], v[240:243], a[84:87], v[200:203]\n"
"ds_read_b128 v[240:243], v153 offset:11264\n"
"v_max3_f32 v157, v157, v138, v139\n"
"v_mfma_f32_16x16x32_bf16 v[200:203], v[244:247], a[88:91], v[200:203]\n"
"ds_read_b128 v[244:247], v153 offset:15360\n"
"v_max3_f32 v159, v159, v140, v141\n"
"v_mfma_f32_16x16x32_bf16 v[200:203], v[248:251], a[92:95], v[200:203]\n"
"ds_read_b128 v[248:251], v153 offset:0\n"
"v_max3_f32 v157, v157, v142, v143\n"
"v_mfma_f32_16x16x32_bf16 v[120:123], a[228:231], v[180:183], 0\n"
"ds_read_b128 a[228:231], v158 offset:0\n"
"v_max3_f32 v159, v159, v144, v145\n"
"v_mfma_f32_16x16x32_bf16 v[96:99], v[0:3], v[180:183], 0\n"
"v_cvt_pk_bf16_f32 v184, v184, v185\n"
"v_cvt_pk_bf16_f32 v185, v186, v187\n"
"v_mfma_f32_16x16x32_bf16 v[104:107], v[16:19], v[180:183], 0\n"
"v_cvt_pk_bf16_f32 v192, v192, v193\n"
"v_cvt_pk_bf16_f32 v193, v194, v195\n"
"v_mfma_f32_16x16x32_bf16 v[112:115], v[32:35], v[180:183], 0\n"
"v_cvt_pk_bf16_f32 v200, v200, v201\n"
"v_cvt_pk_bf16_f32 v201, v202, v203\n"
"v_max3_f32 v157, v157, v146, v147\n"
"v_max_f32 v157, v157, v159\n"
"v_mov_b32 v161, v157\n"
"v_add_f32 v252, v252, v253\n"
"v_rcp_f32 v253, v252\n"
"v_permlane32_swap_b32 v157, v161\n"
"v_max_f32 v157, v157, v161\n"
"v_mov_b32 v161, v157\n"
"s_waitcnt lgkmcnt(5)\n"
"v_lshlrev_b32 v220, 16, v222\n"
"v_and_b32 v221, 0xffff0000, v222\n"
"s_waitcnt lgkmcnt(4)\n"
"v_mfma_f32_16x16x32_bf16 v[164:167], v[236:239], a[52:55], v[164:167]\n"
"s_waitcnt lgkmcnt(3)\n"
"ds_bpermute_b32 v254, v160, v253 offset:0\n"
"v_mfma_f32_16x16x32_bf16 v[164:167], v[240:243], a[56:59], v[164:167]\n"
"s_waitcnt lgkmcnt(3)\n"
"v_permlane16_swap_b32 v157, v161\n"
"v_mfma_f32_16x16x32_bf16 v[164:167], v[244:247], a[60:63], v[164:167]\n"
"v_max_f32 v157, v157, v161\n"
"v_sub_f32 v148, v148, v157\n"
"v_mfma_f32_16x16x32_bf16 v[168:171], v[244:247], a[76:79], 0\n"
"v_sub_f32 v149, v149, v157\n"
"v_sub_f32 v150, v150, v157\n"
"v_mfma_f32_16x16x32_bf16 v[168:171], v[240:243], a[72:75], v[168:171]\n"
"v_sub_f32 v151, v151, v157\n"
"v_sub_f32 v124, v124, v157\n"
"v_mfma_f32_16x16x32_bf16 v[168:171], v[236:239], a[68:71], v[168:171]\n"
"v_exp_f32 v161, v150\n"
"v_mfma_f32_16x16x32_bf16 v[168:171], a[224:227], a[64:67], v[168:171]\n"
"ds_read_b128 a[224:227], v153 offset:4096\n"
"v_cvt_pk_bf16_f32 v186, v164, v165\n"
"v_mfma_f32_16x16x32_bf16 v[172:175], v[236:239], a[84:87], v[172:175]\n"
"ds_read_b128 v[236:239], v153 offset:8192\n"
"v_cvt_pk_bf16_f32 v187, v166, v167\n"
"v_mfma_f32_16x16x32_bf16 v[172:175], v[240:243], a[88:91], v[172:175]\n"
"ds_read_b128 v[240:243], v153 offset:12288\n"
"v_sub_f32 v125, v125, v157\n"
"v_mfma_f32_16x16x32_bf16 v[172:175], v[244:247], a[92:95], v[172:175]\n"
"ds_read_b128 v[244:247], v153 offset:1024\n"
"v_cvt_pk_bf16_f32 v194, v168, v169\n"
"v_mfma_f32_16x16x32_bf16 v[120:123], a[232:235], v[184:187], v[120:123]\n"
"ds_read_b128 a[232:235], v158 offset:2048\n"
"v_cvt_pk_bf16_f32 v195, v170, v171\n"
"v_mfma_f32_16x16x32_bf16 v[96:99], v[4:7], v[184:187], v[96:99]\n"
"v_exp_f32 v148, v148\n"
"v_mfma_f32_16x16x32_bf16 v[100:103], v[12:15], v[184:187], 0\n"
"v_exp_f32 v149, v149\n"
"v_mfma_f32_16x16x32_bf16 v[100:103], v[8:11], v[180:183], v[100:103]\n"
"v_exp_f32 v151, v151\n"
"v_mfma_f32_16x16x32_bf16 v[104:107], v[20:23], v[184:187], v[104:107]\n"
"v_cvt_pk_bf16_f32 v202, v172, v173\n"
"v_cvt_pk_bf16_f32 v203, v174, v175\n"
"v_mfma_f32_16x16x32_bf16 v[108:111], v[28:31], v[184:187], 0\n"
"v_cvt_pk_bf16_f32 v150, v148, v149\n"
"v_add_f32 v148, v148, v149\n"
"v_mfma_f32_16x16x32_bf16 v[108:111], v[24:27], v[180:183], v[108:111]\n"
"v_add_f32 v149, v161, v151\n"
"v_cvt_pk_bf16_f32 v151, v161, v151\n"
"v_mfma_f32_16x16x32_bf16 v[112:115], v[36:39], v[184:187], v[112:115]\n"
"v_exp_f32 v124, v124\n"
"v_mfma_f32_16x16x32_bf16 v[116:119], v[44:47], v[184:187], 0\n"
"v_sub_f32 v126, v126, v157\n"
"v_sub_f32 v127, v127, v157\n"
"v_mfma_f32_16x16x32_bf16 v[116:119], v[40:43], v[180:183], v[116:119]\n"
"s_waitcnt lgkmcnt(3)\n"
"v_exp_f32 v125, v125\n"
"v_mul_f32_dpp v204, v254, v204 quad_perm:[0,0,0,0] row_mask:0xf bank_mask:0xf\n"
"v_mfma_f32_16x16x4_4b_bf16 v[164:179], v[150:151], a[244:245], 0 cbsz:2 abid:0\n"
"s_sub_u32 s74, s74, 1\n"
"v_exp_f32 v126, v126\n"
"v_mul_f32_dpp v205, v254, v205 quad_perm:[1,1,1,1] row_mask:0xf bank_mask:0xf\n"
"v_mul_f32_dpp v206, v254, v206 quad_perm:[2,2,2,2] row_mask:0xf bank_mask:0xf\n"
"v_exp_f32 v127, v127\n"
"v_mul_f32_dpp v207, v254, v207 quad_perm:[3,3,3,3] row_mask:0xf bank_mask:0xf\n"
"v_sub_f32 v128, v128, v157\n"
"s_cbranch_scc1 .Leps%=\n"
"v_mfma_f32_16x16x32_bf16 v[180:183], v[248:251], a[0:3], 0\n"
"v_mul_f32_dpp v208, v254, v208 quad_perm:[0,0,0,0] row_mask:0xf bank_mask:0xf\n"
"v_mul_f32_dpp v209, v254, v209 quad_perm:[1,1,1,1] row_mask:0xf bank_mask:0xf\n"
"v_mfma_f32_16x16x32_bf16 v[180:183], a[224:227], a[4:7], v[180:183]\n"
"v_mul_f32_dpp v210, v254, v210 quad_perm:[2,2,2,2] row_mask:0xf bank_mask:0xf\n"
"v_sub_f32 v129, v129, v157\n"
"v_mfma_f32_16x16x32_bf16 v[180:183], v[236:239], a[8:11], v[180:183]\n"
"s_waitcnt lgkmcnt(0)\n"
"v_exp_f32 v128, v128\n"
"v_mfma_f32_16x16x32_bf16 v[180:183], v[240:243], a[12:15], v[180:183]\n"
"v_mul_f32_dpp v211, v254, v211 quad_perm:[3,3,3,3] row_mask:0xf bank_mask:0xf\n"
"v_cvt_pk_bf16_f32 v124, v124, v125\n"
"v_mfma_f32_16x16x32_bf16 v[204:207], a[32:35], a[160:163], v[204:207]\n"
"v_mul_f32_dpp v212, v254, v212 quad_perm:[0,0,0,0] row_mask:0xf bank_mask:0xf\n"
"v_cvt_pk_bf16_f32 v125, v126, v127\n"
"v_mfma_f32_16x16x32_bf16 v[204:207], a[36:39], a[164:167], v[204:207]\n"
"v_sub_f32 v130, v130, v157\n"
"v_mul_f32_dpp v213, v254, v213 quad_perm:[1,1,1,1] row_mask:0xf bank_mask:0xf\n"
"v_mfma_f32_16x16x32_bf16 v[204:207], a[40:43], a[168:171], v[204:207]\n"
"v_exp_f32 v129, v129\n"
"v_mfma_f32_16x16x32_bf16 v[204:207], a[44:47], a[172:175], v[204:207]\n"
"v_mul_f32_dpp v214, v254, v214 quad_perm:[2,2,2,2] row_mask:0xf bank_mask:0xf\n"
"v_mul_f32_dpp v215, v254, v215 quad_perm:[3,3,3,3] row_mask:0xf bank_mask:0xf\n"
"v_mfma_f32_16x16x32_bf16 v[208:211], a[44:47], a[188:191], v[208:211]\n"
"v_sub_f32 v131, v131, v157\n"
"v_mul_f32_dpp v216, v254, v216 quad_perm:[0,0,0,0] row_mask:0xf bank_mask:0xf\n"
"v_mfma_f32_16x16x32_bf16 v[208:211], a[40:43], a[184:187], v[208:211]\n"
"v_exp_f32 v130, v130\n"
"v_mfma_f32_16x16x32_bf16 v[208:211], a[36:39], a[180:183], v[208:211]\n"
"v_mul_f32_dpp v217, v254, v217 quad_perm:[1,1,1,1] row_mask:0xf bank_mask:0xf\n"
"v_mul_f32_dpp v218, v254, v218 quad_perm:[2,2,2,2] row_mask:0xf bank_mask:0xf\n"
"v_mfma_f32_16x16x32_bf16 v[208:211], a[32:35], a[176:179], v[208:211]\n"
"v_exp_f32 v131, v131\n"
"v_mfma_f32_16x16x32_bf16 v[212:215], a[32:35], a[192:195], v[212:215]\n"
"v_mul_f32_dpp v219, v254, v219 quad_perm:[3,3,3,3] row_mask:0xf bank_mask:0xf\n"
"v_sub_f32 v132, v132, v157\n"
"v_mfma_f32_16x16x32_bf16 v[212:215], a[36:39], a[196:199], v[212:215]\n"
"v_sub_f32 v133, v133, v157\n"
"v_cvt_pk_bf16_f32 v126, v128, v129\n"
"v_mfma_f32_16x16x32_bf16 v[212:215], a[40:43], a[200:203], v[212:215]\n"
"v_exp_f32 v132, v132\n"
"v_mfma_f32_16x16x32_bf16 v[212:215], a[44:47], a[204:207], v[212:215]\n"
"v_cvt_pk_bf16_f32 v127, v130, v131\n"
"v_sub_f32 v134, v134, v157\n"
"v_mfma_f32_16x16x32_bf16 v[216:219], a[44:47], a[220:223], v[216:219]\n"
"s_waitcnt vmcnt(16)\n"
"ds_read_b128 a[44:47], v154 offset:8896\n"
"v_sub_f32 v135, v135, v157\n"
"v_mfma_f32_16x16x32_bf16 v[216:219], a[40:43], a[216:219], v[216:219]\n"
"ds_read_b128 a[40:43], v154 offset:8832\n"
"v_sub_f32 v136, v136, v157\n"
"v_mfma_f32_16x16x32_bf16 v[216:219], a[36:39], a[212:215], v[216:219]\n"
"ds_read_b128 a[36:39], v154 offset:8768\n"
"v_sub_f32 v137, v137, v157\n"
"v_mfma_f32_16x16x32_bf16 v[216:219], a[32:35], a[208:211], v[216:219]\n"
"ds_read_b128 a[32:35], v154 offset:8704\n"
"v_sub_f32 v138, v138, v157\n"
"v_mfma_f32_16x16x32_bf16 v[252:255], a[252:255], v[124:127], v[148:151]\n"
"v_exp_f32 v133, v133\n"
"v_mfma_f32_16x16x32_bf16 v[164:167], v[124:127], v[48:51], v[164:167]\n"
"v_exp_f32 v134, v134\n"
"v_mfma_f32_16x16x32_bf16 v[172:175], v[124:127], v[72:75], v[172:175]\n"
"v_exp_f32 v135, v135\n"
"v_mfma_f32_16x16x32_bf16 v[148:151], a[228:231], v[188:191], 0\n"
"ds_read_b128 a[228:231], v158 offset:0\n"
"v_cvt_pk_bf16_f32 v132, v132, v133\n"
"v_mfma_f32_16x16x32_bf16 v[148:151], a[232:235], v[192:195], v[148:151]\n"
"ds_read_b128 a[232:235], v158 offset:2048\n"
"v_cvt_pk_bf16_f32 v133, v134, v135\n"
"v_mfma_f32_16x16x32_bf16 v[128:131], v[12:15], v[192:195], 0\n"
"v_exp_f32 v136, v136\n"
"v_mfma_f32_16x16x32_bf16 v[128:131], v[8:11], v[188:191], v[128:131]\n"
"v_exp_f32 v137, v137\n"
"v_mfma_f32_16x16x32_bf16 v[184:187], v[244:247], a[0:3], 0\n"
"v_sub_f32 v139, v139, v157\n"
"v_exp_f32 v138, v138\n"
"v_exp_f32 v139, v139\n"
"v_sub_f32 v140, v140, v157\n"
"v_sub_f32 v141, v141, v157\n"
"v_exp_f32 v140, v140\n"
"v_cvt_pk_bf16_f32 v134, v136, v137\n"
"v_cvt_pk_bf16_f32 v135, v138, v139\n"
"v_mfma_f32_16x16x32_bf16 v[136:139], v[28:31], v[192:195], 0\n"
"v_cvt_pk_bf16_f32 v204, v204, v208\n"
"v_sub_f32 v142, v142, v157\n"
"v_mfma_f32_16x16x32_bf16 v[136:139], v[24:27], v[188:191], v[136:139]\n"
"v_exp_f32 v141, v141\n"
"v_mfma_f32_16x16x32_bf16 v[252:255], a[252:255], v[132:135], v[252:255]\n"
"v_cvt_pk_bf16_f32 v208, v205, v209\n"
"v_cvt_pk_bf16_f32 v205, v212, v216\n"
"v_mfma_f32_16x16x32_bf16 v[164:167], v[132:135], v[52:55], v[164:167]\n"
"v_cvt_pk_bf16_f32 v209, v213, v217\n"
"v_sub_f32 v143, v143, v157\n"
"v_mfma_f32_16x16x32_bf16 v[172:175], v[132:135], v[76:79], v[172:175]\n"
"v_pk_max_i16 v204, v204, 0\n"
"v_exp_f32 v142, v142\n"
"s_add_u32 m0, s28, 8704\n"
"v_pk_max_i16 v205, v205, 0\n"
"v_pk_max_i16 v208, v208, 0\n"
"v_exp_f32 v143, v143\n"
"v_pk_max_i16 v209, v209, 0\n"
"v_sub_f32 v144, v144, v157\n"
"v_cvt_pk_bf16_f32 v206, v206, v210\n"
"v_cvt_pk_bf16_f32 v210, v207, v211\n"
"v_cvt_pk_bf16_f32 v207, v214, v218\n"
"v_sub_f32 v145, v145, v157\n"
"v_cvt_pk_bf16_f32 v211, v215, v219\n"
"v_exp_f32 v144, v144\n"
"v_pk_max_i16 v206, v206, 0\n"
"v_cvt_pk_bf16_f32 v140, v140, v141\n"
"v_pk_max_i16 v207, v207, 0\n"
"v_cvt_pk_bf16_f32 v141, v142, v143\n"
"v_sub_f32 v146, v146, v157\n"
"v_exp_f32 v145, v145\n"
"v_pk_max_i16 v210, v210, 0\n"
"v_pk_max_i16 v211, v211, 0\n"
"v_sub_f32 v147, v147, v157\n"
"v_exp_f32 v146, v146\n"
"v_exp_f32 v147, v147\n"
"v_cvt_pk_bf16_f32 v142, v144, v145\n"
"v_max3_f32 v157, v120, v121, v122\n"
"v_cvt_pk_bf16_f32 v143, v146, v147\n"
"v_mfma_f32_16x16x32_bf16 v[144:147], v[44:47], v[192:195], 0\n"
"s_waitcnt lgkmcnt(0)\n"
"buffer_load_dwordx4 v152, s[24:27], s60 offen offset:0 lds\n"
"v_lshlrev_b32 v222, 16, v223\n"
"v_mfma_f32_16x16x32_bf16 v[144:147], v[40:43], v[188:191], v[144:147]\n"
"buffer_load_dwordx4 v152, s[24:27], s61 offen offset:1088 lds\n"
"v_and_b32 v223, 0xffff0000, v223\n"
"v_mfma_f32_16x16x32_bf16 v[252:255], a[252:255], v[140:143], v[252:255]\n"
"buffer_load_dwordx4 v152, s[24:27], s62 offen offset:2176 lds\n"
"v_max3_f32 v159, v123, v96, v97\n"
"v_mfma_f32_16x16x32_bf16 v[164:167], v[140:143], v[56:59], v[164:167]\n"
"buffer_load_dwordx4 v152, s[24:27], s63 offen offset:3264 lds\n"
"v_max3_f32 v157, v157, v98, v99\n"
"v_mfma_f32_16x16x32_bf16 v[168:171], v[140:143], v[68:71], v[168:171]\n"
"v_max3_f32 v159, v159, v100, v101\n"
"v_max3_f32 v157, v157, v102, v103\n"
"v_mfma_f32_16x16x32_bf16 v[168:171], v[132:135], v[64:67], v[168:171]\n"
"v_max3_f32 v159, v159, v104, v105\n"
"v_max3_f32 v157, v157, v106, v107\n"
"v_mfma_f32_16x16x32_bf16 v[168:171], v[124:127], v[60:63], v[168:171]\n"
"buffer_store_dwordx2 v[204:205], v162, s[84:87], s66 offen offset:0 sc0 sc1\n"
"v_mfma_f32_16x16x32_bf16 v[172:175], v[140:143], v[80:83], v[172:175]\n"
"buffer_store_dwordx2 v[208:209], v162, s[84:87], s66 offen offset:256 sc0 sc1\n"
"v_mfma_f32_16x16x32_bf16 v[176:179], v[140:143], v[92:95], v[176:179]\n"
"v_max3_f32 v159, v159, v108, v109\n"
"v_max3_f32 v157, v157, v110, v111\n"
"v_mfma_f32_16x16x32_bf16 v[176:179], v[132:135], v[88:91], v[176:179]\n"
"v_max3_f32 v159, v159, v112, v113\n"
"v_max3_f32 v157, v157, v114, v115\n"
"v_mfma_f32_16x16x32_bf16 v[176:179], v[124:127], v[84:87], v[176:179]\n"
"buffer_store_dwordx2 v[206:207], v162, s[84:87], s66 offen offset:512 sc0 sc1\n"
"v_mfma_f32_16x16x32_bf16 v[124:127], v[0:3], v[188:191], 0\n"
"v_max3_f32 v159, v159, v116, v117\n"
"v_max3_f32 v157, v157, v118, v119\n"
"v_mfma_f32_16x16x32_bf16 v[124:127], v[4:7], v[192:195], v[124:127]\n"
"buffer_store_dwordx2 v[210:211], v162, s[84:87], s66 offen offset:768 sc0 sc1\n"
"v_mfma_f32_16x16x32_bf16 v[132:135], v[16:19], v[188:191], 0\n"
"v_max_f32 v157, v157, v159\n"
"v_mov_b32 v161, v157\n"
"v_mfma_f32_16x16x32_bf16 v[132:135], v[20:23], v[192:195], v[132:135]\n"
"v_add_f32 v252, v252, v253\n"
"v_add_u32 v158, 64, v158\n"
"v_mfma_f32_16x16x32_bf16 v[140:143], v[32:35], v[188:191], 0\n"
"v_permlane32_swap_b32 v157, v161\n"
"v_mfma_f32_16x16x32_bf16 v[140:143], v[36:39], v[192:195], v[140:143]\n"
"v_max_f32 v157, v157, v161\n"
"v_mov_b32 v161, v157\n"
"v_mfma_f32_16x16x32_bf16 v[188:191], v[240:243], a[28:31], 0\n"
"v_rcp_f32 v253, v252\n"
"v_mfma_f32_16x16x32_bf16 v[188:191], v[236:239], a[24:27], v[188:191]\n"
"ds_bpermute_b32 v254, v160, v253 offset:0\n"
"v_mfma_f32_16x16x32_bf16 v[188:191], a[224:227], a[20:23], v[188:191]\n"
"v_permlane16_swap_b32 v157, v161\n"
"v_mfma_f32_16x16x32_bf16 v[188:191], v[248:251], a[16:19], v[188:191]\n"
"v_max_f32 v157, v157, v161\n"
"v_sub_f32 v120, v120, v157\n"
"v_sub_f32 v121, v121, v157\n"
"v_sub_f32 v122, v122, v157\n"
"v_sub_f32 v123, v123, v157\n"
"v_exp_f32 v161, v122\n"
"v_exp_f32 v120, v120\n"
"v_exp_f32 v121, v121\n"
"v_exp_f32 v123, v123\n"
"v_cvt_pk_bf16_f32 v122, v120, v121\n"
"v_add_f32 v120, v120, v121\n"
"v_add_f32 v121, v161, v123\n"
"v_cvt_pk_bf16_f32 v123, v161, v123\n"
"v_sub_f32 v96, v96, v157\n"
"v_sub_f32 v97, v97, v157\n"
"v_exp_f32 v96, v96\n"
"v_mfma_f32_16x16x4_4b_bf16 v[204:219], v[122:123], a[244:245], 0 cbsz:2 abid:0\n"
"v_sub_f32 v98, v98, v157\n"
"v_exp_f32 v97, v97\n"
"v_sub_f32 v99, v99, v157\n"
"v_exp_f32 v98, v98\n"
"v_exp_f32 v99, v99\n"
"v_sub_f32 v100, v100, v157\n"
"v_sub_f32 v101, v101, v157\n"
"v_exp_f32 v100, v100\n"
"v_cvt_pk_bf16_f32 v96, v96, v97\n"
"s_waitcnt lgkmcnt(0)\n"
"v_fmac_f32_dpp v220, v254, v164 quad_perm:[0,0,0,0] row_mask:0xf bank_mask:0xf\n"
"v_mfma_f32_16x16x32_bf16 v[164:167], a[144:147], a[0:3], 0\n"
"v_fmac_f32_dpp v221, v254, v168 quad_perm:[0,0,0,0] row_mask:0xf bank_mask:0xf\n"
"v_fmac_f32_dpp v222, v254, v172 quad_perm:[0,0,0,0] row_mask:0xf bank_mask:0xf\n"
"v_mfma_f32_16x16x32_bf16 v[164:167], a[148:151], a[4:7], v[164:167]\n"
"v_fmac_f32_dpp v223, v254, v176 quad_perm:[0,0,0,0] row_mask:0xf bank_mask:0xf\n"
"v_cvt_pk_bf16_f32 v220, v220, v221\n"
"v_mfma_f32_16x16x32_bf16 v[164:167], a[152:155], a[8:11], v[164:167]\n"
"v_cvt_pk_bf16_f32 v221, v222, v223\n"
"v_cvt_pk_bf16_f32 v97, v98, v99\n"
"v_mfma_f32_16x16x32_bf16 v[164:167], a[156:159], a[12:15], v[164:167]\n"
"v_pk_max_i16 v220, v220, 0\n"
"v_sub_f32 v102, v102, v157\n"
"v_mfma_f32_16x16x32_bf16 v[168:171], a[156:159], a[28:31], 0\n"
"v_pk_max_i16 v221, v221, 0\n"
"v_sub_f32 v103, v103, v157\n"
"v_mfma_f32_16x16x32_bf16 v[168:171], a[152:155], a[24:27], v[168:171]\n"
"v_exp_f32 v101, v101\n"
"v_mfma_f32_16x16x32_bf16 v[168:171], a[148:151], a[20:23], v[168:171]\n"
"v_exp_f32 v102, v102\n"
"v_mfma_f32_16x16x32_bf16 v[168:171], a[144:147], a[16:19], v[168:171]\n"
"buffer_store_dwordx2 v[220:221], v155, s[84:87], s70 offen offset:0 sc0 sc1\n"
"v_mfma_f32_16x16x32_bf16 v[172:175], a[144:147], a[32:35], 0\n"
"v_exp_f32 v103, v103\n"
"v_mfma_f32_16x16x32_bf16 v[172:175], a[148:151], a[36:39], v[172:175]\n"
"v_sub_f32 v104, v104, v157\n"
"v_sub_f32 v105, v105, v157\n"
"v_mfma_f32_16x16x32_bf16 v[172:175], a[152:155], a[40:43], v[172:175]\n"
"v_exp_f32 v104, v104\n"
"v_mfma_f32_16x16x32_bf16 v[172:175], a[156:159], a[44:47], v[172:175]\n"
"v_cvt_pk_bf16_f32 v98, v100, v101\n"
"v_cvt_pk_bf16_f32 v99, v102, v103\n"
"v_mfma_f32_16x16x32_bf16 v[100:103], v[12:15], v[200:203], 0\n"
"v_sub_f32 v106, v106, v157\n"
"v_sub_f32 v107, v107, v157\n"
"v_mfma_f32_16x16x32_bf16 v[100:103], v[8:11], v[196:199], v[100:103]\n"
"v_exp_f32 v105, v105\n"
"v_mfma_f32_16x16x32_bf16 v[252:255], a[252:255], v[96:99], v[120:123]\n"
"v_exp_f32 v106, v106\n"
"v_mfma_f32_16x16x32_bf16 v[204:207], v[96:99], v[48:51], v[204:207]\n"
"v_exp_f32 v107, v107\n"
"v_mfma_f32_16x16x32_bf16 v[212:215], v[96:99], v[72:75], v[212:215]\n"
"v_sub_f32 v108, v108, v157\n"
"v_sub_f32 v109, v109, v157\n"
"v_mfma_f32_16x16x32_bf16 v[120:123], a[228:231], v[196:199], 0\n"
"ds_read_b128 a[228:231], v158 offset:0\n"
"v_cvt_pk_bf16_f32 v104, v104, v105\n"
"v_mfma_f32_16x16x32_bf16 v[120:123], a[232:235], v[200:203], v[120:123]\n"
"ds_read_b128 a[232:235], v158 offset:2048\n"
"v_cvt_pk_bf16_f32 v105, v106, v107\n"
"v_mfma_f32_16x16x32_bf16 v[8:11], a[108:111], a[28:31], 0\n"
"v_exp_f32 v108, v108\n"
"v_mfma_f32_16x16x32_bf16 v[8:11], a[104:107], a[24:27], v[8:11]\n"
"v_sub_f32 v110, v110, v157\n"
"v_sub_f32 v111, v111, v157\n"
"v_mfma_f32_16x16x32_bf16 v[8:11], a[100:103], a[20:23], v[8:11]\n"
"v_exp_f32 v109, v109\n"
"v_mfma_f32_16x16x32_bf16 v[8:11], a[96:99], a[16:19], v[8:11]\n"
"v_exp_f32 v110, v110\n"
"v_mfma_f32_16x16x32_bf16 v[12:15], a[124:127], a[28:31], 0\n"
"v_exp_f32 v111, v111\n"
"v_mfma_f32_16x16x32_bf16 v[12:15], a[120:123], a[24:27], v[12:15]\n"
"v_sub_f32 v112, v112, v157\n"
"v_sub_f32 v113, v113, v157\n"
"v_mfma_f32_16x16x32_bf16 v[12:15], a[116:119], a[20:23], v[12:15]\n"
"v_exp_f32 v112, v112\n"
"v_mfma_f32_16x16x32_bf16 v[12:15], a[112:115], a[16:19], v[12:15]\n"
"v_cvt_pk_bf16_f32 v106, v108, v109\n"
"v_cvt_pk_bf16_f32 v107, v110, v111\n"
"v_mfma_f32_16x16x32_bf16 v[108:111], v[28:31], v[200:203], 0\n"
"v_sub_f32 v114, v114, v157\n"
"v_sub_f32 v115, v115, v157\n"
"v_mfma_f32_16x16x32_bf16 v[108:111], v[24:27], v[196:199], v[108:111]\n"
"v_exp_f32 v113, v113\n"
"v_mfma_f32_16x16x32_bf16 v[252:255], a[252:255], v[104:107], v[252:255]\n"
"v_exp_f32 v114, v114\n"
"v_mfma_f32_16x16x32_bf16 v[204:207], v[104:107], v[52:55], v[204:207]\n"
"v_exp_f32 v115, v115\n"
"v_mfma_f32_16x16x32_bf16 v[212:215], v[104:107], v[76:79], v[212:215]\n"
"v_sub_f32 v116, v116, v157\n"
"v_sub_f32 v117, v117, v157\n"
"v_exp_f32 v116, v116\n"
"v_cvt_pk_bf16_f32 v112, v112, v113\n"
"v_cvt_pk_bf16_f32 v113, v114, v115\n"
"v_sub_f32 v118, v118, v157\n"
"v_exp_f32 v117, v117\n"
"v_sub_f32 v119, v119, v157\n"
"v_exp_f32 v118, v118\n"
"v_exp_f32 v119, v119\n"
"v_cvt_pk_bf16_f32 v114, v116, v117\n"
"v_max3_f32 v157, v148, v149, v150\n"
"v_cvt_pk_bf16_f32 v115, v118, v119\n"
"v_mfma_f32_16x16x32_bf16 v[116:119], v[44:47], v[200:203], 0\n"
"v_max3_f32 v159, v151, v124, v125\n"
"v_max3_f32 v157, v157, v126, v127\n"
"v_mfma_f32_16x16x32_bf16 v[116:119], v[40:43], v[196:199], v[116:119]\n"
"v_max3_f32 v159, v159, v128, v129\n"
"v_max3_f32 v157, v157, v130, v131\n"
"v_mfma_f32_16x16x32_bf16 v[252:255], a[252:255], v[112:115], v[252:255]\n"
"v_max3_f32 v159, v159, v132, v133\n"
"v_max3_f32 v157, v157, v134, v135\n"
"v_mfma_f32_16x16x32_bf16 v[204:207], v[112:115], v[56:59], v[204:207]\n"
"v_max3_f32 v159, v159, v136, v137\n"
"v_max3_f32 v157, v157, v138, v139\n"
"v_mfma_f32_16x16x32_bf16 v[208:211], v[112:115], v[68:71], v[208:211]\n"
"v_max3_f32 v159, v159, v140, v141\n"
"v_max3_f32 v157, v157, v142, v143\n"
"v_mfma_f32_16x16x32_bf16 v[208:211], v[104:107], v[64:67], v[208:211]\n"
"v_max3_f32 v159, v159, v144, v145\n"
"v_max3_f32 v157, v157, v146, v147\n"
"v_mfma_f32_16x16x32_bf16 v[208:211], v[96:99], v[60:63], v[208:211]\n"
"v_max_f32 v157, v157, v159\n"
"v_mov_b32 v161, v157\n"
"v_mfma_f32_16x16x32_bf16 v[212:215], v[112:115], v[80:83], v[212:215]\n"
"v_add_f32 v252, v252, v253\n"
"v_cvt_pk_bf16_f32 v8, v8, v9\n"
"v_mfma_f32_16x16x32_bf16 v[216:219], v[112:115], v[92:95], v[216:219]\n"
"v_permlane32_swap_b32 v157, v161\n"
"v_mfma_f32_16x16x32_bf16 v[216:219], v[104:107], v[88:91], v[216:219]\n"
"v_max_f32 v157, v157, v161\n"
"v_mov_b32 v161, v157\n"
"v_mfma_f32_16x16x32_bf16 v[216:219], v[96:99], v[84:87], v[216:219]\n"
"v_rcp_f32 v253, v252\n"
"v_mfma_f32_16x16x32_bf16 v[96:99], v[0:3], v[196:199], 0\n"
"ds_bpermute_b32 v254, v160, v253 offset:0\n"
"v_mfma_f32_16x16x32_bf16 v[96:99], v[4:7], v[200:203], v[96:99]\n"
"v_permlane16_swap_b32 v157, v161\n"
"v_mfma_f32_16x16x32_bf16 v[104:107], v[16:19], v[196:199], 0\n"
"v_max_f32 v157, v157, v161\n"
"v_sub_f32 v148, v148, v157\n"
"v_mfma_f32_16x16x32_bf16 v[104:107], v[20:23], v[200:203], v[104:107]\n"
"v_sub_f32 v149, v149, v157\n"
"v_sub_f32 v150, v150, v157\n"
"v_mfma_f32_16x16x32_bf16 v[112:115], v[32:35], v[196:199], 0\n"
"v_sub_f32 v151, v151, v157\n"
"v_sub_f32 v124, v124, v157\n"
"v_mfma_f32_16x16x32_bf16 v[112:115], v[36:39], v[200:203], v[112:115]\n"
"v_exp_f32 v161, v150\n"
"v_mfma_f32_16x16x32_bf16 v[0:3], a[96:99], a[0:3], 0\n"
"v_exp_f32 v148, v148\n"
"v_mfma_f32_16x16x32_bf16 v[0:3], a[100:103], a[4:7], v[0:3]\n"
"v_exp_f32 v149, v149\n"
"v_mfma_f32_16x16x32_bf16 v[0:3], a[104:107], a[8:11], v[0:3]\n"
"v_exp_f32 v151, v151\n"
"v_mfma_f32_16x16x32_bf16 v[0:3], a[108:111], a[12:15], v[0:3]\n"
"v_cvt_pk_bf16_f32 v150, v148, v149\n"
"v_add_f32 v148, v148, v149\n"
"v_mfma_f32_16x16x32_bf16 v[16:19], a[96:99], a[32:35], 0\n"
"v_add_f32 v149, v161, v151\n"
"v_cvt_pk_bf16_f32 v151, v161, v151\n"
"v_mfma_f32_16x16x32_bf16 v[16:19], a[100:103], a[36:39], v[16:19]\n"
"v_sub_f32 v125, v125, v157\n"
"v_sub_f32 v126, v126, v157\n"
"v_mfma_f32_16x16x32_bf16 v[16:19], a[104:107], a[40:43], v[16:19]\n"
"v_exp_f32 v124, v124\n"
"v_mfma_f32_16x16x32_bf16 v[16:19], a[108:111], a[44:47], v[16:19]\n"
"v_exp_f32 v125, v125\n"
"v_mfma_f32_16x16x32_bf16 v[4:7], a[112:115], a[0:3], 0\n"
"v_sub_f32 v127, v127, v157\n"
"v_sub_f32 v128, v128, v157\n"
"v_mfma_f32_16x16x32_bf16 v[4:7], a[116:119], a[4:7], v[4:7]\n"
"v_exp_f32 v126, v126\n"
"v_mfma_f32_16x16x32_bf16 v[4:7], a[120:123], a[8:11], v[4:7]\n"
"v_exp_f32 v127, v127\n"
"v_mfma_f32_16x16x32_bf16 v[4:7], a[124:127], a[12:15], v[4:7]\n"
"v_sub_f32 v129, v129, v157\n"
"v_cvt_pk_bf16_f32 v124, v124, v125\n"
"v_mfma_f32_16x16x32_bf16 v[20:23], a[112:115], a[32:35], 0\n"
"v_exp_f32 v128, v128\n"
"v_mfma_f32_16x16x32_bf16 v[20:23], a[116:119], a[36:39], v[20:23]\n"
"v_cvt_pk_bf16_f32 v125, v126, v127\n"
"v_sub_f32 v130, v130, v157\n"
"v_mfma_f32_16x16x32_bf16 v[20:23], a[120:123], a[40:43], v[20:23]\n"
"v_exp_f32 v129, v129\n"
"v_mfma_f32_16x16x32_bf16 v[20:23], a[124:127], a[44:47], v[20:23]\n"
"v_sub_f32 v131, v131, v157\n"
"v_exp_f32 v130, v130\n"
"v_mfma_f32_16x16x4_4b_bf16 v[220:235], v[150:151], a[244:245], 0 cbsz:2 abid:0\n"
"v_exp_f32 v131, v131\n"
"v_sub_f32 v132, v132, v157\n"
"v_sub_f32 v133, v133, v157\n"
"v_exp_f32 v132, v132\n"
"v_cvt_pk_bf16_f32 v126, v128, v129\n"
"v_cvt_pk_bf16_f32 v127, v130, v131\n"
"v_mfma_f32_16x16x32_bf16 v[196:199], v[248:251], a[32:35], 0\n"
"ds_read_b128 v[248:251], v153 offset:5120\n"
"v_sub_f32 v134, v134, v157\n"
"v_mfma_f32_16x16x32_bf16 v[196:199], a[224:227], a[36:39], v[196:199]\n"
"ds_read_b128 a[224:227], v153 offset:9216\n"
"v_sub_f32 v135, v135, v157\n"
"v_mfma_f32_16x16x32_bf16 v[196:199], v[236:239], a[40:43], v[196:199]\n"
"ds_read_b128 v[236:239], v153 offset:13312\n"
"v_sub_f32 v136, v136, v157\n"
"v_mfma_f32_16x16x32_bf16 v[196:199], v[240:243], a[44:47], v[196:199]\n"
"ds_read_b128 v[240:243], v153 offset:2048\n"
"v_sub_f32 v137, v137, v157\n"
"v_mfma_f32_16x16x32_bf16 v[220:223], v[124:127], v[48:51], v[220:223]\n"
"v_exp_f32 v133, v133\n"
"v_mfma_f32_16x16x32_bf16 v[228:231], v[124:127], v[72:75], v[228:231]\n"
"v_exp_f32 v134, v134\n"
"v_mfma_f32_16x16x32_bf16 v[200:203], v[244:247], a[32:35], 0\n"
"v_exp_f32 v135, v135\n"
"v_exp_f32 v136, v136\n"
"v_cvt_pk_bf16_f32 v132, v132, v133\n"
"v_cvt_pk_bf16_f32 v133, v134, v135\n"
"v_sub_f32 v138, v138, v157\n"
"v_exp_f32 v137, v137\n"
"v_sub_f32 v139, v139, v157\n"
"v_exp_f32 v138, v138\n"
"s_waitcnt lgkmcnt(4)\n"
"v_mul_f32_dpp v204, v254, v204 quad_perm:[0,0,0,0] row_mask:0xf bank_mask:0xf\n"
"v_exp_f32 v139, v139\n"
"v_mul_f32_dpp v205, v254, v205 quad_perm:[1,1,1,1] row_mask:0xf bank_mask:0xf\n"
"v_sub_f32 v140, v140, v157\n"
"v_mul_f32_dpp v206, v254, v206 quad_perm:[2,2,2,2] row_mask:0xf bank_mask:0xf\n"
"s_waitcnt lgkmcnt(3)\n"
"v_mfma_f32_16x16x32_bf16 v[184:187], v[248:251], a[4:7], v[184:187]\n"
"s_waitcnt lgkmcnt(2)\n"
"v_mul_f32_dpp v207, v254, v207 quad_perm:[3,3,3,3] row_mask:0xf bank_mask:0xf\n"
"v_mul_f32_dpp v208, v254, v208 quad_perm:[0,0,0,0] row_mask:0xf bank_mask:0xf\n"
"v_mfma_f32_16x16x32_bf16 v[184:187], a[224:227], a[8:11], v[184:187]\n"
"s_waitcnt lgkmcnt(1)\n"
"v_sub_f32 v141, v141, v157\n"
"v_mul_f32_dpp v209, v254, v209 quad_perm:[1,1,1,1] row_mask:0xf bank_mask:0xf\n"
"v_mfma_f32_16x16x32_bf16 v[184:187], v[236:239], a[12:15], v[184:187]\n"
"v_exp_f32 v140, v140\n"
"v_mfma_f32_16x16x32_bf16 v[204:207], a[48:51], a[160:163], v[204:207]\n"
"v_cvt_pk_bf16_f32 v134, v136, v137\n"
"v_mul_f32_dpp v210, v254, v210 quad_perm:[2,2,2,2] row_mask:0xf bank_mask:0xf\n"
"v_mfma_f32_16x16x32_bf16 v[204:207], a[52:55], a[164:167], v[204:207]\n"
"v_cvt_pk_bf16_f32 v135, v138, v139\n"
"v_sub_f32 v142, v142, v157\n"
"v_mfma_f32_16x16x32_bf16 v[204:207], a[56:59], a[168:171], v[204:207]\n"
"v_mul_f32_dpp v211, v254, v211 quad_perm:[3,3,3,3] row_mask:0xf bank_mask:0xf\n"
"v_mul_f32_dpp v212, v254, v212 quad_perm:[0,0,0,0] row_mask:0xf bank_mask:0xf\n"
"v_mfma_f32_16x16x32_bf16 v[204:207], a[60:63], a[172:175], v[204:207]\n"
"v_exp_f32 v141, v141\n"
"v_mfma_f32_16x16x32_bf16 v[208:211], a[60:63], a[188:191], v[208:211]\n"
"v_mul_f32_dpp v213, v254, v213 quad_perm:[1,1,1,1] row_mask:0xf bank_mask:0xf\n"
"v_sub_f32 v143, v143, v157\n"
"v_mfma_f32_16x16x32_bf16 v[208:211], a[56:59], a[184:187], v[208:211]\n"
"v_mul_f32_dpp v214, v254, v214 quad_perm:[2,2,2,2] row_mask:0xf bank_mask:0xf\n"
"v_mul_f32_dpp v215, v254, v215 quad_perm:[3,3,3,3] row_mask:0xf bank_mask:0xf\n"
"v_mfma_f32_16x16x32_bf16 v[208:211], a[52:55], a[180:183], v[208:211]\n"
"v_exp_f32 v142, v142\n"
"v_mfma_f32_16x16x32_bf16 v[208:211], a[48:51], a[176:179], v[208:211]\n"
"v_mul_f32_dpp v216, v254, v216 quad_perm:[0,0,0,0] row_mask:0xf bank_mask:0xf\n"
"v_mul_f32_dpp v217, v254, v217 quad_perm:[1,1,1,1] row_mask:0xf bank_mask:0xf\n"
"v_mfma_f32_16x16x32_bf16 v[212:215], a[48:51], a[192:195], v[212:215]\n"
"v_exp_f32 v143, v143\n"
"v_mfma_f32_16x16x32_bf16 v[212:215], a[52:55], a[196:199], v[212:215]\n"
"v_mul_f32_dpp v218, v254, v218 quad_perm:[2,2,2,2] row_mask:0xf bank_mask:0xf\n"
"v_sub_f32 v144, v144, v157\n"
"v_mfma_f32_16x16x32_bf16 v[212:215], a[56:59], a[200:203], v[212:215]\n"
"v_mul_f32_dpp v219, v254, v219 quad_perm:[3,3,3,3] row_mask:0xf bank_mask:0xf\n"
"v_sub_f32 v145, v145, v157\n"
"v_mfma_f32_16x16x32_bf16 v[212:215], a[60:63], a[204:207], v[212:215]\n"
"v_exp_f32 v144, v144\n"
"v_mfma_f32_16x16x32_bf16 v[216:219], a[60:63], a[220:223], v[216:219]\n"
"s_waitcnt vmcnt(17)\n"
"ds_read_b128 a[60:63], v154 offset:192\n"
"v_cvt_pk_bf16_f32 v140, v140, v141\n"
"v_mfma_f32_16x16x32_bf16 v[216:219], a[56:59], a[216:219], v[216:219]\n"
"ds_read_b128 a[56:59], v154 offset:128\n"
"v_cvt_pk_bf16_f32 v141, v142, v143\n"
"v_mfma_f32_16x16x32_bf16 v[216:219], a[52:55], a[212:215], v[216:219]\n"
"ds_read_b128 a[52:55], v154 offset:64\n"
"v_sub_f32 v146, v146, v157\n"
"v_mfma_f32_16x16x32_bf16 v[216:219], a[48:51], a[208:211], v[216:219]\n"
"ds_read_b128 a[48:51], v154 offset:0\n"
"v_sub_f32 v147, v147, v157\n"
"v_mfma_f32_16x16x32_bf16 v[252:255], a[252:255], v[124:127], v[148:151]\n"
"v_exp_f32 v145, v145\n"
"v_mfma_f32_16x16x32_bf16 v[252:255], a[252:255], v[132:135], v[252:255]\n"
"v_exp_f32 v146, v146\n"
"v_mfma_f32_16x16x32_bf16 v[220:223], v[132:135], v[52:55], v[220:223]\n"
"v_exp_f32 v147, v147\n"
"v_mfma_f32_16x16x32_bf16 v[228:231], v[132:135], v[76:79], v[228:231]\n"
"v_cvt_pk_bf16_f32 v142, v144, v145\n"
"v_cvt_pk_bf16_f32 v143, v146, v147\n"
"v_mfma_f32_16x16x32_bf16 v[192:195], v[236:239], a[28:31], 0\n"
"v_cvt_pk_bf16_f32 v0, v0, v1\n"
"v_cvt_pk_bf16_f32 v1, v2, v3\n"
"v_mfma_f32_16x16x32_bf16 v[192:195], a[224:227], a[24:27], v[192:195]\n"
"v_cvt_pk_bf16_f32 v9, v10, v11\n"
"v_cvt_pk_bf16_f32 v16, v16, v17\n"
"v_mfma_f32_16x16x32_bf16 v[192:195], v[248:251], a[20:23], v[192:195]\n"
"v_cvt_pk_bf16_f32 v17, v18, v19\n"
"v_cvt_pk_bf16_f32 v204, v204, v208\n"
"v_mfma_f32_16x16x32_bf16 v[192:195], v[244:247], a[16:19], v[192:195]\n"
"ds_read_b128 v[244:247], v153 offset:6144\n"
"v_cvt_pk_bf16_f32 v208, v205, v209\n"
"v_mfma_f32_16x16x32_bf16 v[252:255], a[252:255], v[140:143], v[252:255]\n"
"v_cvt_pk_bf16_f32 v205, v212, v216\n"
"v_cvt_pk_bf16_f32 v209, v213, v217\n"
"v_mfma_f32_16x16x32_bf16 v[220:223], v[140:143], v[56:59], v[220:223]\n"
"v_pk_max_i16 v204, v204, 0\n"
"v_pk_max_i16 v205, v205, 0\n"
"v_mfma_f32_16x16x32_bf16 v[224:227], v[140:143], v[68:71], v[224:227]\n"
"v_pk_max_i16 v208, v208, 0\n"
"v_pk_max_i16 v209, v209, 0\n"
"v_mfma_f32_16x16x32_bf16 v[224:227], v[132:135], v[64:67], v[224:227]\n"
"v_cvt_pk_bf16_f32 v206, v206, v210\n"
"v_cvt_pk_bf16_f32 v210, v207, v211\n"
"v_mfma_f32_16x16x32_bf16 v[224:227], v[124:127], v[60:63], v[224:227]\n"
"buffer_store_dwordx2 v[204:205], v162, s[84:87], s67 offen offset:0 sc0 sc1\n"
"v_mfma_f32_16x16x32_bf16 v[228:231], v[140:143], v[80:83], v[228:231]\n"
"buffer_store_dwordx2 v[208:209], v162, s[84:87], s67 offen offset:256 sc0 sc1\n"
"v_mfma_f32_16x16x32_bf16 v[232:235], v[140:143], v[92:95], v[232:235]\n"
"v_max3_f32 v157, v120, v121, v122\n"
"v_cvt_pk_bf16_f32 v207, v214, v218\n"
"v_mfma_f32_16x16x32_bf16 v[232:235], v[132:135], v[88:91], v[232:235]\n"
"s_add_u32 m0, s28, 0\n"
"v_max3_f32 v159, v123, v96, v97\n"
"v_mfma_f32_16x16x32_bf16 v[232:235], v[124:127], v[84:87], v[232:235]\n"
"s_waitcnt lgkmcnt(1)\n"
"v_cvt_pk_bf16_f32 v211, v215, v219\n"
"v_max3_f32 v157, v157, v98, v99\n"
"v_mfma_f32_16x16x32_bf16 v[24:27], a[96:99], a[48:51], 0\n"
"v_pk_max_i16 v206, v206, 0\n"
"v_max3_f32 v159, v159, v100, v101\n"
"v_mfma_f32_16x16x32_bf16 v[24:27], a[100:103], a[52:55], v[24:27]\n"
"v_pk_max_i16 v207, v207, 0\n"
"v_max3_f32 v157, v157, v102, v103\n"
"v_mfma_f32_16x16x32_bf16 v[24:27], a[104:107], a[56:59], v[24:27]\n"
"v_pk_max_i16 v210, v210, 0\n"
"v_max3_f32 v159, v159, v104, v105\n"
"v_mfma_f32_16x16x32_bf16 v[24:27], a[108:111], a[60:63], v[24:27]\n"
"buffer_store_dwordx2 v[206:207], v162, s[84:87], s67 offen offset:512 sc0 sc1\n"
"v_mfma_f32_16x16x32_bf16 v[176:179], a[112:115], a[48:51], 0\n"
"v_cvt_pk_bf16_f32 v2, v4, v5\n"
"v_cvt_pk_bf16_f32 v3, v6, v7\n"
"v_mfma_f32_16x16x32_bf16 v[176:179], a[116:119], a[52:55], v[176:179]\n"
"v_cvt_pk_bf16_f32 v10, v12, v13\n"
"v_cvt_pk_bf16_f32 v11, v14, v15\n"
"v_mfma_f32_16x16x32_bf16 v[176:179], a[120:123], a[56:59], v[176:179]\n"
"v_cvt_pk_bf16_f32 v18, v20, v21\n"
"v_cvt_pk_bf16_f32 v19, v22, v23\n"
"v_mfma_f32_16x16x32_bf16 v[176:179], a[124:127], a[60:63], v[176:179]\n"
"v_pk_max_i16 v211, v211, 0\n"
"v_max3_f32 v157, v157, v106, v107\n"
"v_mfma_f32_16x16x32_bf16 v[4:7], a[128:131], a[0:3], 0\n"
"v_max3_f32 v159, v159, v108, v109\n"
"v_max3_f32 v157, v157, v110, v111\n"
"v_mfma_f32_16x16x32_bf16 v[4:7], a[132:135], a[4:7], v[4:7]\n"
"v_max3_f32 v159, v159, v112, v113\n"
"v_max3_f32 v157, v157, v114, v115\n"
"v_mfma_f32_16x16x32_bf16 v[4:7], a[136:139], a[8:11], v[4:7]\n"
"v_max3_f32 v159, v159, v116, v117\n"
"v_max3_f32 v157, v157, v118, v119\n"
"v_mfma_f32_16x16x32_bf16 v[4:7], a[140:143], a[12:15], v[4:7]\n"
"buffer_store_dwordx2 v[210:211], v162, s[84:87], s67 offen offset:768 sc0 sc1\n"
"v_mfma_f32_16x16x32_bf16 v[12:15], a[140:143], a[28:31], 0\n"
"v_max_f32 v157, v157, v159\n"
"v_mov_b32 v161, v157\n"
"v_mfma_f32_16x16x32_bf16 v[12:15], a[136:139], a[24:27], v[12:15]\n"
"v_add_f32 v252, v252, v253\n"
"v_cvt_pk_bf16_f32 v24, v24, v25\n"
"v_mfma_f32_16x16x32_bf16 v[12:15], a[132:135], a[20:23], v[12:15]\n"
"v_permlane32_swap_b32 v157, v161\n"
"v_mfma_f32_16x16x32_bf16 v[12:15], a[128:131], a[16:19], v[12:15]\n"
"v_max_f32 v157, v157, v161\n"
"v_mov_b32 v161, v157\n"
"v_mfma_f32_16x16x32_bf16 v[20:23], a[128:131], a[32:35], 0\n"
"v_cvt_pk_bf16_f32 v4, v4, v5\n"
"v_cvt_pk_bf16_f32 v5, v6, v7\n"
"v_mfma_f32_16x16x32_bf16 v[20:23], a[132:135], a[36:39], v[20:23]\n"
"v_permlane16_swap_b32 v157, v161\n"
"v_mfma_f32_16x16x32_bf16 v[20:23], a[136:139], a[40:43], v[20:23]\n"
"v_max_f32 v157, v157, v161\n"
"v_sub_f32 v120, v120, v157\n"
"v_mfma_f32_16x16x32_bf16 v[20:23], a[140:143], a[44:47], v[20:23]\n"
"v_sub_f32 v121, v121, v157\n"
"v_sub_f32 v122, v122, v157\n"
"v_mfma_f32_16x16x32_bf16 v[28:31], a[128:131], a[48:51], 0\n"
"v_sub_f32 v123, v123, v157\n"
"v_cvt_pk_bf16_f32 v12, v12, v13\n"
"v_mfma_f32_16x16x32_bf16 v[28:31], a[132:135], a[52:55], v[28:31]\n"
"v_exp_f32 v161, v122\n"
"v_mfma_f32_16x16x32_bf16 v[28:31], a[136:139], a[56:59], v[28:31]\n"
"v_exp_f32 v120, v120\n"
"v_mfma_f32_16x16x32_bf16 v[28:31], a[140:143], a[60:63], v[28:31]\n"
"v_exp_f32 v121, v121\n"
"v_mfma_f32_16x16x32_bf16 v[200:203], v[248:251], a[36:39], v[200:203]\n"
"ds_read_b128 v[248:251], v153 offset:10240\n"
"v_cvt_pk_bf16_f32 v13, v14, v15\n"
"v_mfma_f32_16x16x32_bf16 v[200:203], a[224:227], a[40:43], v[200:203]\n"
"ds_read_b128 a[224:227], v153 offset:14336\n"
"v_cvt_pk_bf16_f32 v20, v20, v21\n"
"v_mfma_f32_16x16x32_bf16 v[200:203], v[236:239], a[44:47], v[200:203]\n"
"ds_read_b128 v[236:239], v153 offset:3072\n"
"s_add_u32 s24, s24, 13107200\n"
"s_addc_u32 s25, s25, 0\n"
"s_sub_u32 s26, s26, 13107200\n"
"s_cselect_b32 s26, 0, s26\n"
"v_exp_f32 v123, v123\n"
"v_cvt_pk_bf16_f32 v21, v22, v23\n"
"v_cvt_pk_bf16_f32 v122, v120, v121\n"
"v_add_f32 v120, v120, v121\n"
"v_add_f32 v121, v161, v123\n"
"v_cvt_pk_bf16_f32 v123, v161, v123\n"
"v_sub_f32 v96, v96, v157\n"
"v_sub_f32 v97, v97, v157\n"
"v_exp_f32 v96, v96\n"
"v_mfma_f32_16x16x4_4b_bf16 v[204:219], v[122:123], a[244:245], 0 cbsz:2 abid:0\n"
"buffer_load_dwordx4 v152, s[24:27], s40 offen offset:0 lds\n"
"v_sub_f32 v98, v98, v157\n"
"v_exp_f32 v97, v97\n"
"v_sub_f32 v99, v99, v157\n"
"v_exp_f32 v98, v98\n"
"v_exp_f32 v99, v99\n"
"v_sub_f32 v100, v100, v157\n"
"v_sub_f32 v101, v101, v157\n"
"v_exp_f32 v100, v100\n"
"v_cvt_pk_bf16_f32 v96, v96, v97\n"
"v_cvt_pk_bf16_f32 v97, v98, v99\n"
"v_sub_f32 v102, v102, v157\n"
"v_exp_f32 v101, v101\n"
"v_cvt_pk_bf16_f32 v6, v164, v165\n"
"v_cvt_pk_bf16_f32 v7, v166, v167\n"
"v_mfma_f32_16x16x32_bf16 v[164:167], a[144:147], a[48:51], 0\n"
"buffer_load_dwordx4 v152, s[24:27], s41 offen offset:1088 lds\n"
"ds_read_b64 a[244:245], v163 offset:0\n"
"v_mfma_f32_16x16x32_bf16 v[164:167], a[148:151], a[52:55], v[164:167]\n"
"buffer_load_dwordx4 v152, s[24:27], s42 offen offset:2176 lds\n"
"v_cvt_pk_bf16_f32 v14, v168, v169\n"
"v_mfma_f32_16x16x32_bf16 v[164:167], a[152:155], a[56:59], v[164:167]\n"
"buffer_load_dwordx4 v152, s[24:27], s43 offen offset:3264 lds\n"
"v_cvt_pk_bf16_f32 v15, v170, v171\n"
"v_mfma_f32_16x16x32_bf16 v[164:167], a[156:159], a[60:63], v[164:167]\n"
"v_cvt_pk_bf16_f32 v22, v172, v173\n"
"v_cvt_pk_bf16_f32 v23, v174, v175\n"
"v_sub_f32 v103, v103, v157\n"
"v_exp_f32 v102, v102\n"
"v_exp_f32 v103, v103\n"
"v_sub_f32 v104, v104, v157\n"
"v_sub_f32 v105, v105, v157\n"
"v_exp_f32 v104, v104\n"
"v_cvt_pk_bf16_f32 v98, v100, v101\n"
"v_cvt_pk_bf16_f32 v99, v102, v103\n"
"v_sub_f32 v106, v106, v157\n"
"v_exp_f32 v105, v105\n"
"v_mfma_f32_16x16x32_bf16 v[204:207], v[96:99], v[48:51], v[204:207]\n"
"v_sub_f32 v107, v107, v157\n"
"v_sub_f32 v108, v108, v157\n"
"v_mfma_f32_16x16x32_bf16 v[212:215], v[96:99], v[72:75], v[212:215]\n"
"v_exp_f32 v106, v106\n"
"v_exp_f32 v107, v107\n"
"v_sub_f32 v109, v109, v157\n"
"v_exp_f32 v108, v108\n"
"v_cvt_pk_bf16_f32 v104, v104, v105\n"
"v_cvt_pk_bf16_f32 v105, v106, v107\n"
"v_sub_f32 v110, v110, v157\n"
"v_exp_f32 v109, v109\n"
"v_sub_f32 v111, v111, v157\n"
"v_rcp_f32 v253, v252\n"
"v_exp_f32 v110, v110\n"
"v_exp_f32 v111, v111\n"
"v_sub_f32 v112, v112, v157\n"
"v_sub_f32 v113, v113, v157\n"
"v_exp_f32 v112, v112\n"
"v_cvt_pk_bf16_f32 v106, v108, v109\n"
"v_cvt_pk_bf16_f32 v107, v110, v111\n"
"v_sub_f32 v114, v114, v157\n"
"v_exp_f32 v113, v113\n"
"v_mfma_f32_16x16x32_bf16 v[204:207], v[104:107], v[52:55], v[204:207]\n"
"ds_bpermute_b32 v254, v160, v253 offset:0\n"
"v_mfma_f32_16x16x32_bf16 v[212:215], v[104:107], v[76:79], v[212:215]\n"
"v_sub_f32 v115, v115, v157\n"
"v_exp_f32 v114, v114\n"
"v_exp_f32 v115, v115\n"
"v_sub_f32 v116, v116, v157\n"
"v_sub_f32 v117, v117, v157\n"
"v_exp_f32 v116, v116\n"
"v_cvt_pk_bf16_f32 v112, v112, v113\n"
"v_cvt_pk_bf16_f32 v113, v114, v115\n"
"v_sub_f32 v118, v118, v157\n"
"v_exp_f32 v117, v117\n"
"v_sub_f32 v119, v119, v157\n"
"v_exp_f32 v118, v118\n"
"v_exp_f32 v119, v119\n"
"v_cvt_pk_bf16_f32 v114, v116, v117\n"
"v_cvt_pk_bf16_f32 v25, v26, v27\n"
"v_cvt_pk_bf16_f32 v115, v118, v119\n"
"v_cvt_pk_bf16_f32 v180, v180, v181\n"
"v_cvt_pk_bf16_f32 v26, v176, v177\n"
"v_cvt_pk_bf16_f32 v181, v182, v183\n"
"v_mfma_f32_16x16x32_bf16 v[204:207], v[112:115], v[56:59], v[204:207]\n"
"v_cvt_pk_bf16_f32 v27, v178, v179\n"
"v_cvt_pk_bf16_f32 v188, v188, v189\n"
"v_mfma_f32_16x16x32_bf16 v[208:211], v[112:115], v[68:71], v[208:211]\n"
"v_cvt_pk_bf16_f32 v189, v190, v191\n"
"v_cvt_pk_bf16_f32 v196, v196, v197\n"
"v_mfma_f32_16x16x32_bf16 v[208:211], v[104:107], v[64:67], v[208:211]\n"
"v_cvt_pk_bf16_f32 v197, v198, v199\n"
"v_cvt_pk_bf16_f32 v28, v28, v29\n"
"v_mfma_f32_16x16x32_bf16 v[208:211], v[96:99], v[60:63], v[208:211]\n"
"s_waitcnt lgkmcnt(0)\n"
"v_mul_f32_dpp v220, v254, v220 quad_perm:[0,0,0,0] row_mask:0xf bank_mask:0xf\n"
"v_mul_f32_dpp v221, v254, v221 quad_perm:[1,1,1,1] row_mask:0xf bank_mask:0xf\n"
"v_mfma_f32_16x16x32_bf16 v[212:215], v[112:115], v[80:83], v[212:215]\n"
"v_mul_f32_dpp v222, v254, v222 quad_perm:[2,2,2,2] row_mask:0xf bank_mask:0xf\n"
"v_mul_f32_dpp v223, v254, v223 quad_perm:[3,3,3,3] row_mask:0xf bank_mask:0xf\n"
"v_mfma_f32_16x16x32_bf16 v[216:219], v[112:115], v[92:95], v[216:219]\n"
"v_mul_f32_dpp v224, v254, v224 quad_perm:[0,0,0,0] row_mask:0xf bank_mask:0xf\n"
"v_mul_f32_dpp v225, v254, v225 quad_perm:[1,1,1,1] row_mask:0xf bank_mask:0xf\n"
"v_mfma_f32_16x16x32_bf16 v[216:219], v[104:107], v[88:91], v[216:219]\n"
"v_mul_f32_dpp v226, v254, v226 quad_perm:[2,2,2,2] row_mask:0xf bank_mask:0xf\n"
"v_mul_f32_dpp v227, v254, v227 quad_perm:[3,3,3,3] row_mask:0xf bank_mask:0xf\n"
"v_mfma_f32_16x16x32_bf16 v[216:219], v[96:99], v[84:87], v[216:219]\n"
"v_mul_f32_dpp v228, v254, v228 quad_perm:[0,0,0,0] row_mask:0xf bank_mask:0xf\n"
"v_mul_f32_dpp v229, v254, v229 quad_perm:[1,1,1,1] row_mask:0xf bank_mask:0xf\n"
"v_mfma_f32_16x16x32_bf16 v[220:223], a[64:67], a[160:163], v[220:223]\n"
"v_mul_f32_dpp v230, v254, v230 quad_perm:[2,2,2,2] row_mask:0xf bank_mask:0xf\n"
"v_mul_f32_dpp v231, v254, v231 quad_perm:[3,3,3,3] row_mask:0xf bank_mask:0xf\n"
"v_mfma_f32_16x16x32_bf16 v[220:223], a[68:71], a[164:167], v[220:223]\n"
"v_mul_f32_dpp v232, v254, v232 quad_perm:[0,0,0,0] row_mask:0xf bank_mask:0xf\n"
"v_mul_f32_dpp v233, v254, v233 quad_perm:[1,1,1,1] row_mask:0xf bank_mask:0xf\n"
"v_mfma_f32_16x16x32_bf16 v[220:223], a[72:75], a[168:171], v[220:223]\n"
"v_mul_f32_dpp v234, v254, v234 quad_perm:[2,2,2,2] row_mask:0xf bank_mask:0xf\n"
"v_mul_f32_dpp v235, v254, v235 quad_perm:[3,3,3,3] row_mask:0xf bank_mask:0xf\n"
"v_mfma_f32_16x16x32_bf16 v[220:223], a[76:79], a[172:175], v[220:223]\n"
"v_cvt_pk_bf16_f32 v182, v184, v185\n"
"v_cvt_pk_bf16_f32 v29, v30, v31\n"
"v_mfma_f32_16x16x32_bf16 v[224:227], a[76:79], a[188:191], v[224:227]\n"
"v_cvt_pk_bf16_f32 v183, v186, v187\n"
"v_cvt_pk_bf16_f32 v190, v192, v193\n"
"v_mfma_f32_16x16x32_bf16 v[224:227], a[72:75], a[184:187], v[224:227]\n"
"v_cvt_pk_bf16_f32 v191, v194, v195\n"
"v_cvt_pk_bf16_f32 v198, v200, v201\n"
"v_mfma_f32_16x16x32_bf16 v[224:227], a[68:71], a[180:183], v[224:227]\n"
"v_cvt_pk_bf16_f32 v199, v202, v203\n"
"v_cvt_pk_bf16_f32 v30, v164, v165\n"
"v_mfma_f32_16x16x32_bf16 v[224:227], a[64:67], a[176:179], v[224:227]\n"
"v_cvt_pk_bf16_f32 v31, v166, v167\n"
"v_add_u32 v156, 512, v156\n"
"v_mfma_f32_16x16x32_bf16 v[228:231], a[64:67], a[192:195], v[228:231]\n"
"v_mfma_f32_16x16x32_bf16 v[228:231], a[68:71], a[196:199], v[228:231]\n"
"v_mfma_f32_16x16x32_bf16 v[228:231], a[72:75], a[200:203], v[228:231]\n"
"v_cvt_pk_bf16_f32 v220, v220, v224\n"
"v_cvt_pk_bf16_f32 v224, v221, v225\n"
"v_mfma_f32_16x16x32_bf16 v[228:231], a[76:79], a[204:207], v[228:231]\n"
"v_pk_max_i16 v220, v220, 0\n"
"v_pk_max_i16 v224, v224, 0\n"
"v_mfma_f32_16x16x32_bf16 v[232:235], a[76:79], a[220:223], v[232:235]\n"
"s_waitcnt vmcnt(18)\n"
"ds_read_b128 a[76:79], v154 offset:4544\n"
"v_cvt_pk_bf16_f32 v222, v222, v226\n"
"v_mfma_f32_16x16x32_bf16 v[232:235], a[72:75], a[216:219], v[232:235]\n"
"ds_read_b128 a[72:75], v154 offset:4480\n"
"v_cvt_pk_bf16_f32 v226, v223, v227\n"
"v_mfma_f32_16x16x32_bf16 v[232:235], a[68:71], a[212:215], v[232:235]\n"
"ds_read_b128 a[68:71], v154 offset:4416\n"
"v_pk_max_i16 v222, v222, 0\n"
"v_mfma_f32_16x16x32_bf16 v[232:235], a[64:67], a[208:211], v[232:235]\n"
"ds_read_b128 a[64:67], v154 offset:4352\n"
"v_pk_max_i16 v226, v226, 0\n"
"v_mfma_f32_16x16x32_bf16 v[252:255], a[252:255], v[96:99], v[120:123]\n"
"v_mfma_f32_16x16x32_bf16 v[252:255], a[252:255], v[104:107], v[252:255]\n"
"v_mfma_f32_16x16x32_bf16 v[252:255], a[252:255], v[112:115], v[252:255]\n"
"v_cvt_pk_bf16_f32 v221, v228, v232\n"
"v_cvt_pk_bf16_f32 v225, v229, v233\n"
"v_mfma_f32_16x16x32_bf16 v[184:187], v[240:243], a[0:3], 0\n"
"v_pk_max_i16 v221, v221, 0\n"
"v_pk_max_i16 v225, v225, 0\n"
"v_mfma_f32_16x16x32_bf16 v[184:187], v[244:247], a[4:7], v[184:187]\n"
"v_cvt_pk_bf16_f32 v223, v230, v234\n"
"v_cvt_pk_bf16_f32 v227, v231, v235\n"
"v_mfma_f32_16x16x32_bf16 v[184:187], v[248:251], a[8:11], v[184:187]\n"
"v_pk_max_i16 v223, v223, 0\n"
"v_pk_max_i16 v227, v227, 0\n"
"v_mfma_f32_16x16x32_bf16 v[184:187], a[224:227], a[12:15], v[184:187]\n"
"s_waitcnt lgkmcnt(3)\n"
"buffer_store_dwordx2 v[220:221], v162, s[84:87], s68 offen offset:0 sc0 sc1\n"
"v_mfma_f32_16x16x32_bf16 v[32:35], a[108:111], a[76:79], 0\n"
"s_waitcnt lgkmcnt(2)\n"
"v_add_f32 v252, v252, v253\n"
"v_mfma_f32_16x16x32_bf16 v[32:35], a[104:107], a[72:75], v[32:35]\n"
"s_waitcnt lgkmcnt(1)\n"
"v_rcp_f32 v253, v252\n"
"v_mfma_f32_16x16x32_bf16 v[32:35], a[100:103], a[68:71], v[32:35]\n"
"s_waitcnt lgkmcnt(0)\n"
"v_cvt_pk_bf16_f32 v184, v184, v185\n"
"v_cvt_pk_bf16_f32 v185, v186, v187\n"
"v_mfma_f32_16x16x32_bf16 v[32:35], a[96:99], a[64:67], v[32:35]\n"
"buffer_store_dwordx2 v[224:225], v162, s[84:87], s68 offen offset:256 sc0 sc1\n"
"v_mfma_f32_16x16x32_bf16 v[172:175], a[124:127], a[76:79], 0\n"
"ds_bpermute_b32 v254, v160, v253 offset:0\n"
"v_mfma_f32_16x16x32_bf16 v[172:175], a[120:123], a[72:75], v[172:175]\n"
"v_mfma_f32_16x16x32_bf16 v[172:175], a[116:119], a[68:71], v[172:175]\n"
"v_cvt_pk_bf16_f32 v32, v32, v33\n"
"v_cvt_pk_bf16_f32 v33, v34, v35\n"
"v_mfma_f32_16x16x32_bf16 v[172:175], a[112:115], a[64:67], v[172:175]\n"
"buffer_store_dwordx2 v[222:223], v162, s[84:87], s68 offen offset:512 sc0 sc1\n"
"v_mfma_f32_16x16x32_bf16 v[36:39], a[140:143], a[76:79], 0\n"
"s_add_u32 m0, s28, 4352\n"
"v_mfma_f32_16x16x32_bf16 v[36:39], a[136:139], a[72:75], v[36:39]\n"
"buffer_load_dwordx4 v152, s[24:27], s44 offen offset:0 lds\n"
"v_mfma_f32_16x16x32_bf16 v[36:39], a[132:135], a[68:71], v[36:39]\n"
"buffer_load_dwordx4 v152, s[24:27], s45 offen offset:1088 lds\n"
"v_cvt_pk_bf16_f32 v34, v172, v173\n"
"v_mfma_f32_16x16x32_bf16 v[36:39], a[128:131], a[64:67], v[36:39]\n"
"buffer_load_dwordx4 v152, s[24:27], s46 offen offset:2176 lds\n"
"v_cvt_pk_bf16_f32 v35, v174, v175\n"
"v_mfma_f32_16x16x32_bf16 v[192:195], a[224:227], a[28:31], 0\n"
"buffer_load_dwordx4 v152, s[24:27], s47 offen offset:3264 lds\n"
"v_mfma_f32_16x16x32_bf16 v[192:195], v[248:251], a[24:27], v[192:195]\n"
"v_mfma_f32_16x16x32_bf16 v[192:195], v[244:247], a[20:23], v[192:195]\n"
"v_cvt_pk_bf16_f32 v36, v36, v37\n"
"v_cvt_pk_bf16_f32 v37, v38, v39\n"
"v_mfma_f32_16x16x32_bf16 v[192:195], v[240:243], a[16:19], v[192:195]\n"
"buffer_store_dwordx2 v[226:227], v162, s[84:87], s68 offen offset:768 sc0 sc1\n"
"v_mfma_f32_16x16x32_bf16 v[168:171], a[156:159], a[76:79], 0\n"
"s_waitcnt lgkmcnt(0)\n"
"v_mul_f32_dpp v204, v254, v204 quad_perm:[0,0,0,0] row_mask:0xf bank_mask:0xf\n"
"v_mul_f32_dpp v205, v254, v205 quad_perm:[1,1,1,1] row_mask:0xf bank_mask:0xf\n"
"v_mfma_f32_16x16x32_bf16 v[168:171], a[152:155], a[72:75], v[168:171]\n"
"v_mul_f32_dpp v206, v254, v206 quad_perm:[2,2,2,2] row_mask:0xf bank_mask:0xf\n"
"v_mul_f32_dpp v207, v254, v207 quad_perm:[3,3,3,3] row_mask:0xf bank_mask:0xf\n"
"v_mfma_f32_16x16x32_bf16 v[168:171], a[148:151], a[68:71], v[168:171]\n"
"v_cvt_pk_bf16_f32 v192, v192, v193\n"
"v_cvt_pk_bf16_f32 v193, v194, v195\n"
"v_mfma_f32_16x16x32_bf16 v[168:171], a[144:147], a[64:67], v[168:171]\n"
"v_mul_f32_dpp v208, v254, v208 quad_perm:[0,0,0,0] row_mask:0xf bank_mask:0xf\n"
"v_mul_f32_dpp v209, v254, v209 quad_perm:[1,1,1,1] row_mask:0xf bank_mask:0xf\n"
"v_mfma_f32_16x16x32_bf16 v[200:203], v[240:243], a[32:35], 0\n"
"ds_read_b128 v[240:243], v153 offset:7168\n"
"v_mul_f32_dpp v210, v254, v210 quad_perm:[2,2,2,2] row_mask:0xf bank_mask:0xf\n"
"v_mfma_f32_16x16x32_bf16 v[200:203], v[244:247], a[36:39], v[200:203]\n"
"ds_read_b128 v[244:247], v153 offset:11264\n"
"v_mul_f32_dpp v211, v254, v211 quad_perm:[3,3,3,3] row_mask:0xf bank_mask:0xf\n"
"v_mfma_f32_16x16x32_bf16 v[200:203], v[248:251], a[40:43], v[200:203]\n"
"ds_read_b128 v[248:251], v153 offset:15360\n"
"v_cvt_pk_bf16_f32 v38, v168, v169\n"
"v_mfma_f32_16x16x32_bf16 v[200:203], a[224:227], a[44:47], v[200:203]\n"
"ds_read_b128 a[224:227], v153 offset:18496\n"
"v_cvt_pk_bf16_f32 v39, v170, v171\n"
"v_mfma_f32_16x16x32_bf16 v[164:167], v[236:239], a[0:3], 0\n"
"v_mul_f32_dpp v212, v254, v212 quad_perm:[0,0,0,0] row_mask:0xf bank_mask:0xf\n"
"v_mul_f32_dpp v213, v254, v213 quad_perm:[1,1,1,1] row_mask:0xf bank_mask:0xf\n"
"v_mfma_f32_16x16x32_bf16 v[172:175], v[236:239], a[32:35], 0\n"
"v_mul_f32_dpp v214, v254, v214 quad_perm:[2,2,2,2] row_mask:0xf bank_mask:0xf\n"
"v_mul_f32_dpp v215, v254, v215 quad_perm:[3,3,3,3] row_mask:0xf bank_mask:0xf\n"
"v_mfma_f32_16x16x32_bf16 v[204:207], a[80:83], a[160:163], v[204:207]\n"
"v_cvt_pk_bf16_f32 v200, v200, v201\n"
"v_cvt_pk_bf16_f32 v201, v202, v203\n"
"v_mfma_f32_16x16x32_bf16 v[204:207], a[84:87], a[164:167], v[204:207]\n"
"v_mul_f32_dpp v216, v254, v216 quad_perm:[0,0,0,0] row_mask:0xf bank_mask:0xf\n"
"v_mul_f32_dpp v217, v254, v217 quad_perm:[1,1,1,1] row_mask:0xf bank_mask:0xf\n"
"v_mfma_f32_16x16x32_bf16 v[204:207], a[88:91], a[168:171], v[204:207]\n"
"v_mul_f32_dpp v218, v254, v218 quad_perm:[2,2,2,2] row_mask:0xf bank_mask:0xf\n"
"v_mul_f32_dpp v219, v254, v219 quad_perm:[3,3,3,3] row_mask:0xf bank_mask:0xf\n"
"v_mfma_f32_16x16x32_bf16 v[204:207], a[92:95], a[172:175], v[204:207]\n"
"v_mfma_f32_16x16x32_bf16 v[208:211], a[92:95], a[188:191], v[208:211]\n"
"v_mfma_f32_16x16x32_bf16 v[208:211], a[88:91], a[184:187], v[208:211]\n"
"v_mfma_f32_16x16x32_bf16 v[208:211], a[84:87], a[180:183], v[208:211]\n"
"v_mfma_f32_16x16x32_bf16 v[208:211], a[80:83], a[176:179], v[208:211]\n"
"s_waitcnt lgkmcnt(3)\n"
"v_mfma_f32_16x16x32_bf16 v[164:167], v[240:243], a[4:7], v[164:167]\n"
"s_waitcnt lgkmcnt(2)\n"
"v_mfma_f32_16x16x32_bf16 v[164:167], v[244:247], a[8:11], v[164:167]\n"
"s_waitcnt lgkmcnt(1)\n"
"v_mfma_f32_16x16x32_bf16 v[164:167], v[248:251], a[12:15], v[164:167]\n"
"v_cvt_pk_bf16_f32 v204, v204, v208\n"
"v_cvt_pk_bf16_f32 v208, v205, v209\n"
"v_mfma_f32_16x16x32_bf16 v[168:171], v[248:251], a[28:31], 0\n"
"v_pk_max_i16 v204, v204, 0\n"
"v_pk_max_i16 v208, v208, 0\n"
"v_mfma_f32_16x16x32_bf16 v[168:171], v[244:247], a[24:27], v[168:171]\n"
"v_cvt_pk_bf16_f32 v206, v206, v210\n"
"v_cvt_pk_bf16_f32 v210, v207, v211\n"
"v_mfma_f32_16x16x32_bf16 v[168:171], v[240:243], a[20:23], v[168:171]\n"
"v_cvt_pk_bf16_f32 v186, v164, v165\n"
"v_cvt_pk_bf16_f32 v187, v166, v167\n"
"v_mfma_f32_16x16x32_bf16 v[168:171], v[236:239], a[16:19], v[168:171]\n"
"ds_read_b128 v[236:239], v153 offset:16384\n"
"v_pk_max_i16 v206, v206, 0\n"
"v_mfma_f32_16x16x32_bf16 v[172:175], v[240:243], a[36:39], v[172:175]\n"
"ds_read_b128 v[240:243], v153 offset:20576\n"
"v_pk_max_i16 v210, v210, 0\n"
"v_mfma_f32_16x16x32_bf16 v[172:175], v[244:247], a[40:43], v[172:175]\n"
"ds_read_b128 v[244:247], v153 offset:24768\n"
"v_mfma_f32_16x16x32_bf16 v[172:175], v[248:251], a[44:47], v[172:175]\n"
"ds_read_b128 v[248:251], v153 offset:28960\n"
"v_cvt_pk_bf16_f32 v194, v168, v169\n"
"v_mfma_f32_16x16x32_bf16 v[212:215], a[80:83], a[192:195], v[212:215]\n"
"v_cvt_pk_bf16_f32 v195, v170, v171\n"
"v_mfma_f32_16x16x32_bf16 v[212:215], a[84:87], a[196:199], v[212:215]\n"
"v_mfma_f32_16x16x32_bf16 v[212:215], a[88:91], a[200:203], v[212:215]\n"
"v_cvt_pk_bf16_f32 v202, v172, v173\n"
"v_cvt_pk_bf16_f32 v203, v174, v175\n"
"v_mfma_f32_16x16x32_bf16 v[212:215], a[92:95], a[204:207], v[212:215]\n"
"v_mfma_f32_16x16x32_bf16 v[216:219], a[92:95], a[220:223], v[216:219]\n"
"s_waitcnt vmcnt(21)\n"
"ds_read_b128 a[92:95], v154 offset:8896\n"
"v_mfma_f32_16x16x32_bf16 v[216:219], a[88:91], a[216:219], v[216:219]\n"
"ds_read_b128 a[88:91], v154 offset:8832\n"
"v_mfma_f32_16x16x32_bf16 v[216:219], a[84:87], a[212:215], v[216:219]\n"
"ds_read_b128 a[84:87], v154 offset:8768\n"
"v_mfma_f32_16x16x32_bf16 v[216:219], a[80:83], a[208:211], v[216:219]\n"
"ds_read_b128 a[80:83], v154 offset:8704\n"
"v_mfma_f32_16x16x32_bf16 v[120:123], a[228:231], v[180:183], 0\n"
"ds_read_b128 a[228:231], v158 offset:0\n"
"v_mfma_f32_16x16x32_bf16 v[120:123], a[232:235], v[184:187], v[120:123]\n"
"ds_read_b128 a[232:235], v158 offset:2048\n"
"v_mfma_f32_16x16x32_bf16 v[96:99], v[0:3], v[180:183], 0\n"
"v_cvt_pk_bf16_f32 v205, v212, v216\n"
"v_cvt_pk_bf16_f32 v209, v213, v217\n"
"v_mfma_f32_16x16x32_bf16 v[96:99], v[4:7], v[184:187], v[96:99]\n"
"v_pk_max_i16 v205, v205, 0\n"
"v_pk_max_i16 v209, v209, 0\n"
"v_mfma_f32_16x16x32_bf16 v[100:103], v[12:15], v[184:187], 0\n"
"v_cvt_pk_bf16_f32 v207, v214, v218\n"
"v_cvt_pk_bf16_f32 v211, v215, v219\n"
"v_mfma_f32_16x16x32_bf16 v[100:103], v[8:11], v[180:183], v[100:103]\n"
"buffer_store_dwordx2 v[204:205], v162, s[84:87], s69 offen offset:0 sc0 sc1\n"
"v_mfma_f32_16x16x32_bf16 v[104:107], v[16:19], v[180:183], 0\n"
"v_pk_max_i16 v207, v207, 0\n"
"v_pk_max_i16 v211, v211, 0\n"
"v_mfma_f32_16x16x32_bf16 v[104:107], v[20:23], v[184:187], v[104:107]\n"
"buffer_store_dwordx2 v[208:209], v162, s[84:87], s69 offen offset:256 sc0 sc1\n"
"v_mfma_f32_16x16x32_bf16 v[108:111], v[28:31], v[184:187], 0\n"
"v_max3_f32 v157, v120, v121, v122\n"
"v_max3_f32 v159, v123, v96, v97\n"
"v_mfma_f32_16x16x32_bf16 v[108:111], v[24:27], v[180:183], v[108:111]\n"
"s_waitcnt lgkmcnt(2)\n"
"buffer_store_dwordx2 v[206:207], v162, s[84:87], s69 offen offset:512 sc0 sc1\n"
"v_mfma_f32_16x16x32_bf16 v[40:43], a[96:99], a[80:83], 0\n"
"v_max3_f32 v157, v157, v98, v99\n"
"v_max3_f32 v159, v159, v100, v101\n"
"v_mfma_f32_16x16x32_bf16 v[40:43], a[100:103], a[84:87], v[40:43]\n"
"v_max3_f32 v157, v157, v102, v103\n"
"v_max3_f32 v159, v159, v104, v105\n"
"v_mfma_f32_16x16x32_bf16 v[40:43], a[104:107], a[88:91], v[40:43]\n"
"v_max3_f32 v157, v157, v106, v107\n"
"v_max3_f32 v159, v159, v108, v109\n"
"v_mfma_f32_16x16x32_bf16 v[40:43], a[108:111], a[92:95], v[40:43]\n"
"buffer_store_dwordx2 v[210:211], v162, s[84:87], s69 offen offset:768 sc0 sc1\n"
"s_branch .Lp2s%=\n"
".Leps%=:\n"
"s_nop 0\n"
"v_mfma_f32_16x16x32_bf16 v[204:207], a[32:35], a[160:163], v[204:207]\n"
"v_sub_f32 v129, v129, v157\n"
"v_sub_f32 v130, v130, v157\n"
"v_mfma_f32_16x16x32_bf16 v[204:207], a[36:39], a[164:167], v[204:207]\n"
"v_sub_f32 v131, v131, v157\n"
"v_sub_f32 v132, v132, v157\n"
"v_mfma_f32_16x16x32_bf16 v[204:207], a[40:43], a[168:171], v[204:207]\n"
"v_exp_f32 v128, v128\n"
"v_mfma_f32_16x16x32_bf16 v[204:207], a[44:47], a[172:175], v[204:207]\n"
"v_exp_f32 v129, v129\n"
"v_exp_f32 v130, v130\n"
"v_exp_f32 v131, v131\n"
"v_sub_f32 v133, v133, v157\n"
"v_cvt_pk_bf16_f32 v124, v124, v125\n"
"v_exp_f32 v132, v132\n"
"v_sub_f32 v134, v134, v157\n"
"v_exp_f32 v133, v133\n"
"v_sub_f32 v135, v135, v157\n"
"v_cvt_pk_bf16_f32 v125, v126, v127\n"
"v_exp_f32 v134, v134\n"
"v_exp_f32 v135, v135\n"
"v_sub_f32 v136, v136, v157\n"
"v_sub_f32 v137, v137, v157\n"
"v_sub_f32 v138, v138, v157\n"
"v_sub_f32 v139, v139, v157\n"
"v_cvt_pk_bf16_f32 v126, v128, v129\n"
"v_cvt_pk_bf16_f32 v127, v130, v131\n"
"v_mfma_f32_16x16x32_bf16 v[128:131], v[12:15], v[192:195], 0\n"
"v_exp_f32 v136, v136\n"
"v_mfma_f32_16x16x32_bf16 v[128:131], v[8:11], v[188:191], v[128:131]\n"
"v_exp_f32 v137, v137\n"
"v_mfma_f32_16x16x32_bf16 v[164:167], v[124:127], v[48:51], v[164:167]\n"
"v_exp_f32 v138, v138\n"
"v_mfma_f32_16x16x32_bf16 v[172:175], v[124:127], v[72:75], v[172:175]\n"
"v_exp_f32 v139, v139\n"
"v_sub_f32 v140, v140, v157\n"
"v_sub_f32 v141, v141, v157\n"
"v_cvt_pk_bf16_f32 v132, v132, v133\n"
"v_exp_f32 v140, v140\n"
"v_sub_f32 v142, v142, v157\n"
"v_exp_f32 v141, v141\n"
"v_sub_f32 v143, v143, v157\n"
"v_mul_f32_dpp v208, v254, v208 quad_perm:[0,0,0,0] row_mask:0xf bank_mask:0xf\n"
"v_mul_f32_dpp v209, v254, v209 quad_perm:[1,1,1,1] row_mask:0xf bank_mask:0xf\n"
"v_mul_f32_dpp v210, v254, v210 quad_perm:[2,2,2,2] row_mask:0xf bank_mask:0xf\n"
"v_mul_f32_dpp v211, v254, v211 quad_perm:[3,3,3,3] row_mask:0xf bank_mask:0xf\n"
"v_mul_f32_dpp v212, v254, v212 quad_perm:[0,0,0,0] row_mask:0xf bank_mask:0xf\n"
"v_mul_f32_dpp v213, v254, v213 quad_perm:[1,1,1,1] row_mask:0xf bank_mask:0xf\n"
"v_mul_f32_dpp v214, v254, v214 quad_perm:[2,2,2,2] row_mask:0xf bank_mask:0xf\n"
"v_mfma_f32_16x16x32_bf16 v[208:211], a[44:47], a[188:191], v[208:211]\n"
"v_mul_f32_dpp v215, v254, v215 quad_perm:[3,3,3,3] row_mask:0xf bank_mask:0xf\n"
"v_mul_f32_dpp v216, v254, v216 quad_perm:[0,0,0,0] row_mask:0xf bank_mask:0xf\n"
"v_mfma_f32_16x16x32_bf16 v[208:211], a[40:43], a[184:187], v[208:211]\n"
"v_mul_f32_dpp v217, v254, v217 quad_perm:[1,1,1,1] row_mask:0xf bank_mask:0xf\n"
"v_mul_f32_dpp v218, v254, v218 quad_perm:[2,2,2,2] row_mask:0xf bank_mask:0xf\n"
"v_mfma_f32_16x16x32_bf16 v[208:211], a[36:39], a[180:183], v[208:211]\n"
"v_mul_f32_dpp v219, v254, v219 quad_perm:[3,3,3,3] row_mask:0xf bank_mask:0xf\n"
"v_cvt_pk_bf16_f32 v133, v134, v135\n"
"v_mfma_f32_16x16x32_bf16 v[208:211], a[32:35], a[176:179], v[208:211]\n"
"v_exp_f32 v142, v142\n"
"v_mfma_f32_16x16x32_bf16 v[252:255], a[252:255], v[124:127], v[148:151]\n"
"v_exp_f32 v143, v143\n"
"v_mfma_f32_16x16x32_bf16 v[212:215], a[32:35], a[192:195], v[212:215]\n"
"v_sub_f32 v144, v144, v157\n"
"v_sub_f32 v145, v145, v157\n"
"v_mfma_f32_16x16x32_bf16 v[212:215], a[36:39], a[196:199], v[212:215]\n"
"v_sub_f32 v146, v146, v157\n"
"v_sub_f32 v147, v147, v157\n"
"v_mfma_f32_16x16x32_bf16 v[212:215], a[40:43], a[200:203], v[212:215]\n"
"v_cvt_pk_bf16_f32 v134, v136, v137\n"
"v_cvt_pk_bf16_f32 v135, v138, v139\n"
"v_mfma_f32_16x16x32_bf16 v[212:215], a[44:47], a[204:207], v[212:215]\n"
"v_exp_f32 v144, v144\n"
"v_mfma_f32_16x16x32_bf16 v[252:255], a[252:255], v[132:135], v[252:255]\n"
"v_exp_f32 v145, v145\n"
"v_mfma_f32_16x16x32_bf16 v[164:167], v[132:135], v[52:55], v[164:167]\n"
"v_exp_f32 v146, v146\n"
"v_mfma_f32_16x16x32_bf16 v[172:175], v[132:135], v[76:79], v[172:175]\n"
"v_exp_f32 v147, v147\n"
"v_mfma_f32_16x16x32_bf16 v[216:219], a[44:47], a[220:223], v[216:219]\n"
"v_cvt_pk_bf16_f32 v140, v140, v141\n"
"v_cvt_pk_bf16_f32 v141, v142, v143\n"
"v_mfma_f32_16x16x32_bf16 v[216:219], a[40:43], a[216:219], v[216:219]\n"
"v_cvt_pk_bf16_f32 v142, v144, v145\n"
"v_cvt_pk_bf16_f32 v143, v146, v147\n"
"v_mfma_f32_16x16x32_bf16 v[216:219], a[36:39], a[212:215], v[216:219]\n"
"v_max3_f32 v157, v120, v121, v122\n"
"v_max3_f32 v159, v123, v96, v97\n"
"v_mfma_f32_16x16x32_bf16 v[216:219], a[32:35], a[208:211], v[216:219]\n"
"v_max3_f32 v157, v157, v98, v99\n"
"v_max3_f32 v159, v159, v100, v101\n"
"v_mfma_f32_16x16x32_bf16 v[252:255], a[252:255], v[140:143], v[252:255]\n"
"v_max3_f32 v157, v157, v102, v103\n"
"v_max3_f32 v159, v159, v104, v105\n"
"v_mfma_f32_16x16x32_bf16 v[168:171], v[140:143], v[68:71], v[168:171]\n"
"v_max3_f32 v157, v157, v106, v107\n"
"v_max3_f32 v159, v159, v108, v109\n"
"v_mfma_f32_16x16x32_bf16 v[168:171], v[132:135], v[64:67], v[168:171]\n"
"v_max3_f32 v157, v157, v110, v111\n"
"v_max3_f32 v159, v159, v112, v113\n"
"v_mfma_f32_16x16x32_bf16 v[168:171], v[124:127], v[60:63], v[168:171]\n"
"v_add_f32 v252, v252, v253\n"
"v_max3_f32 v157, v157, v114, v115\n"
"v_mfma_f32_16x16x32_bf16 v[176:179], v[140:143], v[92:95], v[176:179]\n"
"v_rcp_f32 v253, v252\n"
"v_mfma_f32_16x16x32_bf16 v[176:179], v[132:135], v[88:91], v[176:179]\n"
"ds_bpermute_b32 v254, v160, v253 offset:0\n"
"v_mfma_f32_16x16x32_bf16 v[176:179], v[124:127], v[84:87], v[176:179]\n"
"v_max3_f32 v159, v159, v116, v117\n"
"v_max3_f32 v157, v157, v118, v119\n"
"v_mfma_f32_16x16x32_bf16 v[164:167], v[140:143], v[56:59], v[164:167]\n"
"v_max_f32 v157, v157, v159\n"
"v_mov_b32 v161, v157\n"
"v_mfma_f32_16x16x32_bf16 v[172:175], v[140:143], v[80:83], v[172:175]\n"
"v_lshlrev_b32 v222, 16, v223\n"
"v_and_b32 v223, 0xffff0000, v223\n"
"v_mfma_f32_16x16x32_bf16 v[148:151], a[228:231], v[188:191], 0\n"
"s_waitcnt lgkmcnt(1)\n"
"ds_read_b128 a[228:231], v158 offset:0\n"
"v_cvt_pk_bf16_f32 v204, v204, v208\n"
"v_mfma_f32_16x16x32_bf16 v[148:151], a[232:235], v[192:195], v[148:151]\n"
"ds_read_b128 a[232:235], v158 offset:2048\n"
"v_cvt_pk_bf16_f32 v206, v206, v210\n"
"v_mfma_f32_16x16x32_bf16 v[124:127], v[0:3], v[188:191], 0\n"
"v_permlane32_swap_b32 v157, v161\n"
"v_mfma_f32_16x16x32_bf16 v[124:127], v[4:7], v[192:195], v[124:127]\n"
"v_max_f32 v157, v157, v161\n"
"v_mov_b32 v161, v157\n"
"v_mfma_f32_16x16x32_bf16 v[132:135], v[16:19], v[188:191], 0\n"
"v_cvt_pk_bf16_f32 v208, v205, v209\n"
"v_cvt_pk_bf16_f32 v210, v207, v211\n"
"v_mfma_f32_16x16x32_bf16 v[132:135], v[20:23], v[192:195], v[132:135]\n"
"v_permlane16_swap_b32 v157, v161\n"
"v_mfma_f32_16x16x32_bf16 v[136:139], v[28:31], v[192:195], 0\n"
"v_max_f32 v157, v157, v161\n"
"v_sub_f32 v120, v120, v157\n"
"v_mfma_f32_16x16x32_bf16 v[136:139], v[24:27], v[188:191], v[136:139]\n"
"v_sub_f32 v121, v121, v157\n"
"v_sub_f32 v96, v96, v157\n"
"v_mfma_f32_16x16x32_bf16 v[140:143], v[32:35], v[188:191], 0\n"
"v_exp_f32 v120, v120\n"
"v_mfma_f32_16x16x32_bf16 v[140:143], v[36:39], v[192:195], v[140:143]\n"
"v_exp_f32 v121, v121\n"
"v_mfma_f32_16x16x32_bf16 v[144:147], v[44:47], v[192:195], 0\n"
"v_sub_f32 v97, v97, v157\n"
"v_sub_f32 v122, v122, v157\n"
"v_mfma_f32_16x16x32_bf16 v[144:147], v[40:43], v[188:191], v[144:147]\n"
"v_sub_f32 v123, v123, v157\n"
"v_exp_f32 v96, v96\n"
"v_sub_f32 v98, v98, v157\n"
"v_exp_f32 v97, v97\n"
"v_sub_f32 v99, v99, v157\n"
"v_exp_f32 v161, v122\n"
"v_exp_f32 v123, v123\n"
"v_exp_f32 v98, v98\n"
"v_exp_f32 v99, v99\n"
"v_sub_f32 v100, v100, v157\n"
"v_sub_f32 v101, v101, v157\n"
"v_sub_f32 v102, v102, v157\n"
"v_sub_f32 v103, v103, v157\n"
"v_exp_f32 v100, v100\n"
"v_exp_f32 v101, v101\n"
"v_exp_f32 v102, v102\n"
"v_exp_f32 v103, v103\n"
"v_cvt_pk_bf16_f32 v122, v120, v121\n"
"v_sub_f32 v104, v104, v157\n"
"v_sub_f32 v105, v105, v157\n"
"v_add_f32 v120, v120, v121\n"
"v_cvt_pk_bf16_f32 v96, v96, v97\n"
"v_exp_f32 v104, v104\n"
"v_sub_f32 v106, v106, v157\n"
"v_exp_f32 v105, v105\n"
"v_sub_f32 v107, v107, v157\n"
"v_add_f32 v121, v161, v123\n"
"v_cvt_pk_bf16_f32 v97, v98, v99\n"
"v_exp_f32 v106, v106\n"
"v_exp_f32 v107, v107\n"
"v_sub_f32 v108, v108, v157\n"
"v_sub_f32 v109, v109, v157\n"
"v_sub_f32 v110, v110, v157\n"
"v_sub_f32 v111, v111, v157\n"
"v_cvt_pk_bf16_f32 v123, v161, v123\n"
"v_cvt_pk_bf16_f32 v98, v100, v101\n"
"v_cvt_pk_bf16_f32 v99, v102, v103\n"
"v_mfma_f32_16x16x32_bf16 v[100:103], v[12:15], v[200:203], 0\n"
"v_exp_f32 v108, v108\n"
"v_mfma_f32_16x16x32_bf16 v[100:103], v[8:11], v[196:199], v[100:103]\n"
"v_exp_f32 v109, v109\n"
"v_exp_f32 v110, v110\n"
"v_exp_f32 v111, v111\n"
"v_sub_f32 v112, v112, v157\n"
"v_sub_f32 v113, v113, v157\n"
"v_cvt_pk_bf16_f32 v104, v104, v105\n"
"v_exp_f32 v112, v112\n"
"v_sub_f32 v114, v114, v157\n"
"v_exp_f32 v113, v113\n"
"v_sub_f32 v115, v115, v157\n"
"s_waitcnt lgkmcnt(0)\n"
"v_fmac_f32_dpp v220, v254, v164 quad_perm:[0,0,0,0] row_mask:0xf bank_mask:0xf\n"
"v_fmac_f32_dpp v221, v254, v168 quad_perm:[0,0,0,0] row_mask:0xf bank_mask:0xf\n"
"v_fmac_f32_dpp v222, v254, v172 quad_perm:[0,0,0,0] row_mask:0xf bank_mask:0xf\n"
"v_fmac_f32_dpp v223, v254, v176 quad_perm:[0,0,0,0] row_mask:0xf bank_mask:0xf\n"
"v_mfma_f32_16x16x32_bf16 v[252:255], a[252:255], v[96:99], v[120:123]\n"
"v_cvt_pk_bf16_f32 v105, v106, v107\n"
"v_exp_f32 v114, v114\n"
"v_exp_f32 v115, v115\n"
"v_sub_f32 v116, v116, v157\n"
"v_sub_f32 v117, v117, v157\n"
"v_sub_f32 v118, v118, v157\n"
"v_sub_f32 v119, v119, v157\n"
"v_cvt_pk_bf16_f32 v106, v108, v109\n"
"v_cvt_pk_bf16_f32 v107, v110, v111\n"
"v_mfma_f32_16x16x32_bf16 v[108:111], v[28:31], v[200:203], 0\n"
"v_exp_f32 v116, v116\n"
"v_mfma_f32_16x16x32_bf16 v[108:111], v[24:27], v[196:199], v[108:111]\n"
"v_exp_f32 v117, v117\n"
"v_mfma_f32_16x16x32_bf16 v[252:255], a[252:255], v[104:107], v[252:255]\n"
"v_exp_f32 v118, v118\n"
"v_exp_f32 v119, v119\n"
"v_cvt_pk_bf16_f32 v112, v112, v113\n"
"v_cvt_pk_bf16_f32 v113, v114, v115\n"
"v_cvt_pk_bf16_f32 v114, v116, v117\n"
"v_cvt_pk_bf16_f32 v115, v118, v119\n"
"v_mfma_f32_16x16x32_bf16 v[116:119], v[44:47], v[200:203], 0\n"
"v_cvt_pk_bf16_f32 v205, v212, v216\n"
"v_cvt_pk_bf16_f32 v209, v213, v217\n"
"v_mfma_f32_16x16x32_bf16 v[116:119], v[40:43], v[196:199], v[116:119]\n"
"v_cvt_pk_bf16_f32 v207, v214, v218\n"
"v_cvt_pk_bf16_f32 v211, v215, v219\n"
"v_mfma_f32_16x16x32_bf16 v[252:255], a[252:255], v[112:115], v[252:255]\n"
"v_pk_max_i16 v204, v204, 0\n"
"v_pk_max_i16 v205, v205, 0\n"
"v_pk_max_i16 v208, v208, 0\n"
"v_pk_max_i16 v209, v209, 0\n"
"v_pk_max_i16 v206, v206, 0\n"
"v_pk_max_i16 v207, v207, 0\n"
"v_pk_max_i16 v210, v210, 0\n"
"v_pk_max_i16 v211, v211, 0\n"
"v_max3_f32 v157, v148, v149, v150\n"
"v_max3_f32 v159, v151, v124, v125\n"
"v_max3_f32 v157, v157, v126, v127\n"
"v_add_f32 v252, v252, v253\n"
"v_rcp_f32 v253, v252\n"
"v_max3_f32 v159, v159, v128, v129\n"
"v_max3_f32 v157, v157, v130, v131\n"
"v_max3_f32 v159, v159, v132, v133\n"
"v_max3_f32 v157, v157, v134, v135\n"
"v_max3_f32 v159, v159, v136, v137\n"
"v_max3_f32 v157, v157, v138, v139\n"
"v_max3_f32 v159, v159, v140, v141\n"
"v_max3_f32 v157, v157, v142, v143\n"
"v_max3_f32 v159, v159, v144, v145\n"
"v_max3_f32 v157, v157, v146, v147\n"
"v_max_f32 v157, v157, v159\n"
"v_mov_b32 v161, v157\n"
"v_cvt_pk_bf16_f32 v220, v220, v221\n"
"v_cvt_pk_bf16_f32 v221, v222, v223\n"
"v_pk_max_i16 v220, v220, 0\n"
"v_permlane32_swap_b32 v157, v161\n"
"v_max_f32 v157, v157, v161\n"
"v_mov_b32 v161, v157\n"
"v_pk_max_i16 v221, v221, 0\n"
"v_add_u32 v158, 64, v158\n"
"ds_bpermute_b32 v254, v160, v253 offset:0\n"
"buffer_store_dwordx2 v[204:205], v162, s[84:87], s66 offen offset:0 sc0 sc1\n"
"v_permlane16_swap_b32 v157, v161\n"
"v_max_f32 v157, v157, v161\n"
"v_sub_f32 v148, v148, v157\n"
"v_sub_f32 v149, v149, v157\n"
"v_exp_f32 v148, v148\n"
"v_exp_f32 v149, v149\n"
"v_sub_f32 v124, v124, v157\n"
"v_sub_f32 v125, v125, v157\n"
"v_sub_f32 v150, v150, v157\n"
"v_sub_f32 v151, v151, v157\n"
"v_exp_f32 v124, v124\n"
"v_sub_f32 v126, v126, v157\n"
"v_exp_f32 v125, v125\n"
"v_sub_f32 v127, v127, v157\n"
"v_exp_f32 v161, v150\n"
"v_exp_f32 v151, v151\n"
"v_exp_f32 v126, v126\n"
"v_exp_f32 v127, v127\n"
"v_sub_f32 v128, v128, v157\n"
"v_sub_f32 v129, v129, v157\n"
"v_sub_f32 v130, v130, v157\n"
"v_sub_f32 v131, v131, v157\n"
"v_exp_f32 v128, v128\n"
"v_exp_f32 v129, v129\n"
"v_exp_f32 v130, v130\n"
"v_exp_f32 v131, v131\n"
"v_cvt_pk_bf16_f32 v150, v148, v149\n"
"v_sub_f32 v132, v132, v157\n"
"v_sub_f32 v133, v133, v157\n"
"v_add_f32 v148, v148, v149\n"
"v_cvt_pk_bf16_f32 v124, v124, v125\n"
"v_exp_f32 v132, v132\n"
"v_sub_f32 v134, v134, v157\n"
"v_exp_f32 v133, v133\n"
"v_sub_f32 v135, v135, v157\n"
"v_add_f32 v149, v161, v151\n"
"v_cvt_pk_bf16_f32 v125, v126, v127\n"
"v_exp_f32 v134, v134\n"
"v_exp_f32 v135, v135\n"
"v_sub_f32 v136, v136, v157\n"
"v_sub_f32 v137, v137, v157\n"
"v_sub_f32 v138, v138, v157\n"
"v_sub_f32 v139, v139, v157\n"
"v_cvt_pk_bf16_f32 v151, v161, v151\n"
"v_cvt_pk_bf16_f32 v126, v128, v129\n"
"v_cvt_pk_bf16_f32 v127, v130, v131\n"
"v_exp_f32 v136, v136\n"
"v_exp_f32 v137, v137\n"
"v_exp_f32 v138, v138\n"
"v_exp_f32 v139, v139\n"
"v_sub_f32 v140, v140, v157\n"
"v_sub_f32 v141, v141, v157\n"
"v_cvt_pk_bf16_f32 v132, v132, v133\n"
"v_exp_f32 v140, v140\n"
"v_sub_f32 v142, v142, v157\n"
"v_exp_f32 v141, v141\n"
"v_sub_f32 v143, v143, v157\n"
"v_cvt_pk_bf16_f32 v133, v134, v135\n"
"v_exp_f32 v142, v142\n"
"v_exp_f32 v143, v143\n"
"v_sub_f32 v144, v144, v157\n"
"v_sub_f32 v145, v145, v157\n"
"v_sub_f32 v146, v146, v157\n"
"v_sub_f32 v147, v147, v157\n"
"v_cvt_pk_bf16_f32 v134, v136, v137\n"
"v_cvt_pk_bf16_f32 v135, v138, v139\n"
"v_exp_f32 v144, v144\n"
"v_exp_f32 v145, v145\n"
"v_exp_f32 v146, v146\n"
"v_exp_f32 v147, v147\n"
"v_cvt_pk_bf16_f32 v140, v140, v141\n"
"v_cvt_pk_bf16_f32 v141, v142, v143\n"
"v_cvt_pk_bf16_f32 v142, v144, v145\n"
"v_cvt_pk_bf16_f32 v143, v146, v147\n"
"buffer_store_dwordx2 v[208:209], v162, s[84:87], s66 offen offset:256 sc0 sc1\n"
"buffer_store_dwordx2 v[206:207], v162, s[84:87], s66 offen offset:512 sc0 sc1\n"
"buffer_store_dwordx2 v[210:211], v162, s[84:87], s66 offen offset:768 sc0 sc1\n"
"s_nop 1\n"
"v_mfma_f32_16x16x4_4b_bf16 v[204:219], v[122:123], a[244:245], 0 cbsz:2 abid:0\n"
"buffer_store_dwordx2 v[220:221], v155, s[84:87], s70 offen offset:0 sc0 sc1\n"
"v_mfma_f32_16x16x32_bf16 v[120:123], a[228:231], v[196:199], 0\n"
"v_mfma_f32_16x16x32_bf16 v[120:123], a[232:235], v[200:203], v[120:123]\n"
"v_mfma_f32_16x16x32_bf16 v[204:207], v[96:99], v[48:51], v[204:207]\n"
"v_mfma_f32_16x16x32_bf16 v[204:207], v[104:107], v[52:55], v[204:207]\n"
"v_mfma_f32_16x16x32_bf16 v[204:207], v[112:115], v[56:59], v[204:207]\n"
"v_max3_f32 v157, v120, v121, v122\n"
"v_mfma_f32_16x16x32_bf16 v[208:211], v[112:115], v[68:71], v[208:211]\n"
"v_mfma_f32_16x16x32_bf16 v[208:211], v[104:107], v[64:67], v[208:211]\n"
"v_mfma_f32_16x16x32_bf16 v[208:211], v[96:99], v[60:63], v[208:211]\n"
"s_waitcnt lgkmcnt(0)\n"
"v_mul_f32_dpp v204, v254, v204 quad_perm:[0,0,0,0] row_mask:0xf bank_mask:0xf\n"
"v_mul_f32_dpp v205, v254, v205 quad_perm:[1,1,1,1] row_mask:0xf bank_mask:0xf\n"
"v_mfma_f32_16x16x32_bf16 v[212:215], v[96:99], v[72:75], v[212:215]\n"
"v_mul_f32_dpp v206, v254, v206 quad_perm:[2,2,2,2] row_mask:0xf bank_mask:0xf\n"
"v_mul_f32_dpp v207, v254, v207 quad_perm:[3,3,3,3] row_mask:0xf bank_mask:0xf\n"
"v_mfma_f32_16x16x32_bf16 v[212:215], v[104:107], v[76:79], v[212:215]\n"
"v_mfma_f32_16x16x32_bf16 v[212:215], v[112:115], v[80:83], v[212:215]\n"
"v_mul_f32_dpp v208, v254, v208 quad_perm:[0,0,0,0] row_mask:0xf bank_mask:0xf\n"
"v_mul_f32_dpp v209, v254, v209 quad_perm:[1,1,1,1] row_mask:0xf bank_mask:0xf\n"
"v_mfma_f32_16x16x32_bf16 v[216:219], v[112:115], v[92:95], v[216:219]\n"
"v_mul_f32_dpp v210, v254, v210 quad_perm:[2,2,2,2] row_mask:0xf bank_mask:0xf\n"
"v_mul_f32_dpp v211, v254, v211 quad_perm:[3,3,3,3] row_mask:0xf bank_mask:0xf\n"
"v_mfma_f32_16x16x32_bf16 v[216:219], v[104:107], v[88:91], v[216:219]\n"
"v_mfma_f32_16x16x32_bf16 v[216:219], v[96:99], v[84:87], v[216:219]\n"
"v_mul_f32_dpp v212, v254, v212 quad_perm:[0,0,0,0] row_mask:0xf bank_mask:0xf\n"
"v_mul_f32_dpp v213, v254, v213 quad_perm:[1,1,1,1] row_mask:0xf bank_mask:0xf\n"
"v_mfma_f32_16x16x32_bf16 v[96:99], v[0:3], v[196:199], 0\n"
"v_mul_f32_dpp v214, v254, v214 quad_perm:[2,2,2,2] row_mask:0xf bank_mask:0xf\n"
"v_mul_f32_dpp v215, v254, v215 quad_perm:[3,3,3,3] row_mask:0xf bank_mask:0xf\n"
"v_mfma_f32_16x16x32_bf16 v[96:99], v[4:7], v[200:203], v[96:99]\n"
"v_mfma_f32_16x16x32_bf16 v[104:107], v[16:19], v[196:199], 0\n"
"v_mul_f32_dpp v216, v254, v216 quad_perm:[0,0,0,0] row_mask:0xf bank_mask:0xf\n"
"v_mul_f32_dpp v217, v254, v217 quad_perm:[1,1,1,1] row_mask:0xf bank_mask:0xf\n"
"v_mfma_f32_16x16x32_bf16 v[104:107], v[20:23], v[200:203], v[104:107]\n"
"v_mul_f32_dpp v218, v254, v218 quad_perm:[2,2,2,2] row_mask:0xf bank_mask:0xf\n"
"v_mul_f32_dpp v219, v254, v219 quad_perm:[3,3,3,3] row_mask:0xf bank_mask:0xf\n"
"v_mfma_f32_16x16x32_bf16 v[252:255], a[252:255], v[124:127], v[148:151]\n"
"v_max3_f32 v159, v123, v96, v97\n"
"v_max3_f32 v157, v157, v98, v99\n"
"v_mfma_f32_16x16x32_bf16 v[252:255], a[252:255], v[132:135], v[252:255]\n"
"v_max3_f32 v159, v159, v100, v101\n"
"v_max3_f32 v157, v157, v102, v103\n"
"v_mfma_f32_16x16x32_bf16 v[252:255], a[252:255], v[140:143], v[252:255]\n"
"v_max3_f32 v159, v159, v104, v105\n"
"v_max3_f32 v157, v157, v106, v107\n"
"v_mfma_f32_16x16x32_bf16 v[112:115], v[32:35], v[196:199], 0\n"
"v_max3_f32 v159, v159, v108, v109\n"
"v_max3_f32 v157, v157, v110, v111\n"
"v_mfma_f32_16x16x32_bf16 v[112:115], v[36:39], v[200:203], v[112:115]\n"
"v_mfma_f32_16x16x4_4b_bf16 v[220:235], v[150:151], a[244:245], 0 cbsz:2 abid:0\n"
"v_add_f32 v252, v252, v253\n"
"v_rcp_f32 v253, v252\n"
"s_nop 0\n"
"ds_bpermute_b32 v254, v160, v253 offset:0\n"
"v_mfma_f32_16x16x32_bf16 v[204:207], a[48:51], a[160:163], v[204:207]\n"
"v_max3_f32 v159, v159, v112, v113\n"
"v_max3_f32 v157, v157, v114, v115\n"
"v_mfma_f32_16x16x32_bf16 v[204:207], a[52:55], a[164:167], v[204:207]\n"
"v_max3_f32 v159, v159, v116, v117\n"
"v_max3_f32 v157, v157, v118, v119\n"
"v_mfma_f32_16x16x32_bf16 v[204:207], a[56:59], a[168:171], v[204:207]\n"
"v_max_f32 v157, v157, v159\n"
"v_mov_b32 v161, v157\n"
"v_mfma_f32_16x16x32_bf16 v[204:207], a[60:63], a[172:175], v[204:207]\n"
"s_nop 0\n"
"v_permlane32_swap_b32 v157, v161\n"
"v_mfma_f32_16x16x32_bf16 v[220:223], v[124:127], v[48:51], v[220:223]\n"
"v_max_f32 v157, v157, v161\n"
"v_mov_b32 v161, v157\n"
"v_mfma_f32_16x16x32_bf16 v[220:223], v[132:135], v[52:55], v[220:223]\n"
"v_mfma_f32_16x16x32_bf16 v[220:223], v[140:143], v[56:59], v[220:223]\n"
"v_permlane16_swap_b32 v157, v161\n"
"v_mfma_f32_16x16x32_bf16 v[224:227], v[140:143], v[68:71], v[224:227]\n"
"v_max_f32 v157, v157, v161\n"
"v_sub_f32 v120, v120, v157\n"
"v_mfma_f32_16x16x32_bf16 v[224:227], v[132:135], v[64:67], v[224:227]\n"
"v_sub_f32 v121, v121, v157\n"
"v_sub_f32 v96, v96, v157\n"
"v_mfma_f32_16x16x32_bf16 v[224:227], v[124:127], v[60:63], v[224:227]\n"
"v_exp_f32 v120, v120\n"
"v_mfma_f32_16x16x32_bf16 v[228:231], v[124:127], v[72:75], v[228:231]\n"
"v_exp_f32 v121, v121\n"
"v_mfma_f32_16x16x32_bf16 v[228:231], v[132:135], v[76:79], v[228:231]\n"
"v_sub_f32 v97, v97, v157\n"
"v_sub_f32 v122, v122, v157\n"
"v_mfma_f32_16x16x32_bf16 v[228:231], v[140:143], v[80:83], v[228:231]\n"
"v_sub_f32 v123, v123, v157\n"
"v_sub_f32 v98, v98, v157\n"
"v_mfma_f32_16x16x32_bf16 v[232:235], v[140:143], v[92:95], v[232:235]\n"
"v_exp_f32 v96, v96\n"
"v_mfma_f32_16x16x32_bf16 v[232:235], v[132:135], v[88:91], v[232:235]\n"
"v_exp_f32 v97, v97\n"
"v_mfma_f32_16x16x32_bf16 v[232:235], v[124:127], v[84:87], v[232:235]\n"
"v_sub_f32 v99, v99, v157\n"
"v_sub_f32 v100, v100, v157\n"
"v_mfma_f32_16x16x32_bf16 v[208:211], a[60:63], a[188:191], v[208:211]\n"
"v_exp_f32 v161, v122\n"
"v_mfma_f32_16x16x32_bf16 v[208:211], a[56:59], a[184:187], v[208:211]\n"
"v_exp_f32 v123, v123\n"
"v_mfma_f32_16x16x32_bf16 v[208:211], a[52:55], a[180:183], v[208:211]\n"
"v_exp_f32 v98, v98\n"
"v_mfma_f32_16x16x32_bf16 v[208:211], a[48:51], a[176:179], v[208:211]\n"
"v_exp_f32 v99, v99\n"
"v_mfma_f32_16x16x32_bf16 v[212:215], a[48:51], a[192:195], v[212:215]\n"
"v_sub_f32 v101, v101, v157\n"
"v_sub_f32 v102, v102, v157\n"
"v_mfma_f32_16x16x32_bf16 v[212:215], a[52:55], a[196:199], v[212:215]\n"
"v_sub_f32 v103, v103, v157\n"
"v_cvt_pk_bf16_f32 v122, v120, v121\n"
"v_mfma_f32_16x16x32_bf16 v[212:215], a[56:59], a[200:203], v[212:215]\n"
"v_exp_f32 v100, v100\n"
"v_mfma_f32_16x16x32_bf16 v[212:215], a[60:63], a[204:207], v[212:215]\n"
"v_exp_f32 v101, v101\n"
"v_mfma_f32_16x16x32_bf16 v[216:219], a[60:63], a[220:223], v[216:219]\n"
"v_exp_f32 v102, v102\n"
"v_mfma_f32_16x16x32_bf16 v[216:219], a[56:59], a[216:219], v[216:219]\n"
"v_exp_f32 v103, v103\n"
"v_mfma_f32_16x16x32_bf16 v[216:219], a[52:55], a[212:215], v[216:219]\n"
"v_sub_f32 v104, v104, v157\n"
"v_sub_f32 v105, v105, v157\n"
"v_mfma_f32_16x16x32_bf16 v[216:219], a[48:51], a[208:211], v[216:219]\n"
"v_add_f32 v120, v120, v121\n"
"v_cvt_pk_bf16_f32 v96, v96, v97\n"
"v_exp_f32 v104, v104\n"
"v_sub_f32 v106, v106, v157\n"
"v_exp_f32 v105, v105\n"
"v_sub_f32 v107, v107, v157\n"
"v_add_f32 v121, v161, v123\n"
"v_cvt_pk_bf16_f32 v97, v98, v99\n"
"v_exp_f32 v106, v106\n"
"v_exp_f32 v107, v107\n"
"v_sub_f32 v108, v108, v157\n"
"v_sub_f32 v109, v109, v157\n"
"v_sub_f32 v110, v110, v157\n"
"v_sub_f32 v111, v111, v157\n"
"v_cvt_pk_bf16_f32 v123, v161, v123\n"
"v_cvt_pk_bf16_f32 v98, v100, v101\n"
"v_cvt_pk_bf16_f32 v99, v102, v103\n"
"v_exp_f32 v108, v108\n"
"v_exp_f32 v109, v109\n"
"v_exp_f32 v110, v110\n"
"v_exp_f32 v111, v111\n"
"v_sub_f32 v112, v112, v157\n"
"v_sub_f32 v113, v113, v157\n"
"v_cvt_pk_bf16_f32 v104, v104, v105\n"
"v_exp_f32 v112, v112\n"
"v_sub_f32 v114, v114, v157\n"
"v_exp_f32 v113, v113\n"
"v_sub_f32 v115, v115, v157\n"
"v_cvt_pk_bf16_f32 v105, v106, v107\n"
"s_waitcnt lgkmcnt(0)\n"
"v_mul_f32_dpp v220, v254, v220 quad_perm:[0,0,0,0] row_mask:0xf bank_mask:0xf\n"
"v_mul_f32_dpp v221, v254, v221 quad_perm:[1,1,1,1] row_mask:0xf bank_mask:0xf\n"
"v_mul_f32_dpp v222, v254, v222 quad_perm:[2,2,2,2] row_mask:0xf bank_mask:0xf\n"
"v_mul_f32_dpp v223, v254, v223 quad_perm:[3,3,3,3] row_mask:0xf bank_mask:0xf\n"
"v_mul_f32_dpp v224, v254, v224 quad_perm:[0,0,0,0] row_mask:0xf bank_mask:0xf\n"
"v_mul_f32_dpp v225, v254, v225 quad_perm:[1,1,1,1] row_mask:0xf bank_mask:0xf\n"
"v_mul_f32_dpp v226, v254, v226 quad_perm:[2,2,2,2] row_mask:0xf bank_mask:0xf\n"
"v_mfma_f32_16x16x32_bf16 v[220:223], a[64:67], a[160:163], v[220:223]\n"
"v_mul_f32_dpp v227, v254, v227 quad_perm:[3,3,3,3] row_mask:0xf bank_mask:0xf\n"
"v_mul_f32_dpp v228, v254, v228 quad_perm:[0,0,0,0] row_mask:0xf bank_mask:0xf\n"
"v_mfma_f32_16x16x32_bf16 v[220:223], a[68:71], a[164:167], v[220:223]\n"
"v_mul_f32_dpp v229, v254, v229 quad_perm:[1,1,1,1] row_mask:0xf bank_mask:0xf\n"
"v_mul_f32_dpp v230, v254, v230 quad_perm:[2,2,2,2] row_mask:0xf bank_mask:0xf\n"
"v_mfma_f32_16x16x32_bf16 v[220:223], a[72:75], a[168:171], v[220:223]\n"
"v_mul_f32_dpp v231, v254, v231 quad_perm:[3,3,3,3] row_mask:0xf bank_mask:0xf\n"
"v_mul_f32_dpp v232, v254, v232 quad_perm:[0,0,0,0] row_mask:0xf bank_mask:0xf\n"
"v_mfma_f32_16x16x32_bf16 v[220:223], a[76:79], a[172:175], v[220:223]\n"
"v_mul_f32_dpp v233, v254, v233 quad_perm:[1,1,1,1] row_mask:0xf bank_mask:0xf\n"
"v_mul_f32_dpp v234, v254, v234 quad_perm:[2,2,2,2] row_mask:0xf bank_mask:0xf\n"
"v_mfma_f32_16x16x32_bf16 v[224:227], a[76:79], a[188:191], v[224:227]\n"
"v_mul_f32_dpp v235, v254, v235 quad_perm:[3,3,3,3] row_mask:0xf bank_mask:0xf\n"
"v_sub_f32 v116, v116, v157\n"
"v_mfma_f32_16x16x32_bf16 v[224:227], a[72:75], a[184:187], v[224:227]\n"
"v_exp_f32 v114, v114\n"
"v_mfma_f32_16x16x32_bf16 v[224:227], a[68:71], a[180:183], v[224:227]\n"
"v_exp_f32 v115, v115\n"
"v_mfma_f32_16x16x32_bf16 v[224:227], a[64:67], a[176:179], v[224:227]\n"
"v_sub_f32 v117, v117, v157\n"
"v_sub_f32 v118, v118, v157\n"
"v_mfma_f32_16x16x32_bf16 v[252:255], a[252:255], v[96:99], v[120:123]\n"
"v_sub_f32 v119, v119, v157\n"
"v_cvt_pk_bf16_f32 v106, v108, v109\n"
"v_mfma_f32_16x16x32_bf16 v[228:231], a[64:67], a[192:195], v[228:231]\n"
"v_cvt_pk_bf16_f32 v107, v110, v111\n"
"v_cvt_pk_bf16_f32 v112, v112, v113\n"
"v_mfma_f32_16x16x32_bf16 v[228:231], a[68:71], a[196:199], v[228:231]\n"
"v_exp_f32 v116, v116\n"
"v_mfma_f32_16x16x32_bf16 v[228:231], a[72:75], a[200:203], v[228:231]\n"
"v_exp_f32 v117, v117\n"
"v_mfma_f32_16x16x32_bf16 v[228:231], a[76:79], a[204:207], v[228:231]\n"
"v_exp_f32 v118, v118\n"
"v_mfma_f32_16x16x32_bf16 v[252:255], a[252:255], v[104:107], v[252:255]\n"
"v_exp_f32 v119, v119\n"
"v_mfma_f32_16x16x32_bf16 v[232:235], a[76:79], a[220:223], v[232:235]\n"
"v_cvt_pk_bf16_f32 v113, v114, v115\n"
"v_cvt_pk_bf16_f32 v114, v116, v117\n"
"v_mfma_f32_16x16x32_bf16 v[232:235], a[72:75], a[216:219], v[232:235]\n"
"v_cvt_pk_bf16_f32 v115, v118, v119\n"
"v_cvt_pk_bf16_f32 v204, v204, v208\n"
"v_mfma_f32_16x16x32_bf16 v[232:235], a[68:71], a[212:215], v[232:235]\n"
"v_cvt_pk_bf16_f32 v206, v206, v210\n"
"v_cvt_pk_bf16_f32 v208, v205, v209\n"
"v_mfma_f32_16x16x32_bf16 v[232:235], a[64:67], a[208:211], v[232:235]\n"
"v_cvt_pk_bf16_f32 v210, v207, v211\n"
"v_cvt_pk_bf16_f32 v205, v212, v216\n"
"v_mfma_f32_16x16x32_bf16 v[252:255], a[252:255], v[112:115], v[252:255]\n"
"v_cvt_pk_bf16_f32 v209, v213, v217\n"
"v_cvt_pk_bf16_f32 v207, v214, v218\n"
"v_cvt_pk_bf16_f32 v211, v215, v219\n"
"v_pk_max_i16 v204, v204, 0\n"
"v_pk_max_i16 v205, v205, 0\n"
"v_pk_max_i16 v208, v208, 0\n"
"v_pk_max_i16 v209, v209, 0\n"
"v_pk_max_i16 v206, v206, 0\n"
"v_pk_max_i16 v207, v207, 0\n"
"v_pk_max_i16 v210, v210, 0\n"
"v_pk_max_i16 v211, v211, 0\n"
"v_add_f32 v252, v252, v253\n"
"v_rcp_f32 v253, v252\n"
"v_cvt_pk_bf16_f32 v220, v220, v224\n"
"v_cvt_pk_bf16_f32 v222, v222, v226\n"
"v_cvt_pk_bf16_f32 v224, v221, v225\n"
"v_cvt_pk_bf16_f32 v226, v223, v227\n"
"v_cvt_pk_bf16_f32 v221, v228, v232\n"
"v_cvt_pk_bf16_f32 v225, v229, v233\n"
"v_cvt_pk_bf16_f32 v223, v230, v234\n"
"v_cvt_pk_bf16_f32 v227, v231, v235\n"
"v_pk_max_i16 v220, v220, 0\n"
"v_pk_max_i16 v221, v221, 0\n"
"v_pk_max_i16 v224, v224, 0\n"
"v_pk_max_i16 v225, v225, 0\n"
"v_pk_max_i16 v222, v222, 0\n"
"v_pk_max_i16 v223, v223, 0\n"
"v_pk_max_i16 v226, v226, 0\n"
"v_pk_max_i16 v227, v227, 0\n"
"ds_bpermute_b32 v254, v160, v253 offset:0\n"
"buffer_store_dwordx2 v[204:205], v162, s[84:87], s67 offen offset:0 sc0 sc1\n"
"buffer_store_dwordx2 v[208:209], v162, s[84:87], s67 offen offset:256 sc0 sc1\n"
"buffer_store_dwordx2 v[206:207], v162, s[84:87], s67 offen offset:512 sc0 sc1\n"
"buffer_store_dwordx2 v[210:211], v162, s[84:87], s67 offen offset:768 sc0 sc1\n"
"s_nop 1\n"
"v_mfma_f32_16x16x4_4b_bf16 v[204:219], v[122:123], a[244:245], 0 cbsz:2 abid:0\n"
"buffer_store_dwordx2 v[220:221], v162, s[84:87], s68 offen offset:0 sc0 sc1\n"
"buffer_store_dwordx2 v[224:225], v162, s[84:87], s68 offen offset:256 sc0 sc1\n"
"s_nop 5\n"
"v_mfma_f32_16x16x32_bf16 v[204:207], v[96:99], v[48:51], v[204:207]\n"
"s_nop 1\n"
"v_mfma_f32_16x16x32_bf16 v[204:207], v[104:107], v[52:55], v[204:207]\n"
"v_mfma_f32_16x16x32_bf16 v[204:207], v[112:115], v[56:59], v[204:207]\n"
"buffer_store_dwordx2 v[222:223], v162, s[84:87], s68 offen offset:512 sc0 sc1\n"
"v_mfma_f32_16x16x32_bf16 v[208:211], v[112:115], v[68:71], v[208:211]\n"
"v_mfma_f32_16x16x32_bf16 v[208:211], v[104:107], v[64:67], v[208:211]\n"
"v_mfma_f32_16x16x32_bf16 v[208:211], v[96:99], v[60:63], v[208:211]\n"
"buffer_store_dwordx2 v[226:227], v162, s[84:87], s68 offen offset:768 sc0 sc1\n"
"v_mfma_f32_16x16x32_bf16 v[212:215], v[96:99], v[72:75], v[212:215]\n"
"s_waitcnt lgkmcnt(0)\n"
"v_mul_f32_dpp v204, v254, v204 quad_perm:[0,0,0,0] row_mask:0xf bank_mask:0xf\n"
"v_mul_f32_dpp v205, v254, v205 quad_perm:[1,1,1,1] row_mask:0xf bank_mask:0xf\n"
"v_mfma_f32_16x16x32_bf16 v[212:215], v[104:107], v[76:79], v[212:215]\n"
"v_mul_f32_dpp v206, v254, v206 quad_perm:[2,2,2,2] row_mask:0xf bank_mask:0xf\n"
"v_mul_f32_dpp v207, v254, v207 quad_perm:[3,3,3,3] row_mask:0xf bank_mask:0xf\n"
"v_mfma_f32_16x16x32_bf16 v[212:215], v[112:115], v[80:83], v[212:215]\n"
"v_mul_f32_dpp v208, v254, v208 quad_perm:[0,0,0,0] row_mask:0xf bank_mask:0xf\n"
"v_mul_f32_dpp v209, v254, v209 quad_perm:[1,1,1,1] row_mask:0xf bank_mask:0xf\n"
"v_mfma_f32_16x16x32_bf16 v[216:219], v[112:115], v[92:95], v[216:219]\n"
"v_mul_f32_dpp v210, v254, v210 quad_perm:[2,2,2,2] row_mask:0xf bank_mask:0xf\n"
"v_mul_f32_dpp v211, v254, v211 quad_perm:[3,3,3,3] row_mask:0xf bank_mask:0xf\n"
"v_mfma_f32_16x16x32_bf16 v[216:219], v[104:107], v[88:91], v[216:219]\n"
"v_mfma_f32_16x16x32_bf16 v[216:219], v[96:99], v[84:87], v[216:219]\n"
"v_mul_f32_dpp v212, v254, v212 quad_perm:[0,0,0,0] row_mask:0xf bank_mask:0xf\n"
"v_mul_f32_dpp v213, v254, v213 quad_perm:[1,1,1,1] row_mask:0xf bank_mask:0xf\n"
"v_mfma_f32_16x16x32_bf16 v[204:207], a[80:83], a[160:163], v[204:207]\n"
"v_mul_f32_dpp v214, v254, v214 quad_perm:[2,2,2,2] row_mask:0xf bank_mask:0xf\n"
"v_mul_f32_dpp v215, v254, v215 quad_perm:[3,3,3,3] row_mask:0xf bank_mask:0xf\n"
"v_mfma_f32_16x16x32_bf16 v[204:207], a[84:87], a[164:167], v[204:207]\n"
"v_mfma_f32_16x16x32_bf16 v[204:207], a[88:91], a[168:171], v[204:207]\n"
"v_mul_f32_dpp v216, v254, v216 quad_perm:[0,0,0,0] row_mask:0xf bank_mask:0xf\n"
"v_mul_f32_dpp v217, v254, v217 quad_perm:[1,1,1,1] row_mask:0xf bank_mask:0xf\n"
"v_mfma_f32_16x16x32_bf16 v[204:207], a[92:95], a[172:175], v[204:207]\n"
"v_mul_f32_dpp v218, v254, v218 quad_perm:[2,2,2,2] row_mask:0xf bank_mask:0xf\n"
"v_mul_f32_dpp v219, v254, v219 quad_perm:[3,3,3,3] row_mask:0xf bank_mask:0xf\n"
"v_mfma_f32_16x16x32_bf16 v[208:211], a[92:95], a[188:191], v[208:211]\n"
"v_mfma_f32_16x16x32_bf16 v[208:211], a[88:91], a[184:187], v[208:211]\n"
"v_mfma_f32_16x16x32_bf16 v[208:211], a[84:87], a[180:183], v[208:211]\n"
"v_mfma_f32_16x16x32_bf16 v[208:211], a[80:83], a[176:179], v[208:211]\n"
"v_mfma_f32_16x16x32_bf16 v[212:215], a[80:83], a[192:195], v[212:215]\n"
"v_mfma_f32_16x16x32_bf16 v[212:215], a[84:87], a[196:199], v[212:215]\n"
"v_mfma_f32_16x16x32_bf16 v[212:215], a[88:91], a[200:203], v[212:215]\n"
"v_cvt_pk_bf16_f32 v204, v204, v208\n"
"v_cvt_pk_bf16_f32 v206, v206, v210\n"
"v_mfma_f32_16x16x32_bf16 v[212:215], a[92:95], a[204:207], v[212:215]\n"
"v_cvt_pk_bf16_f32 v208, v205, v209\n"
"v_cvt_pk_bf16_f32 v210, v207, v211\n"
"v_mfma_f32_16x16x32_bf16 v[216:219], a[92:95], a[220:223], v[216:219]\n"
"v_pk_max_i16 v204, v204, 0\n"
"v_pk_max_i16 v208, v208, 0\n"
"v_mfma_f32_16x16x32_bf16 v[216:219], a[88:91], a[216:219], v[216:219]\n"
"v_pk_max_i16 v206, v206, 0\n"
"v_pk_max_i16 v210, v210, 0\n"
"v_mfma_f32_16x16x32_bf16 v[216:219], a[84:87], a[212:215], v[216:219]\n"
"v_mfma_f32_16x16x32_bf16 v[216:219], a[80:83], a[208:211], v[216:219]\n"
"s_nop 7\n"
"v_cvt_pk_bf16_f32 v205, v212, v216\n"
"v_cvt_pk_bf16_f32 v209, v213, v217\n"
"v_cvt_pk_bf16_f32 v207, v214, v218\n"
"v_cvt_pk_bf16_f32 v211, v215, v219\n"
"v_pk_max_i16 v205, v205, 0\n"
"v_pk_max_i16 v209, v209, 0\n"
"v_pk_max_i16 v207, v207, 0\n"
"v_pk_max_i16 v211, v211, 0\n"
"buffer_store_dwordx2 v[204:205], v162, s[84:87], s69 offen offset:0 sc0 sc1\n"
"buffer_store_dwordx2 v[208:209], v162, s[84:87], s69 offen offset:256 sc0 sc1\n"
"buffer_store_dwordx2 v[206:207], v162, s[84:87], s69 offen offset:512 sc0 sc1\n"
"buffer_store_dwordx2 v[210:211], v162, s[84:87], s69 offen offset:768 sc0 sc1\n"
".Lend%=:\n"
    :
    : [wave] "s"(wave), [lds] "s"(lds), [ka] "s"(ka), [bid] "s"(bid)
    : "v0","v1","v2","v3","v4","v5","v6","v7","v8","v9","v10","v11","v12","v13","v14","v15","v16","v17","v18","v19","v20","v21","v22","v23","v24","v25","v26","v27","v28","v29","v30","v31","v32","v33","v34","v35","v36","v37","v38","v39","v40","v41","v42","v43","v44","v45","v46","v47","v48","v49","v50","v51","v52","v53","v54","v55","v56","v57","v58","v59","v60","v61","v62","v63","v64","v65","v66","v67","v68","v69","v70","v71","v72","v73","v74","v75","v76","v77","v78","v79","v80","v81","v82","v83","v84","v85","v86","v87","v88","v89","v90","v91","v92","v93","v94","v95","v96","v97","v98","v99","v100","v101","v102","v103","v104","v105","v106","v107","v108","v109","v110","v111","v112","v113","v114","v115","v116","v117","v118","v119","v120","v121","v122","v123","v124","v125","v126","v127","v128","v129","v130","v131","v132","v133","v134","v135","v136","v137","v138","v139","v140","v141","v142","v143","v144","v145","v146","v147","v148","v149","v150","v151","v152","v153","v154","v155","v156","v157","v158","v159","v160","v161","v162","v163","v164","v165","v166","v167","v168","v169","v170","v171","v172","v173","v174","v175","v176","v177","v178","v179","v180","v181","v182","v183","v184","v185","v186","v187","v188","v189","v190","v191","v192","v193","v194","v195","v196","v197","v198","v199","v200","v201","v202","v203","v204","v205","v206","v207","v208","v209","v210","v211","v212","v213","v214","v215","v216","v217","v218","v219","v220","v221","v222","v223","v224","v225","v226","v227","v228","v229","v230","v231","v232","v233","v234","v235","v236","v237","v238","v239","v240","v241","v242","v243","v244","v245","v246","v247","v248","v249","v250","v251","v252","v253","v254","v255","a0","a1","a2","a3","a4","a5","a6","a7","a8","a9","a10","a11","a12","a13","a14","a15","a16","a17","a18","a19","a20","a21","a22","a23","a24","a25","a26","a27","a28","a29","a30","a31","a32","a33","a34","a35","a36","a37","a38","a39","a40","a41","a42","a43","a44","a45","a46","a47","a48","a49","a50","a51","a52","a53","a54","a55","a56","a57","a58","a59","a60","a61","a62","a63","a64","a65","a66","a67","a68","a69","a70","a71","a72","a73","a74","a75","a76","a77","a78","a79","a80","a81","a82","a83","a84","a85","a86","a87","a88","a89","a90","a91","a92","a93","a94","a95","a96","a97","a98","a99","a100","a101","a102","a103","a104","a105","a106","a107","a108","a109","a110","a111","a112","a113","a114","a115","a116","a117","a118","a119","a120","a121","a122","a123","a124","a125","a126","a127","a128","a129","a130","a131","a132","a133","a134","a135","a136","a137","a138","a139","a140","a141","a142","a143","a144","a145","a146","a147","a148","a149","a150","a151","a152","a153","a154","a155","a156","a157","a158","a159","a160","a161","a162","a163","a164","a165","a166","a167","a168","a169","a170","a171","a172","a173","a174","a175","a176","a177","a178","a179","a180","a181","a182","a183","a184","a185","a186","a187","a188","a189","a190","a191","a192","a193","a194","a195","a196","a197","a198","a199","a200","a201","a202","a203","a204","a205","a206","a207","a208","a209","a210","a211","a212","a213","a214","a215","a216","a217","a218","a219","a220","a221","a222","a223","a224","a225","a226","a227","a228","a229","a230","a231","a232","a233","a234","a235","a236","a237","a238","a239","a240","a241","a242","a243","a244","a245","a246","a247","a248","a249","a250","a251","a252","a253","a254","a255","s8","s9","s10","s11","s12","s13","s14","s15","s16","s17","s18","s19","s20","s21","s22","s23","s24","s25","s26","s27","s28","s29","s40","s41","s42","s43","s44","s45","s46","s47","s48","s49","s50","s51","s52","s53","s54","s55","s56","s57","s58","s59","s60","s61","s62","s63","s64","s65","s66","s67","s68","s69","s70","s71","s72","s73","s74","s75","s76","s77","s78","s79","s80","s81","s82","s83","s84","s85","s86","s87","s88","s89","s90","s91","s92","s93","s94","s95","scc","vcc","memory");
  STAMP_END();
}

extern "C" void kernel_launch(void* const* d_in, const int* in_sizes, int n_in, void* d_out, int out_size, void* d_ws,
                              size_t ws_size, hipStream_t stream) {
  autoint_r_kernel<<<256, 256, 0, stream>>>((const char*)d_in[0], (const char*)d_in[1], (const char*)d_in[2],
                                            (const char*)d_in[3], (const char*)d_in[4], (char*)d_out, (char*)d_ws);
}
```

```cpp
#include <hip/hip_runtime.h>
#include <stdint.h>

#define STAMP_BEGIN()
#define STAMP_END()

__global__ __launch_bounds__(256, 1)
void autoint_r_kernel(const char* __restrict__ e, const char* __restrict__ wq, const char* __restrict__ wk,
                      const char* __restrict__ wv, const char* __restrict__ wres, char* __restrict__ out,
                      char* __restrict__ ws) {
  __shared__ __attribute__((aligned(1024))) uint8_t lds_raw[163840];
  STAMP_BEGIN();
  const uint32_t wave = __builtin_amdgcn_readfirstlane(threadIdx.x >> 6);
  const uint32_t lds = (uint32_t)(uintptr_t)(__attribute__((address_space(3))) uint8_t*)lds_raw;
  const uint64_t ka = (uint64_t)(uintptr_t)__builtin_amdgcn_kernarg_segment_ptr();
  const uint32_t bid = blockIdx.x;
  asm volatile(
"s_load_dwordx8 s[8:15], %[ka], 0x0\n"
"s_load_dwordx4 s[16:19], %[ka], 0x20\n"
"s_lshl_b32 s88, %[wave], 3\n"
"s_load_dwordx2 s[22:23], %[ka], s88 offset:0x8\n"
"v_mbcnt_lo_u32_b32 v239, -1, 0\n"
"v_mbcnt_hi_u32_b32 v239, -1, v239\n"
"s_getpc_b64 s[76:77]\n"
".Lpc0%=:\n"
"s_bfe_u32 s89, %[bid], 0x40003\n"
"s_lshl_b32 s89, s89, 2\n"
"s_add_u32 s89, s89, %[wave]\n"
"s_lshl_b32 s89, s89, 9\n"
"v_lshl_add_u32 v254, v239, 7, s89\n"
"s_mov_b64 exec, 0xf\n"
"global_load_dword v253, v254, s[76:77] offset:128\n"
"s_mov_b64 exec, -1\n"
"v_lshrrev_b32 v249, 4, v239\n"
"s_lshl_b32 s74, %[wave], 12\n"
"s_bfe_u32 s92, %[bid], 0x10003\n"
"s_lshl_b32 s93, s92, 14\n"
"s_add_u32 s93, s93, s74\n"
"v_lshlrev_b32 v240, 1, v249\n"
"v_xor_b32 v240, v240, v239\n"
"v_and_b32 v240, 7, v240\n"
"v_lshlrev_b32 v240, 4, v240\n"
"v_bfe_u32 v241, v239, 3, 2\n"
"v_lshrrev_b32 v242, 5, v239\n"
"v_lshl_add_u32 v241, v242, 3, v241\n"
"v_lshl_add_u32 v44, v241, 7, v240\n"
"v_add_u32 v44, s93, v44\n"
"s_bfe_u32 s88, %[bid], 0x50003\n"
"s_lshl_b32 s88, s88, 10\n"
"v_lshl_add_u32 v255, v239, 7, s88\n"
"s_and_b32 s90, %[bid], 7\n"
"s_lshl_b32 s90, s90, 4\n"
"s_lshr_b32 s91, %[bid], 4\n"
"s_add_u32 s90, s90, s91\n"
"s_lshl_b32 s90, s90, 2\n"
"s_add_u32 s90, s90, %[wave]\n"
"s_mul_i32 s91, s90, 25600\n"
"v_xor_b32 v152, v249, v239\n"
"v_lshlrev_b32 v152, 4, v152\n"
"s_mul_i32 s28, %[wave], 13056\n"
"s_add_u32 s28, s28, 66304\n"
"s_mul_i32 s29, %[wave], 10752\n"
"s_add_u32 s29, s29, 118528\n"
"s_mul_i32 s75, %[wave], 4192\n"
"s_mov_b32 s40, 192\n"
"s_mov_b32 s41, 128\n"
"s_mov_b32 s42, 64\n"
"s_mov_b32 s43, 0\n"
"s_mov_b32 s26, 104857600\n"
"s_mov_b32 s27, 0x20000\n"
"s_waitcnt lgkmcnt(0)\n"
"s_add_u32 m0, s75, 16384\n"
"s_nop 0\n"
"global_load_lds_dwordx4 v44, s[14:15] offset:0\n"
"global_load_lds_dwordx4 v44, s[14:15] offset:2048\n"
"s_add_u32 s8, s8, s91\n"
"s_addc_u32 s9, s9, 0\n"
"s_sub_u32 s24, s8, 192\n"
"s_subb_u32 s25, s9, 0\n"
"s_and_b32 s25, s25, 0xffff\n"
"s_mov_b32 s44, 4288\n"
"s_mov_b32 s45, 4224\n"
"s_mov_b32 s46, 4160\n"
"s_mov_b32 s47, 4096\n"
"s_mov_b32 s48, 8384\n"
"s_mov_b32 s49, 8320\n"
"s_mov_b32 s50, 8256\n"
"s_add_u32 m0, s75, 16896\n"
"s_nop 0\n"
"global_load_lds_dwordx4 v44, s[14:15] offset:512\n"
"s_mov_b32 s51, 8192\n"
"s_mov_b32 s52, 12480\n"
"s_mov_b32 s53, 12416\n"
"s_mov_b32 s54, 12352\n"
"s_mov_b32 s55, 12288\n"
"s_mov_b32 s56, 16576\n"
"s_mov_b32 s57, 16512\n"
"s_mov_b32 s58, 16448\n"
"s_mov_b32 s59, 16384\n"
"s_mov_b32 s60, 20672\n"
"s_mov_b32 s61, 20608\n"
"s_mov_b32 s62, 20544\n"
"global_load_lds_dwordx4 v44, s[14:15] offset:2560\n"
"s_mov_b32 s63, 20480\n"
"v_and_b32 v248, 15, v239\n"
"v_and_b32 v232, 3, v248\n"
"v_lshrrev_b32 v251, 2, v248\n"
"s_lshl_b32 s88, %[wave], 13\n"
"s_lshl_b32 s92, s92, 7\n"
"s_add_u32 s88, s88, s92\n"
"v_lshl_add_u32 v135, v241, 8, v240\n"
"v_add_u32 v135, s88, v135\n"
"v_add_u32 v252, 0x1000, v135\n"
"v_lshlrev_b32 v153, 4, v239\n"
"s_mov_b32 s65, 4096\n"
"s_mov_b64 exec, 0xff\n"
"global_load_dword v255, v255, s[22:23]\n"
"s_mov_b64 exec, -1\n"
"s_mov_b32 s66, 8192\n"
"s_mov_b32 s67, 12288\n"
"s_mov_b32 s68, 16384\n"
"s_mov_b32 s69, 20480\n"
"s_mov_b32 s70, 24576\n"
"v_bfe_u32 v240, v239, 2, 3\n"
"v_lshl_add_u32 v240, v242, 4, v240\n"
"v_lshlrev_b32 v240, 7, v240\n"
"v_lshl_add_u32 v240, v232, 3, v240\n"
"v_bfe_u32 v241, v239, 3, 2\n"
"v_xor_b32 v243, 0, v241\n"
"v_lshl_add_u32 v40, v243, 5, v240\n"
"s_add_u32 m0, s74, 33152\n"
"s_nop 0\n"
"global_load_lds_dwordx4 v44, s[12:13] offset:0\n"
"v_add_u32 v32, s74, v40\n"
"v_add_u32 v36, s75, v40\n"
"v_xor_b32 v243, 1, v241\n"
"v_lshl_add_u32 v41, v243, 5, v240\n"
"v_add_u32 v33, s74, v41\n"
"v_add_u32 v37, s75, v41\n"
"v_xor_b32 v243, 2, v241\n"
"v_lshl_add_u32 v42, v243, 5, v240\n"
"v_add_u32 v34, s74, v42\n"
"v_add_u32 v38, s75, v42\n"
"v_xor_b32 v243, 3, v241\n"
"v_lshl_add_u32 v43, v243, 5, v240\n"
"global_load_lds_dwordx4 v44, s[12:13] offset:2048\n"
"v_add_u32 v35, s74, v43\n"
"v_add_u32 v39, s75, v43\n"
"v_add_u32 v45, s74, v153\n"
"v_mul_u32_u24 v240, 1056, v232\n"
"v_lshl_add_u32 v240, v251, 4, v240\n"
"v_lshl_add_u32 v240, v249, 8, v240\n"
"v_add_u32 v46, s75, v240\n"
"v_mul_u32_u24 v240, 1088, v251\n"
"v_lshl_add_u32 v240, v232, 8, v240\n"
"v_xor_b32 v241, v249, v232\n"
"v_lshl_add_u32 v240, v241, 4, v240\n"
"v_add_u32 v154, s28, v240\n"
"s_add_u32 m0, s74, 33664\n"
"s_nop 0\n"
"global_load_lds_dwordx4 v44, s[12:13] offset:512\n"
"v_add_u32 v47, s29, v240\n"
"v_lshlrev_b32 v243, 6, v249\n"
"v_lshl_add_u32 v132, v249, 9, s29\n"
"v_lshl_add_u32 v132, v248, 4, v132\n"
"v_min_u32 v240, 1, v251\n"
"v_lshl_add_u32 v161, v240, 20, v132\n"
"v_lshl_add_u32 v158, v249, 10, s29\n"
"v_bfe_u32 v240, v239, 1, 2\n"
"v_lshl_add_u32 v158, v240, 4, v158\n"
"v_and_b32 v240, 1, v239\n"
"v_lshl_add_u32 v158, v240, 2, v158\n"
"v_bfe_u32 v240, v239, 3, 1\n"
"global_load_lds_dwordx4 v44, s[12:13] offset:2560\n"
"v_lshl_add_u32 v158, v240, 20, v158\n"
"s_add_u32 s89, s29, 4096\n"
"v_lshl_add_u32 v133, v232, 4, v243\n"
"v_lshl_add_u32 v133, v251, 9, v133\n"
"v_add_u32 v133, s89, v133\n"
"v_lshl_add_u32 v156, v249, 7, s89\n"
"v_lshl_add_u32 v156, v251, 4, v156\n"
"v_and_b32 v240, 1, v239\n"
"v_lshl_add_u32 v156, v240, 3, v156\n"
"v_bfe_u32 v241, v239, 1, 1\n"
"v_lshl_add_u32 v156, v241, 20, v156\n"
"s_add_u32 s89, s74, 33152\n"
"s_add_u32 m0, s29, 0\n"
"s_mov_b32 s94, 24768\n"
"buffer_load_dwordx4 v152, s[24:27], s94 offen offset:0 lds\n"
"v_lshl_add_u32 v134, v249, 9, s89\n"
"v_lshl_add_u32 v134, v248, 3, v134\n"
"v_lshl_add_u32 v163, v239, 3, s89\n"
"s_add_u32 s89, s29, 8192\n"
"v_lshl_add_u32 v244, v240, 1, s89\n"
"v_lshl_add_u32 v244, v241, 4, v244\n"
"v_add_u32 v244, v243, v244\n"
"v_bfe_u32 v245, v239, 2, 1\n"
"v_lshl_add_u32 v244, v245, 3, v244\n"
"v_bfe_u32 v245, v239, 3, 1\n"
"v_lshl_add_u32 v244, v245, 8, v244\n"
"v_accvgpr_write_b32 a240, v244\n"
"s_mov_b32 s94, 13130880\n"
"buffer_load_dwordx4 v152, s[24:27], s94 offen offset:1088 lds\n"
"v_add_u32 v244, s89, v153\n"
"v_accvgpr_write_b32 a241, v244\n"
"v_lshl_add_u32 v244, v248, 4, s89\n"
"v_add_u32 v244, 2048, v244\n"
"v_min_u32 v245, 1, v232\n"
"v_lshl_add_u32 v244, v245, 20, v244\n"
"v_accvgpr_write_b32 a242, v244\n"
"v_lshl_add_u32 v244, v239, 3, s89\n"
"v_mov_b32 v246, 0\n"
"v_mov_b32 v247, 0\n"
"ds_write_b64 v244, v[246:247] offset:1024\n"
"v_lshlrev_b32 v162, 3, v248\n"
"s_mov_b32 s94, 26236992\n"
"buffer_load_dwordx4 v152, s[24:27], s94 offen offset:2176 lds\n"
"v_lshl_add_u32 v155, v249, 8, v162\n"
"v_lshl_add_u32 v162, v249, 10, v162\n"
"v_lshl_add_u32 v160, v249, 2, v232\n"
"v_lshlrev_b32 v160, 2, v160\n"
"v_mul_u32_u24 v157, 0xaaaa, v240\n"
"v_add_u32 v157, 0x4444, v157\n"
"v_mov_b32 v159, 0\n"
"v_mov_b32 v243, 0x3f803f80\n"
"v_xor_b32 v242, v242, v241\n"
"v_subrev_u32 v242, 1, v242\n"
"v_and_b32 v242, v243, v242\n"
"v_accvgpr_write_b32 a248, v243\n"
"s_mov_b32 s94, 39343104\n"
"buffer_load_dwordx4 v152, s[24:27], s94 offen offset:3264 lds\n"
"v_accvgpr_write_b32 a249, v243\n"
"v_accvgpr_write_b32 a250, v243\n"
"v_accvgpr_write_b32 a251, v243\n"
"v_accvgpr_write_b32 a252, v242\n"
"v_accvgpr_write_b32 a253, v242\n"
"v_accvgpr_write_b32 a254, v242\n"
"v_accvgpr_write_b32 a255, v242\n"
"s_mov_b32 s72, 0x3e38aa3b\n"
"s_getpc_b64 s[80:81]\n"
".Lpc3%=:\n"
"s_add_u32 s80, s80, .Lp2f%=-.Lpc3%=\n"
"s_addc_u32 s81, s81, 0\n"
"s_and_b32 s81, s81, 0xffff\n"
"s_mov_b32 s82, .Lcend%=-.Lp2f%=\n"
"s_mov_b32 s83, 0x20000\n"
"s_add_u32 s91, s91, s92\n"
"s_add_u32 s18, s18, s91\n"
"s_addc_u32 s19, s19, 0\n"
"s_sub_u32 s84, s18, 13107200\n"
"s_subb_u32 s85, s19, 0\n"
"s_and_b32 s85, s85, 0xffff\n"
"s_mov_b32 s86, 25472\n"
"s_mov_b32 s87, 0x20000\n"
"s_bfe_u32 s90, %[bid], 0x20004\n"
"s_lshl_b32 s90, s90, 12\n"
"s_bfe_u32 s91, %[bid], 0x10003\n"
"s_lshl_b32 s91, s91, 9\n"
"s_lshl_b32 s88, %[wave], 7\n"
"s_add_u32 s88, s88, s90\n"
"s_add_u32 s88, s88, s91\n"
"s_waitcnt lgkmcnt(0)\n"
".Lhead%=:\n"
"s_mov_b64 vcc, -1\n"
"s_cmp_eq_u32 %[wave], 0\n"
"s_cbranch_scc1 .Lpad0_%=\n"
"s_cmp_eq_u32 %[wave], 1\n"
"s_cbranch_scc1 .Lpad1_%=\n"
"s_cmp_eq_u32 %[wave], 2\n"
"s_cbranch_scc1 .Lpad3_%=\n"
"s_cmp_eq_u32 %[wave], 3\n"
"s_cbranch_scc1 .Lpad4_%=\n"
".Lback0_%=:\n"
"s_mov_b64 vcc, 0\n"
"s_add_u32 m0, s74, 0\n"
"s_nop 0\n"
"global_load_lds_dwordx4 v44, s[10:11] offset:0\n"
"global_load_lds_dwordx4 v44, s[10:11] offset:2048\n"
"s_add_u32 m0, s74, 512\n"
"s_nop 0\n"
"global_load_lds_dwordx4 v44, s[10:11] offset:512\n"
"s_waitcnt vmcnt(12)\n"
"ds_read_b64_tr_b16 v[236:237], v36 offset:16384\n"
"ds_read_b64_tr_b16 v[238:239], v36 offset:17408\n"
"global_load_lds_dwordx4 v44, s[10:11] offset:2560\n"
"ds_read_b64_tr_b16 v[240:241], v37 offset:16384\n"
"ds_read_b64_tr_b16 v[242:243], v37 offset:17408\n"
"ds_read_b64_tr_b16 v[244:245], v38 offset:16384\n"
"ds_read_b64_tr_b16 v[246:247], v38 offset:17408\n"
"ds_read_b64_tr_b16 v[248:249], v39 offset:16384\n"
"ds_read_b64_tr_b16 v[250:251], v39 offset:17408\n"
"s_add_u32 m0, s28, 8704\n"
"s_mov_b32 s94, 52453568\n"
"buffer_load_dwordx4 v152, s[24:27], s94 offen offset:0 lds\n"
"s_mov_b32 s94, 65559680\n"
"buffer_load_dwordx4 v152, s[24:27], s94 offen offset:1088 lds\n"
"s_waitcnt lgkmcnt(6)\n"
"ds_write_b128 v46, v[236:239] offset:16384\n"
"s_waitcnt lgkmcnt(5)\n"
"ds_write_b128 v46, v[240:243] offset:16448\n"
"s_mov_b32 s94, 78665792\n"
"buffer_load_dwordx4 v152, s[24:27], s94 offen offset:2176 lds\n"
"s_waitcnt lgkmcnt(4)\n"
"ds_write_b128 v46, v[244:247] offset:16512\n"
"s_mov_b32 s94, 91771904\n"
"buffer_load_dwordx4 v152, s[24:27], s94 offen offset:3264 lds\n"
"s_waitcnt lgkmcnt(3)\n"
"ds_write_b128 v46, v[248:251] offset:16576\n"
"s_add_u32 m0, s75, 49536\n"
"s_nop 0\n"
"global_load_lds_dwordx4 v135, s[16:17] offset:0\n"
"global_load_lds_dwordx4 v135, s[16:17] offset:1024\n"
"v_mov_b32 v135, 0\n"
"s_waitcnt vmcnt(14) lgkmcnt(0)\n"
"s_barrier\n"
"ds_read_b64_tr_b16 a[96:97], v40 offset:33152\n"
"ds_read_b64_tr_b16 a[98:99], v40 offset:34176\n"
"ds_read_b64_tr_b16 a[100:101], v40 offset:37248\n"
"ds_read_b64_tr_b16 a[102:103], v40 offset:38272\n"
"ds_read_b64_tr_b16 a[104:105], v40 offset:41344\n"
"ds_read_b64_tr_b16 a[106:107], v40 offset:42368\n"
"ds_read_b64_tr_b16 a[108:109], v40 offset:45440\n"
"ds_read_b64_tr_b16 a[110:111], v40 offset:46464\n"
"ds_read_b64_tr_b16 a[112:113], v41 offset:33152\n"
"ds_read_b64_tr_b16 a[114:115], v41 offset:34176\n"
"ds_read_b64_tr_b16 a[116:117], v41 offset:37248\n"
"ds_read_b64_tr_b16 a[118:119], v41 offset:38272\n"
"ds_read_b64_tr_b16 a[120:121], v41 offset:41344\n"
"ds_read_b64_tr_b16 a[122:123], v41 offset:42368\n"
"ds_read_b64_tr_b16 a[124:125], v41 offset:45440\n"
"s_add_u32 m0, s75, 51584\n"
"s_nop 0\n"
"global_load_lds_dwordx4 v252, s[16:17] offset:0\n"
"ds_read_b64_tr_b16 a[126:127], v41 offset:46464\n"
"ds_read_b64_tr_b16 a[128:129], v42 offset:33152\n"
"ds_read_b64_tr_b16 a[130:131], v42 offset:34176\n"
"ds_read_b64_tr_b16 a[132:133], v42 offset:37248\n"
"ds_read_b64_tr_b16 a[134:135], v42 offset:38272\n"
"ds_read_b64_tr_b16 a[136:137], v42 offset:41344\n"
"ds_read_b64_tr_b16 a[138:139], v42 offset:42368\n"
"ds_read_b64_tr_b16 a[140:141], v42 offset:45440\n"
"ds_read_b64_tr_b16 a[142:143], v42 offset:46464\n"
"ds_read_b64_tr_b16 a[144:145], v43 offset:33152\n"
"ds_read_b64_tr_b16 a[146:147], v43 offset:34176\n"
"ds_read_b64_tr_b16 a[148:149], v43 offset:37248\n"
"ds_read_b64_tr_b16 a[150:151], v43 offset:38272\n"
"ds_read_b64_tr_b16 a[152:153], v43 offset:41344\n"
"ds_read_b64_tr_b16 a[154:155], v43 offset:42368\n"
"ds_read_b64_tr_b16 a[156:157], v43 offset:45440\n"
"ds_read_b64_tr_b16 a[158:159], v43 offset:46464\n"
"ds_read_b128 a[160:163], v153 offset:16384\n"
"global_load_lds_dwordx4 v252, s[16:17] offset:1024\n"
"ds_read_b128 a[164:167], v153 offset:20576\n"
"ds_read_b128 a[168:171], v153 offset:24768\n"
"ds_read_b128 a[172:175], v153 offset:28960\n"
"ds_read_b128 a[176:179], v153 offset:17440\n"
"ds_read_b128 a[180:183], v153 offset:21632\n"
"ds_read_b128 a[184:187], v153 offset:25824\n"
"ds_read_b128 a[188:191], v153 offset:30016\n"
"ds_read_b128 a[192:195], v153 offset:18496\n"
"ds_read_b128 a[196:199], v153 offset:22688\n"
"ds_read_b128 a[200:203], v153 offset:26880\n"
"s_waitcnt vmcnt(12)\n"
"ds_read_b128 v[204:207], v47 offset:0\n"
"ds_read_b128 v[208:211], v47 offset:64\n"
"ds_read_b128 v[212:215], v47 offset:128\n"
"ds_read_b128 v[216:219], v47 offset:192\n"
"ds_read_b128 a[204:207], v153 offset:31072\n"
"ds_read_b128 a[208:211], v153 offset:19552\n"
"ds_read_b128 a[212:215], v153 offset:23744\n"
"ds_read_b128 a[216:219], v153 offset:27936\n"
"ds_read_b128 a[220:223], v153 offset:32128\n"
"s_waitcnt lgkmcnt(8)\n"
"v_mfma_f32_16x16x32_bf16 v[96:99], a[96:99], v[204:207], 0\n"
"s_waitcnt lgkmcnt(7)\n"
"v_mfma_f32_16x16x32_bf16 v[96:99], a[100:103], v[208:211], v[96:99]\n"
"s_waitcnt lgkmcnt(6)\n"
"v_mfma_f32_16x16x32_bf16 v[96:99], a[104:107], v[212:215], v[96:99]\n"
"s_waitcnt lgkmcnt(5)\n"
"v_mfma_f32_16x16x32_bf16 v[96:99], a[108:111], v[216:219], v[96:99]\n"
"s_waitcnt vmcnt(8)\n"
"ds_read_b64_tr_b16 v[112:113], v32 offset:0\n"
"ds_read_b64_tr_b16 v[114:115], v32 offset:1024\n"
"v_mfma_f32_16x16x32_bf16 v[100:103], a[112:115], v[204:207], 0\n"
"ds_read_b64_tr_b16 v[116:117], v33 offset:0\n"
"ds_read_b64_tr_b16 v[118:119], v33 offset:1024\n"
"v_mfma_f32_16x16x32_bf16 v[100:103], a[116:119], v[208:211], v[100:103]\n"
"s_add_u32 m0, s28, 0\n"
"s_nop 0\n"
"buffer_load_dwordx4 v152, s[24:27], s40 offen offset:0 lds\n"
"v_mfma_f32_16x16x32_bf16 v[100:103], a[120:123], v[212:215], v[100:103]\n"
"ds_read_b64_tr_b16 v[120:121], v34 offset:0\n"
"ds_read_b64_tr_b16 v[122:123], v34 offset:1024\n"
"v_mfma_f32_16x16x32_bf16 v[100:103], a[124:127], v[216:219], v[100:103]\n"
"ds_read_b64_tr_b16 v[124:125], v35 offset:0\n"
"ds_read_b64_tr_b16 v[126:127], v35 offset:1024\n"
"v_mfma_f32_16x16x32_bf16 v[104:107], a[128:131], v[204:207], 0\n"
"v_cvt_pk_bf16_f32 v96, v96, v97\n"
"v_cvt_pk_bf16_f32 v97, v98, v99\n"
"v_mfma_f32_16x16x32_bf16 v[104:107], a[132:135], v[208:211], v[104:107]\n"
"v_mfma_f32_16x16x32_bf16 v[104:107], a[136:139], v[212:215], v[104:107]\n"
"v_cvt_pk_bf16_f32 v98, v100, v101\n"
"v_cvt_pk_bf16_f32 v99, v102, v103\n"
"v_mfma_f32_16x16x32_bf16 v[104:107], a[140:143], v[216:219], v[104:107]\n"
"ds_write_b128 v132, v[96:99] offset:0\n"
"v_mfma_f32_16x16x32_bf16 v[108:111], a[144:147], v[204:207], 0\n"
"s_waitcnt lgkmcnt(8)\n"
"v_lshlrev_b32 v128, 16, v112\n"
"v_mfma_f32_16x16x32_bf16 v[108:111], a[148:151], v[208:211], v[108:111]\n"
"buffer_load_dwordx4 v152, s[24:27], s41 offen offset:1088 lds\n"
"v_cvt_pk_bf16_f32 v104, v104, v105\n"
"v_mfma_f32_16x16x32_bf16 v[108:111], a[152:155], v[212:215], v[108:111]\n"
"v_cvt_pk_bf16_f32 v105, v106, v107\n"
"v_and_b32 v112, 0xffff0000, v112\n"
"v_mfma_f32_16x16x32_bf16 v[108:111], a[156:159], v[216:219], v[108:111]\n"
"s_barrier\n"
"v_mfma_f32_16x16x32_bf16 v[48:51], v[204:207], a[160:163], 0\n"
"ds_read_b64_tr_b16 a[48:49], v40 offset:0\n"
"ds_read_b64_tr_b16 a[50:51], v40 offset:1024\n"
"v_mfma_f32_16x16x32_bf16 v[48:51], v[208:211], a[164:167], v[48:51]\n"
"ds_read_b64_tr_b16 a[52:53], v40 offset:4096\n"
"ds_read_b64_tr_b16 a[54:55], v40 offset:5120\n"
"v_mfma_f32_16x16x32_bf16 v[48:51], v[212:215], a[168:171], v[48:51]\n"
"ds_read_b64_tr_b16 a[56:57], v40 offset:8192\n"
"ds_read_b64_tr_b16 a[58:59], v40 offset:9216\n"
"v_mfma_f32_16x16x32_bf16 v[48:51], v[216:219], a[172:175], v[48:51]\n"
"ds_read_b64_tr_b16 a[60:61], v40 offset:12288\n"
"buffer_load_dwordx4 v152, s[24:27], s42 offen offset:2176 lds\n"
"v_mfma_f32_16x16x32_bf16 v[52:55], v[204:207], a[176:179], 0\n"
"ds_read_b64_tr_b16 a[62:63], v40 offset:13312\n"
"ds_read_b64_tr_b16 a[64:65], v41 offset:0\n"
"v_mfma_f32_16x16x32_bf16 v[52:55], v[208:211], a[180:183], v[52:55]\n"
"ds_read_b64_tr_b16 a[66:67], v41 offset:1024\n"
"ds_read_b64_tr_b16 a[68:69], v41 offset:4096\n"
"v_mfma_f32_16x16x32_bf16 v[52:55], v[212:215], a[184:187], v[52:55]\n"
"ds_read_b64_tr_b16 a[70:71], v41 offset:5120\n"
"ds_read_b64_tr_b16 a[72:73], v41 offset:8192\n"
"v_mfma_f32_16x16x32_bf16 v[52:55], v[216:219], a[188:191], v[52:55]\n"
"ds_read_b64_tr_b16 a[74:75], v41 offset:9216\n"
"ds_read_b64_tr_b16 a[76:77], v41 offset:12288\n"
"v_mfma_f32_16x16x32_bf16 v[60:63], v[204:207], a[192:195], 0\n"
"ds_read_b64_tr_b16 a[78:79], v41 offset:13312\n"
"ds_read_b64_tr_b16 a[80:81], v42 offset:0\n"
"v_mfma_f32_16x16x32_bf16 v[60:63], v[208:211], a[196:199], v[60:63]\n"
"ds_read_b64_tr_b16 a[82:83], v42 offset:1024\n"
"ds_read_b64_tr_b16 a[84:85], v42 offset:4096\n"
"v_mfma_f32_16x16x32_bf16 v[60:63], v[212:215], a[200:203], v[60:63]\n"
"ds_read_b64_tr_b16 a[86:87], v42 offset:5120\n"
"v_cvt_pk_bf16_f32 v106, v108, v109\n"
"v_mfma_f32_16x16x32_bf16 v[60:63], v[216:219], a[204:207], v[60:63]\n"
"ds_read_b64_tr_b16 a[88:89], v42 offset:8192\n"
"ds_read_b64_tr_b16 a[90:91], v42 offset:9216\n"
"v_mfma_f32_16x16x32_bf16 v[64:67], v[204:207], a[208:211], 0\n"
"s_waitcnt vmcnt(7)\n"
"ds_read_b128 v[220:223], v154 offset:8704\n"
"buffer_load_dwordx4 v152, s[24:27], s43 offen offset:3264 lds\n"
"v_mfma_f32_16x16x32_bf16 v[64:67], v[208:211], a[212:215], v[64:67]\n"
"ds_read_b128 v[224:227], v154 offset:8768\n"
"v_cvt_pk_bf16_f32 v107, v110, v111\n"
"v_mfma_f32_16x16x32_bf16 v[64:67], v[212:215], a[216:219], v[64:67]\n"
"ds_read_b128 v[228:231], v154 offset:8832\n"
"v_cvt_pk_bf16_f32 v48, v48, v49\n"
"v_mfma_f32_16x16x32_bf16 v[64:67], v[216:219], a[220:223], v[64:67]\n"
"s_waitcnt lgkmcnt(14)\n"
"ds_read_b128 v[232:235], v154 offset:8896\n"
"v_cvt_pk_bf16_f32 v49, v50, v51\n"
"v_mfma_f32_16x16x32_bf16 v[136:139], a[48:51], v[204:207], 0\n"
"ds_read_b64_tr_b16 a[92:93], v42 offset:12288\n"
"v_cvt_pk_bf16_f32 v52, v52, v53\n"
"v_mfma_f32_16x16x32_bf16 v[136:139], a[52:55], v[208:211], v[136:139]\n"
"ds_read_b64_tr_b16 a[94:95], v42 offset:13312\n"
"ds_read_b64_tr_b16 a[224:225], v43 offset:0\n"
"v_mfma_f32_16x16x32_bf16 v[136:139], a[56:59], v[212:215], v[136:139]\n"
"ds_read_b64_tr_b16 a[226:227], v43 offset:1024\n"
"ds_read_b64_tr_b16 a[228:229], v43 offset:4096\n"
"v_mfma_f32_16x16x32_bf16 v[136:139], a[60:63], v[216:219], v[136:139]\n"
"ds_read_b64_tr_b16 a[230:231], v43 offset:5120\n"
"v_cvt_pk_bf16_f32 v53, v54, v55\n"
"v_mfma_f32_16x16x32_bf16 v[140:143], a[64:67], v[204:207], 0\n"
"s_waitcnt lgkmcnt(13)\n"
"ds_read_b64_tr_b16 a[232:233], v43 offset:8192\n"
"ds_read_b64_tr_b16 a[234:235], v43 offset:9216\n"
"v_mfma_f32_16x16x32_bf16 v[140:143], a[68:71], v[208:211], v[140:143]\n"
"ds_read_b64_tr_b16 a[236:237], v43 offset:12288\n"
"v_cvt_pk_bf16_f32 v60, v60, v61\n"
"v_mfma_f32_16x16x32_bf16 v[140:143], a[72:75], v[212:215], v[140:143]\n"
"ds_read_b64_tr_b16 a[238:239], v43 offset:13312\n"
"v_cvt_pk_bf16_f32 v61, v62, v63\n"
"v_mfma_f32_16x16x32_bf16 v[140:143], a[76:79], v[216:219], v[140:143]\n"
"ds_write_b128 v132, v[104:107] offset:2048\n"
"v_mfma_f32_16x16x32_bf16 v[144:147], a[80:83], v[204:207], 0\n"
"s_waitcnt lgkmcnt(13)\n"
"v_cvt_pk_bf16_f32 v64, v64, v65\n"
"v_cvt_pk_bf16_f32 v65, v66, v67\n"
"v_mfma_f32_16x16x32_bf16 v[144:147], a[84:87], v[208:211], v[144:147]\n"
"v_mul_f32 v136, s72, v136\n"
"v_mul_f32 v137, s72, v137\n"
"v_mfma_f32_16x16x32_bf16 v[144:147], a[88:91], v[212:215], v[144:147]\n"
"s_waitcnt lgkmcnt(11)\n"
"ds_write_b64 v134, v[60:61] offset:256\n"
"v_mfma_f32_16x16x32_bf16 v[96:99], a[108:111], v[232:235], 0\n"
"s_waitcnt vmcnt(4)\n"
"ds_read_b64_tr_b16 v[236:237], v36 offset:49536\n"
"ds_read_b64_tr_b16 v[238:239], v36 offset:50560\n"
"v_mfma_f32_16x16x32_bf16 v[96:99], a[104:107], v[228:231], v[96:99]\n"
"ds_read_b64_tr_b16 v[240:241], v37 offset:49536\n"
"ds_read_b64_tr_b16 v[242:243], v37 offset:50560\n"
"v_mfma_f32_16x16x32_bf16 v[96:99], a[100:103], v[224:227], v[96:99]\n"
"ds_read_b64_tr_b16 v[244:245], v38 offset:49536\n"
"ds_read_b64_tr_b16 v[246:247], v38 offset:50560\n"
"v_mfma_f32_16x16x32_bf16 v[96:99], a[96:99], v[220:223], v[96:99]\n"
"s_waitcnt lgkmcnt(10)\n"
"ds_write_b64 v134, v[64:65] offset:384\n"
"v_mfma_f32_16x16x32_bf16 v[144:147], a[92:95], v[216:219], v[144:147]\n"
"ds_write_b64 v134, v[48:49] offset:0\n"
"v_mfma_f32_16x16x32_bf16 v[148:151], a[224:227], v[204:207], 0\n"
"v_mul_f32 v138, s72, v138\n"
"v_mul_f32 v139, s72, v139\n"
"v_mfma_f32_16x16x32_bf16 v[148:151], a[228:231], v[208:211], v[148:151]\n"
"ds_read_b64_tr_b16 v[248:249], v39 offset:49536\n"
"ds_read_b64_tr_b16 v[250:251], v39 offset:50560\n"
"v_mfma_f32_16x16x32_bf16 v[148:151], a[232:235], v[212:215], v[148:151]\n"
"s_waitcnt lgkmcnt(11)\n"
"v_mul_f32 v140, s72, v140\n"
"v_mul_f32 v141, s72, v141\n"
"v_mfma_f32_16x16x32_bf16 v[148:151], a[236:239], v[216:219], v[148:151]\n"
"ds_write_b64 v134, v[52:53] offset:128\n"
"v_mfma_f32_16x16x32_bf16 v[100:103], a[124:127], v[232:235], 0\n"
"v_mul_f32 v142, s72, v142\n"
"v_mul_f32 v143, s72, v143\n"
"v_mfma_f32_16x16x32_bf16 v[100:103], a[120:123], v[228:231], v[100:103]\n"
"v_cvt_pk_bf16_f32 v136, v136, v137\n"
"v_cvt_pk_bf16_f32 v137, v140, v141\n"
"v_mfma_f32_16x16x32_bf16 v[100:103], a[116:119], v[224:227], v[100:103]\n"
"v_cvt_pk_bf16_f32 v138, v138, v139\n"
"v_cvt_pk_bf16_f32 v139, v142, v143\n"
"v_mfma_f32_16x16x32_bf16 v[100:103], a[112:115], v[220:223], v[100:103]\n"
"ds_write_b128 v133, v[136:139] offset:0\n"
"v_mfma_f32_16x16x32_bf16 v[104:107], a[140:143], v[232:235], 0\n"
"s_add_u32 m0, s28, 4352\n"
"s_nop 0\n"
"buffer_load_dwordx4 v152, s[24:27], s44 offen offset:0 lds\n"
"v_mfma_f32_16x16x32_bf16 v[104:107], a[136:139], v[228:231], v[104:107]\n"
"v_mul_f32 v144, s72, v144\n"
"v_mul_f32 v145, s72, v145\n"
"v_mfma_f32_16x16x32_bf16 v[104:107], a[132:135], v[224:227], v[104:107]\n"
"v_mul_f32 v146, s72, v146\n"
"v_mul_f32 v147, s72, v147\n"
"v_mfma_f32_16x16x32_bf16 v[104:107], a[128:131], v[220:223], v[104:107]\n"
"s_waitcnt lgkmcnt(4)\n"
"ds_write_b128 v46, v[236:239] offset:49536\n"
"v_mfma_f32_16x16x32_bf16 v[108:111], a[156:159], v[232:235], 0\n"
"ds_read_b128 v[236:239], v153 offset:16384\n"
"v_mul_f32 v148, s72, v148\n"
"v_mfma_f32_16x16x32_bf16 v[108:111], a[152:155], v[228:231], v[108:111]\n"
"v_mul_f32 v149, s72, v149\n"
"v_mul_f32 v150, s72, v150\n"
"v_mfma_f32_16x16x32_bf16 v[108:111], a[148:151], v[224:227], v[108:111]\n"
"v_mul_f32 v151, s72, v151\n"
"v_cvt_pk_bf16_f32 v144, v144, v145\n"
"v_mfma_f32_16x16x32_bf16 v[108:111], a[144:147], v[220:223], v[108:111]\n"
"ds_write_b128 v46, v[240:243] offset:49600\n"
"v_mfma_f32_16x16x32_bf16 v[72:75], v[232:235], a[172:175], 0\n"
"ds_read_b128 v[240:243], v153 offset:20576\n"
"v_cvt_pk_bf16_f32 v145, v148, v149\n"
"v_mfma_f32_16x16x32_bf16 v[72:75], v[228:231], a[168:171], v[72:75]\n"
"buffer_load_dwordx4 v152, s[24:27], s45 offen offset:1088 lds\n"
"v_cvt_pk_bf16_f32 v146, v146, v147\n"
"v_mfma_f32_16x16x32_bf16 v[72:75], v[224:227], a[164:167], v[72:75]\n"
"v_cvt_pk_bf16_f32 v147, v150, v151\n"
"v_cvt_pk_bf16_f32 v96, v96, v97\n"
"v_mfma_f32_16x16x32_bf16 v[72:75], v[220:223], a[160:163], v[72:75]\n"
"ds_write_b128 v133, v[144:147] offset:256\n"
"v_mfma_f32_16x16x32_bf16 v[76:79], v[232:235], a[188:191], 0\n"
"v_cvt_pk_bf16_f32 v97, v98, v99\n"
"v_cvt_pk_bf16_f32 v98, v100, v101\n"
"v_mfma_f32_16x16x32_bf16 v[76:79], v[228:231], a[184:187], v[76:79]\n"
"v_cvt_pk_bf16_f32 v99, v102, v103\n"
"v_cvt_pk_bf16_f32 v104, v104, v105\n"
"v_mfma_f32_16x16x32_bf16 v[76:79], v[224:227], a[180:183], v[76:79]\n"
"v_cvt_pk_bf16_f32 v105, v106, v107\n"
"v_cvt_pk_bf16_f32 v106, v108, v109\n"
"v_mfma_f32_16x16x32_bf16 v[76:79], v[220:223], a[176:179], v[76:79]\n"
"ds_write_b128 v132, v[96:99] offset:256\n"
"v_mfma_f32_16x16x32_bf16 v[84:87], v[232:235], a[204:207], 0\n"
"v_cvt_pk_bf16_f32 v107, v110, v111\n"
"v_cvt_pk_bf16_f32 v72, v72, v73\n"
"v_mfma_f32_16x16x32_bf16 v[84:87], v[228:231], a[200:203], v[84:87]\n"
"v_cvt_pk_bf16_f32 v73, v74, v75\n"
"v_cvt_pk_bf16_f32 v76, v76, v77\n"
"v_mfma_f32_16x16x32_bf16 v[84:87], v[224:227], a[196:199], v[84:87]\n"
"v_cvt_pk_bf16_f32 v77, v78, v79\n"
"v_mul_f32 v128, s72, v128\n"
"v_mfma_f32_16x16x32_bf16 v[84:87], v[220:223], a[192:195], v[84:87]\n"
"buffer_load_dwordx4 v152, s[24:27], s46 offen offset:2176 lds\n"
"ds_write_b128 v132, v[104:107] offset:2304\n"
"v_mfma_f32_16x16x32_bf16 v[88:91], v[232:235], a[220:223], 0\n"
"v_mov_b32 v132, 0\n"
"v_mul_f32 v112, s72, v112\n"
"v_mfma_f32_16x16x32_bf16 v[88:91], v[228:231], a[216:219], v[88:91]\n"
"s_nop 0\n"
"v_cvt_pk_bf16_f32 v84, v84, v85\n"
"v_cvt_pk_bf16_f32 v85, v86, v87\n"
"v_mfma_f32_16x16x32_bf16 v[88:91], v[224:227], a[212:215], v[88:91]\n"
"v_cvt_pk_bf16_f32 v112, v128, v112\n"
"v_lshlrev_b32 v129, 16, v113\n"
"v_mfma_f32_16x16x32_bf16 v[88:91], v[220:223], a[208:211], v[88:91]\n"
"ds_write_b64 v134, v[72:73] offset:2048\n"
"v_mfma_f32_16x16x32_bf16 v[136:139], a[60:63], v[232:235], 0\n"
"v_and_b32 v113, 0xffff0000, v113\n"
"v_mul_f32 v129, s72, v129\n"
"v_mfma_f32_16x16x32_bf16 v[136:139], a[56:59], v[228:231], v[136:139]\n"
".Lpad0_%=:\n"
"s_cbranch_vccnz .Lpad5_%=\n"
"v_mul_f32 v113, s72, v113\n"
"v_cvt_pk_bf16_f32 v113, v129, v113\n"
"v_mfma_f32_16x16x32_bf16 v[136:139], a[52:55], v[224:227], v[136:139]\n"
"v_cvt_pk_bf16_f32 v88, v88, v89\n"
"v_cvt_pk_bf16_f32 v89, v90, v91\n"
"v_mfma_f32_16x16x32_bf16 v[136:139], a[48:51], v[220:223], v[136:139]\n"
"ds_write_b64 v134, v[76:77] offset:2176\n"
"v_mfma_f32_16x16x32_bf16 v[140:143], a[76:79], v[232:235], 0\n"
"s_waitcnt vmcnt(3)\n"
"buffer_load_dwordx4 v152, s[24:27], s47 offen offset:3264 lds\n"
"ds_read_b128 a[0:3], v154 offset:0\n"
"v_mfma_f32_16x16x32_bf16 v[140:143], a[72:75], v[228:231], v[140:143]\n"
"ds_read_b128 a[4:7], v154 offset:64\n"
"ds_read_b128 a[8:11], v154 offset:128\n"
"v_mfma_f32_16x16x32_bf16 v[140:143], a[68:71], v[224:227], v[140:143]\n"
"ds_read_b128 a[12:15], v154 offset:192\n"
"v_mul_f32 v136, s72, v136\n"
"v_mfma_f32_16x16x32_bf16 v[140:143], a[64:67], v[220:223], v[140:143]\n"
"ds_write_b64 v134, v[84:85] offset:2304\n"
"v_mfma_f32_16x16x32_bf16 v[144:147], a[92:95], v[232:235], 0\n"
"v_mul_f32 v137, s72, v137\n"
"v_mul_f32 v138, s72, v138\n"
"v_mfma_f32_16x16x32_bf16 v[144:147], a[88:91], v[228:231], v[144:147]\n"
"v_mul_f32 v139, s72, v139\n"
"v_cvt_pk_bf16_f32 v136, v136, v137\n"
"v_mfma_f32_16x16x32_bf16 v[144:147], a[84:87], v[224:227], v[144:147]\n"
"v_mul_f32 v140, s72, v140\n"
"v_mul_f32 v141, s72, v141\n"
"v_mfma_f32_16x16x32_bf16 v[144:147], a[80:83], v[220:223], v[144:147]\n"
"ds_write_b64 v134, v[88:89] offset:2432\n"
"v_mfma_f32_16x16x32_bf16 v[148:151], a[236:239], v[232:235], 0\n"
"ds_read_b64 a[244:245], v163 offset:0\n"
"v_mul_f32 v142, s72, v142\n"
"v_mfma_f32_16x16x32_bf16 v[148:151], a[232:235], v[228:231], v[148:151]\n"
"v_mul_f32 v143, s72, v143\n"
"v_cvt_pk_bf16_f32 v137, v140, v141\n"
"v_mfma_f32_16x16x32_bf16 v[148:151], a[228:231], v[224:227], v[148:151]\n"
"v_cvt_pk_bf16_f32 v138, v138, v139\n"
"v_cvt_pk_bf16_f32 v139, v142, v143\n"
"v_mfma_f32_16x16x32_bf16 v[148:151], a[224:227], v[220:223], v[148:151]\n"
"s_waitcnt lgkmcnt(6)\n"
"ds_write_b128 v133, v[136:139] offset:2048\n"
"v_mfma_f32_16x16x32_bf16 v[0:3], a[96:99], a[0:3], 0\n"
"s_waitcnt lgkmcnt(5)\n"
"ds_read_b128 v[136:139], v153 offset:18496\n"
"v_mul_f32 v144, s72, v144\n"
"v_mfma_f32_16x16x32_bf16 v[0:3], a[100:103], a[4:7], v[0:3]\n"
"ds_read_b128 v[140:143], v153 offset:22688\n"
"v_mul_f32 v145, s72, v145\n"
"v_mfma_f32_16x16x32_bf16 v[0:3], a[104:107], a[8:11], v[0:3]\n"
"s_waitcnt lgkmcnt(5)\n"
"v_mul_f32 v146, s72, v146\n"
"v_mul_f32 v147, s72, v147\n"
"v_mfma_f32_16x16x32_bf16 v[0:3], a[108:111], a[12:15], v[0:3]\n"
"ds_write_b128 v46, v[244:247] offset:49664\n"
"v_mfma_f32_16x16x32_bf16 v[176:179], a[112:115], a[0:3], 0\n"
"v_mul_f32 v148, s72, v148\n"
"v_mul_f32 v149, s72, v149\n"
"v_mfma_f32_16x16x32_bf16 v[176:179], a[116:119], a[4:7], v[176:179]\n"
"ds_read_b128 v[244:247], v153 offset:24768\n"
"v_mul_f32 v150, s72, v150\n"
"v_mfma_f32_16x16x32_bf16 v[176:179], a[120:123], a[8:11], v[176:179]\n"
"v_mul_f32 v151, s72, v151\n"
"v_cvt_pk_bf16_f32 v144, v144, v145\n"
"v_mfma_f32_16x16x32_bf16 v[176:179], a[124:127], a[12:15], v[176:179]\n"
"ds_write_b128 v46, v[248:251] offset:49728\n"
"v_mfma_f32_16x16x32_bf16 v[4:7], a[128:131], a[0:3], 0\n"
"ds_read_b128 v[248:251], v153 offset:28960\n"
"v_cvt_pk_bf16_f32 v145, v148, v149\n"
"v_mfma_f32_16x16x32_bf16 v[4:7], a[132:135], a[4:7], v[4:7]\n"
"s_add_u32 m0, s28, 8704\n"
"s_nop 0\n"
"buffer_load_dwordx4 v152, s[24:27], s48 offen offset:0 lds\n"
"v_mfma_f32_16x16x32_bf16 v[4:7], a[136:139], a[8:11], v[4:7]\n"
"v_cvt_pk_bf16_f32 v146, v146, v147\n"
"v_cvt_pk_bf16_f32 v147, v150, v151\n"
"v_mfma_f32_16x16x32_bf16 v[4:7], a[140:143], a[12:15], v[4:7]\n"
"ds_write_b128 v133, v[144:147] offset:2304\n"
"v_mfma_f32_16x16x32_bf16 v[164:167], a[144:147], a[0:3], 0\n"
"ds_read_b128 v[144:147], v153 offset:26880\n"
"v_mov_b32 v133, 0\n"
"v_mfma_f32_16x16x32_bf16 v[164:167], a[148:151], a[4:7], v[164:167]\n"
"ds_read_b128 v[148:151], v153 offset:31072\n"
"v_cvt_pk_bf16_f32 v0, v0, v1\n"
"v_mfma_f32_16x16x32_bf16 v[164:167], a[152:155], a[8:11], v[164:167]\n"
"v_cvt_pk_bf16_f32 v1, v2, v3\n"
"v_cvt_pk_bf16_f32 v2, v176, v177\n"
"v_mfma_f32_16x16x32_bf16 v[164:167], a[156:159], a[12:15], v[164:167]\n"
"v_cvt_pk_bf16_f32 v3, v178, v179\n"
"v_cvt_pk_bf16_f32 v4, v4, v5\n"
"v_mfma_f32_16x16x32_bf16 v[180:183], a[48:51], a[0:3], 0\n"
"v_cvt_pk_bf16_f32 v5, v6, v7\n"
"v_lshlrev_b32 v130, 16, v114\n"
"v_mfma_f32_16x16x32_bf16 v[180:183], a[52:55], a[4:7], v[180:183]\n"
"v_and_b32 v114, 0xffff0000, v114\n"
"v_mul_f32 v130, s72, v130\n"
"v_mfma_f32_16x16x32_bf16 v[180:183], a[56:59], a[8:11], v[180:183]\n"
"v_cvt_pk_bf16_f32 v6, v164, v165\n"
"v_cvt_pk_bf16_f32 v7, v166, v167\n"
"v_mfma_f32_16x16x32_bf16 v[180:183], a[60:63], a[12:15], v[180:183]\n"
"buffer_load_dwordx4 v152, s[24:27], s49 offen offset:1088 lds\n"
"v_mul_f32 v114, s72, v114\n"
"v_mfma_f32_16x16x32_bf16 v[176:179], a[64:67], a[0:3], 0\n"
"v_cvt_pk_bf16_f32 v114, v130, v114\n"
"v_lshlrev_b32 v131, 16, v115\n"
"v_mfma_f32_16x16x32_bf16 v[176:179], a[68:71], a[4:7], v[176:179]\n"
"v_and_b32 v115, 0xffff0000, v115\n"
"v_mul_f32 v131, s72, v131\n"
"v_mfma_f32_16x16x32_bf16 v[176:179], a[72:75], a[8:11], v[176:179]\n"
"v_mul_f32 v180, s72, v180\n"
"v_mul_f32 v181, s72, v181\n"
"v_mfma_f32_16x16x32_bf16 v[176:179], a[76:79], a[12:15], v[176:179]\n"
"s_waitcnt lgkmcnt(2)\n"
"s_barrier\n"
"v_mfma_f32_16x16x32_bf16 v[184:187], a[80:83], a[0:3], 0\n"
"v_mul_f32 v182, s72, v182\n"
"v_mul_f32 v183, s72, v183\n"
"v_mfma_f32_16x16x32_bf16 v[184:187], a[84:87], a[4:7], v[184:187]\n"
"v_cvt_pk_bf16_f32 v180, v180, v181\n"
"v_cvt_pk_bf16_f32 v181, v182, v183\n"
"v_mfma_f32_16x16x32_bf16 v[184:187], a[88:91], a[8:11], v[184:187]\n"
"v_mul_f32 v176, s72, v176\n"
"v_mul_f32 v177, s72, v177\n"
"v_mfma_f32_16x16x32_bf16 v[184:187], a[92:95], a[12:15], v[184:187]\n"
"v_mul_f32 v178, s72, v178\n"
"v_mul_f32 v179, s72, v179\n"
"v_mfma_f32_16x16x32_bf16 v[168:171], a[224:227], a[0:3], 0\n"
"buffer_load_dwordx4 v152, s[24:27], s50 offen offset:2176 lds\n"
"v_cvt_pk_bf16_f32 v182, v176, v177\n"
"v_mfma_f32_16x16x32_bf16 v[168:171], a[228:231], a[4:7], v[168:171]\n"
"v_cvt_pk_bf16_f32 v183, v178, v179\n"
"v_mul_f32 v115, s72, v115\n"
"v_mfma_f32_16x16x32_bf16 v[168:171], a[232:235], a[8:11], v[168:171]\n"
"v_mul_f32 v184, s72, v184\n"
"v_mul_f32 v185, s72, v185\n"
"v_mfma_f32_16x16x32_bf16 v[168:171], a[236:239], a[12:15], v[168:171]\n"
"v_mul_f32 v186, s72, v186\n"
"v_mul_f32 v187, s72, v187\n"
"v_mfma_f32_16x16x32_bf16 v[48:51], a[0:3], a[160:163], 0\n"
"v_cvt_pk_bf16_f32 v184, v184, v185\n"
"v_cvt_pk_bf16_f32 v185, v186, v187\n"
"v_mfma_f32_16x16x32_bf16 v[48:51], a[4:7], a[164:167], v[48:51]\n"
"v_cvt_pk_bf16_f32 v115, v131, v115\n"
"v_lshlrev_b32 v128, 16, v116\n"
"v_mfma_f32_16x16x32_bf16 v[48:51], a[8:11], a[168:171], v[48:51]\n"
"v_mul_f32 v168, s72, v168\n"
"v_mul_f32 v169, s72, v169\n"
"v_mfma_f32_16x16x32_bf16 v[48:51], a[12:15], a[172:175], v[48:51]\n"
"ds_write_b128 v45, v[112:115] offset:0\n"
"v_mfma_f32_16x16x32_bf16 v[60:63], a[0:3], a[176:179], 0\n"
"v_mul_f32 v170, s72, v170\n"
"v_mul_f32 v171, s72, v171\n"
"v_mfma_f32_16x16x32_bf16 v[60:63], a[4:7], a[180:183], v[60:63]\n"
"v_cvt_pk_bf16_f32 v186, v168, v169\n"
"v_cvt_pk_bf16_f32 v187, v170, v171\n"
"v_mfma_f32_16x16x32_bf16 v[60:63], a[8:11], a[184:187], v[60:63]\n"
"v_cvt_pk_bf16_f32 v48, v48, v49\n"
"v_cvt_pk_bf16_f32 v49, v50, v51\n"
"v_mfma_f32_16x16x32_bf16 v[60:63], a[12:15], a[188:191], v[60:63]\n"
"s_waitcnt vmcnt(3)\n"
"buffer_load_dwordx4 v152, s[24:27], s51 offen offset:3264 lds\n"
"ds_read_b128 a[16:19], v154 offset:4352\n"
"v_mfma_f32_16x16x32_bf16 v[72:75], a[0:3], a[192:195], 0\n"
"ds_read_b128 a[20:23], v154 offset:4416\n"
"ds_read_b128 a[24:27], v154 offset:4480\n"
"v_mfma_f32_16x16x32_bf16 v[72:75], a[4:7], a[196:199], v[72:75]\n"
"ds_read_b128 a[28:31], v154 offset:4544\n"
"v_and_b32 v116, 0xffff0000, v116\n"
"v_mfma_f32_16x16x32_bf16 v[72:75], a[8:11], a[200:203], v[72:75]\n"
"v_cvt_pk_bf16_f32 v60, v60, v61\n"
"v_cvt_pk_bf16_f32 v61, v62, v63\n"
"v_mfma_f32_16x16x32_bf16 v[72:75], a[12:15], a[204:207], v[72:75]\n"
"v_mul_f32 v128, s72, v128\n"
"v_mul_f32 v116, s72, v116\n"
"v_mfma_f32_16x16x32_bf16 v[84:87], a[0:3], a[208:211], 0\n"
"v_cvt_pk_bf16_f32 v116, v128, v116\n"
"v_lshlrev_b32 v129, 16, v117\n"
"v_mfma_f32_16x16x32_bf16 v[84:87], a[4:7], a[212:215], v[84:87]\n"
"v_and_b32 v117, 0xffff0000, v117\n"
"v_mul_f32 v129, s72, v129\n"
"v_mfma_f32_16x16x32_bf16 v[84:87], a[8:11], a[216:219], v[84:87]\n"
"v_cvt_pk_bf16_f32 v72, v72, v73\n"
"v_cvt_pk_bf16_f32 v73, v74, v75\n"
"v_mfma_f32_16x16x32_bf16 v[84:87], a[12:15], a[220:223], v[84:87]\n"
"v_mul_f32 v117, s72, v117\n"
"v_cvt_pk_bf16_f32 v117, v129, v117\n"
"v_lshlrev_b32 v130, 16, v118\n"
"v_and_b32 v118, 0xffff0000, v118\n"
"v_mul_f32 v130, s72, v130\n"
"v_mul_f32 v118, s72, v118\n"
"v_cvt_pk_bf16_f32 v118, v130, v118\n"
"v_lshlrev_b32 v131, 16, v119\n"
"v_and_b32 v119, 0xffff0000, v119\n"
"v_mul_f32 v131, s72, v131\n"
"v_mul_f32 v119, s72, v119\n"
"s_waitcnt lgkmcnt(3)\n"
"v_mfma_f32_16x16x32_bf16 v[8:11], a[96:99], a[16:19], 0\n"
"s_waitcnt lgkmcnt(2)\n"
"v_cvt_pk_bf16_f32 v84, v84, v85\n"
"v_cvt_pk_bf16_f32 v85, v86, v87\n"
"v_mfma_f32_16x16x32_bf16 v[8:11], a[100:103], a[20:23], v[8:11]\n"
"s_waitcnt lgkmcnt(1)\n"
"v_cvt_pk_bf16_f32 v119, v131, v119\n"
"v_lshlrev_b32 v128, 16, v120\n"
"v_mfma_f32_16x16x32_bf16 v[8:11], a[104:107], a[24:27], v[8:11]\n"
"s_waitcnt lgkmcnt(0)\n"
"v_and_b32 v120, 0xffff0000, v120\n"
"v_mul_f32 v128, s72, v128\n"
"v_mfma_f32_16x16x32_bf16 v[8:11], a[108:111], a[28:31], v[8:11]\n"
"ds_write_b128 v45, v[116:119] offset:1024\n"
"v_mfma_f32_16x16x32_bf16 v[176:179], a[112:115], a[16:19], 0\n"
"v_mul_f32 v120, s72, v120\n"
"v_cvt_pk_bf16_f32 v120, v128, v120\n"
"v_mfma_f32_16x16x32_bf16 v[176:179], a[116:119], a[20:23], v[176:179]\n"
"s_nop 0\n"
"v_cvt_pk_bf16_f32 v8, v8, v9\n"
"v_cvt_pk_bf16_f32 v9, v10, v11\n"
"v_mfma_f32_16x16x32_bf16 v[176:179], a[120:123], a[24:27], v[176:179]\n"
"v_lshlrev_b32 v129, 16, v121\n"
"v_and_b32 v121, 0xffff0000, v121\n"
"v_mfma_f32_16x16x32_bf16 v[176:179], a[124:127], a[28:31], v[176:179]\n"
"v_mul_f32 v129, s72, v129\n"
"v_mul_f32 v121, s72, v121\n"
"v_mfma_f32_16x16x32_bf16 v[12:15], a[128:131], a[16:19], 0\n"
"v_cvt_pk_bf16_f32 v121, v129, v121\n"
"v_lshlrev_b32 v130, 16, v122\n"
"v_mfma_f32_16x16x32_bf16 v[12:15], a[132:135], a[20:23], v[12:15]\n"
"v_and_b32 v122, 0xffff0000, v122\n"
"v_mul_f32 v130, s72, v130\n"
"v_mfma_f32_16x16x32_bf16 v[12:15], a[136:139], a[24:27], v[12:15]\n"
"v_cvt_pk_bf16_f32 v10, v176, v177\n"
"v_cvt_pk_bf16_f32 v11, v178, v179\n"
"v_mfma_f32_16x16x32_bf16 v[12:15], a[140:143], a[28:31], v[12:15]\n"
"s_add_u32 m0, s28, 0\n"
"s_nop 0\n"
"buffer_load_dwordx4 v152, s[24:27], s52 offen offset:0 lds\n"
"v_mfma_f32_16x16x32_bf16 v[168:171], a[144:147], a[16:19], 0\n"
"v_mul_f32 v122, s72, v122\n"
"v_cvt_pk_bf16_f32 v122, v130, v122\n"
"v_mfma_f32_16x16x32_bf16 v[168:171], a[148:151], a[20:23], v[168:171]\n"
"v_lshlrev_b32 v131, 16, v123\n"
"v_and_b32 v123, 0xffff0000, v123\n"
"v_mfma_f32_16x16x32_bf16 v[168:171], a[152:155], a[24:27], v[168:171]\n"
"v_cvt_pk_bf16_f32 v12, v12, v13\n"
"v_cvt_pk_bf16_f32 v13, v14, v15\n"
"v_mfma_f32_16x16x32_bf16 v[168:171], a[156:159], a[28:31], v[168:171]\n"
"v_mul_f32 v131, s72, v131\n"
"v_mul_f32 v123, s72, v123\n"
"v_mfma_f32_16x16x32_bf16 v[188:191], a[48:51], a[16:19], 0\n"
"v_cvt_pk_bf16_f32 v123, v131, v123\n"
"v_lshlrev_b32 v128, 16, v124\n"
"v_mfma_f32_16x16x32_bf16 v[188:191], a[52:55], a[20:23], v[188:191]\n"
"v_and_b32 v124, 0xffff0000, v124\n"
"v_mul_f32 v128, s72, v128\n"
"v_mfma_f32_16x16x32_bf16 v[188:191], a[56:59], a[24:27], v[188:191]\n"
"v_cvt_pk_bf16_f32 v14, v168, v169\n"
"v_cvt_pk_bf16_f32 v15, v170, v171\n"
"v_mfma_f32_16x16x32_bf16 v[188:191], a[60:63], a[28:31], v[188:191]\n"
"ds_write_b128 v45, v[120:123] offset:2048\n"
"v_mfma_f32_16x16x32_bf16 v[176:179], a[64:67], a[16:19], 0\n"
"v_mul_f32 v124, s72, v124\n"
"v_cvt_pk_bf16_f32 v124, v128, v124\n"
"v_mfma_f32_16x16x32_bf16 v[176:179], a[68:71], a[20:23], v[176:179]\n"
"buffer_load_dwordx4 v152, s[24:27], s53 offen offset:1088 lds\n"
"v_mov_b32 v128, 0\n"
"v_mfma_f32_16x16x32_bf16 v[176:179], a[72:75], a[24:27], v[176:179]\n"
"v_mul_f32 v188, s72, v188\n"
"v_mul_f32 v189, s72, v189\n"
"v_mfma_f32_16x16x32_bf16 v[176:179], a[76:79], a[28:31], v[176:179]\n"
"v_mul_f32 v190, s72, v190\n"
"v_mul_f32 v191, s72, v191\n"
"v_mfma_f32_16x16x32_bf16 v[192:195], a[80:83], a[16:19], 0\n"
"v_cvt_pk_bf16_f32 v188, v188, v189\n"
"v_cvt_pk_bf16_f32 v189, v190, v191\n"
"v_mfma_f32_16x16x32_bf16 v[192:195], a[84:87], a[20:23], v[192:195]\n"
"v_lshlrev_b32 v129, 16, v125\n"
"v_and_b32 v125, 0xffff0000, v125\n"
"v_mfma_f32_16x16x32_bf16 v[192:195], a[88:91], a[24:27], v[192:195]\n"
"v_mul_f32 v176, s72, v176\n"
"v_mul_f32 v177, s72, v177\n"
"v_mfma_f32_16x16x32_bf16 v[192:195], a[92:95], a[28:31], v[192:195]\n"
"v_mul_f32 v178, s72, v178\n"
"v_mul_f32 v179, s72, v179\n"
"v_mfma_f32_16x16x32_bf16 v[164:167], a[224:227], a[16:19], 0\n"
"v_cvt_pk_bf16_f32 v190, v176, v177\n"
"v_cvt_pk_bf16_f32 v191, v178, v179\n"
"v_mfma_f32_16x16x32_bf16 v[164:167], a[228:231], a[20:23], v[164:167]\n"
"v_mul_f32 v129, s72, v129\n"
"v_mul_f32 v125, s72, v125\n"
"v_mfma_f32_16x16x32_bf16 v[164:167], a[232:235], a[24:27], v[164:167]\n"
"v_mul_f32 v192, s72, v192\n"
"v_mul_f32 v193, s72, v193\n"
"v_mfma_f32_16x16x32_bf16 v[164:167], a[236:239], a[28:31], v[164:167]\n"
"v_mul_f32 v194, s72, v194\n"
"v_mul_f32 v195, s72, v195\n"
"v_mfma_f32_16x16x32_bf16 v[172:175], a[28:31], a[172:175], 0\n"
"buffer_load_dwordx4 v152, s[24:27], s54 offen offset:2176 lds\n"
"v_cvt_pk_bf16_f32 v192, v192, v193\n"
"v_mfma_f32_16x16x32_bf16 v[172:175], a[24:27], a[168:171], v[172:175]\n"
"v_cvt_pk_bf16_f32 v193, v194, v195\n"
"v_cvt_pk_bf16_f32 v125, v129, v125\n"
"v_mfma_f32_16x16x32_bf16 v[172:175], a[20:23], a[164:167], v[172:175]\n"
"v_mov_b32 v129, 0\n"
"v_mul_f32 v164, s72, v164\n"
"v_mfma_f32_16x16x32_bf16 v[172:175], a[16:19], a[160:163], v[172:175]\n"
"v_mul_f32 v165, s72, v165\n"
"v_mul_f32 v166, s72, v166\n"
"v_mfma_f32_16x16x32_bf16 v[176:179], a[28:31], a[188:191], 0\n"
"v_mul_f32 v167, s72, v167\n"
"v_cvt_pk_bf16_f32 v194, v164, v165\n"
"v_mfma_f32_16x16x32_bf16 v[176:179], a[24:27], a[184:187], v[176:179]\n"
"v_cvt_pk_bf16_f32 v195, v166, v167\n"
"v_lshlrev_b32 v130, 16, v126\n"
"v_mfma_f32_16x16x32_bf16 v[176:179], a[20:23], a[180:183], v[176:179]\n"
"v_cvt_pk_bf16_f32 v50, v172, v173\n"
"v_cvt_pk_bf16_f32 v51, v174, v175\n"
"v_mfma_f32_16x16x32_bf16 v[176:179], a[16:19], a[176:179], v[176:179]\n"
"v_and_b32 v126, 0xffff0000, v126\n"
"v_mul_f32 v130, s72, v130\n"
"v_mfma_f32_16x16x32_bf16 v[172:175], a[28:31], a[204:207], 0\n"
"v_mul_f32 v126, s72, v126\n"
"v_cvt_pk_bf16_f32 v126, v130, v126\n"
"v_mfma_f32_16x16x32_bf16 v[172:175], a[24:27], a[200:203], v[172:175]\n"
"v_mov_b32 v130, 0\n"
"v_lshlrev_b32 v131, 16, v127\n"
"v_mfma_f32_16x16x32_bf16 v[172:175], a[20:23], a[196:199], v[172:175]\n"
"v_cvt_pk_bf16_f32 v62, v176, v177\n"
"v_cvt_pk_bf16_f32 v63, v178, v179\n"
"v_mfma_f32_16x16x32_bf16 v[172:175], a[16:19], a[192:195], v[172:175]\n"
"v_and_b32 v127, 0xffff0000, v127\n"
"v_mul_f32 v131, s72, v131\n"
"v_mfma_f32_16x16x32_bf16 v[176:179], a[28:31], a[220:223], 0\n"
"s_waitcnt vmcnt(3)\n"
"buffer_load_dwordx4 v152, s[24:27], s55 offen offset:3264 lds\n"
"ds_read_b128 a[32:35], v154 offset:8704\n"
"v_mfma_f32_16x16x32_bf16 v[176:179], a[24:27], a[216:219], v[176:179]\n"
"ds_read_b128 a[36:39], v154 offset:8768\n"
"ds_read_b128 a[40:43], v154 offset:8832\n"
"v_mfma_f32_16x16x32_bf16 v[176:179], a[20:23], a[212:215], v[176:179]\n"
"ds_read_b128 a[44:47], v154 offset:8896\n"
"v_cvt_pk_bf16_f32 v74, v172, v173\n"
"v_mfma_f32_16x16x32_bf16 v[176:179], a[16:19], a[208:211], v[176:179]\n"
"v_cvt_pk_bf16_f32 v75, v174, v175\n"
"v_mul_f32 v127, s72, v127\n"
"v_cvt_pk_bf16_f32 v127, v131, v127\n"
"ds_write_b128 v45, v[124:127] offset:3072\n"
"v_mov_b32 v131, 0\n"
"s_nop 2\n"
"v_cvt_pk_bf16_f32 v86, v176, v177\n"
"v_cvt_pk_bf16_f32 v87, v178, v179\n"
"s_waitcnt lgkmcnt(4)\n"
"v_mfma_f32_16x16x32_bf16 v[196:199], a[48:51], a[32:35], 0\n"
"s_waitcnt lgkmcnt(3)\n"
"v_mfma_f32_16x16x32_bf16 v[196:199], a[52:55], a[36:39], v[196:199]\n"
"s_waitcnt lgkmcnt(2)\n"
"v_mfma_f32_16x16x32_bf16 v[196:199], a[56:59], a[40:43], v[196:199]\n"
"s_waitcnt lgkmcnt(1)\n"
"v_mfma_f32_16x16x32_bf16 v[196:199], a[60:63], a[44:47], v[196:199]\n"
"v_mfma_f32_16x16x32_bf16 v[176:179], a[64:67], a[32:35], 0\n"
"v_mfma_f32_16x16x32_bf16 v[176:179], a[68:71], a[36:39], v[176:179]\n"
"v_mfma_f32_16x16x32_bf16 v[176:179], a[72:75], a[40:43], v[176:179]\n"
"v_mul_f32 v196, s72, v196\n"
"v_mul_f32 v197, s72, v197\n"
"v_mfma_f32_16x16x32_bf16 v[176:179], a[76:79], a[44:47], v[176:179]\n"
"v_mul_f32 v198, s72, v198\n"
"v_mul_f32 v199, s72, v199\n"
"v_mfma_f32_16x16x32_bf16 v[200:203], a[80:83], a[32:35], 0\n"
"v_cvt_pk_bf16_f32 v196, v196, v197\n"
"v_cvt_pk_bf16_f32 v197, v198, v199\n"
"v_mfma_f32_16x16x32_bf16 v[200:203], a[84:87], a[36:39], v[200:203]\n"
"v_mfma_f32_16x16x32_bf16 v[200:203], a[88:91], a[40:43], v[200:203]\n"
"v_mul_f32 v176, s72, v176\n"
"v_mul_f32 v177, s72, v177\n"
"v_mfma_f32_16x16x32_bf16 v[200:203], a[92:95], a[44:47], v[200:203]\n"
"v_mul_f32 v178, s72, v178\n"
"v_mul_f32 v179, s72, v179\n"
"v_mfma_f32_16x16x32_bf16 v[168:171], a[224:227], a[32:35], 0\n"
"s_add_u32 m0, s28, 4352\n"
"s_nop 0\n"
"buffer_load_dwordx4 v152, s[24:27], s56 offen offset:0 lds\n"
"v_mfma_f32_16x16x32_bf16 v[168:171], a[228:231], a[36:39], v[168:171]\n"
"ds_read_b128 a[228:231], v161 offset:0\n"
"v_cvt_pk_bf16_f32 v198, v176, v177\n"
"v_mfma_f32_16x16x32_bf16 v[168:171], a[232:235], a[40:43], v[168:171]\n"
"ds_read_b128 a[232:235], v161 offset:2048\n"
"v_cvt_pk_bf16_f32 v199, v178, v179\n"
"v_mfma_f32_16x16x32_bf16 v[168:171], a[236:239], a[44:47], v[168:171]\n"
"ds_read_b64 a[236:237], v156 offset:0\n"
"ds_read_b64 a[238:239], v156 offset:64\n"
"v_mfma_f32_16x16x32_bf16 v[16:19], a[96:99], a[32:35], 0\n"
"v_mul_f32 v200, s72, v200\n"
"v_mul_f32 v201, s72, v201\n"
"v_mfma_f32_16x16x32_bf16 v[16:19], a[100:103], a[36:39], v[16:19]\n"
"v_mul_f32 v202, s72, v202\n"
"v_mul_f32 v203, s72, v203\n"
"v_mfma_f32_16x16x32_bf16 v[16:19], a[104:107], a[40:43], v[16:19]\n"
"v_cvt_pk_bf16_f32 v200, v200, v201\n"
"v_cvt_pk_bf16_f32 v201, v202, v203\n"
"v_mfma_f32_16x16x32_bf16 v[16:19], a[108:111], a[44:47], v[16:19]\n"
"v_mul_f32 v168, s72, v168\n"
"v_mul_f32 v169, s72, v169\n"
"v_mfma_f32_16x16x32_bf16 v[176:179], a[112:115], a[32:35], 0\n"
"v_mul_f32 v170, s72, v170\n"
"v_mul_f32 v171, s72, v171\n"
"v_mfma_f32_16x16x32_bf16 v[176:179], a[116:119], a[36:39], v[176:179]\n"
"v_cvt_pk_bf16_f32 v202, v168, v169\n"
"v_cvt_pk_bf16_f32 v203, v170, v171\n"
"v_mfma_f32_16x16x32_bf16 v[176:179], a[120:123], a[40:43], v[176:179]\n"
"v_cvt_pk_bf16_f32 v16, v16, v17\n"
"v_cvt_pk_bf16_f32 v17, v18, v19\n"
"v_mfma_f32_16x16x32_bf16 v[176:179], a[124:127], a[44:47], v[176:179]\n"
"buffer_load_dwordx4 v152, s[24:27], s57 offen offset:1088 lds\n"
"v_add_u32 v156, 512, v156\n"
"v_mfma_f32_16x16x32_bf16 v[20:23], a[128:131], a[32:35], 0\n"
"v_add_u32 v161, 64, v161\n"
"v_mfma_f32_16x16x32_bf16 v[20:23], a[132:135], a[36:39], v[20:23]\n"
"v_mfma_f32_16x16x32_bf16 v[20:23], a[136:139], a[40:43], v[20:23]\n"
"v_cvt_pk_bf16_f32 v18, v176, v177\n"
"v_cvt_pk_bf16_f32 v19, v178, v179\n"
"v_mfma_f32_16x16x32_bf16 v[20:23], a[140:143], a[44:47], v[20:23]\n"
"v_mfma_f32_16x16x32_bf16 v[164:167], a[144:147], a[32:35], 0\n"
"v_mfma_f32_16x16x32_bf16 v[164:167], a[148:151], a[36:39], v[164:167]\n"
"v_mfma_f32_16x16x32_bf16 v[164:167], a[152:155], a[40:43], v[164:167]\n"
"v_cvt_pk_bf16_f32 v20, v20, v21\n"
"v_cvt_pk_bf16_f32 v21, v22, v23\n"
"v_mfma_f32_16x16x32_bf16 v[164:167], a[156:159], a[44:47], v[164:167]\n"
"v_mfma_f32_16x16x32_bf16 v[52:55], a[32:35], a[160:163], 0\n"
"v_mfma_f32_16x16x32_bf16 v[52:55], a[36:39], a[164:167], v[52:55]\n"
"buffer_load_dwordx4 v152, s[24:27], s58 offen offset:2176 lds\n"
"v_mfma_f32_16x16x32_bf16 v[52:55], a[40:43], a[168:171], v[52:55]\n"
"v_cvt_pk_bf16_f32 v22, v164, v165\n"
"v_cvt_pk_bf16_f32 v23, v166, v167\n"
"v_mfma_f32_16x16x32_bf16 v[52:55], a[44:47], a[172:175], v[52:55]\n"
"v_mfma_f32_16x16x32_bf16 v[64:67], a[32:35], a[176:179], 0\n"
"v_mfma_f32_16x16x32_bf16 v[64:67], a[36:39], a[180:183], v[64:67]\n"
"v_mfma_f32_16x16x32_bf16 v[64:67], a[40:43], a[184:187], v[64:67]\n"
"v_cvt_pk_bf16_f32 v52, v52, v53\n"
"v_cvt_pk_bf16_f32 v53, v54, v55\n"
"v_mfma_f32_16x16x32_bf16 v[64:67], a[44:47], a[188:191], v[64:67]\n"
"v_mfma_f32_16x16x32_bf16 v[76:79], a[32:35], a[192:195], 0\n"
"v_mfma_f32_16x16x32_bf16 v[76:79], a[36:39], a[196:199], v[76:79]\n"
"v_mfma_f32_16x16x32_bf16 v[76:79], a[40:43], a[200:203], v[76:79]\n"
"v_cvt_pk_bf16_f32 v64, v64, v65\n"
"v_cvt_pk_bf16_f32 v65, v66, v67\n"
"v_mfma_f32_16x16x32_bf16 v[76:79], a[44:47], a[204:207], v[76:79]\n"
"v_mfma_f32_16x16x32_bf16 v[88:91], a[32:35], a[208:211], 0\n"
"v_mfma_f32_16x16x32_bf16 v[88:91], a[36:39], a[212:215], v[88:91]\n"
"s_waitcnt vmcnt(3)\n"
"buffer_load_dwordx4 v152, s[24:27], s59 offen offset:3264 lds\n"
"ds_read_b128 a[48:51], v154 offset:0\n"
"v_mfma_f32_16x16x32_bf16 v[88:91], a[40:43], a[216:219], v[88:91]\n"
"ds_read_b128 a[52:55], v154 offset:64\n"
"ds_read_b128 a[56:59], v154 offset:128\n"
"v_mfma_f32_16x16x32_bf16 v[88:91], a[44:47], a[220:223], v[88:91]\n"
"ds_read_b128 a[60:63], v154 offset:192\n"
"v_cvt_pk_bf16_f32 v76, v76, v77\n"
"v_cvt_pk_bf16_f32 v77, v78, v79\n"
"s_nop 4\n"
"v_cvt_pk_bf16_f32 v88, v88, v89\n"
"v_cvt_pk_bf16_f32 v89, v90, v91\n"
"s_waitcnt lgkmcnt(3)\n"
"v_mfma_f32_16x16x32_bf16 v[24:27], a[96:99], a[48:51], 0\n"
"s_waitcnt lgkmcnt(2)\n"
"v_mfma_f32_16x16x32_bf16 v[24:27], a[100:103], a[52:55], v[24:27]\n"
"s_waitcnt lgkmcnt(1)\n"
"v_mfma_f32_16x16x32_bf16 v[24:27], a[104:107], a[56:59], v[24:27]\n"
"s_waitcnt lgkmcnt(0)\n"
"v_mfma_f32_16x16x32_bf16 v[24:27], a[108:111], a[60:63], v[24:27]\n"
"v_mfma_f32_16x16x32_bf16 v[172:175], a[60:63], a[172:175], 0\n"
"ds_read_b128 a[172:175], v153 offset:62112\n"
"v_mfma_f32_16x16x32_bf16 v[172:175], a[56:59], a[168:171], v[172:175]\n"
"ds_read_b128 a[168:171], v153 offset:57920\n"
"v_mfma_f32_16x16x32_bf16 v[172:175], a[52:55], a[164:167], v[172:175]\n"
"ds_read_b128 a[164:167], v153 offset:53728\n"
"v_cvt_pk_bf16_f32 v24, v24, v25\n"
"v_mfma_f32_16x16x32_bf16 v[172:175], a[48:51], a[160:163], v[172:175]\n"
"ds_read_b128 a[160:163], v153 offset:49536\n"
"v_cvt_pk_bf16_f32 v25, v26, v27\n"
"v_mfma_f32_16x16x32_bf16 v[176:179], a[60:63], a[188:191], 0\n"
"ds_read_b128 a[188:191], v153 offset:63168\n"
"v_mfma_f32_16x16x32_bf16 v[176:179], a[56:59], a[184:187], v[176:179]\n"
"ds_read_b128 a[184:187], v153 offset:58976\n"
"v_mfma_f32_16x16x32_bf16 v[176:179], a[52:55], a[180:183], v[176:179]\n"
"ds_read_b128 a[180:183], v153 offset:54784\n"
"v_cvt_pk_bf16_f32 v54, v172, v173\n"
"v_mfma_f32_16x16x32_bf16 v[176:179], a[48:51], a[176:179], v[176:179]\n"
"ds_read_b128 a[176:179], v153 offset:50592\n"
"v_cvt_pk_bf16_f32 v55, v174, v175\n"
"v_mfma_f32_16x16x32_bf16 v[172:175], a[60:63], a[204:207], 0\n"
"s_add_u32 m0, s28, 8704\n"
"s_nop 0\n"
"buffer_load_dwordx4 v152, s[24:27], s60 offen offset:0 lds\n"
"v_mfma_f32_16x16x32_bf16 v[172:175], a[56:59], a[200:203], v[172:175]\n"
"ds_read_b128 a[200:203], v153 offset:60032\n"
"ds_read_b128 a[204:207], v153 offset:64224\n"
"v_mfma_f32_16x16x32_bf16 v[172:175], a[52:55], a[196:199], v[172:175]\n"
"ds_read_b128 a[196:199], v153 offset:55840\n"
"v_cvt_pk_bf16_f32 v66, v176, v177\n"
"v_mfma_f32_16x16x32_bf16 v[172:175], a[48:51], a[192:195], v[172:175]\n"
"ds_read_b128 a[192:195], v153 offset:51648\n"
"v_cvt_pk_bf16_f32 v67, v178, v179\n"
"v_mfma_f32_16x16x32_bf16 v[176:179], a[60:63], a[220:223], 0\n"
"ds_read_b128 a[220:223], v153 offset:65280\n"
"v_mfma_f32_16x16x32_bf16 v[176:179], a[56:59], a[216:219], v[176:179]\n"
"ds_read_b128 a[216:219], v153 offset:61088\n"
"v_mfma_f32_16x16x32_bf16 v[176:179], a[52:55], a[212:215], v[176:179]\n"
"ds_read_b128 a[212:215], v153 offset:56896\n"
"v_cvt_pk_bf16_f32 v78, v172, v173\n"
"v_mfma_f32_16x16x32_bf16 v[176:179], a[48:51], a[208:211], v[176:179]\n"
"ds_read_b128 a[208:211], v153 offset:52704\n"
"v_cvt_pk_bf16_f32 v79, v174, v175\n"
"v_mfma_f32_16x16x32_bf16 v[28:31], a[128:131], a[48:51], 0\n"
"v_mfma_f32_16x16x32_bf16 v[28:31], a[132:135], a[52:55], v[28:31]\n"
"v_mfma_f32_16x16x32_bf16 v[28:31], a[136:139], a[56:59], v[28:31]\n"
"v_cvt_pk_bf16_f32 v90, v176, v177\n"
"v_cvt_pk_bf16_f32 v91, v178, v179\n"
"v_mfma_f32_16x16x32_bf16 v[28:31], a[140:143], a[60:63], v[28:31]\n"
"buffer_load_dwordx4 v152, s[24:27], s61 offen offset:1088 lds\n"
"v_mfma_f32_16x16x32_bf16 v[176:179], a[112:115], a[48:51], 0\n"
"v_mfma_f32_16x16x32_bf16 v[176:179], a[116:119], a[52:55], v[176:179]\n"
"v_mfma_f32_16x16x32_bf16 v[176:179], a[120:123], a[56:59], v[176:179]\n"
"v_cvt_pk_bf16_f32 v28, v28, v29\n"
"v_cvt_pk_bf16_f32 v29, v30, v31\n"
"v_mfma_f32_16x16x32_bf16 v[176:179], a[124:127], a[60:63], v[176:179]\n"
"v_mfma_f32_16x16x32_bf16 v[168:171], a[144:147], a[48:51], 0\n"
"v_mfma_f32_16x16x32_bf16 v[168:171], a[148:151], a[52:55], v[168:171]\n"
"v_mfma_f32_16x16x32_bf16 v[168:171], a[152:155], a[56:59], v[168:171]\n"
"v_cvt_pk_bf16_f32 v26, v176, v177\n"
"v_cvt_pk_bf16_f32 v27, v178, v179\n"
"v_mfma_f32_16x16x32_bf16 v[168:171], a[156:159], a[60:63], v[168:171]\n"
"s_waitcnt lgkmcnt(0)\n"
"s_barrier\n"
"v_mfma_f32_16x16x32_bf16 v[96:99], v[204:207], a[160:163], 0\n"
"buffer_load_dwordx4 v152, s[24:27], s62 offen offset:2176 lds\n"
"ds_read_b128 a[224:227], v153 offset:13312\n"
"v_mfma_f32_16x16x32_bf16 v[96:99], v[208:211], a[164:167], v[96:99]\n"
"s_nop 0\n"
"v_cvt_pk_bf16_f32 v30, v168, v169\n"
"v_cvt_pk_bf16_f32 v31, v170, v171\n"
"v_mfma_f32_16x16x32_bf16 v[96:99], v[212:215], a[168:171], v[96:99]\n"
"v_mfma_f32_16x16x32_bf16 v[96:99], v[216:219], a[172:175], v[96:99]\n"
"v_mfma_f32_16x16x32_bf16 v[100:103], v[204:207], a[176:179], 0\n"
"v_mfma_f32_16x16x32_bf16 v[100:103], v[208:211], a[180:183], v[100:103]\n"
"v_mfma_f32_16x16x32_bf16 v[100:103], v[212:215], a[184:187], v[100:103]\n"
"v_mfma_f32_16x16x32_bf16 v[100:103], v[216:219], a[188:191], v[100:103]\n"
"v_mfma_f32_16x16x32_bf16 v[104:107], v[204:207], a[192:195], 0\n"
"v_mfma_f32_16x16x32_bf16 v[104:107], v[208:211], a[196:199], v[104:107]\n"
"v_mfma_f32_16x16x32_bf16 v[104:107], v[212:215], a[200:203], v[104:107]\n"
"v_cvt_pk_bf16_f32 v96, v96, v100\n"
"v_cvt_pk_bf16_f32 v100, v97, v101\n"
"v_mfma_f32_16x16x32_bf16 v[104:107], v[216:219], a[204:207], v[104:107]\n"
"s_waitcnt vmcnt(3)\n"
"buffer_load_dwordx4 v152, s[24:27], s63 offen offset:3264 lds\n"
"ds_read_b128 a[64:67], v154 offset:4352\n"
"v_mfma_f32_16x16x32_bf16 v[108:111], v[204:207], a[208:211], 0\n"
"ds_read_b128 a[68:71], v154 offset:4416\n"
"ds_read_b128 a[72:75], v154 offset:4480\n"
"v_mfma_f32_16x16x32_bf16 v[108:111], v[208:211], a[212:215], v[108:111]\n"
"ds_read_b128 a[76:79], v154 offset:4544\n"
"v_cvt_pk_bf16_f32 v98, v98, v102\n"
"v_mfma_f32_16x16x32_bf16 v[108:111], v[212:215], a[216:219], v[108:111]\n"
"v_cvt_pk_bf16_f32 v102, v99, v103\n"
"v_mfma_f32_16x16x32_bf16 v[108:111], v[216:219], a[220:223], v[108:111]\n"
"s_add_u32 s24, s24, 13107200\n"
"s_addc_u32 s25, s25, 0\n"
"s_sub_u32 s26, s26, 13107200\n"
"s_cselect_b32 s26, 0, s26\n"
"v_mfma_f32_16x16x32_bf16 v[112:115], v[232:235], a[172:175], 0\n"
"v_accvgpr_read_b32 v219, a240\n"
"v_mfma_f32_16x16x32_bf16 v[112:115], v[228:231], a[168:171], v[112:115]\n"
"v_cvt_pk_bf16_f32 v97, v104, v108\n"
"v_mfma_f32_16x16x32_bf16 v[112:115], v[224:227], a[164:167], v[112:115]\n"
"v_cvt_pk_bf16_f32 v101, v105, v109\n"
"v_cvt_pk_bf16_f32 v99, v106, v110\n"
"v_mfma_f32_16x16x32_bf16 v[112:115], v[220:223], a[160:163], v[112:115]\n"
"ds_write_b64 v134, v[96:97] offset:16384\n"
"v_mfma_f32_16x16x32_bf16 v[116:119], v[232:235], a[188:191], 0\n"
"v_cvt_pk_bf16_f32 v103, v107, v111\n"
"v_mfma_f32_16x16x32_bf16 v[116:119], v[228:231], a[184:187], v[116:119]\n"
"v_mfma_f32_16x16x32_bf16 v[116:119], v[224:227], a[180:183], v[116:119]\n"
"v_mfma_f32_16x16x32_bf16 v[116:119], v[220:223], a[176:179], v[116:119]\n"
"ds_write_b64 v134, v[100:101] offset:16512\n"
"v_mfma_f32_16x16x32_bf16 v[108:111], v[0:3], v[188:191], 0\n"
"v_mfma_f32_16x16x32_bf16 v[108:111], v[4:7], v[192:195], v[108:111]\n"
"ds_write_b64 v134, v[98:99] offset:16640\n"
".Lpad1_%=:\n"
"s_cbranch_vccnz .Lpad6_%=\n"
"v_mfma_f32_16x16x32_bf16 v[96:99], v[0:3], v[180:183], 0\n"
"v_cvt_pk_bf16_f32 v112, v112, v116\n"
"v_cvt_pk_bf16_f32 v116, v113, v117\n"
"v_mfma_f32_16x16x32_bf16 v[96:99], v[4:7], v[184:187], v[96:99]\n"
"ds_write_b64 v134, v[102:103] offset:16768\n"
"v_mfma_f32_16x16x32_bf16 v[100:103], v[16:19], v[180:183], 0\n"
"v_exp_f32 v108, v108\n"
"v_mfma_f32_16x16x32_bf16 v[100:103], v[20:23], v[184:187], v[100:103]\n"
"v_exp_f32 v109, v109\n"
"v_mfma_f32_16x16x32_bf16 v[120:123], v[232:235], a[204:207], 0\n"
"v_exp_f32 v96, v96\n"
"v_mfma_f32_16x16x32_bf16 v[120:123], v[228:231], a[200:203], v[120:123]\n"
"v_exp_f32 v97, v97\n"
"v_mfma_f32_16x16x32_bf16 v[120:123], v[224:227], a[196:199], v[120:123]\n"
"v_exp_f32 v98, v98\n"
"v_mfma_f32_16x16x32_bf16 v[120:123], v[220:223], a[192:195], v[120:123]\n"
"s_add_u32 m0, s28, 0\n"
"s_nop 0\n"
"buffer_load_dwordx4 v152, s[24:27], s40 offen offset:0 lds\n"
"v_mfma_f32_16x16x32_bf16 v[124:127], v[232:235], a[220:223], 0\n"
"v_exp_f32 v99, v99\n"
"v_mfma_f32_16x16x32_bf16 v[124:127], v[228:231], a[216:219], v[124:127]\n"
"v_mov_b32 v228, 0\n"
"v_mov_b32 v229, 0\n"
"v_mfma_f32_16x16x32_bf16 v[124:127], v[224:227], a[212:215], v[124:127]\n"
"v_mov_b32 v224, 0\n"
"v_mov_b32 v225, 0\n"
"v_mfma_f32_16x16x32_bf16 v[124:127], v[220:223], a[208:211], v[124:127]\n"
"v_mov_b32 v220, 0\n"
"v_mov_b32 v221, 0\n"
"v_smfmac_f32_16x16x64_bf16 v[224:227], a[236:239], v[8:15], v157\n"
"v_cvt_pk_bf16_f32 v96, v96, v97\n"
"v_smfmac_f32_16x16x64_bf16 v[220:223], a[236:239], v[0:7], v157\n"
"v_cvt_pk_bf16_f32 v97, v98, v99\n"
"v_smfmac_f32_16x16x64_bf16 v[228:231], a[236:239], v[16:23], v157\n"
"s_waitcnt lgkmcnt(0)\n"
"v_exp_f32 v100, v100\n"
"v_mfma_f32_16x16x32_bf16 v[32:35], a[96:99], a[64:67], 0\n"
"v_add_f32 v209, v224, v225\n"
"v_add_f32 v208, v220, v221\n"
"v_mfma_f32_16x16x32_bf16 v[32:35], a[100:103], a[68:71], v[32:35]\n"
"v_exp_f32 v208, v208\n"
"v_mfma_f32_16x16x32_bf16 v[32:35], a[104:107], a[72:75], v[32:35]\n"
"v_exp_f32 v209, v209\n"
"v_mfma_f32_16x16x32_bf16 v[32:35], a[108:111], a[76:79], v[32:35]\n"
"buffer_load_dwordx4 v152, s[24:27], s41 offen offset:1088 lds\n"
"v_cvt_pk_bf16_f32 v215, v208, v209\n"
"v_mfma_f32_16x16x32_bf16 v[176:179], a[112:115], a[64:67], 0\n"
"ds_write_b16 v219, v215 offset:0\n"
"v_mfma_f32_16x16x32_bf16 v[176:179], a[116:119], a[68:71], v[176:179]\n"
"ds_write_b16_d16_hi v219, v215 offset:4\n"
"v_mfma_f32_16x16x32_bf16 v[176:179], a[120:123], a[72:75], v[176:179]\n"
"v_add_f32 v210, v228, v229\n"
"v_mov_b32 v220, 0\n"
"v_mfma_f32_16x16x32_bf16 v[176:179], a[124:127], a[76:79], v[176:179]\n"
"v_exp_f32 v210, v210\n"
"v_mfma_f32_16x16x32_bf16 v[36:39], a[128:131], a[64:67], 0\n"
"v_mov_b32 v221, 0\n"
"v_mov_b32 v224, 0\n"
"v_mfma_f32_16x16x32_bf16 v[36:39], a[132:135], a[68:71], v[36:39]\n"
"v_mov_b32 v225, 0\n"
"v_mov_b32 v228, 0\n"
"v_mfma_f32_16x16x32_bf16 v[36:39], a[136:139], a[72:75], v[36:39]\n"
"v_mov_b32 v229, 0\n"
"v_add_f32 v208, v209, v208\n"
"v_mfma_f32_16x16x32_bf16 v[36:39], a[140:143], a[76:79], v[36:39]\n"
"v_exp_f32 v101, v101\n"
"v_smfmac_f32_16x16x64_bf16 v[220:223], a[236:239], v[24:31], v157\n"
"v_add_f32 v208, v210, v208\n"
"v_mfma_f32_16x16x32_bf16 v[164:167], a[144:147], a[64:67], 0\n"
"v_exp_f32 v102, v102\n"
"v_mfma_f32_16x16x32_bf16 v[164:167], a[148:151], a[68:71], v[164:167]\n"
"buffer_load_dwordx4 v152, s[24:27], s42 offen offset:2176 lds\n"
"v_cvt_pk_bf16_f32 v100, v100, v101\n"
"v_mfma_f32_16x16x32_bf16 v[164:167], a[152:155], a[72:75], v[164:167]\n"
"v_add_f32 v211, v220, v221\n"
"v_mov_b32 v220, 0\n"
"v_mfma_f32_16x16x32_bf16 v[164:167], a[156:159], a[76:79], v[164:167]\n"
"v_exp_f32 v211, v211\n"
"v_mfma_f32_16x16x32_bf16 v[56:59], a[64:67], v[236:239], 0\n"
"v_cvt_pk_bf16_f32 v216, v210, v211\n"
"v_mov_b32 v221, 0\n"
"v_mfma_f32_16x16x32_bf16 v[56:59], a[68:71], v[240:243], v[56:59]\n"
"ds_write_b16 v219, v216 offset:512\n"
"v_mfma_f32_16x16x32_bf16 v[56:59], a[72:75], v[244:247], v[56:59]\n"
"ds_write_b16_d16_hi v219, v216 offset:516\n"
"v_mfma_f32_16x16x32_bf16 v[56:59], a[76:79], v[248:251], v[56:59]\n"
"v_exp_f32 v103, v103\n"
"v_smfmac_f32_16x16x64_bf16 v[220:223], a[236:239], a[228:235], v157\n"
"ds_read_b128 a[228:231], v161 offset:0\n"
"v_mfma_f32_16x16x32_bf16 v[80:83], a[64:67], v[136:139], 0\n"
"ds_read_b128 a[232:235], v161 offset:2048\n"
"v_add_f32 v208, v211, v208\n"
"v_mfma_f32_16x16x32_bf16 v[80:83], a[68:71], v[140:143], v[80:83]\n"
"v_cvt_pk_bf16_f32 v101, v102, v103\n"
"v_cvt_pk_bf16_f32 v108, v108, v109\n"
"v_mfma_f32_16x16x32_bf16 v[80:83], a[72:75], v[144:147], v[80:83]\n"
"v_add_f32 v214, v220, v221\n"
"v_cvt_pk_bf16_f32 v113, v120, v124\n"
"v_mfma_f32_16x16x32_bf16 v[80:83], a[76:79], v[148:151], v[80:83]\n"
"s_waitcnt vmcnt(3)\n"
"buffer_load_dwordx4 v152, s[24:27], s43 offen offset:3264 lds\n"
"ds_read_b128 a[80:83], v154 offset:8704\n"
"ds_read_b128 a[84:87], v154 offset:8768\n"
"ds_read_b128 a[88:91], v154 offset:8832\n"
"v_exp_f32 v214, v214\n"
"ds_read_b128 a[92:95], v154 offset:8896\n"
"ds_write_b64 v134, v[112:113] offset:18432\n"
"v_cvt_pk_bf16_f32 v218, v214, v214\n"
"v_exp_f32 v110, v110\n"
"v_exp_f32 v111, v111\n"
"ds_write_b16 v219, v218 offset:2048\n"
"v_cvt_pk_bf16_f32 v109, v110, v111\n"
"v_cvt_pk_bf16_f32 v117, v121, v125\n"
"ds_write_b64 v134, v[116:117] offset:18560\n"
"v_cvt_pk_bf16_f32 v114, v114, v118\n"
"v_cvt_pk_bf16_f32 v118, v115, v119\n"
"v_cvt_pk_bf16_f32 v115, v122, v126\n"
"ds_write_b64 v134, v[114:115] offset:18688\n"
"v_mfma_f32_16x16x32_bf16 v[112:115], v[16:19], v[188:191], 0\n"
"v_cvt_pk_bf16_f32 v119, v123, v127\n"
"v_cvt_pk_bf16_f32 v32, v32, v33\n"
"v_mfma_f32_16x16x32_bf16 v[112:115], v[20:23], v[192:195], v[112:115]\n"
"ds_write_b64 v134, v[118:119] offset:18816\n"
"v_mfma_f32_16x16x32_bf16 v[120:123], v[12:15], v[184:187], 0\n"
"ds_read_b64 v[206:207], v163 offset:16384\n"
"v_mov_b32 v134, 0\n"
"v_mfma_f32_16x16x32_bf16 v[120:123], v[8:11], v[180:183], v[120:123]\n"
"v_cvt_pk_bf16_f32 v33, v34, v35\n"
"v_cvt_pk_bf16_f32 v34, v176, v177\n"
"v_mfma_f32_16x16x32_bf16 v[124:127], v[28:31], v[184:187], 0\n"
"v_exp_f32 v112, v112\n"
"v_mfma_f32_16x16x32_bf16 v[124:127], v[24:27], v[180:183], v[124:127]\n"
"s_waitcnt lgkmcnt(7)\n"
"v_exp_f32 v113, v113\n"
"v_mfma_f32_16x16x32_bf16 v[40:43], a[96:99], a[80:83], 0\n"
"v_exp_f32 v120, v120\n"
"v_mfma_f32_16x16x32_bf16 v[40:43], a[100:103], a[84:87], v[40:43]\n"
"v_exp_f32 v121, v121\n"
"v_mfma_f32_16x16x32_bf16 v[40:43], a[104:107], a[88:91], v[40:43]\n"
"s_waitcnt lgkmcnt(4)\n"
"v_exp_f32 v122, v122\n"
"v_mfma_f32_16x16x32_bf16 v[40:43], a[108:111], a[92:95], v[40:43]\n"
"v_exp_f32 v123, v123\n"
"v_mfma_f32_16x16x32_bf16 v[44:47], a[128:131], a[80:83], 0\n"
"v_cvt_pk_bf16_f32 v98, v120, v121\n"
"v_cvt_pk_bf16_f32 v99, v122, v123\n"
"v_mfma_f32_16x16x32_bf16 v[44:47], a[132:135], a[84:87], v[44:47]\n"
"v_exp_f32 v124, v124\n"
"v_mfma_f32_16x16x32_bf16 v[44:47], a[136:139], a[88:91], v[44:47]\n"
"v_exp_f32 v125, v125\n"
"v_mfma_f32_16x16x32_bf16 v[44:47], a[140:143], a[92:95], v[44:47]\n"
"s_add_u32 m0, s28, 4352\n"
"s_nop 0\n"
"buffer_load_dwordx4 v152, s[24:27], s44 offen offset:0 lds\n"
"v_mfma_f32_16x16x32_bf16 v[168:171], a[144:147], a[80:83], 0\n"
"v_exp_f32 v126, v126\n"
"v_mfma_f32_16x16x32_bf16 v[168:171], a[148:151], a[84:87], v[168:171]\n"
"v_exp_f32 v127, v127\n"
"v_mfma_f32_16x16x32_bf16 v[168:171], a[152:155], a[88:91], v[168:171]\n"
"v_cvt_pk_bf16_f32 v102, v124, v125\n"
"v_cvt_pk_bf16_f32 v103, v126, v127\n"
"v_mfma_f32_16x16x32_bf16 v[168:171], a[156:159], a[92:95], v[168:171]\n"
"v_exp_f32 v114, v114\n"
"v_mfma_f32_16x16x32_bf16 v[124:127], v[12:15], v[192:195], 0\n"
"v_exp_f32 v115, v115\n"
"v_mfma_f32_16x16x32_bf16 v[124:127], v[8:11], v[188:191], v[124:127]\n"
"v_cvt_pk_bf16_f32 v112, v112, v113\n"
"v_cvt_pk_bf16_f32 v113, v114, v115\n"
"v_mfma_f32_16x16x32_bf16 v[68:71], a[92:95], v[248:251], 0\n"
"ds_read_b128 v[248:251], v153 offset:30016\n"
"v_cvt_pk_bf16_f32 v35, v178, v179\n"
"v_mfma_f32_16x16x32_bf16 v[68:71], a[88:91], v[244:247], v[68:71]\n"
"ds_read_b128 v[244:247], v153 offset:25824\n"
"v_cvt_pk_bf16_f32 v36, v36, v37\n"
"v_mfma_f32_16x16x32_bf16 v[68:71], a[84:87], v[240:243], v[68:71]\n"
"ds_read_b128 v[240:243], v153 offset:21632\n"
"v_cvt_pk_bf16_f32 v37, v38, v39\n"
"v_mfma_f32_16x16x32_bf16 v[68:71], a[80:83], v[236:239], v[68:71]\n"
"ds_read_b128 v[236:239], v153 offset:17440\n"
"v_exp_f32 v124, v124\n"
"v_mfma_f32_16x16x32_bf16 v[104:107], v[32:35], v[180:183], 0\n"
"v_exp_f32 v125, v125\n"
"v_mfma_f32_16x16x32_bf16 v[116:119], v[32:35], v[188:191], 0\n"
"buffer_load_dwordx4 v152, s[24:27], s45 offen offset:1088 lds\n"
"v_cvt_pk_bf16_f32 v110, v124, v125\n"
"v_mfma_f32_16x16x32_bf16 v[176:179], a[112:115], a[80:83], 0\n"
"v_exp_f32 v126, v126\n"
"v_mfma_f32_16x16x32_bf16 v[176:179], a[116:119], a[84:87], v[176:179]\n"
"v_exp_f32 v127, v127\n"
"v_mfma_f32_16x16x32_bf16 v[176:179], a[120:123], a[88:91], v[176:179]\n"
"v_cvt_pk_bf16_f32 v111, v126, v127\n"
"v_cvt_pk_bf16_f32 v38, v164, v165\n"
"v_mfma_f32_16x16x32_bf16 v[176:179], a[124:127], a[92:95], v[176:179]\n"
"v_cvt_pk_bf16_f32 v39, v166, v167\n"
"v_cvt_pk_bf16_f32 v40, v40, v41\n"
"v_mfma_f32_16x16x32_bf16 v[92:95], a[92:95], v[148:151], 0\n"
"ds_read_b128 v[148:151], v153 offset:32128\n"
"v_cvt_pk_bf16_f32 v41, v42, v43\n"
"v_mfma_f32_16x16x32_bf16 v[92:95], a[88:91], v[144:147], v[92:95]\n"
"ds_read_b128 v[144:147], v153 offset:27936\n"
"v_cvt_pk_bf16_f32 v44, v44, v45\n"
"v_mfma_f32_16x16x32_bf16 v[92:95], a[84:87], v[140:143], v[92:95]\n"
"ds_read_b128 v[140:143], v153 offset:23744\n"
"v_cvt_pk_bf16_f32 v42, v176, v177\n"
"v_mfma_f32_16x16x32_bf16 v[92:95], a[80:83], v[136:139], v[92:95]\n"
"ds_read_b128 v[136:139], v153 offset:19552\n"
"v_cvt_pk_bf16_f32 v43, v178, v179\n"
"v_smfmac_f32_16x16x64_bf16 v[224:227], a[236:239], v[32:39], v157\n"
"v_cvt_pk_bf16_f32 v45, v46, v47\n"
"v_mfma_f32_16x16x32_bf16 v[104:107], v[36:39], v[184:187], v[104:107]\n"
"v_cvt_pk_bf16_f32 v46, v168, v169\n"
"v_cvt_pk_bf16_f32 v47, v170, v171\n"
"v_mfma_f32_16x16x32_bf16 v[116:119], v[36:39], v[192:195], v[116:119]\n"
"buffer_load_dwordx4 v152, s[24:27], s46 offen offset:2176 lds\n"
"v_cvt_pk_bf16_f32 v56, v56, v57\n"
"v_smfmac_f32_16x16x64_bf16 v[228:231], a[236:239], v[40:47], v157\n"
"ds_read_b64 a[236:237], v156 offset:0\n"
"v_mfma_f32_16x16x32_bf16 v[120:123], v[44:47], v[184:187], 0\n"
"ds_read_b64 a[238:239], v156 offset:64\n"
"v_add_f32 v212, v224, v225\n"
"v_mfma_f32_16x16x32_bf16 v[120:123], v[40:43], v[180:183], v[120:123]\n"
"v_exp_f32 v212, v212\n"
"v_mfma_f32_16x16x32_bf16 v[124:127], v[44:47], v[192:195], 0\n"
"v_add_f32 v213, v228, v229\n"
"v_add_f32 v208, v212, v208\n"
"v_mfma_f32_16x16x32_bf16 v[124:127], v[40:43], v[188:191], v[124:127]\n"
"s_waitcnt lgkmcnt(6)\n"
"v_exp_f32 v213, v213\n"
"v_mfma_f32_16x16x32_bf16 v[172:175], a[92:95], v[248:251], 0\n"
"v_cvt_pk_bf16_f32 v217, v212, v213\n"
"v_add_f32 v208, v213, v208\n"
"v_mfma_f32_16x16x32_bf16 v[172:175], a[88:91], v[244:247], v[172:175]\n"
"ds_write_b16 v219, v217 offset:1536\n"
"v_mfma_f32_16x16x32_bf16 v[172:175], a[84:87], v[240:243], v[172:175]\n"
"ds_write_b16_d16_hi v219, v217 offset:1540\n"
"v_mfma_f32_16x16x32_bf16 v[172:175], a[80:83], v[236:239], v[172:175]\n"
"s_waitcnt lgkmcnt(4)\n"
"v_accvgpr_read_b32 v219, a241\n"
"v_add_f32_dpp v208, v214, v208 quad_perm:[0,1,2,3] row_mask:0xf bank_mask:0x1\n"
"v_mfma_f32_16x16x32_bf16 v[176:179], a[92:95], v[148:151], 0\n"
"ds_read_b128 v[220:223], v219 offset:0\n"
"buffer_load_dwordx4 v152, s[24:27], s47 offen offset:3264 lds\n"
"v_mfma_f32_16x16x32_bf16 v[176:179], a[88:91], v[144:147], v[176:179]\n"
"ds_read_b128 v[224:227], v219 offset:1024\n"
"v_accvgpr_read_b32 v219, a242\n"
"v_mfma_f32_16x16x32_bf16 v[176:179], a[84:87], v[140:143], v[176:179]\n"
"ds_read2_b32 v[228:229], v219 offset0:0 offset1:4\n"
"v_add_f32_dpp v208, v208, v208 row_ror:8 row_mask:0xf bank_mask:0xf\n"
"v_mfma_f32_16x16x32_bf16 v[176:179], a[80:83], v[136:139], v[176:179]\n"
"v_exp_f32 v104, v104\n"
"v_add_f32_dpp v208, v208, v208 row_ror:4 row_mask:0xf bank_mask:0xf\n"
"v_exp_f32 v105, v105\n"
"v_exp_f32 v106, v106\n"
"v_add_f32_dpp v208, v208, v208 row_ror:2 row_mask:0xf bank_mask:0xf\n"
"v_exp_f32 v107, v107\n"
"v_cvt_pk_bf16_f32 v104, v104, v105\n"
"v_add_f32_dpp v208, v208, v208 row_ror:1 row_mask:0xf bank_mask:0xf\n"
"v_rcp_f32 v209, v208\n"
"v_exp_f32 v120, v120\n"
"v_maximum3_f32 v159, v159, v208, v209\n"
"v_cvt_pk_bf16_f32 v105, v106, v107\n"
"v_exp_f32 v121, v121\n"
"v_exp_f32 v122, v122\n"
"v_exp_f32 v123, v123\n"
"v_cvt_pk_bf16_f32 v106, v120, v121\n"
"v_cvt_pk_bf16_f32 v57, v58, v59\n"
"v_cvt_pk_bf16_f32 v107, v122, v123\n"
"v_mfma_f32_16x16x32_bf16 v[120:123], v[28:31], v[192:195], 0\n"
"v_cvt_pk_bf16_f32 v58, v68, v69\n"
"v_cvt_pk_bf16_f32 v59, v70, v71\n"
"v_mfma_f32_16x16x32_bf16 v[120:123], v[24:27], v[188:191], v[120:123]\n"
"v_cvt_pk_bf16_f32 v80, v80, v81\n"
"v_cvt_pk_bf16_f32 v81, v82, v83\n"
"v_mfma_f32_16x16x32_bf16 v[68:71], a[64:67], v[236:239], 0\n"
"ds_read2_b32 v[236:237], v158 offset0:0 offset1:2\n"
"ds_read2_b32 v[238:239], v158 offset0:128 offset1:130\n"
"v_mfma_f32_16x16x32_bf16 v[68:71], a[68:71], v[240:243], v[68:71]\n"
"v_mov_b32_dpp v158, v158 row_ror:8 row_mask:0xf bank_mask:0xf\n"
"v_cvt_pk_bf16_f32 v82, v92, v93\n"
"v_mfma_f32_16x16x32_bf16 v[68:71], a[72:75], v[244:247], v[68:71]\n"
"ds_read2_b32 v[240:241], v158 offset0:0 offset1:2\n"
"ds_read2_b32 v[242:243], v158 offset0:128 offset1:130\n"
"v_mfma_f32_16x16x32_bf16 v[68:71], a[76:79], v[248:251], v[68:71]\n"
"ds_read_b128 v[244:247], v153 offset:5120\n"
"ds_read_b128 v[248:251], v153 offset:9216\n"
"v_exp_f32 v120, v120\n"
"v_exp_f32 v121, v121\n"
"v_cvt_pk_bf16_f32 v83, v94, v95\n"
"v_mfma_f32_16x16x32_bf16 v[92:95], a[64:67], v[136:139], 0\n"
"ds_read_b128 v[136:139], v153 offset:2048\n"
"v_add_u32 v163, 512, v163\n"
"v_mfma_f32_16x16x32_bf16 v[92:95], a[68:71], v[140:143], v[92:95]\n"
"ds_read_b128 v[140:143], v153 offset:0\n"
"v_cvt_pk_bf16_f32 v68, v68, v69\n"
"v_mfma_f32_16x16x32_bf16 v[92:95], a[72:75], v[144:147], v[92:95]\n"
"ds_read_b128 v[144:147], v153 offset:4096\n"
"v_cvt_pk_bf16_f32 v69, v70, v71\n"
"v_mfma_f32_16x16x32_bf16 v[92:95], a[76:79], v[148:151], v[92:95]\n"
"ds_read_b128 v[148:151], v153 offset:8192\n"
"v_cvt_pk_bf16_f32 v70, v172, v173\n"
"v_cvt_pk_bf16_f32 v71, v174, v175\n"
"s_add_u32 s84, s84, 13107200\n"
"s_addc_u32 s85, s85, 0\n"
"v_lshlrev_b32 v204, 16, v206\n"
"v_and_b32 v205, 0xffff0000, v206\n"
"v_lshlrev_b32 v206, 16, v207\n"
"v_and_b32 v207, 0xffff0000, v207\n"
"s_waitcnt lgkmcnt(8)\n"
"v_smfmac_f32_16x16x64_bf16 v[128:131], v[236:239], v[180:187], v157\n"
"s_add_u32 m0, s28, 8704\n"
"s_nop 0\n"
"buffer_load_dwordx4 v152, s[24:27], s48 offen offset:0 lds\n"
"v_smfmac_f32_16x16x64_bf16 v[132:135], v[236:239], v[196:203], v157\n"
"s_waitcnt lgkmcnt(6)\n"
"ds_read_b128 v[236:239], v153 offset:12288\n"
"v_smfmac_f32_16x16x64_bf16 v[128:131], v[240:243], v[188:195], v157\n"
"ds_read_b128 v[240:243], v153 offset:1024\n"
"v_cvt_pk_bf16_f32 v92, v92, v93\n"
"v_cvt_pk_bf16_f32 v93, v94, v95\n"
"v_cvt_pk_bf16_f32 v94, v176, v177\n"
"v_cvt_pk_bf16_f32 v95, v178, v179\n"
"v_mfma_f32_16x16x4_4b_bf16 v[164:179], v[228:229], a[244:245], 0 cbsz:2 abid:0\n"
"s_nop 1\n"
"v_add_f32 v128, v128, v129\n"
"v_add_f32 v130, v130, v131\n"
"v_exp_f32 v128, v128\n"
"v_exp_f32 v130, v130\n"
"s_nop 0\n"
"v_cvt_pk_bf16_f32 v129, v128, v130\n"
"v_add_f32 v128, v128, v130\n"
"s_nop 1\n"
"v_smfmac_f32_16x16x64_bf16 v[164:167], v[220:223], v[48:55], v157\n"
"v_smfmac_f32_16x16x64_bf16 v[164:167], v[224:227], v[52:59], v157\n"
"v_permlane16_swap_b32 v128, v129\n"
"v_smfmac_f32_16x16x64_bf16 v[168:171], v[224:227], v[64:71], v157\n"
"v_mov_b32 v252, v128\n"
"v_smfmac_f32_16x16x64_bf16 v[168:171], v[220:223], v[60:67], v157\n"
"v_mov_b32 v253, v129\n"
"v_smfmac_f32_16x16x64_bf16 v[172:175], v[220:223], v[72:79], v157\n"
"buffer_load_dwordx4 v152, s[24:27], s49 offen offset:1088 lds\n"
"v_smfmac_f32_16x16x64_bf16 v[172:175], v[224:227], v[76:83], v157\n"
"v_fmac_f32 v204, v209, v164\n"
"v_smfmac_f32_16x16x64_bf16 v[176:179], v[224:227], v[88:95], v157\n"
"v_fmac_f32 v205, v209, v168\n"
"v_smfmac_f32_16x16x64_bf16 v[176:179], v[220:223], v[84:91], v157\n"
"v_fmac_f32 v204, v209, v165\n"
"v_smfmac_f32_16x16x64_bf16 v[252:255], a[248:251], v[96:103], v157\n"
"v_fmac_f32 v205, v209, v169\n"
"v_mfma_f32_16x16x4_4b_bf16 v[220:235], v[128:129], a[244:245], 0 cbsz:2 abid:1\n"
"buffer_load_dwordx4 v152, s[24:27], s50 offen offset:2176 lds\n"
"buffer_load_dwordx4 v152, s[24:27], s51 offen offset:3264 lds\n"
"buffer_load_dword a243, v162, s[80:83], s70 offen\n"
"buffer_load_dword a243, v162, s[80:83], s70 offen\n"
"buffer_load_dword a243, v162, s[80:83], s70 offen\n"
"buffer_load_dword a243, v162, s[80:83], s70 offen\n"
"s_waitcnt vmcnt(12) lgkmcnt(0)\n"
"s_mov_b32 s74, 7\n"
"s_nop 11\n"
"s_branch .Lp2b%=\n"
".p2align 12\n"
".Lp2f%=:\n"
".Lp2b%=:\n"
"v_exp_f32 v122, v122\n"
"v_mfma_f32_16x16x32_bf16 v[180:183], v[140:143], a[48:51], 0\n"
".Lpad2_%=:\n"
"s_cbranch_vccnz .Lback0_%=\n"
"v_mov_b32 v254, v128\n"
"v_mov_b32 v255, v129\n"
"v_mfma_f32_16x16x32_bf16 v[180:183], v[144:147], a[52:55], v[180:183]\n"
"v_exp_f32 v123, v123\n"
"v_mfma_f32_16x16x32_bf16 v[180:183], v[148:151], a[56:59], v[180:183]\n"
"s_waitcnt lgkmcnt(0)\n"
"v_fmac_f32 v206, v209, v172\n"
"v_fmac_f32 v207, v209, v176\n"
"v_mfma_f32_16x16x32_bf16 v[180:183], v[236:239], a[60:63], v[180:183]\n"
"v_fmac_f32 v206, v209, v173\n"
"v_fmac_f32 v207, v209, v177\n"
"v_mfma_f32_16x16x32_bf16 v[220:223], v[96:99], v[48:51], v[220:223]\n"
"v_cvt_pk_bf16_f32 v114, v120, v121\n"
"v_cvt_pk_bf16_f32 v115, v122, v123\n"
"v_mfma_f32_16x16x32_bf16 v[220:223], v[100:103], v[52:55], v[220:223]\n"
"v_exp_f32 v116, v116\n"
"v_mfma_f32_16x16x32_bf16 v[220:223], v[104:107], v[56:59], v[220:223]\n"
"v_cvt_pk_bf16_f32 v204, v204, v205\n"
"v_cvt_pk_bf16_f32 v205, v206, v207\n"
"v_mfma_f32_16x16x32_bf16 v[224:227], v[104:107], v[68:71], v[224:227]\n"
"v_exp_f32 v117, v117\n"
"v_mfma_f32_16x16x32_bf16 v[224:227], v[100:103], v[64:67], v[224:227]\n"
"v_pk_max_i16 v204, v204, 0\n"
"v_pk_max_i16 v205, v205, 0\n"
"v_mfma_f32_16x16x32_bf16 v[224:227], v[96:99], v[60:63], v[224:227]\n"
"buffer_store_dwordx2 v[204:205], v155, s[84:87], s70 offen offset:0 sc0 sc1\n"
"v_smfmac_f32_16x16x64_bf16 v[252:255], a[252:255], v[104:111], v157\n"
"v_cvt_pk_bf16_f32 v116, v116, v117\n"
"v_mfma_f32_16x16x32_bf16 v[228:231], v[96:99], v[72:75], v[228:231]\n"
"v_exp_f32 v118, v118\n"
"v_mfma_f32_16x16x32_bf16 v[228:231], v[100:103], v[76:79], v[228:231]\n"
"v_exp_f32 v119, v119\n"
"v_mfma_f32_16x16x32_bf16 v[228:231], v[104:107], v[80:83], v[228:231]\n"
"v_add_f32 v130, v252, v253\n"
"v_cvt_pk_bf16_f32 v117, v118, v119\n"
"v_mfma_f32_16x16x32_bf16 v[232:235], v[104:107], v[92:95], v[232:235]\n"
"v_rcp_f32 v131, v130\n"
"v_mfma_f32_16x16x32_bf16 v[232:235], v[100:103], v[88:91], v[232:235]\n"
"v_maximum3_f32 v159, v159, v130, v131\n"
"v_cvt_pk_bf16_f32 v180, v180, v181\n"
"v_mfma_f32_16x16x32_bf16 v[232:235], v[96:99], v[84:87], v[232:235]\n"
"ds_bpermute_b32 v130, v160, v131 offset:0\n"
"v_mfma_f32_16x16x32_bf16 v[96:99], v[0:3], v[196:199], 0\n"
"v_exp_f32 v124, v124\n"
"v_mfma_f32_16x16x32_bf16 v[96:99], v[4:7], v[200:203], v[96:99]\n"
"v_exp_f32 v125, v125\n"
"v_mfma_f32_16x16x32_bf16 v[120:123], v[12:15], v[200:203], 0\n"
"v_exp_f32 v126, v126\n"
"v_mfma_f32_16x16x32_bf16 v[120:123], v[8:11], v[196:199], v[120:123]\n"
"v_exp_f32 v127, v127\n"
"v_mfma_f32_16x16x32_bf16 v[100:103], v[16:19], v[196:199], 0\n"
"v_cvt_pk_bf16_f32 v118, v124, v125\n"
"v_cvt_pk_bf16_f32 v119, v126, v127\n"
"v_mfma_f32_16x16x32_bf16 v[100:103], v[20:23], v[200:203], v[100:103]\n"
"v_exp_f32 v96, v96\n"
"v_mfma_f32_16x16x32_bf16 v[124:127], v[28:31], v[200:203], 0\n"
"v_exp_f32 v97, v97\n"
"v_mfma_f32_16x16x32_bf16 v[124:127], v[24:27], v[196:199], v[124:127]\n"
"v_exp_f32 v98, v98\n"
"v_mfma_f32_16x16x32_bf16 v[104:107], v[32:35], v[196:199], 0\n"
"v_exp_f32 v99, v99\n"
"v_mfma_f32_16x16x32_bf16 v[104:107], v[36:39], v[200:203], v[104:107]\n"
"v_cvt_pk_bf16_f32 v96, v96, v97\n"
"v_exp_f32 v120, v120\n"
"v_mfma_f32_16x16x4_4b_bf16 v[204:219], v[128:129], a[244:245], 0 cbsz:2 abid:3\n"
"s_waitcnt lgkmcnt(0)\n"
"v_mul_f32_dpp v220, v130, v220 quad_perm:[0,0,0,0] row_mask:0xf bank_mask:0xf\n"
"v_mul_f32_dpp v221, v130, v221 quad_perm:[1,1,1,1] row_mask:0xf bank_mask:0xf\n"
"v_mul_f32_dpp v222, v130, v222 quad_perm:[2,2,2,2] row_mask:0xf bank_mask:0xf\n"
"v_mul_f32_dpp v223, v130, v223 quad_perm:[3,3,3,3] row_mask:0xf bank_mask:0xf\n"
"v_mul_f32_dpp v224, v130, v224 quad_perm:[0,0,0,0] row_mask:0xf bank_mask:0xf\n"
"v_mul_f32_dpp v225, v130, v225 quad_perm:[1,1,1,1] row_mask:0xf bank_mask:0xf\n"
"v_mul_f32_dpp v226, v130, v226 quad_perm:[2,2,2,2] row_mask:0xf bank_mask:0xf\n"
"v_mul_f32_dpp v227, v130, v227 quad_perm:[3,3,3,3] row_mask:0xf bank_mask:0xf\n"
"v_mfma_f32_16x16x32_bf16 v[220:223], a[0:3], a[160:163], v[220:223]\n"
"v_mul_f32_dpp v228, v130, v228 quad_perm:[0,0,0,0] row_mask:0xf bank_mask:0xf\n"
"v_mul_f32_dpp v229, v130, v229 quad_perm:[1,1,1,1] row_mask:0xf bank_mask:0xf\n"
"v_mfma_f32_16x16x32_bf16 v[220:223], a[4:7], a[164:167], v[220:223]\n"
"v_mul_f32_dpp v230, v130, v230 quad_perm:[2,2,2,2] row_mask:0xf bank_mask:0xf\n"
"v_mul_f32_dpp v231, v130, v231 quad_perm:[3,3,3,3] row_mask:0xf bank_mask:0xf\n"
"v_mfma_f32_16x16x32_bf16 v[220:223], a[8:11], a[168:171], v[220:223]\n"
"v_mul_f32_dpp v232, v130, v232 quad_perm:[0,0,0,0] row_mask:0xf bank_mask:0xf\n"
"v_mul_f32_dpp v233, v130, v233 quad_perm:[1,1,1,1] row_mask:0xf bank_mask:0xf\n"
"v_mfma_f32_16x16x32_bf16 v[220:223], a[12:15], a[172:175], v[220:223]\n"
"v_mul_f32_dpp v234, v130, v234 quad_perm:[2,2,2,2] row_mask:0xf bank_mask:0xf\n"
"v_mul_f32_dpp v235, v130, v235 quad_perm:[3,3,3,3] row_mask:0xf bank_mask:0xf\n"
"v_mfma_f32_16x16x32_bf16 v[224:227], a[12:15], a[188:191], v[224:227]\n"
"v_cvt_pk_bf16_f32 v97, v98, v99\n"
"v_mfma_f32_16x16x32_bf16 v[224:227], a[8:11], a[184:187], v[224:227]\n"
"v_exp_f32 v121, v121\n"
"v_mfma_f32_16x16x32_bf16 v[224:227], a[4:7], a[180:183], v[224:227]\n"
"v_exp_f32 v122, v122\n"
"v_mfma_f32_16x16x32_bf16 v[224:227], a[0:3], a[176:179], v[224:227]\n"
"v_exp_f32 v123, v123\n"
"v_mfma_f32_16x16x32_bf16 v[228:231], a[0:3], a[192:195], v[228:231]\n"
"v_cvt_pk_bf16_f32 v98, v120, v121\n"
"v_cvt_pk_bf16_f32 v99, v122, v123\n"
"v_mfma_f32_16x16x32_bf16 v[228:231], a[4:7], a[196:199], v[228:231]\n"
"v_exp_f32 v100, v100\n"
"v_mfma_f32_16x16x32_bf16 v[228:231], a[8:11], a[200:203], v[228:231]\n"
"buffer_load_dword a243, v162, s[80:83], s88 offen sc1\n"
"v_mfma_f32_16x16x32_bf16 v[228:231], a[12:15], a[204:207], v[228:231]\n"
"v_cvt_pk_bf16_f32 v220, v220, v224\n"
"v_cvt_pk_bf16_f32 v224, v221, v225\n"
"v_mfma_f32_16x16x32_bf16 v[232:235], a[12:15], a[220:223], v[232:235]\n"
"s_waitcnt vmcnt(17)\n"
"ds_read_b128 a[12:15], v154 offset:192\n"
"v_pk_max_i16 v220, v220, 0\n"
"v_mfma_f32_16x16x32_bf16 v[232:235], a[8:11], a[216:219], v[232:235]\n"
"ds_read_b128 a[8:11], v154 offset:128\n"
"v_pk_max_i16 v224, v224, 0\n"
"v_mfma_f32_16x16x32_bf16 v[232:235], a[4:7], a[212:215], v[232:235]\n"
"ds_read_b128 a[4:7], v154 offset:64\n"
"v_cvt_pk_bf16_f32 v222, v222, v226\n"
"v_mfma_f32_16x16x32_bf16 v[232:235], a[0:3], a[208:211], v[232:235]\n"
"ds_read_b128 a[0:3], v154 offset:0\n"
"v_cvt_pk_bf16_f32 v226, v223, v227\n"
"v_mfma_f32_16x16x32_bf16 v[120:123], v[44:47], v[200:203], 0\n"
"v_pk_max_i16 v222, v222, 0\n"
"v_pk_max_i16 v226, v226, 0\n"
"v_mfma_f32_16x16x32_bf16 v[120:123], v[40:43], v[196:199], v[120:123]\n"
"v_exp_f32 v101, v101\n"
"v_mfma_f32_16x16x32_bf16 v[204:207], v[108:111], v[48:51], v[204:207]\n"
"v_cvt_pk_bf16_f32 v221, v228, v232\n"
"v_cvt_pk_bf16_f32 v225, v229, v233\n"
"v_mfma_f32_16x16x32_bf16 v[204:207], v[112:115], v[52:55], v[204:207]\n"
"v_pk_max_i16 v221, v221, 0\n"
"v_pk_max_i16 v225, v225, 0\n"
"v_mfma_f32_16x16x32_bf16 v[204:207], v[116:119], v[56:59], v[204:207]\n"
"buffer_store_dwordx2 v[220:221], v162, s[84:87], 0 offen offset:0 sc0 sc1\n"
"v_mfma_f32_16x16x32_bf16 v[208:211], v[116:119], v[68:71], v[208:211]\n"
"v_cvt_pk_bf16_f32 v223, v230, v234\n"
"v_cvt_pk_bf16_f32 v227, v231, v235\n"
"v_mfma_f32_16x16x32_bf16 v[208:211], v[112:115], v[64:67], v[208:211]\n"
"v_pk_max_i16 v223, v223, 0\n"
"v_pk_max_i16 v227, v227, 0\n"
"v_mfma_f32_16x16x32_bf16 v[208:211], v[108:111], v[60:63], v[208:211]\n"
"v_exp_f32 v102, v102\n"
"v_smfmac_f32_16x16x64_bf16 v[252:255], a[248:251], v[112:119], v157\n"
"v_cvt_pk_bf16_f32 v100, v100, v101\n"
"v_mfma_f32_16x16x32_bf16 v[188:191], v[236:239], a[76:79], 0\n"
"s_add_u32 m0, s28, 0\n"
"v_cvt_pk_bf16_f32 v181, v182, v183\n"
"v_mfma_f32_16x16x32_bf16 v[188:191], v[148:151], a[72:75], v[188:191]\n"
"s_waitcnt lgkmcnt(0)\n"
"buffer_load_dwordx4 v152, s[24:27], s52 offen offset:0 lds\n"
"v_mfma_f32_16x16x32_bf16 v[188:191], v[144:147], a[68:71], v[188:191]\n"
"v_exp_f32 v103, v103\n"
"v_mfma_f32_16x16x32_bf16 v[188:191], v[140:143], a[64:67], v[188:191]\n"
"v_exp_f32 v124, v124\n"
"v_mfma_f32_16x16x32_bf16 v[196:199], v[140:143], a[80:83], 0\n"
"ds_read_b128 v[140:143], v153 offset:6144\n"
"v_cvt_pk_bf16_f32 v101, v102, v103\n"
"v_mfma_f32_16x16x32_bf16 v[196:199], v[144:147], a[84:87], v[196:199]\n"
"ds_read_b128 v[144:147], v153 offset:10240\n"
"v_add_f32 v130, v254, v255\n"
"v_mfma_f32_16x16x32_bf16 v[196:199], v[148:151], a[88:91], v[196:199]\n"
"ds_read_b128 v[148:151], v153 offset:14336\n"
"v_cvt_pk_bf16_f32 v188, v188, v189\n"
"v_mfma_f32_16x16x32_bf16 v[196:199], v[236:239], a[92:95], v[196:199]\n"
"ds_read_b128 v[236:239], v153 offset:3072\n"
"v_cvt_pk_bf16_f32 v189, v190, v191\n"
"v_mfma_f32_16x16x32_bf16 v[212:215], v[108:111], v[72:75], v[212:215]\n"
"v_exp_f32 v125, v125\n"
"v_mfma_f32_16x16x32_bf16 v[212:215], v[112:115], v[76:79], v[212:215]\n"
"v_exp_f32 v126, v126\n"
"v_mfma_f32_16x16x32_bf16 v[212:215], v[116:119], v[80:83], v[212:215]\n"
"buffer_store_dwordx2 v[224:225], v162, s[84:87], 0 offen offset:256 sc0 sc1\n"
"v_mfma_f32_16x16x32_bf16 v[216:219], v[116:119], v[92:95], v[216:219]\n"
"v_cvt_pk_bf16_f32 v196, v196, v197\n"
"v_cvt_pk_bf16_f32 v197, v198, v199\n"
"v_mfma_f32_16x16x32_bf16 v[216:219], v[112:115], v[88:91], v[216:219]\n"
"buffer_load_dwordx4 v152, s[24:27], s53 offen offset:1088 lds\n"
"v_mfma_f32_16x16x32_bf16 v[216:219], v[108:111], v[84:87], v[216:219]\n"
"v_exp_f32 v127, v127\n"
"v_mfma_f32_16x16x32_bf16 v[184:187], v[240:243], a[48:51], 0\n"
"v_rcp_f32 v131, v130\n"
"v_mfma_f32_16x16x32_bf16 v[184:187], v[244:247], a[52:55], v[184:187]\n"
"v_maximum3_f32 v159, v159, v130, v131\n"
"v_cvt_pk_bf16_f32 v102, v124, v125\n"
"v_mfma_f32_16x16x32_bf16 v[184:187], v[248:251], a[56:59], v[184:187]\n"
"ds_bpermute_b32 v130, v160, v131 offset:128\n"
"v_mfma_f32_16x16x32_bf16 v[184:187], a[224:227], a[60:63], v[184:187]\n"
"v_exp_f32 v104, v104\n"
"v_mfma_f32_16x16x32_bf16 v[192:195], a[224:227], a[76:79], 0\n"
"v_cvt_pk_bf16_f32 v103, v126, v127\n"
"v_mov_b32 v128, 0\n"
"v_mfma_f32_16x16x32_bf16 v[192:195], v[248:251], a[72:75], v[192:195]\n"
"v_exp_f32 v105, v105\n"
"v_mfma_f32_16x16x32_bf16 v[192:195], v[244:247], a[68:71], v[192:195]\n"
"v_cvt_pk_bf16_f32 v182, v184, v185\n"
"v_cvt_pk_bf16_f32 v183, v186, v187\n"
"v_mfma_f32_16x16x32_bf16 v[192:195], v[240:243], a[64:67], v[192:195]\n"
"v_exp_f32 v106, v106\n"
"v_mfma_f32_16x16x32_bf16 v[200:203], v[240:243], a[80:83], 0\n"
"ds_read_b128 v[240:243], v153 offset:7168\n"
"v_cvt_pk_bf16_f32 v104, v104, v105\n"
"v_mfma_f32_16x16x32_bf16 v[200:203], v[244:247], a[84:87], v[200:203]\n"
"ds_read_b128 v[244:247], v153 offset:11264\n"
"v_mov_b32 v129, 0\n"
"v_mfma_f32_16x16x32_bf16 v[200:203], v[248:251], a[88:91], v[200:203]\n"
"ds_read_b128 v[248:251], v153 offset:15360\n"
"v_cvt_pk_bf16_f32 v190, v192, v193\n"
"v_mfma_f32_16x16x32_bf16 v[200:203], a[224:227], a[92:95], v[200:203]\n"
"buffer_load_dwordx4 v152, s[24:27], s54 offen offset:2176 lds\n"
"v_mfma_f32_16x16x32_bf16 v[184:187], v[136:139], a[48:51], 0\n"
"s_waitcnt lgkmcnt(4)\n"
"ds_read_b128 a[224:227], v153 offset:9216\n"
"v_cvt_pk_bf16_f32 v191, v194, v195\n"
"v_mfma_f32_16x16x32_bf16 v[184:187], v[140:143], a[52:55], v[184:187]\n"
"v_exp_f32 v107, v107\n"
"v_mfma_f32_16x16x32_bf16 v[184:187], v[144:147], a[56:59], v[184:187]\n"
"v_cvt_pk_bf16_f32 v198, v200, v201\n"
"v_cvt_pk_bf16_f32 v199, v202, v203\n"
"v_mfma_f32_16x16x32_bf16 v[184:187], v[148:151], a[60:63], v[184:187]\n"
"buffer_store_dwordx2 v[222:223], v162, s[84:87], 0 offen offset:512 sc0 sc1\n"
"v_mfma_f32_16x16x32_bf16 v[192:195], v[148:151], a[76:79], 0\n"
"s_waitcnt lgkmcnt(4)\n"
"v_mul_f32_dpp v204, v130, v204 quad_perm:[0,0,0,0] row_mask:0xf bank_mask:0xf\n"
"v_mul_f32_dpp v205, v130, v205 quad_perm:[1,1,1,1] row_mask:0xf bank_mask:0xf\n"
"v_mfma_f32_16x16x32_bf16 v[192:195], v[144:147], a[72:75], v[192:195]\n"
"v_mul_f32_dpp v206, v130, v206 quad_perm:[2,2,2,2] row_mask:0xf bank_mask:0xf\n"
"v_mul_f32_dpp v207, v130, v207 quad_perm:[3,3,3,3] row_mask:0xf bank_mask:0xf\n"
"v_mfma_f32_16x16x32_bf16 v[192:195], v[140:143], a[68:71], v[192:195]\n"
"v_cvt_pk_bf16_f32 v184, v184, v185\n"
"v_cvt_pk_bf16_f32 v185, v186, v187\n"
"v_mfma_f32_16x16x32_bf16 v[192:195], v[136:139], a[64:67], v[192:195]\n"
"v_mul_f32_dpp v208, v130, v208 quad_perm:[0,0,0,0] row_mask:0xf bank_mask:0xf\n"
"v_mul_f32_dpp v209, v130, v209 quad_perm:[1,1,1,1] row_mask:0xf bank_mask:0xf\n"
"v_mfma_f32_16x16x32_bf16 v[200:203], v[136:139], a[80:83], 0\n"
"ds_read2_b32 v[136:137], v158 offset0:0 offset1:2\n"
"ds_read2_b32 v[138:139], v158 offset0:128 offset1:130\n"
"v_mfma_f32_16x16x32_bf16 v[200:203], v[140:143], a[84:87], v[200:203]\n"
"v_mul_f32_dpp v210, v130, v210 quad_perm:[2,2,2,2] row_mask:0xf bank_mask:0xf\n"
"v_mul_f32_dpp v211, v130, v211 quad_perm:[3,3,3,3] row_mask:0xf bank_mask:0xf\n"
"v_mfma_f32_16x16x32_bf16 v[200:203], v[144:147], a[88:91], v[200:203]\n"
"ds_read_b128 v[144:147], v153 offset:0\n"
"v_cvt_pk_bf16_f32 v192, v192, v193\n"
"v_mfma_f32_16x16x32_bf16 v[200:203], v[148:151], a[92:95], v[200:203]\n"
"buffer_load_dwordx4 v152, s[24:27], s55 offen offset:3264 lds\n"
"v_mfma_f32_16x16x32_bf16 v[164:167], v[236:239], a[48:51], 0\n"
"s_waitcnt lgkmcnt(3)\n"
"ds_read_b128 v[148:151], v153 offset:4096\n"
"v_cvt_pk_bf16_f32 v193, v194, v195\n"
"v_mfma_f32_16x16x32_bf16 v[164:167], v[240:243], a[52:55], v[164:167]\n"
"v_exp_f32 v120, v120\n"
"v_mfma_f32_16x16x32_bf16 v[164:167], v[244:247], a[56:59], v[164:167]\n"
"v_cvt_pk_bf16_f32 v200, v200, v201\n"
"v_cvt_pk_bf16_f32 v201, v202, v203\n"
"v_mfma_f32_16x16x32_bf16 v[164:167], v[248:251], a[60:63], v[164:167]\n"
"v_mul_f32_dpp v212, v130, v212 quad_perm:[0,0,0,0] row_mask:0xf bank_mask:0xf\n"
"v_cvt_pk_bf16_f32 v105, v106, v107\n"
"v_mfma_f32_16x16x32_bf16 v[168:171], v[248:251], a[76:79], 0\n"
"v_mul_f32_dpp v213, v130, v213 quad_perm:[1,1,1,1] row_mask:0xf bank_mask:0xf\n"
"v_mul_f32_dpp v214, v130, v214 quad_perm:[2,2,2,2] row_mask:0xf bank_mask:0xf\n"
"v_mfma_f32_16x16x32_bf16 v[168:171], v[244:247], a[72:75], v[168:171]\n"
"v_mul_f32_dpp v215, v130, v215 quad_perm:[3,3,3,3] row_mask:0xf bank_mask:0xf\n"
"v_mul_f32_dpp v216, v130, v216 quad_perm:[0,0,0,0] row_mask:0xf bank_mask:0xf\n"
"v_mfma_f32_16x16x32_bf16 v[168:171], v[240:243], a[68:71], v[168:171]\n"
"v_exp_f32 v121, v121\n"
"v_mfma_f32_16x16x32_bf16 v[168:171], v[236:239], a[64:67], v[168:171]\n"
"buffer_store_dwordx2 v[226:227], v162, s[84:87], 0 offen offset:768 sc0 sc1\n"
"v_mfma_f32_16x16x32_bf16 v[172:175], v[236:239], a[80:83], 0\n"
"ds_read_b128 v[236:239], v153 offset:8192\n"
"v_mul_f32_dpp v217, v130, v217 quad_perm:[1,1,1,1] row_mask:0xf bank_mask:0xf\n"
"v_mfma_f32_16x16x32_bf16 v[172:175], v[240:243], a[84:87], v[172:175]\n"
"ds_read_b128 v[240:243], v153 offset:12288\n"
"v_mul_f32_dpp v218, v130, v218 quad_perm:[2,2,2,2] row_mask:0xf bank_mask:0xf\n"
"v_mfma_f32_16x16x32_bf16 v[172:175], v[244:247], a[88:91], v[172:175]\n"
"ds_read_b128 v[244:247], v153 offset:1024\n"
"v_mul_f32_dpp v219, v130, v219 quad_perm:[3,3,3,3] row_mask:0xf bank_mask:0xf\n"
"v_mfma_f32_16x16x32_bf16 v[172:175], v[248:251], a[92:95], v[172:175]\n"
"ds_read_b128 v[248:251], v153 offset:5120\n"
"v_cvt_pk_bf16_f32 v186, v164, v165\n"
"v_mfma_f32_16x16x32_bf16 v[204:207], a[16:19], a[160:163], v[204:207]\n"
"v_exp_f32 v122, v122\n"
"v_mfma_f32_16x16x32_bf16 v[204:207], a[20:23], a[164:167], v[204:207]\n"
"v_exp_f32 v123, v123\n"
"v_mfma_f32_16x16x32_bf16 v[204:207], a[24:27], a[168:171], v[204:207]\n"
"v_cvt_pk_bf16_f32 v187, v166, v167\n"
"v_cvt_pk_bf16_f32 v194, v168, v169\n"
"v_mfma_f32_16x16x32_bf16 v[204:207], a[28:31], a[172:175], v[204:207]\n"
"s_waitcnt lgkmcnt(4)\n"
"v_cvt_pk_bf16_f32 v195, v170, v171\n"
"v_cvt_pk_bf16_f32 v202, v172, v173\n"
"v_smfmac_f32_16x16x64_bf16 v[132:135], v[136:139], v[180:187], v157\n"
"v_cvt_pk_bf16_f32 v203, v174, v175\n"
"v_mfma_f32_16x16x32_bf16 v[208:211], a[28:31], a[188:191], v[208:211]\n"
"v_cvt_pk_bf16_f32 v106, v120, v121\n"
"v_cvt_pk_bf16_f32 v107, v122, v123\n"
"v_mfma_f32_16x16x32_bf16 v[208:211], a[24:27], a[184:187], v[208:211]\n"
"v_mov_b32_dpp v158, v158 row_ror:8 row_mask:0xf bank_mask:0xf\n"
"v_mov_b32 v130, 0\n"
"v_mfma_f32_16x16x32_bf16 v[208:211], a[20:23], a[180:183], v[208:211]\n"
"ds_read2_b32 v[140:141], v158 offset0:0 offset1:2\n"
"ds_read2_b32 v[142:143], v158 offset0:128 offset1:130\n"
"v_mfma_f32_16x16x32_bf16 v[208:211], a[16:19], a[176:179], v[208:211]\n"
"v_add_f32 v132, v132, v133\n"
"v_add_f32 v134, v134, v135\n"
"v_mfma_f32_16x16x32_bf16 v[212:215], a[16:19], a[192:195], v[212:215]\n"
"v_exp_f32 v132, v132\n"
"v_mfma_f32_16x16x32_bf16 v[212:215], a[20:23], a[196:199], v[212:215]\n"
"v_exp_f32 v134, v134\n"
"v_mfma_f32_16x16x32_bf16 v[212:215], a[24:27], a[200:203], v[212:215]\n"
"v_cvt_pk_bf16_f32 v133, v132, v134\n"
"v_add_f32 v132, v132, v134\n"
"v_mfma_f32_16x16x32_bf16 v[212:215], a[28:31], a[204:207], v[212:215]\n"
"v_mov_b32 v131, 0\n"
"v_cvt_pk_bf16_f32 v204, v204, v208\n"
"v_mfma_f32_16x16x32_bf16 v[216:219], a[28:31], a[220:223], v[216:219]\n"
"s_waitcnt vmcnt(14)\n"
"ds_read_b128 a[28:31], v154 offset:4544\n"
"v_cvt_pk_bf16_f32 v208, v205, v209\n"
"v_mfma_f32_16x16x32_bf16 v[216:219], a[24:27], a[216:219], v[216:219]\n"
"ds_read_b128 a[24:27], v154 offset:4480\n"
"v_pk_max_i16 v204, v204, 0\n"
"v_mfma_f32_16x16x32_bf16 v[216:219], a[20:23], a[212:215], v[216:219]\n"
"ds_read_b128 a[20:23], v154 offset:4416\n"
"v_pk_max_i16 v208, v208, 0\n"
"v_mfma_f32_16x16x32_bf16 v[216:219], a[16:19], a[208:211], v[216:219]\n"
"ds_read_b128 a[16:19], v154 offset:4352\n"
"v_cvt_pk_bf16_f32 v206, v206, v210\n"
"v_mfma_f32_16x16x32_bf16 v[108:111], v[0:3], v[180:183], 0\n"
"v_permlane16_swap_b32 v132, v133\n"
"v_mfma_f32_16x16x32_bf16 v[108:111], v[4:7], v[184:187], v[108:111]\n"
"s_waitcnt lgkmcnt(4)\n"
"v_cvt_pk_bf16_f32 v210, v207, v211\n"
"v_pk_max_i16 v206, v206, 0\n"
"v_smfmac_f32_16x16x64_bf16 v[128:131], v[140:143], v[188:195], v157\n"
"ds_read_b128 v[140:143], v153 offset:16384\n"
"v_smfmac_f32_16x16x64_bf16 v[128:131], v[136:139], v[196:203], v157\n"
"ds_read_b128 v[136:139], v153 offset:13312\n"
"v_mfma_f32_16x16x32_bf16 v[124:127], v[12:15], v[184:187], 0\n"
"v_cvt_pk_bf16_f32 v205, v212, v216\n"
"v_cvt_pk_bf16_f32 v209, v213, v217\n"
"v_mfma_f32_16x16x32_bf16 v[124:127], v[8:11], v[180:183], v[124:127]\n"
"v_pk_max_i16 v205, v205, 0\n"
"v_pk_max_i16 v209, v209, 0\n"
"v_mfma_f32_16x16x32_bf16 v[112:115], v[16:19], v[180:183], 0\n"
"v_add_f32 v128, v128, v129\n"
"v_add_f32 v130, v130, v131\n"
"v_mfma_f32_16x16x32_bf16 v[112:115], v[20:23], v[184:187], v[112:115]\n"
"buffer_store_dwordx2 v[204:205], v162, s[84:87], s65 offen offset:0 sc0 sc1\n"
"v_mfma_f32_16x16x4_4b_bf16 v[220:235], v[132:133], a[244:245], 0 cbsz:2 abid:1\n"
"v_exp_f32 v128, v128\n"
"v_exp_f32 v130, v130\n"
"s_add_u32 m0, s28, 4352\n"
"v_cvt_pk_bf16_f32 v207, v214, v218\n"
"v_cvt_pk_bf16_f32 v129, v128, v130\n"
"v_add_f32 v128, v128, v130\n"
"v_mfma_f32_16x16x32_bf16 v[120:123], v[28:31], v[184:187], 0\n"
"s_waitcnt lgkmcnt(2)\n"
"buffer_load_dwordx4 v152, s[24:27], s56 offen offset:0 lds\n"
"v_mfma_f32_16x16x32_bf16 v[120:123], v[24:27], v[180:183], v[120:123]\n"
"v_permlane16_swap_b32 v128, v129\n"
"v_mfma_f32_16x16x32_bf16 v[220:223], v[96:99], v[48:51], v[220:223]\n"
"v_cvt_pk_bf16_f32 v211, v215, v219\n"
"v_pk_max_i16 v207, v207, 0\n"
"v_mfma_f32_16x16x32_bf16 v[220:223], v[100:103], v[52:55], v[220:223]\n"
"v_pk_max_i16 v210, v210, 0\n"
"v_pk_max_i16 v211, v211, 0\n"
"v_mfma_f32_16x16x32_bf16 v[220:223], v[104:107], v[56:59], v[220:223]\n"
"v_mov_b32 v252, v132\n"
"v_mov_b32 v253, v133\n"
"v_mfma_f32_16x16x32_bf16 v[224:227], v[104:107], v[68:71], v[224:227]\n"
"v_exp_f32 v108, v108\n"
"v_mfma_f32_16x16x32_bf16 v[224:227], v[100:103], v[64:67], v[224:227]\n"
"v_exp_f32 v109, v109\n"
"v_mfma_f32_16x16x32_bf16 v[224:227], v[96:99], v[60:63], v[224:227]\n"
"v_exp_f32 v110, v110\n"
"v_smfmac_f32_16x16x64_bf16 v[252:255], a[248:251], v[96:103], v157\n"
"v_add_u32 v158, 64, v158\n"
"v_mfma_f32_16x16x32_bf16 v[228:231], v[96:99], v[72:75], v[228:231]\n"
"v_exp_f32 v111, v111\n"
"v_mfma_f32_16x16x32_bf16 v[228:231], v[100:103], v[76:79], v[228:231]\n"
"v_cvt_pk_bf16_f32 v108, v108, v109\n"
"v_cvt_pk_bf16_f32 v109, v110, v111\n"
"v_mfma_f32_16x16x32_bf16 v[228:231], v[104:107], v[80:83], v[228:231]\n"
"buffer_load_dwordx4 v152, s[24:27], s57 offen offset:1088 lds\n"
"v_mfma_f32_16x16x32_bf16 v[232:235], v[104:107], v[92:95], v[232:235]\n"
"v_mov_b32 v254, v132\n"
"v_mov_b32 v255, v133\n"
"v_mfma_f32_16x16x32_bf16 v[232:235], v[100:103], v[88:91], v[232:235]\n"
"v_exp_f32 v124, v124\n"
"v_mfma_f32_16x16x32_bf16 v[232:235], v[96:99], v[84:87], v[232:235]\n"
"buffer_store_dwordx2 v[208:209], v162, s[84:87], s65 offen offset:256 sc0 sc1\n"
"v_mfma_f32_16x16x32_bf16 v[116:119], v[32:35], v[180:183], 0\n"
"v_exp_f32 v125, v125\n"
"v_mfma_f32_16x16x32_bf16 v[116:119], v[36:39], v[184:187], v[116:119]\n"
"v_exp_f32 v126, v126\n"
"v_mfma_f32_16x16x32_bf16 v[96:99], v[0:3], v[188:191], 0\n"
"v_exp_f32 v127, v127\n"
"v_mfma_f32_16x16x32_bf16 v[96:99], v[4:7], v[192:195], v[96:99]\n"
"v_cvt_pk_bf16_f32 v110, v124, v125\n"
"v_cvt_pk_bf16_f32 v111, v126, v127\n"
"v_mfma_f32_16x16x32_bf16 v[124:127], v[44:47], v[184:187], 0\n"
"v_exp_f32 v112, v112\n"
"v_mfma_f32_16x16x32_bf16 v[124:127], v[40:43], v[180:183], v[124:127]\n"
"v_exp_f32 v113, v113\n"
"v_smfmac_f32_16x16x64_bf16 v[252:255], a[252:255], v[104:111], v157\n"
"v_cvt_pk_bf16_f32 v112, v112, v113\n"
"v_mfma_f32_16x16x32_bf16 v[100:103], v[16:19], v[188:191], 0\n"
"v_exp_f32 v114, v114\n"
"v_mfma_f32_16x16x32_bf16 v[100:103], v[20:23], v[192:195], v[100:103]\n"
"buffer_load_dwordx4 v152, s[24:27], s58 offen offset:2176 lds\n"
"v_mfma_f32_16x16x32_bf16 v[104:107], v[32:35], v[188:191], 0\n"
"v_add_f32 v134, v252, v253\n"
"v_mfma_f32_16x16x32_bf16 v[104:107], v[36:39], v[192:195], v[104:107]\n"
"s_sub_u32 s74, s74, 1\n"
"v_rcp_f32 v135, v134\n"
"s_cbranch_scc1 .Lepf%=\n"
"v_mfma_f32_16x16x32_bf16 v[164:167], a[144:147], a[0:3], 0\n"
"v_maximum3_f32 v159, v159, v134, v135\n"
"v_mfma_f32_16x16x32_bf16 v[164:167], a[148:151], a[4:7], v[164:167]\n"
"ds_bpermute_b32 v134, v160, v135 offset:0\n"
"v_mfma_f32_16x16x32_bf16 v[164:167], a[152:155], a[8:11], v[164:167]\n"
"v_exp_f32 v115, v115\n"
"v_mfma_f32_16x16x32_bf16 v[164:167], a[156:159], a[12:15], v[164:167]\n"
"buffer_store_dwordx2 v[206:207], v162, s[84:87], s65 offen offset:512 sc0 sc1\n"
"v_mfma_f32_16x16x32_bf16 v[168:171], a[156:159], a[28:31], 0\n"
"v_exp_f32 v120, v120\n"
"v_mfma_f32_16x16x32_bf16 v[168:171], a[152:155], a[24:27], v[168:171]\n"
"v_cvt_pk_bf16_f32 v113, v114, v115\n"
"v_mfma_f32_16x16x32_bf16 v[168:171], a[148:151], a[20:23], v[168:171]\n"
"v_exp_f32 v121, v121\n"
"v_mfma_f32_16x16x32_bf16 v[168:171], a[144:147], a[16:19], v[168:171]\n"
"v_exp_f32 v122, v122\n"
"v_mfma_f32_16x16x32_bf16 v[180:183], v[144:147], a[0:3], 0\n"
"v_exp_f32 v123, v123\n"
"v_mfma_f32_16x16x32_bf16 v[180:183], v[148:151], a[4:7], v[180:183]\n"
"v_cvt_pk_bf16_f32 v114, v120, v121\n"
"v_cvt_pk_bf16_f32 v115, v122, v123\n"
"v_mfma_f32_16x16x32_bf16 v[180:183], v[236:239], a[8:11], v[180:183]\n"
"buffer_load_dwordx4 v152, s[24:27], s59 offen offset:3264 lds\n"
"v_mfma_f32_16x16x32_bf16 v[180:183], v[240:243], a[12:15], v[180:183]\n"
"v_exp_f32 v116, v116\n"
"v_mfma_f32_16x16x32_bf16 v[120:123], v[12:15], v[192:195], 0\n"
"s_waitcnt lgkmcnt(0)\n"
"v_mul_f32_dpp v220, v134, v220 quad_perm:[0,0,0,0] row_mask:0xf bank_mask:0xf\n"
"v_mul_f32_dpp v221, v134, v221 quad_perm:[1,1,1,1] row_mask:0xf bank_mask:0xf\n"
"v_mfma_f32_16x16x32_bf16 v[120:123], v[8:11], v[188:191], v[120:123]\n"
"v_mul_f32_dpp v222, v134, v222 quad_perm:[2,2,2,2] row_mask:0xf bank_mask:0xf\n"
"v_mul_f32_dpp v223, v134, v223 quad_perm:[3,3,3,3] row_mask:0xf bank_mask:0xf\n"
"v_mfma_f32_16x16x32_bf16 v[184:187], v[244:247], a[0:3], 0\n"
"v_mul_f32_dpp v224, v134, v224 quad_perm:[0,0,0,0] row_mask:0xf bank_mask:0xf\n"
"v_mul_f32_dpp v225, v134, v225 quad_perm:[1,1,1,1] row_mask:0xf bank_mask:0xf\n"
"v_mfma_f32_16x16x32_bf16 v[184:187], v[248:251], a[4:7], v[184:187]\n"
"v_mul_f32_dpp v226, v134, v226 quad_perm:[2,2,2,2] row_mask:0xf bank_mask:0xf\n"
"v_mul_f32_dpp v227, v134, v227 quad_perm:[3,3,3,3] row_mask:0xf bank_mask:0xf\n"
"v_mfma_f32_16x16x32_bf16 v[184:187], a[224:227], a[8:11], v[184:187]\n"
"v_mul_f32_dpp v228, v134, v228 quad_perm:[0,0,0,0] row_mask:0xf bank_mask:0xf\n"
"v_mul_f32_dpp v229, v134, v229 quad_perm:[1,1,1,1] row_mask:0xf bank_mask:0xf\n"
"v_mfma_f32_16x16x32_bf16 v[184:187], v[136:139], a[12:15], v[184:187]\n"
"buffer_store_dwordx2 v[210:211], v162, s[84:87], s65 offen offset:768 sc0 sc1\n"
"v_mfma_f32_16x16x32_bf16 v[220:223], a[32:35], a[160:163], v[220:223]\n"
"v_mul_f32_dpp v230, v134, v230 quad_perm:[2,2,2,2] row_mask:0xf bank_mask:0xf\n"
"v_mul_f32_dpp v231, v134, v231 quad_perm:[3,3,3,3] row_mask:0xf bank_mask:0xf\n"
"v_mfma_f32_16x16x32_bf16 v[220:223], a[36:39], a[164:167], v[220:223]\n"
"v_mul_f32_dpp v232, v134, v232 quad_perm:[0,0,0,0] row_mask:0xf bank_mask:0xf\n"
"v_mul_f32_dpp v233, v134, v233 quad_perm:[1,1,1,1] row_mask:0xf bank_mask:0xf\n"
"v_mfma_f32_16x16x32_bf16 v[220:223], a[40:43], a[168:171], v[220:223]\n"
"v_mul_f32_dpp v234, v134, v234 quad_perm:[2,2,2,2] row_mask:0xf bank_mask:0xf\n"
"v_mul_f32_dpp v235, v134, v235 quad_perm:[3,3,3,3] row_mask:0xf bank_mask:0xf\n"
"v_mfma_f32_16x16x32_bf16 v[220:223], a[44:47], a[172:175], v[220:223]\n"
"v_exp_f32 v117, v117\n"
"v_mfma_f32_16x16x32_bf16 v[224:227], a[44:47], a[188:191], v[224:227]\n"
"v_exp_f32 v118, v118\n"
"v_mfma_f32_16x16x32_bf16 v[224:227], a[40:43], a[184:187], v[224:227]\n"
"v_exp_f32 v119, v119\n"
"v_mfma_f32_16x16x32_bf16 v[224:227], a[36:39], a[180:183], v[224:227]\n"
"v_cvt_pk_bf16_f32 v116, v116, v117\n"
"v_cvt_pk_bf16_f32 v117, v118, v119\n"
"v_mfma_f32_16x16x32_bf16 v[224:227], a[32:35], a[176:179], v[224:227]\n"
"v_exp_f32 v124, v124\n"
"v_mfma_f32_16x16x32_bf16 v[228:231], a[32:35], a[192:195], v[228:231]\n"
"v_exp_f32 v125, v125\n"
"v_mfma_f32_16x16x32_bf16 v[228:231], a[36:39], a[196:199], v[228:231]\n"
"v_exp_f32 v126, v126\n"
"v_mfma_f32_16x16x32_bf16 v[228:231], a[40:43], a[200:203], v[228:231]\n"
"v_cvt_pk_bf16_f32 v220, v220, v224\n"
"v_cvt_pk_bf16_f32 v224, v221, v225\n"
"v_mfma_f32_16x16x32_bf16 v[228:231], a[44:47], a[204:207], v[228:231]\n"
"v_pk_max_i16 v220, v220, 0\n"
"v_pk_max_i16 v224, v224, 0\n"
"v_mfma_f32_16x16x32_bf16 v[232:235], a[44:47], a[220:223], v[232:235]\n"
"s_waitcnt vmcnt(18)\n"
"ds_read_b128 a[44:47], v154 offset:8896\n"
"v_cvt_pk_bf16_f32 v222, v222, v226\n"
"v_mfma_f32_16x16x32_bf16 v[232:235], a[40:43], a[216:219], v[232:235]\n"
"ds_read_b128 a[40:43], v154 offset:8832\n"
"v_cvt_pk_bf16_f32 v226, v223, v227\n"
"v_mfma_f32_16x16x32_bf16 v[232:235], a[36:39], a[212:215], v[232:235]\n"
"ds_read_b128 a[36:39], v154 offset:8768\n"
"v_pk_max_i16 v222, v222, 0\n"
"v_mfma_f32_16x16x32_bf16 v[232:235], a[32:35], a[208:211], v[232:235]\n"
"ds_read_b128 a[32:35], v154 offset:8704\n"
"v_exp_f32 v127, v127\n"
"v_mfma_f32_16x16x4_4b_bf16 v[204:219], v[132:133], a[244:245], 0 cbsz:2 abid:3\n"
"v_pk_max_i16 v226, v226, 0\n"
"v_cvt_pk_bf16_f32 v118, v124, v125\n"
"v_cvt_pk_bf16_f32 v119, v126, v127\n"
"v_exp_f32 v96, v96\n"
"v_exp_f32 v97, v97\n"
"v_cvt_pk_bf16_f32 v221, v228, v232\n"
"v_mfma_f32_16x16x32_bf16 v[124:127], v[28:31], v[192:195], 0\n"
"v_cvt_pk_bf16_f32 v225, v229, v233\n"
"v_pk_max_i16 v221, v221, 0\n"
"v_mfma_f32_16x16x32_bf16 v[124:127], v[24:27], v[188:191], v[124:127]\n"
"buffer_store_dwordx2 v[220:221], v162, s[84:87], s66 offen offset:0 sc0 sc1\n"
"v_mfma_f32_16x16x32_bf16 v[204:207], v[108:111], v[48:51], v[204:207]\n"
"v_pk_max_i16 v225, v225, 0\n"
"v_cvt_pk_bf16_f32 v223, v230, v234\n"
"v_mfma_f32_16x16x32_bf16 v[204:207], v[112:115], v[52:55], v[204:207]\n"
"v_cvt_pk_bf16_f32 v227, v231, v235\n"
"v_pk_max_i16 v223, v223, 0\n"
"v_mfma_f32_16x16x32_bf16 v[204:207], v[116:119], v[56:59], v[204:207]\n"
"v_pk_max_i16 v227, v227, 0\n"
"v_cvt_pk_bf16_f32 v96, v96, v97\n"
"v_mfma_f32_16x16x32_bf16 v[208:211], v[116:119], v[68:71], v[208:211]\n"
"v_exp_f32 v98, v98\n"
"v_mfma_f32_16x16x32_bf16 v[208:211], v[112:115], v[64:67], v[208:211]\n"
"s_add_u32 m0, s28, 8704\n"
"v_mfma_f32_16x16x32_bf16 v[208:211], v[108:111], v[60:63], v[208:211]\n"
"s_waitcnt lgkmcnt(0)\n"
"buffer_load_dwordx4 v152, s[24:27], s60 offen offset:0 lds\n"
"v_smfmac_f32_16x16x64_bf16 v[252:255], a[248:251], v[112:119], v157\n"
"v_exp_f32 v99, v99\n"
"v_mfma_f32_16x16x32_bf16 v[212:215], v[108:111], v[72:75], v[212:215]\n"
"v_exp_f32 v120, v120\n"
"v_mfma_f32_16x16x32_bf16 v[212:215], v[112:115], v[76:79], v[212:215]\n"
"v_cvt_pk_bf16_f32 v97, v98, v99\n"
"v_mfma_f32_16x16x32_bf16 v[212:215], v[116:119], v[80:83], v[212:215]\n"
"v_exp_f32 v121, v121\n"
"v_mfma_f32_16x16x32_bf16 v[216:219], v[116:119], v[92:95], v[216:219]\n"
"v_exp_f32 v122, v122\n"
"v_mfma_f32_16x16x32_bf16 v[216:219], v[112:115], v[88:91], v[216:219]\n"
"v_exp_f32 v123, v123\n"
"v_mfma_f32_16x16x32_bf16 v[216:219], v[108:111], v[84:87], v[216:219]\n"
"buffer_store_dwordx2 v[224:225], v162, s[84:87], s66 offen offset:256 sc0 sc1\n"
"v_mfma_f32_16x16x32_bf16 v[108:111], v[0:3], v[196:199], 0\n"
"v_cvt_pk_bf16_f32 v98, v120, v121\n"
"v_cvt_pk_bf16_f32 v99, v122, v123\n"
"v_mfma_f32_16x16x32_bf16 v[108:111], v[4:7], v[200:203], v[108:111]\n"
"v_exp_f32 v100, v100\n"
"v_mfma_f32_16x16x32_bf16 v[120:123], v[44:47], v[192:195], 0\n"
"v_exp_f32 v101, v101\n"
"v_mfma_f32_16x16x32_bf16 v[120:123], v[40:43], v[188:191], v[120:123]\n"
"buffer_load_dwordx4 v152, s[24:27], s61 offen offset:1088 lds\n"
"v_mfma_f32_16x16x32_bf16 v[112:115], v[16:19], v[196:199], 0\n"
"v_exp_f32 v102, v102\n"
"v_mfma_f32_16x16x32_bf16 v[112:115], v[20:23], v[200:203], v[112:115]\n"
"v_exp_f32 v103, v103\n"
"v_mfma_f32_16x16x32_bf16 v[116:119], v[32:35], v[196:199], 0\n"
"v_add_f32 v134, v254, v255\n"
"v_cvt_pk_bf16_f32 v100, v100, v101\n"
"v_mfma_f32_16x16x32_bf16 v[116:119], v[36:39], v[200:203], v[116:119]\n"
"v_rcp_f32 v135, v134\n"
"v_mfma_f32_16x16x32_bf16 v[0:3], a[96:99], a[0:3], 0\n"
"v_maximum3_f32 v159, v159, v134, v135\n"
"v_cvt_pk_bf16_f32 v101, v102, v103\n"
"v_mfma_f32_16x16x32_bf16 v[0:3], a[100:103], a[4:7], v[0:3]\n"
"ds_bpermute_b32 v134, v160, v135 offset:128\n"
"v_mfma_f32_16x16x32_bf16 v[0:3], a[104:107], a[8:11], v[0:3]\n"
"v_exp_f32 v124, v124\n"
"v_mfma_f32_16x16x32_bf16 v[0:3], a[108:111], a[12:15], v[0:3]\n"
"v_exp_f32 v125, v125\n"
"v_mfma_f32_16x16x32_bf16 v[16:19], a[96:99], a[32:35], 0\n"
"v_exp_f32 v126, v126\n"
"v_mfma_f32_16x16x32_bf16 v[16:19], a[100:103], a[36:39], v[16:19]\n"
"v_exp_f32 v127, v127\n"
"v_mfma_f32_16x16x32_bf16 v[16:19], a[104:107], a[40:43], v[16:19]\n"
"v_cvt_pk_bf16_f32 v102, v124, v125\n"
"v_cvt_pk_bf16_f32 v103, v126, v127\n"
"v_mfma_f32_16x16x32_bf16 v[16:19], a[108:111], a[44:47], v[16:19]\n"
"buffer_store_dwordx2 v[222:223], v162, s[84:87], s66 offen offset:512 sc0 sc1\n"
"v_mfma_f32_16x16x32_bf16 v[124:127], v[12:15], v[200:203], 0\n"
"buffer_load_dwordx4 v152, s[24:27], s62 offen offset:2176 lds\n"
"v_mfma_f32_16x16x32_bf16 v[124:127], v[8:11], v[196:199], v[124:127]\n"
".Lpad3_%=:\n"
"s_cbranch_vccnz .Lpad2_%=\n"
"v_exp_f32 v104, v104\n"
"v_mfma_f32_16x16x32_bf16 v[8:11], a[108:111], a[28:31], 0\n"
"v_exp_f32 v105, v105\n"
"v_mfma_f32_16x16x32_bf16 v[8:11], a[104:107], a[24:27], v[8:11]\n"
"v_exp_f32 v106, v106\n"
"v_mfma_f32_16x16x32_bf16 v[8:11], a[100:103], a[20:23], v[8:11]\n"
"s_waitcnt lgkmcnt(0)\n"
"v_mul_f32_dpp v204, v134, v204 quad_perm:[0,0,0,0] row_mask:0xf bank_mask:0xf\n"
"v_mul_f32_dpp v205, v134, v205 quad_perm:[1,1,1,1] row_mask:0xf bank_mask:0xf\n"
"v_mfma_f32_16x16x32_bf16 v[8:11], a[96:99], a[16:19], v[8:11]\n"
"v_mul_f32_dpp v206, v134, v206 quad_perm:[2,2,2,2] row_mask:0xf bank_mask:0xf\n"
"v_mul_f32_dpp v207, v134, v207 quad_perm:[3,3,3,3] row_mask:0xf bank_mask:0xf\n"
"v_mfma_f32_16x16x32_bf16 v[4:7], a[112:115], a[0:3], 0\n"
"v_mul_f32_dpp v208, v134, v208 quad_perm:[0,0,0,0] row_mask:0xf bank_mask:0xf\n"
"v_mul_f32_dpp v209, v134, v209 quad_perm:[1,1,1,1] row_mask:0xf bank_mask:0xf\n"
"v_mfma_f32_16x16x32_bf16 v[4:7], a[116:119], a[4:7], v[4:7]\n"
"v_mul_f32_dpp v210, v134, v210 quad_perm:[2,2,2,2] row_mask:0xf bank_mask:0xf\n"
"v_mul_f32_dpp v211, v134, v211 quad_perm:[3,3,3,3] row_mask:0xf bank_mask:0xf\n"
"v_mfma_f32_16x16x32_bf16 v[4:7], a[120:123], a[8:11], v[4:7]\n"
"v_mul_f32_dpp v212, v134, v212 quad_perm:[0,0,0,0] row_mask:0xf bank_mask:0xf\n"
"v_mul_f32_dpp v213, v134, v213 quad_perm:[1,1,1,1] row_mask:0xf bank_mask:0xf\n"
"v_mfma_f32_16x16x32_bf16 v[4:7], a[124:127], a[12:15], v[4:7]\n"
"v_mul_f32_dpp v214, v134, v214 quad_perm:[2,2,2,2] row_mask:0xf bank_mask:0xf\n"
"v_mul_f32_dpp v215, v134, v215 quad_perm:[3,3,3,3] row_mask:0xf bank_mask:0xf\n"
"v_mfma_f32_16x16x32_bf16 v[204:207], a[48:51], a[160:163], v[204:207]\n"
"v_mul_f32_dpp v216, v134, v216 quad_perm:[0,0,0,0] row_mask:0xf bank_mask:0xf\n"
"v_mul_f32_dpp v217, v134, v217 quad_perm:[1,1,1,1] row_mask:0xf bank_mask:0xf\n"
"v_mfma_f32_16x16x32_bf16 v[204:207], a[52:55], a[164:167], v[204:207]\n"
"v_mul_f32_dpp v218, v134, v218 quad_perm:[2,2,2,2] row_mask:0xf bank_mask:0xf\n"
"v_mul_f32_dpp v219, v134, v219 quad_perm:[3,3,3,3] row_mask:0xf bank_mask:0xf\n"
"v_mfma_f32_16x16x32_bf16 v[204:207], a[56:59], a[168:171], v[204:207]\n"
"buffer_load_dwordx4 v152, s[24:27], s63 offen offset:3264 lds\n"
"v_mfma_f32_16x16x32_bf16 v[204:207], a[60:63], a[172:175], v[204:207]\n"
"buffer_store_dwordx2 v[226:227], v162, s[84:87], s66 offen offset:768 sc0 sc1\n"
"v_mfma_f32_16x16x32_bf16 v[208:211], a[60:63], a[188:191], v[208:211]\n"
"v_exp_f32 v107, v107\n"
"v_mfma_f32_16x16x32_bf16 v[208:211], a[56:59], a[184:187], v[208:211]\n"
"v_cvt_pk_bf16_f32 v104, v104, v105\n"
"v_cvt_pk_bf16_f32 v105, v106, v107\n"
"v_mfma_f32_16x16x32_bf16 v[208:211], a[52:55], a[180:183], v[208:211]\n"
"v_exp_f32 v120, v120\n"
"v_mfma_f32_16x16x32_bf16 v[208:211], a[48:51], a[176:179], v[208:211]\n"
"v_exp_f32 v121, v121\n"
"v_mfma_f32_16x16x32_bf16 v[212:215], a[48:51], a[192:195], v[212:215]\n"
"v_exp_f32 v122, v122\n"
"v_mfma_f32_16x16x32_bf16 v[212:215], a[52:55], a[196:199], v[212:215]\n"
"v_exp_f32 v123, v123\n"
"v_mfma_f32_16x16x32_bf16 v[212:215], a[56:59], a[200:203], v[212:215]\n"
"v_exp_f32 v108, v108\n"
"v_mfma_f32_16x16x32_bf16 v[212:215], a[60:63], a[204:207], v[212:215]\n"
"v_cvt_pk_bf16_f32 v106, v120, v121\n"
"v_cvt_pk_bf16_f32 v107, v122, v123\n"
"v_mfma_f32_16x16x32_bf16 v[120:123], v[28:31], v[200:203], 0\n"
"v_exp_f32 v109, v109\n"
"v_mfma_f32_16x16x32_bf16 v[120:123], v[24:27], v[196:199], v[120:123]\n"
"v_exp_f32 v110, v110\n"
"v_mfma_f32_16x16x32_bf16 v[216:219], a[60:63], a[220:223], v[216:219]\n"
"s_waitcnt vmcnt(17)\n"
"ds_read_b128 a[60:63], v154 offset:192\n"
"v_mov_b32 v252, v128\n"
"v_mfma_f32_16x16x32_bf16 v[216:219], a[56:59], a[216:219], v[216:219]\n"
"ds_read_b128 a[56:59], v154 offset:128\n"
"v_mov_b32 v253, v129\n"
"v_mfma_f32_16x16x32_bf16 v[216:219], a[52:55], a[212:215], v[216:219]\n"
"ds_read_b128 a[52:55], v154 offset:64\n"
"v_cvt_pk_bf16_f32 v108, v108, v109\n"
"v_mfma_f32_16x16x32_bf16 v[216:219], a[48:51], a[208:211], v[216:219]\n"
"ds_read_b128 a[48:51], v154 offset:0\n"
"v_cvt_pk_bf16_f32 v204, v204, v208\n"
"v_smfmac_f32_16x16x64_bf16 v[252:255], a[248:251], v[96:103], v157\n"
"v_exp_f32 v111, v111\n"
"v_mfma_f32_16x16x4_4b_bf16 v[220:235], v[128:129], a[244:245], 0 cbsz:2 abid:1\n"
"v_exp_f32 v124, v124\n"
"v_cvt_pk_bf16_f32 v109, v110, v111\n"
"s_add_u32 s24, s24, 13107200\n"
"s_addc_u32 s25, s25, 0\n"
"s_sub_u32 s26, s26, 13107200\n"
"s_cselect_b32 s26, 0, s26\n"
"v_mov_b32 v254, v128\n"
"v_mfma_f32_16x16x32_bf16 v[12:15], a[124:127], a[28:31], 0\n"
"v_mov_b32 v255, v129\n"
"v_cvt_pk_bf16_f32 v208, v205, v209\n"
"v_mfma_f32_16x16x32_bf16 v[12:15], a[120:123], a[24:27], v[12:15]\n"
"v_exp_f32 v125, v125\n"
"v_mfma_f32_16x16x32_bf16 v[12:15], a[116:119], a[20:23], v[12:15]\n"
"v_cvt_pk_bf16_f32 v205, v212, v216\n"
"v_cvt_pk_bf16_f32 v209, v213, v217\n"
"v_mfma_f32_16x16x32_bf16 v[12:15], a[112:115], a[16:19], v[12:15]\n"
"v_pk_max_i16 v204, v204, 0\n"
"v_pk_max_i16 v205, v205, 0\n"
"v_mfma_f32_16x16x32_bf16 v[220:223], v[96:99], v[48:51], v[220:223]\n"
"v_exp_f32 v126, v126\n"
"v_mfma_f32_16x16x32_bf16 v[220:223], v[100:103], v[52:55], v[220:223]\n"
"v_pk_max_i16 v208, v208, 0\n"
"v_pk_max_i16 v209, v209, 0\n"
"v_mfma_f32_16x16x32_bf16 v[220:223], v[104:107], v[56:59], v[220:223]\n"
"buffer_store_dwordx2 v[204:205], v162, s[84:87], s67 offen offset:0 sc0 sc1\n"
"v_mfma_f32_16x16x32_bf16 v[224:227], v[104:107], v[68:71], v[224:227]\n"
"v_cvt_pk_bf16_f32 v206, v206, v210\n"
"v_cvt_pk_bf16_f32 v210, v207, v211\n"
"v_mfma_f32_16x16x32_bf16 v[224:227], v[100:103], v[64:67], v[224:227]\n"
"v_cvt_pk_bf16_f32 v207, v214, v218\n"
"v_cvt_pk_bf16_f32 v211, v215, v219\n"
"v_mfma_f32_16x16x32_bf16 v[224:227], v[96:99], v[60:63], v[224:227]\n"
"v_exp_f32 v127, v127\n"
"v_mfma_f32_16x16x32_bf16 v[228:231], v[96:99], v[72:75], v[228:231]\n"
"v_pk_max_i16 v206, v206, 0\n"
"v_pk_max_i16 v207, v207, 0\n"
"v_mfma_f32_16x16x32_bf16 v[228:231], v[100:103], v[76:79], v[228:231]\n"
"v_pk_max_i16 v210, v210, 0\n"
"v_pk_max_i16 v211, v211, 0\n"
"v_mfma_f32_16x16x32_bf16 v[228:231], v[104:107], v[80:83], v[228:231]\n"
"v_cvt_pk_bf16_f32 v110, v124, v125\n"
"v_cvt_pk_bf16_f32 v111, v126, v127\n"
"v_mfma_f32_16x16x32_bf16 v[232:235], v[104:107], v[92:95], v[232:235]\n"
"v_exp_f32 v112, v112\n"
"v_mfma_f32_16x16x32_bf16 v[232:235], v[100:103], v[88:91], v[232:235]\n"
"v_exp_f32 v113, v113\n"
"v_mfma_f32_16x16x32_bf16 v[232:235], v[96:99], v[84:87], v[232:235]\n"
"v_exp_f32 v114, v114\n"
"v_mfma_f32_16x16x32_bf16 v[124:127], v[44:47], v[200:203], 0\n"
"v_exp_f32 v115, v115\n"
"v_mfma_f32_16x16x32_bf16 v[124:127], v[40:43], v[196:199], v[124:127]\n"
"buffer_store_dwordx2 v[208:209], v162, s[84:87], s67 offen offset:256 sc0 sc1\n"
"v_smfmac_f32_16x16x64_bf16 v[252:255], a[252:255], v[104:111], v157\n"
"v_cvt_pk_bf16_f32 v112, v112, v113\n"
"v_mfma_f32_16x16x32_bf16 v[20:23], a[112:115], a[32:35], 0\n"
"v_exp_f32 v120, v120\n"
"v_mfma_f32_16x16x32_bf16 v[20:23], a[116:119], a[36:39], v[20:23]\n"
"v_cvt_pk_bf16_f32 v113, v114, v115\n"
"v_mfma_f32_16x16x32_bf16 v[20:23], a[120:123], a[40:43], v[20:23]\n"
"v_add_f32 v130, v252, v253\n"
"v_mfma_f32_16x16x32_bf16 v[20:23], a[124:127], a[44:47], v[20:23]\n"
"v_rcp_f32 v131, v130\n"
"v_mfma_f32_16x16x32_bf16 v[172:175], a[144:147], a[32:35], 0\n"
"v_maximum3_f32 v159, v159, v130, v131\n"
"v_mfma_f32_16x16x32_bf16 v[172:175], a[148:151], a[36:39], v[172:175]\n"
"ds_bpermute_b32 v130, v160, v131 offset:0\n"
"v_mfma_f32_16x16x32_bf16 v[172:175], a[152:155], a[40:43], v[172:175]\n"
"v_exp_f32 v121, v121\n"
"v_mfma_f32_16x16x32_bf16 v[172:175], a[156:159], a[44:47], v[172:175]\n"
"s_waitcnt lgkmcnt(1)\n"
"v_exp_f32 v122, v122\n"
"v_mfma_f32_16x16x32_bf16 v[24:27], a[96:99], a[48:51], 0\n"
"v_exp_f32 v123, v123\n"
"v_mfma_f32_16x16x32_bf16 v[24:27], a[100:103], a[52:55], v[24:27]\n"
"v_cvt_pk_bf16_f32 v114, v120, v121\n"
"v_cvt_pk_bf16_f32 v115, v122, v123\n"
"v_mfma_f32_16x16x32_bf16 v[24:27], a[104:107], a[56:59], v[24:27]\n"
"v_exp_f32 v116, v116\n"
"v_mfma_f32_16x16x32_bf16 v[24:27], a[108:111], a[60:63], v[24:27]\n"
"buffer_store_dwordx2 v[206:207], v162, s[84:87], s67 offen offset:512 sc0 sc1\n"
"v_mfma_f32_16x16x32_bf16 v[188:191], v[240:243], a[28:31], 0\n"
"s_add_u32 m0, s28, 0\n"
"v_cvt_pk_bf16_f32 v0, v0, v1\n"
"v_mfma_f32_16x16x32_bf16 v[188:191], v[236:239], a[24:27], v[188:191]\n"
"buffer_load_dwordx4 v152, s[24:27], s40 offen offset:0 lds\n"
"v_mfma_f32_16x16x32_bf16 v[188:191], v[148:151], a[20:23], v[188:191]\n"
"v_exp_f32 v117, v117\n"
"v_mfma_f32_16x16x32_bf16 v[188:191], v[144:147], a[16:19], v[188:191]\n"
"v_exp_f32 v118, v118\n"
"v_mfma_f32_16x16x32_bf16 v[196:199], v[144:147], a[32:35], 0\n"
"s_waitcnt lgkmcnt(0)\n"
"ds_read_b128 v[144:147], v153 offset:20576\n"
"v_mul_f32_dpp v220, v130, v220 quad_perm:[0,0,0,0] row_mask:0xf bank_mask:0xf\n"
"v_mfma_f32_16x16x32_bf16 v[196:199], v[148:151], a[36:39], v[196:199]\n"
"ds_read_b128 v[148:151], v153 offset:24768\n"
"v_mul_f32_dpp v221, v130, v221 quad_perm:[1,1,1,1] row_mask:0xf bank_mask:0xf\n"
"v_mfma_f32_16x16x32_bf16 v[196:199], v[236:239], a[40:43], v[196:199]\n"
"ds_read_b128 v[236:239], v153 offset:28960\n"
"v_mul_f32_dpp v222, v130, v222 quad_perm:[2,2,2,2] row_mask:0xf bank_mask:0xf\n"
"v_mfma_f32_16x16x32_bf16 v[196:199], v[240:243], a[44:47], v[196:199]\n"
"ds_read_b128 v[240:243], v153 offset:2048\n"
"v_mul_f32_dpp v223, v130, v223 quad_perm:[3,3,3,3] row_mask:0xf bank_mask:0xf\n"
"v_mfma_f32_16x16x32_bf16 v[176:179], a[112:115], a[48:51], 0\n"
"v_mul_f32_dpp v224, v130, v224 quad_perm:[0,0,0,0] row_mask:0xf bank_mask:0xf\n"
"v_mul_f32_dpp v225, v130, v225 quad_perm:[1,1,1,1] row_mask:0xf bank_mask:0xf\n"
"v_mfma_f32_16x16x32_bf16 v[176:179], a[116:119], a[52:55], v[176:179]\n"
"v_mul_f32_dpp v226, v130, v226 quad_perm:[2,2,2,2] row_mask:0xf bank_mask:0xf\n"
"v_mul_f32_dpp v227, v130, v227 quad_perm:[3,3,3,3] row_mask:0xf bank_mask:0xf\n"
"v_mfma_f32_16x16x32_bf16 v[176:179], a[120:123], a[56:59], v[176:179]\n"
"v_mul_f32_dpp v228, v130, v228 quad_perm:[0,0,0,0] row_mask:0xf bank_mask:0xf\n"
"v_mul_f32_dpp v229, v130, v229 quad_perm:[1,1,1,1] row_mask:0xf bank_mask:0xf\n"
"v_mfma_f32_16x16x32_bf16 v[176:179], a[124:127], a[60:63], v[176:179]\n"
"buffer_load_dwordx4 v152, s[24:27], s41 offen offset:1088 lds\n"
"v_mfma_f32_16x16x32_bf16 v[220:223], a[64:67], a[160:163], v[220:223]\n"
"v_mul_f32_dpp v230, v130, v230 quad_perm:[2,2,2,2] row_mask:0xf bank_mask:0xf\n"
"v_mul_f32_dpp v231, v130, v231 quad_perm:[3,3,3,3] row_mask:0xf bank_mask:0xf\n"
"v_mfma_f32_16x16x32_bf16 v[220:223], a[68:71], a[164:167], v[220:223]\n"
"v_mul_f32_dpp v232, v130, v232 quad_perm:[0,0,0,0] row_mask:0xf bank_mask:0xf\n"
"v_mul_f32_dpp v233, v130, v233 quad_perm:[1,1,1,1] row_mask:0xf bank_mask:0xf\n"
"v_mfma_f32_16x16x32_bf16 v[220:223], a[72:75], a[168:171], v[220:223]\n"
"v_mul_f32_dpp v234, v130, v234 quad_perm:[2,2,2,2] row_mask:0xf bank_mask:0xf\n"
"v_mul_f32_dpp v235, v130, v235 quad_perm:[3,3,3,3] row_mask:0xf bank_mask:0xf\n"
"v_mfma_f32_16x16x32_bf16 v[220:223], a[76:79], a[172:175], v[220:223]\n"
"buffer_store_dwordx2 v[210:211], v162, s[84:87], s67 offen offset:768 sc0 sc1\n"
"v_mfma_f32_16x16x32_bf16 v[224:227], a[76:79], a[188:191], v[224:227]\n"
"v_exp_f32 v119, v119\n"
"v_mfma_f32_16x16x32_bf16 v[224:227], a[72:75], a[184:187], v[224:227]\n"
"v_cvt_pk_bf16_f32 v116, v116, v117\n"
"v_cvt_pk_bf16_f32 v117, v118, v119\n"
"v_mfma_f32_16x16x32_bf16 v[224:227], a[68:71], a[180:183], v[224:227]\n"
"v_exp_f32 v124, v124\n"
"v_mfma_f32_16x16x32_bf16 v[224:227], a[64:67], a[176:179], v[224:227]\n"
"v_exp_f32 v125, v125\n"
"v_mfma_f32_16x16x32_bf16 v[228:231], a[64:67], a[192:195], v[228:231]\n"
"v_exp_f32 v126, v126\n"
"v_mfma_f32_16x16x32_bf16 v[228:231], a[68:71], a[196:199], v[228:231]\n"
"v_exp_f32 v127, v127\n"
"v_mfma_f32_16x16x32_bf16 v[228:231], a[72:75], a[200:203], v[228:231]\n"
"v_cvt_pk_bf16_f32 v118, v124, v125\n"
"v_cvt_pk_bf16_f32 v119, v126, v127\n"
"v_mfma_f32_16x16x32_bf16 v[228:231], a[76:79], a[204:207], v[228:231]\n"
"v_cvt_pk_bf16_f32 v220, v220, v224\n"
"v_cvt_pk_bf16_f32 v224, v221, v225\n"
"v_mfma_f32_16x16x32_bf16 v[232:235], a[76:79], a[220:223], v[232:235]\n"
"buffer_load_dwordx4 v152, s[24:27], s42 offen offset:2176 lds\n"
"v_mfma_f32_16x16x32_bf16 v[232:235], a[72:75], a[216:219], v[232:235]\n"
"s_waitcnt vmcnt(16)\n"
"ds_read_b128 a[72:75], v154 offset:4480\n"
"ds_read_b128 a[76:79], v154 offset:4544\n"
"v_mfma_f32_16x16x32_bf16 v[232:235], a[68:71], a[212:215], v[232:235]\n"
"ds_read_b128 a[68:71], v154 offset:4416\n"
"v_pk_max_i16 v220, v220, 0\n"
"v_mfma_f32_16x16x32_bf16 v[232:235], a[64:67], a[208:211], v[232:235]\n"
"ds_read_b128 a[64:67], v154 offset:4352\n"
"v_pk_max_i16 v224, v224, 0\n"
"v_cvt_pk_bf16_f32 v222, v222, v226\n"
"v_mfma_f32_16x16x4_4b_bf16 v[204:219], v[128:129], a[244:245], 0 cbsz:2 abid:3\n"
"ds_read_b64 a[244:245], v163 offset:0\n"
"v_cvt_pk_bf16_f32 v226, v223, v227\n"
"v_pk_max_i16 v222, v222, 0\n"
"v_pk_max_i16 v226, v226, 0\n"
"v_cvt_pk_bf16_f32 v1, v2, v3\n"
"v_cvt_pk_bf16_f32 v8, v8, v9\n"
"v_cvt_pk_bf16_f32 v9, v10, v11\n"
"v_cvt_pk_bf16_f32 v221, v228, v232\n"
"v_smfmac_f32_16x16x64_bf16 v[252:255], a[248:251], v[112:119], v157\n"
"v_cvt_pk_bf16_f32 v225, v229, v233\n"
"v_mfma_f32_16x16x32_bf16 v[192:195], v[136:139], a[28:31], 0\n"
"v_pk_max_i16 v221, v221, 0\n"
"v_pk_max_i16 v225, v225, 0\n"
"v_mfma_f32_16x16x32_bf16 v[192:195], a[224:227], a[24:27], v[192:195]\n"
"v_cvt_pk_bf16_f32 v223, v230, v234\n"
"v_cvt_pk_bf16_f32 v227, v231, v235\n"
"v_mfma_f32_16x16x32_bf16 v[192:195], v[248:251], a[20:23], v[192:195]\n"
"v_pk_max_i16 v223, v223, 0\n"
"v_pk_max_i16 v227, v227, 0\n"
"v_mfma_f32_16x16x32_bf16 v[192:195], v[244:247], a[16:19], v[192:195]\n"
"buffer_store_dwordx2 v[220:221], v162, s[84:87], s68 offen offset:0 sc0 sc1\n"
"v_mfma_f32_16x16x32_bf16 v[204:207], v[108:111], v[48:51], v[204:207]\n"
"buffer_load_dwordx4 v152, s[24:27], s43 offen offset:3264 lds\n"
"v_mfma_f32_16x16x32_bf16 v[204:207], v[112:115], v[52:55], v[204:207]\n"
"v_add_f32 v130, v254, v255\n"
"v_cvt_pk_bf16_f32 v16, v16, v17\n"
"v_mfma_f32_16x16x32_bf16 v[204:207], v[116:119], v[56:59], v[204:207]\n"
"v_rcp_f32 v131, v130\n"
"v_mfma_f32_16x16x32_bf16 v[208:211], v[116:119], v[68:71], v[208:211]\n"
"v_maximum3_f32 v159, v159, v130, v131\n"
"v_cvt_pk_bf16_f32 v17, v18, v19\n"
"v_mfma_f32_16x16x32_bf16 v[208:211], v[112:115], v[64:67], v[208:211]\n"
"ds_bpermute_b32 v130, v160, v131 offset:128\n"
"v_mfma_f32_16x16x32_bf16 v[208:211], v[108:111], v[60:63], v[208:211]\n"
"v_cvt_pk_bf16_f32 v2, v4, v5\n"
"v_cvt_pk_bf16_f32 v3, v6, v7\n"
"v_mfma_f32_16x16x32_bf16 v[212:215], v[108:111], v[72:75], v[212:215]\n"
"v_cvt_pk_bf16_f32 v10, v12, v13\n"
"v_cvt_pk_bf16_f32 v11, v14, v15\n"
"v_mfma_f32_16x16x32_bf16 v[212:215], v[112:115], v[76:79], v[212:215]\n"
"v_cvt_pk_bf16_f32 v18, v20, v21\n"
"v_cvt_pk_bf16_f32 v19, v22, v23\n"
"v_mfma_f32_16x16x32_bf16 v[212:215], v[116:119], v[80:83], v[212:215]\n"
"v_cvt_pk_bf16_f32 v24, v24, v25\n"
"v_cvt_pk_bf16_f32 v25, v26, v27\n"
"v_mfma_f32_16x16x32_bf16 v[216:219], v[116:119], v[92:95], v[216:219]\n"
"v_cvt_pk_bf16_f32 v180, v180, v181\n"
"v_cvt_pk_bf16_f32 v181, v182, v183\n"
"v_mfma_f32_16x16x32_bf16 v[216:219], v[112:115], v[88:91], v[216:219]\n"
"v_cvt_pk_bf16_f32 v188, v188, v189\n"
"v_cvt_pk_bf16_f32 v189, v190, v191\n"
"v_mfma_f32_16x16x32_bf16 v[216:219], v[108:111], v[84:87], v[216:219]\n"
"v_cvt_pk_bf16_f32 v196, v196, v197\n"
"v_cvt_pk_bf16_f32 v197, v198, v199\n"
"v_mfma_f32_16x16x32_bf16 v[4:7], a[128:131], a[0:3], 0\n"
"v_cvt_pk_bf16_f32 v26, v176, v177\n"
"v_cvt_pk_bf16_f32 v27, v178, v179\n"
"v_mfma_f32_16x16x32_bf16 v[4:7], a[132:135], a[4:7], v[4:7]\n"
"v_add_u32 v156, 512, v156\n"
"v_mov_b32 v220, 0\n"
"v_mfma_f32_16x16x32_bf16 v[4:7], a[136:139], a[8:11], v[4:7]\n"
"v_add_u32 v161, 64, v161\n"
"v_mov_b32 v221, 0\n"
"v_mfma_f32_16x16x32_bf16 v[4:7], a[140:143], a[12:15], v[4:7]\n"
"buffer_store_dwordx2 v[224:225], v162, s[84:87], s68 offen offset:256 sc0 sc1\n"
"v_mfma_f32_16x16x32_bf16 v[12:15], a[140:143], a[28:31], 0\n"
"s_waitcnt lgkmcnt(0)\n"
"v_mul_f32_dpp v204, v130, v204 quad_perm:[0,0,0,0] row_mask:0xf bank_mask:0xf\n"
"v_mul_f32_dpp v205, v130, v205 quad_perm:[1,1,1,1] row_mask:0xf bank_mask:0xf\n"
"v_mfma_f32_16x16x32_bf16 v[12:15], a[136:139], a[24:27], v[12:15]\n"
"v_mul_f32_dpp v206, v130, v206 quad_perm:[2,2,2,2] row_mask:0xf bank_mask:0xf\n"
"v_mul_f32_dpp v207, v130, v207 quad_perm:[3,3,3,3] row_mask:0xf bank_mask:0xf\n"
"v_mfma_f32_16x16x32_bf16 v[12:15], a[132:135], a[20:23], v[12:15]\n"
"v_mul_f32_dpp v208, v130, v208 quad_perm:[0,0,0,0] row_mask:0xf bank_mask:0xf\n"
"v_mul_f32_dpp v209, v130, v209 quad_perm:[1,1,1,1] row_mask:0xf bank_mask:0xf\n"
"v_mfma_f32_16x16x32_bf16 v[12:15], a[128:131], a[16:19], v[12:15]\n"
"v_mul_f32_dpp v210, v130, v210 quad_perm:[2,2,2,2] row_mask:0xf bank_mask:0xf\n"
"v_mul_f32_dpp v211, v130, v211 quad_perm:[3,3,3,3] row_mask:0xf bank_mask:0xf\n"
"v_mfma_f32_16x16x32_bf16 v[20:23], a[128:131], a[32:35], 0\n"
"v_mul_f32_dpp v212, v130, v212 quad_perm:[0,0,0,0] row_mask:0xf bank_mask:0xf\n"
"v_mul_f32_dpp v213, v130, v213 quad_perm:[1,1,1,1] row_mask:0xf bank_mask:0xf\n"
"v_mfma_f32_16x16x32_bf16 v[20:23], a[132:135], a[36:39], v[20:23]\n"
"v_mul_f32_dpp v214, v130, v214 quad_perm:[2,2,2,2] row_mask:0xf bank_mask:0xf\n"
"v_mul_f32_dpp v215, v130, v215 quad_perm:[3,3,3,3] row_mask:0xf bank_mask:0xf\n"
"v_mfma_f32_16x16x32_bf16 v[20:23], a[136:139], a[40:43], v[20:23]\n"
"v_mul_f32_dpp v216, v130, v216 quad_perm:[0,0,0,0] row_mask:0xf bank_mask:0xf\n"
"v_mul_f32_dpp v217, v130, v217 quad_perm:[1,1,1,1] row_mask:0xf bank_mask:0xf\n"
"v_mfma_f32_16x16x32_bf16 v[20:23], a[140:143], a[44:47], v[20:23]\n"
"v_mul_f32_dpp v218, v130, v218 quad_perm:[2,2,2,2] row_mask:0xf bank_mask:0xf\n"
"v_mul_f32_dpp v219, v130, v219 quad_perm:[3,3,3,3] row_mask:0xf bank_mask:0xf\n"
"v_mfma_f32_16x16x32_bf16 v[204:207], a[80:83], a[160:163], v[204:207]\n"
"v_cvt_pk_bf16_f32 v4, v4, v5\n"
"v_cvt_pk_bf16_f32 v5, v6, v7\n"
"v_mfma_f32_16x16x32_bf16 v[204:207], a[84:87], a[164:167], v[204:207]\n"
"v_cvt_pk_bf16_f32 v12, v12, v13\n"
"v_cvt_pk_bf16_f32 v13, v14, v15\n"
"v_mfma_f32_16x16x32_bf16 v[204:207], a[88:91], a[168:171], v[204:207]\n"
"v_cvt_pk_bf16_f32 v20, v20, v21\n"
"v_cvt_pk_bf16_f32 v21, v22, v23\n"
"v_mfma_f32_16x16x32_bf16 v[204:207], a[92:95], a[172:175], v[204:207]\n"
"v_cvt_pk_bf16_f32 v6, v164, v165\n"
"v_cvt_pk_bf16_f32 v7, v166, v167\n"
"v_mfma_f32_16x16x32_bf16 v[208:211], a[92:95], a[188:191], v[208:211]\n"
"v_cvt_pk_bf16_f32 v14, v168, v169\n"
"v_cvt_pk_bf16_f32 v15, v170, v171\n"
"v_mfma_f32_16x16x32_bf16 v[208:211], a[88:91], a[184:187], v[208:211]\n"
"v_cvt_pk_bf16_f32 v22, v172, v173\n"
"v_cvt_pk_bf16_f32 v23, v174, v175\n"
"v_mfma_f32_16x16x32_bf16 v[208:211], a[84:87], a[180:183], v[208:211]\n"
"v_cvt_pk_bf16_f32 v182, v184, v185\n"
"v_cvt_pk_bf16_f32 v183, v186, v187\n"
"v_mfma_f32_16x16x32_bf16 v[208:211], a[80:83], a[176:179], v[208:211]\n"
"buffer_store_dwordx2 v[222:223], v162, s[84:87], s68 offen offset:512 sc0 sc1\n"
"v_mfma_f32_16x16x32_bf16 v[212:215], a[80:83], a[192:195], v[212:215]\n"
"v_cvt_pk_bf16_f32 v190, v192, v193\n"
"v_cvt_pk_bf16_f32 v191, v194, v195\n"
"v_mfma_f32_16x16x32_bf16 v[212:215], a[84:87], a[196:199], v[212:215]\n"
"v_mov_b32 v224, 0\n"
"v_mov_b32 v225, 0\n"
"v_mfma_f32_16x16x32_bf16 v[212:215], a[88:91], a[200:203], v[212:215]\n"
"v_cvt_pk_bf16_f32 v204, v204, v208\n"
"v_cvt_pk_bf16_f32 v208, v205, v209\n"
"v_mfma_f32_16x16x32_bf16 v[212:215], a[92:95], a[204:207], v[212:215]\n"
"v_pk_max_i16 v204, v204, 0\n"
"v_pk_max_i16 v208, v208, 0\n"
"v_mfma_f32_16x16x32_bf16 v[216:219], a[92:95], a[220:223], v[216:219]\n"
"v_cvt_pk_bf16_f32 v206, v206, v210\n"
"v_cvt_pk_bf16_f32 v210, v207, v211\n"
"v_mfma_f32_16x16x32_bf16 v[216:219], a[88:91], a[216:219], v[216:219]\n"
"v_pk_max_i16 v206, v206, 0\n"
"v_pk_max_i16 v210, v210, 0\n"
"v_mfma_f32_16x16x32_bf16 v[216:219], a[84:87], a[212:215], v[216:219]\n"
"v_mov_b32 v228, 0\n"
"v_mov_b32 v229, 0\n"
"v_mfma_f32_16x16x32_bf16 v[216:219], a[80:83], a[208:211], v[216:219]\n"
"v_mov_b32 v128, 0\n"
"v_mov_b32 v129, 0\n"
"v_mfma_f32_16x16x32_bf16 v[32:35], a[108:111], a[76:79], 0\n"
"v_mov_b32 v130, 0\n"
"v_mov_b32 v131, 0\n"
"v_mfma_f32_16x16x32_bf16 v[32:35], a[104:107], a[72:75], v[32:35]\n"
"v_mov_b32 v132, 0\n"
"v_mov_b32 v133, 0\n"
"v_mfma_f32_16x16x32_bf16 v[32:35], a[100:103], a[68:71], v[32:35]\n"
"v_cvt_pk_bf16_f32 v205, v212, v216\n"
"v_cvt_pk_bf16_f32 v209, v213, v217\n"
"v_mfma_f32_16x16x32_bf16 v[32:35], a[96:99], a[64:67], v[32:35]\n"
"v_pk_max_i16 v205, v205, 0\n"
"v_pk_max_i16 v209, v209, 0\n"
"v_mfma_f32_16x16x32_bf16 v[172:175], a[124:127], a[76:79], 0\n"
"v_cvt_pk_bf16_f32 v207, v214, v218\n"
"v_cvt_pk_bf16_f32 v211, v215, v219\n"
"v_mfma_f32_16x16x32_bf16 v[172:175], a[120:123], a[72:75], v[172:175]\n"
"v_pk_max_i16 v207, v207, 0\n"
"v_pk_max_i16 v211, v211, 0\n"
"v_mfma_f32_16x16x32_bf16 v[172:175], a[116:119], a[68:71], v[172:175]\n"
"v_cvt_pk_bf16_f32 v32, v32, v33\n"
"v_cvt_pk_bf16_f32 v33, v34, v35\n"
"v_mfma_f32_16x16x32_bf16 v[172:175], a[112:115], a[64:67], v[172:175]\n"
"buffer_store_dwordx2 v[226:227], v162, s[84:87], s68 offen offset:768 sc0 sc1\n"
"v_smfmac_f32_16x16x64_bf16 v[220:223], a[236:239], v[0:7], v157\n"
"v_accvgpr_read_b32 v219, a240\n"
"v_mfma_f32_16x16x32_bf16 v[200:203], v[244:247], a[32:35], 0\n"
"s_waitcnt vmcnt(13)\n"
"ds_read_b128 v[244:247], v153 offset:6144\n"
"ds_read_b128 a[80:83], v154 offset:8704\n"
"v_mfma_f32_16x16x32_bf16 v[200:203], v[248:251], a[36:39], v[200:203]\n"
"ds_read_b128 a[84:87], v154 offset:8768\n"
"ds_read_b128 a[88:91], v154 offset:8832\n"
"v_mfma_f32_16x16x32_bf16 v[200:203], a[224:227], a[40:43], v[200:203]\n"
"ds_read_b128 a[92:95], v154 offset:8896\n"
"ds_read_b128 v[248:251], v153 offset:10240\n"
"v_mfma_f32_16x16x32_bf16 v[200:203], v[136:139], a[44:47], v[200:203]\n"
"ds_read_b128 a[224:227], v153 offset:14336\n"
"ds_read_b128 v[136:139], v153 offset:3072\n"
"v_mfma_f32_16x16x32_bf16 v[28:31], a[128:131], a[48:51], 0\n"
"v_cvt_pk_bf16_f32 v34, v172, v173\n"
"v_cvt_pk_bf16_f32 v35, v174, v175\n"
"v_mfma_f32_16x16x32_bf16 v[28:31], a[132:135], a[52:55], v[28:31]\n"
"v_mov_b32 v134, 0\n"
"v_mov_b32 v135, 0\n"
"v_mfma_f32_16x16x32_bf16 v[28:31], a[136:139], a[56:59], v[28:31]\n"
"v_cvt_pk_bf16_f32 v198, v200, v201\n"
"v_cvt_pk_bf16_f32 v199, v202, v203\n"
"v_mfma_f32_16x16x32_bf16 v[28:31], a[140:143], a[60:63], v[28:31]\n"
"v_mfma_f32_16x16x32_bf16 v[36:39], a[140:143], a[76:79], 0\n"
"v_mfma_f32_16x16x32_bf16 v[36:39], a[136:139], a[72:75], v[36:39]\n"
"v_mfma_f32_16x16x32_bf16 v[36:39], a[132:135], a[68:71], v[36:39]\n"
"v_cvt_pk_bf16_f32 v28, v28, v29\n"
"v_cvt_pk_bf16_f32 v29, v30, v31\n"
"v_mfma_f32_16x16x32_bf16 v[36:39], a[128:131], a[64:67], v[36:39]\n"
"buffer_store_dwordx2 v[204:205], v162, s[84:87], s69 offen offset:0 sc0 sc1\n"
"v_mfma_f32_16x16x32_bf16 v[48:51], a[0:3], v[140:143], 0\n"
"v_mfma_f32_16x16x32_bf16 v[48:51], a[4:7], v[144:147], v[48:51]\n"
"v_mfma_f32_16x16x32_bf16 v[48:51], a[8:11], v[148:151], v[48:51]\n"
"v_cvt_pk_bf16_f32 v36, v36, v37\n"
"v_cvt_pk_bf16_f32 v37, v38, v39\n"
"v_mfma_f32_16x16x32_bf16 v[48:51], a[12:15], v[236:239], v[48:51]\n"
"v_mfma_f32_16x16x32_bf16 v[60:63], a[28:31], v[236:239], 0\n"
"v_mfma_f32_16x16x32_bf16 v[60:63], a[24:27], v[148:151], v[60:63]\n"
"v_mfma_f32_16x16x32_bf16 v[60:63], a[20:23], v[144:147], v[60:63]\n"
"v_cvt_pk_bf16_f32 v48, v48, v49\n"
"v_cvt_pk_bf16_f32 v49, v50, v51\n"
"v_mfma_f32_16x16x32_bf16 v[60:63], a[16:19], v[140:143], v[60:63]\n"
"buffer_store_dwordx2 v[208:209], v162, s[84:87], s69 offen offset:256 sc0 sc1\n"
"v_mfma_f32_16x16x32_bf16 v[52:55], a[32:35], v[140:143], 0\n"
"s_nop 0\n"
"v_add_f32 v208, v220, v221\n"
"v_mov_b32 v220, 0\n"
"v_mfma_f32_16x16x32_bf16 v[52:55], a[36:39], v[144:147], v[52:55]\n"
"v_exp_f32 v208, v208\n"
"v_mfma_f32_16x16x32_bf16 v[52:55], a[40:43], v[148:151], v[52:55]\n"
"v_cvt_pk_bf16_f32 v50, v60, v61\n"
"v_cvt_pk_bf16_f32 v51, v62, v63\n"
"v_mfma_f32_16x16x32_bf16 v[52:55], a[44:47], v[236:239], v[52:55]\n"
"v_mov_b32 v221, 0\n"
"v_mfma_f32_16x16x32_bf16 v[64:67], a[60:63], v[236:239], 0\n"
"v_mfma_f32_16x16x32_bf16 v[64:67], a[56:59], v[148:151], v[64:67]\n"
"v_mfma_f32_16x16x32_bf16 v[64:67], a[52:55], v[144:147], v[64:67]\n"
"v_cvt_pk_bf16_f32 v52, v52, v53\n"
"v_cvt_pk_bf16_f32 v53, v54, v55\n"
"v_mfma_f32_16x16x32_bf16 v[64:67], a[48:51], v[140:143], v[64:67]\n"
"buffer_store_dwordx2 v[206:207], v162, s[84:87], s69 offen offset:512 sc0 sc1\n"
"v_mfma_f32_16x16x32_bf16 v[56:59], a[64:67], v[140:143], 0\n"
"s_nop 0\n"
"ds_read_b64 v[206:207], v163 offset:16384\n"
"v_add_u32 v163, 512, v163\n"
"v_mfma_f32_16x16x32_bf16 v[56:59], a[68:71], v[144:147], v[56:59]\n"
"v_mfma_f32_16x16x32_bf16 v[56:59], a[72:75], v[148:151], v[56:59]\n"
"v_cvt_pk_bf16_f32 v54, v64, v65\n"
"v_cvt_pk_bf16_f32 v55, v66, v67\n"
"v_mfma_f32_16x16x32_bf16 v[56:59], a[76:79], v[236:239], v[56:59]\n"
"s_waitcnt lgkmcnt(1)\n"
"v_mfma_f32_16x16x32_bf16 v[68:71], a[92:95], v[236:239], 0\n"
"ds_read_b128 v[236:239], v153 offset:18496\n"
"v_mfma_f32_16x16x32_bf16 v[68:71], a[88:91], v[148:151], v[68:71]\n"
"ds_read_b128 v[148:151], v153 offset:15360\n"
"v_mfma_f32_16x16x32_bf16 v[68:71], a[84:87], v[144:147], v[68:71]\n"
"ds_read_b128 v[144:147], v153 offset:11264\n"
"v_cvt_pk_bf16_f32 v56, v56, v57\n"
"v_mfma_f32_16x16x32_bf16 v[68:71], a[80:83], v[140:143], v[68:71]\n"
"buffer_store_dwordx2 v[210:211], v162, s[84:87], s69 offen offset:768 sc0 sc1\n"
"v_smfmac_f32_16x16x64_bf16 v[224:227], a[236:239], v[8:15], v157\n"
"ds_read_b128 v[140:143], v153 offset:7168\n"
"v_mfma_f32_16x16x32_bf16 v[184:187], v[240:243], a[0:3], 0\n"
"v_cvt_pk_bf16_f32 v57, v58, v59\n"
"v_mfma_f32_16x16x32_bf16 v[184:187], v[244:247], a[4:7], v[184:187]\n"
"v_cvt_pk_bf16_f32 v58, v68, v69\n"
"v_cvt_pk_bf16_f32 v59, v70, v71\n"
"v_mfma_f32_16x16x32_bf16 v[184:187], v[248:251], a[8:11], v[184:187]\n"
"v_add_f32 v209, v224, v225\n"
"v_mov_b32 v224, 0\n"
"v_mfma_f32_16x16x32_bf16 v[184:187], a[224:227], a[12:15], v[184:187]\n"
"v_exp_f32 v209, v209\n"
"v_mfma_f32_16x16x32_bf16 v[192:195], a[224:227], a[28:31], 0\n"
"v_cvt_pk_bf16_f32 v215, v208, v209\n"
"s_add_u32 m0, s28, 4352\n"
"v_mfma_f32_16x16x32_bf16 v[192:195], v[248:251], a[24:27], v[192:195]\n"
"buffer_load_dwordx4 v152, s[24:27], s44 offen offset:0 lds\n"
"v_mfma_f32_16x16x32_bf16 v[192:195], v[244:247], a[20:23], v[192:195]\n"
"ds_write_b16 v219, v215 offset:0\n"
"v_mfma_f32_16x16x32_bf16 v[192:195], v[240:243], a[16:19], v[192:195]\n"
"ds_write_b16_d16_hi v219, v215 offset:4\n"
"v_mfma_f32_16x16x32_bf16 v[200:203], v[240:243], a[32:35], 0\n"
"ds_read_b128 v[240:243], v153 offset:22688\n"
"v_cvt_pk_bf16_f32 v184, v184, v185\n"
"v_mfma_f32_16x16x32_bf16 v[200:203], v[244:247], a[36:39], v[200:203]\n"
"ds_read_b128 v[244:247], v153 offset:26880\n"
"v_cvt_pk_bf16_f32 v185, v186, v187\n"
"v_mfma_f32_16x16x32_bf16 v[200:203], v[248:251], a[40:43], v[200:203]\n"
"ds_read_b128 v[248:251], v153 offset:31072\n"
"v_cvt_pk_bf16_f32 v192, v192, v193\n"
"v_mfma_f32_16x16x32_bf16 v[200:203], a[224:227], a[44:47], v[200:203]\n"
"ds_read_b128 a[224:227], v153 offset:27936\n"
"v_cvt_pk_bf16_f32 v193, v194, v195\n"
"v_mfma_f32_16x16x32_bf16 v[40:43], a[96:99], a[80:83], 0\n"
"v_mov_b32 v225, 0\n"
"v_add_f32 v208, v209, v208\n"
"v_mfma_f32_16x16x32_bf16 v[40:43], a[100:103], a[84:87], v[40:43]\n"
"s_waitcnt lgkmcnt(6)\n"
"v_lshlrev_b32 v204, 16, v206\n"
"v_and_b32 v205, 0xffff0000, v206\n"
"v_mfma_f32_16x16x32_bf16 v[40:43], a[104:107], a[88:91], v[40:43]\n"
"v_cvt_pk_bf16_f32 v200, v200, v201\n"
"v_cvt_pk_bf16_f32 v201, v202, v203\n"
"v_mfma_f32_16x16x32_bf16 v[40:43], a[108:111], a[92:95], v[40:43]\n"
"v_lshlrev_b32 v206, 16, v207\n"
"v_and_b32 v207, 0xffff0000, v207\n"
"v_mfma_f32_16x16x32_bf16 v[164:167], v[136:139], a[0:3], 0\n"
"v_mfma_f32_16x16x32_bf16 v[164:167], v[140:143], a[4:7], v[164:167]\n"
"v_mfma_f32_16x16x32_bf16 v[164:167], v[144:147], a[8:11], v[164:167]\n"
"buffer_load_dwordx4 v152, s[24:27], s45 offen offset:1088 lds\n"
"v_mfma_f32_16x16x32_bf16 v[164:167], v[148:151], a[12:15], v[164:167]\n"
"v_cvt_pk_bf16_f32 v40, v40, v41\n"
"v_cvt_pk_bf16_f32 v41, v42, v43\n"
"v_mfma_f32_16x16x32_bf16 v[168:171], v[148:151], a[28:31], 0\n"
"v_mfma_f32_16x16x32_bf16 v[168:171], v[144:147], a[24:27], v[168:171]\n"
"v_mfma_f32_16x16x32_bf16 v[168:171], v[140:143], a[20:23], v[168:171]\n"
"v_cvt_pk_bf16_f32 v186, v164, v165\n"
"v_cvt_pk_bf16_f32 v187, v166, v167\n"
"v_mfma_f32_16x16x32_bf16 v[168:171], v[136:139], a[16:19], v[168:171]\n"
"v_mfma_f32_16x16x32_bf16 v[172:175], v[136:139], a[32:35], 0\n"
"ds_read_b128 v[136:139], v153 offset:17440\n"
"v_mfma_f32_16x16x32_bf16 v[172:175], v[140:143], a[36:39], v[172:175]\n"
"ds_read_b128 v[140:143], v153 offset:21632\n"
"v_mfma_f32_16x16x32_bf16 v[172:175], v[144:147], a[40:43], v[172:175]\n"
"ds_read_b128 v[144:147], v153 offset:25824\n"
"v_cvt_pk_bf16_f32 v194, v168, v169\n"
"v_mfma_f32_16x16x32_bf16 v[172:175], v[148:151], a[44:47], v[172:175]\n"
"ds_read_b128 v[148:151], v153 offset:30016\n"
"v_cvt_pk_bf16_f32 v195, v170, v171\n"
"v_mfma_f32_16x16x32_bf16 v[164:167], a[144:147], a[48:51], 0\n"
"v_mfma_f32_16x16x32_bf16 v[164:167], a[148:151], a[52:55], v[164:167]\n"
"v_mfma_f32_16x16x32_bf16 v[164:167], a[152:155], a[56:59], v[164:167]\n"
"v_cvt_pk_bf16_f32 v202, v172, v173\n"
"v_cvt_pk_bf16_f32 v203, v174, v175\n"
"v_mfma_f32_16x16x32_bf16 v[164:167], a[156:159], a[60:63], v[164:167]\n"
"v_mfma_f32_16x16x32_bf16 v[168:171], a[156:159], a[76:79], 0\n"
"buffer_load_dwordx4 v152, s[24:27], s46 offen offset:2176 lds\n"
"v_mfma_f32_16x16x32_bf16 v[168:171], a[152:155], a[72:75], v[168:171]\n"
"v_mfma_f32_16x16x32_bf16 v[168:171], a[148:151], a[68:71], v[168:171]\n"
".Lpad4_%=:\n"
"s_cbranch_vccnz .Lback0_%=\n"
"v_cvt_pk_bf16_f32 v30, v164, v165\n"
"v_cvt_pk_bf16_f32 v31, v166, v167\n"
"v_mfma_f32_16x16x32_bf16 v[168:171], a[144:147], a[64:67], v[168:171]\n"
"v_smfmac_f32_16x16x64_bf16 v[228:231], a[236:239], v[16:23], v157\n"
"v_mfma_f32_16x16x32_bf16 v[176:179], a[112:115], a[80:83], 0\n"
"v_mfma_f32_16x16x32_bf16 v[176:179], a[116:119], a[84:87], v[176:179]\n"
"v_cvt_pk_bf16_f32 v38, v168, v169\n"
"v_cvt_pk_bf16_f32 v39, v170, v171\n"
"v_mfma_f32_16x16x32_bf16 v[176:179], a[120:123], a[88:91], v[176:179]\n"
"v_add_f32 v210, v228, v229\n"
"v_mov_b32 v228, 0\n"
"v_mfma_f32_16x16x32_bf16 v[176:179], a[124:127], a[92:95], v[176:179]\n"
"v_exp_f32 v210, v210\n"
"v_smfmac_f32_16x16x64_bf16 v[220:223], a[236:239], v[24:31], v157\n"
"v_mov_b32 v229, 0\n"
"v_mfma_f32_16x16x32_bf16 v[96:99], v[0:3], v[180:183], 0\n"
"v_add_f32 v208, v210, v208\n"
"v_mfma_f32_16x16x32_bf16 v[96:99], v[4:7], v[184:187], v[96:99]\n"
"v_cvt_pk_bf16_f32 v42, v176, v177\n"
"v_cvt_pk_bf16_f32 v43, v178, v179\n"
"v_mfma_f32_16x16x32_bf16 v[44:47], a[128:131], a[80:83], 0\n"
"v_add_f32 v211, v220, v221\n"
"v_mov_b32 v220, 0\n"
"v_mfma_f32_16x16x32_bf16 v[44:47], a[132:135], a[84:87], v[44:47]\n"
"v_exp_f32 v211, v211\n"
"v_mfma_f32_16x16x32_bf16 v[44:47], a[136:139], a[88:91], v[44:47]\n"
"buffer_load_dwordx4 v152, s[24:27], s47 offen offset:3264 lds\n"
"v_mfma_f32_16x16x32_bf16 v[44:47], a[140:143], a[92:95], v[44:47]\n"
"v_cvt_pk_bf16_f32 v216, v210, v211\n"
"v_mov_b32 v221, 0\n"
"v_mfma_f32_16x16x32_bf16 v[120:123], v[12:15], v[184:187], 0\n"
"ds_write_b16 v219, v216 offset:512\n"
"v_mfma_f32_16x16x32_bf16 v[120:123], v[8:11], v[180:183], v[120:123]\n"
"ds_write_b16_d16_hi v219, v216 offset:516\n"
"v_smfmac_f32_16x16x64_bf16 v[224:227], a[236:239], v[32:39], v157\n"
"v_cvt_pk_bf16_f32 v44, v44, v45\n"
"v_mfma_f32_16x16x32_bf16 v[164:167], a[144:147], a[80:83], 0\n"
"v_cvt_pk_bf16_f32 v45, v46, v47\n"
"v_add_f32 v208, v211, v208\n"
"v_mfma_f32_16x16x32_bf16 v[164:167], a[148:151], a[84:87], v[164:167]\n"
"v_exp_f32 v96, v96\n"
"v_mfma_f32_16x16x32_bf16 v[164:167], a[152:155], a[88:91], v[164:167]\n"
"v_add_f32 v212, v224, v225\n"
"v_mfma_f32_16x16x32_bf16 v[164:167], a[156:159], a[92:95], v[164:167]\n"
"v_exp_f32 v212, v212\n"
"v_mfma_f32_16x16x32_bf16 v[100:103], v[16:19], v[180:183], 0\n"
"v_exp_f32 v97, v97\n"
"v_mfma_f32_16x16x32_bf16 v[100:103], v[20:23], v[184:187], v[100:103]\n"
"v_exp_f32 v98, v98\n"
"v_mfma_f32_16x16x32_bf16 v[72:75], a[0:3], v[236:239], 0\n"
"s_waitcnt lgkmcnt(2)\n"
"v_cvt_pk_bf16_f32 v46, v164, v165\n"
"v_cvt_pk_bf16_f32 v47, v166, v167\n"
"v_mfma_f32_16x16x32_bf16 v[72:75], a[4:7], v[240:243], v[72:75]\n"
"v_exp_f32 v99, v99\n"
"v_mfma_f32_16x16x32_bf16 v[72:75], a[8:11], v[244:247], v[72:75]\n"
"v_cvt_pk_bf16_f32 v96, v96, v97\n"
"v_cvt_pk_bf16_f32 v97, v98, v99\n"
"v_mfma_f32_16x16x32_bf16 v[72:75], a[12:15], v[248:251], v[72:75]\n"
"v_exp_f32 v120, v120\n"
"v_mfma_f32_16x16x32_bf16 v[84:87], a[28:31], v[248:251], 0\n"
"s_add_u32 m0, s28, 8704\n"
"v_add_f32 v208, v212, v208\n"
"v_mfma_f32_16x16x32_bf16 v[84:87], a[24:27], v[244:247], v[84:87]\n"
"buffer_load_dwordx4 v152, s[24:27], s48 offen offset:0 lds\n"
"v_mfma_f32_16x16x32_bf16 v[84:87], a[20:23], v[240:243], v[84:87]\n"
"v_cvt_pk_bf16_f32 v72, v72, v73\n"
"v_cvt_pk_bf16_f32 v73, v74, v75\n"
"v_mfma_f32_16x16x32_bf16 v[84:87], a[16:19], v[236:239], v[84:87]\n"
"v_exp_f32 v121, v121\n"
"v_mfma_f32_16x16x32_bf16 v[76:79], a[32:35], v[236:239], 0\n"
"v_exp_f32 v122, v122\n"
"v_mfma_f32_16x16x32_bf16 v[76:79], a[36:39], v[240:243], v[76:79]\n"
"v_exp_f32 v123, v123\n"
"v_mfma_f32_16x16x32_bf16 v[76:79], a[40:43], v[244:247], v[76:79]\n"
"v_cvt_pk_bf16_f32 v74, v84, v85\n"
"v_cvt_pk_bf16_f32 v75, v86, v87\n"
"v_mfma_f32_16x16x32_bf16 v[76:79], a[44:47], v[248:251], v[76:79]\n"
"v_cvt_pk_bf16_f32 v98, v120, v121\n"
"v_cvt_pk_bf16_f32 v99, v122, v123\n"
"v_mfma_f32_16x16x32_bf16 v[88:91], a[60:63], v[248:251], 0\n"
"v_exp_f32 v100, v100\n"
"v_mfma_f32_16x16x32_bf16 v[88:91], a[56:59], v[244:247], v[88:91]\n"
"v_exp_f32 v101, v101\n"
"v_mfma_f32_16x16x32_bf16 v[88:91], a[52:55], v[240:243], v[88:91]\n"
"v_cvt_pk_bf16_f32 v76, v76, v77\n"
"v_cvt_pk_bf16_f32 v77, v78, v79\n"
"v_mfma_f32_16x16x32_bf16 v[88:91], a[48:51], v[236:239], v[88:91]\n"
"v_exp_f32 v102, v102\n"
"v_mfma_f32_16x16x32_bf16 v[80:83], a[64:67], v[236:239], 0\n"
"v_exp_f32 v103, v103\n"
"v_mfma_f32_16x16x32_bf16 v[80:83], a[68:71], v[240:243], v[80:83]\n"
"v_cvt_pk_bf16_f32 v100, v100, v101\n"
"v_cvt_pk_bf16_f32 v101, v102, v103\n"
"v_mfma_f32_16x16x32_bf16 v[80:83], a[72:75], v[244:247], v[80:83]\n"
"buffer_load_dwordx4 v152, s[24:27], s49 offen offset:1088 lds\n"
"v_mfma_f32_16x16x32_bf16 v[80:83], a[76:79], v[248:251], v[80:83]\n"
"v_cvt_pk_bf16_f32 v78, v88, v89\n"
"v_cvt_pk_bf16_f32 v79, v90, v91\n"
"v_mfma_f32_16x16x32_bf16 v[92:95], a[92:95], v[248:251], 0\n"
"ds_read_b128 v[248:251], v153 offset:23744\n"
"v_mfma_f32_16x16x32_bf16 v[92:95], a[88:91], v[244:247], v[92:95]\n"
"ds_read_b128 v[244:247], v153 offset:19552\n"
"v_mfma_f32_16x16x32_bf16 v[92:95], a[84:87], v[240:243], v[92:95]\n"
"v_cvt_pk_bf16_f32 v80, v80, v81\n"
"v_cvt_pk_bf16_f32 v81, v82, v83\n"
"v_mfma_f32_16x16x32_bf16 v[92:95], a[80:83], v[236:239], v[92:95]\n"
"ds_read2_b32 v[236:237], v158 offset0:0 offset1:2\n"
"ds_read2_b32 v[238:239], v158 offset0:128 offset1:130\n"
"v_smfmac_f32_16x16x64_bf16 v[228:231], a[236:239], v[40:47], v157\n"
"v_mov_b32_dpp v158, v158 row_ror:8 row_mask:0xf bank_mask:0xf\n"
"v_mfma_f32_16x16x32_bf16 v[124:127], v[28:31], v[184:187], 0\n"
"ds_read2_b32 v[240:241], v158 offset0:0 offset1:2\n"
"ds_read2_b32 v[242:243], v158 offset0:128 offset1:130\n"
"v_mfma_f32_16x16x32_bf16 v[124:127], v[24:27], v[180:183], v[124:127]\n"
"v_cvt_pk_bf16_f32 v82, v92, v93\n"
"v_cvt_pk_bf16_f32 v83, v94, v95\n"
"v_smfmac_f32_16x16x64_bf16 v[220:223], a[236:239], a[228:235], v157\n"
"ds_read_b64 a[236:237], v156 offset:0\n"
"v_mfma_f32_16x16x32_bf16 v[104:107], v[32:35], v[180:183], 0\n"
"ds_read_b64 a[238:239], v156 offset:64\n"
"ds_read_b128 a[228:231], v161 offset:0\n"
"v_mfma_f32_16x16x32_bf16 v[104:107], v[36:39], v[184:187], v[104:107]\n"
"ds_read_b128 a[232:235], v161 offset:2048\n"
"v_add_f32 v213, v228, v229\n"
"v_mfma_f32_16x16x32_bf16 v[120:123], v[44:47], v[184:187], 0\n"
"v_exp_f32 v213, v213\n"
"v_mfma_f32_16x16x32_bf16 v[120:123], v[40:43], v[180:183], v[120:123]\n"
"buffer_load_dwordx4 v152, s[24:27], s50 offen offset:2176 lds\n"
"v_mfma_f32_16x16x32_bf16 v[60:63], a[0:3], v[136:139], 0\n"
"v_cvt_pk_bf16_f32 v217, v212, v213\n"
"v_add_f32 v214, v220, v221\n"
"v_mfma_f32_16x16x32_bf16 v[60:63], a[4:7], v[140:143], v[60:63]\n"
"ds_write_b16 v219, v217 offset:1536\n"
"v_mfma_f32_16x16x32_bf16 v[60:63], a[8:11], v[144:147], v[60:63]\n"
"ds_write_b16_d16_hi v219, v217 offset:1540\n"
"v_mfma_f32_16x16x32_bf16 v[60:63], a[12:15], v[148:151], v[60:63]\n"
"v_exp_f32 v214, v214\n"
"v_mfma_f32_16x16x32_bf16 v[164:167], a[28:31], v[148:151], 0\n"
"v_cvt_pk_bf16_f32 v218, v214, v214\n"
"v_add_f32 v208, v213, v208\n"
"v_mfma_f32_16x16x32_bf16 v[164:167], a[24:27], v[144:147], v[164:167]\n"
"ds_write_b16 v219, v218 offset:2048\n"
"v_mfma_f32_16x16x32_bf16 v[164:167], a[20:23], v[140:143], v[164:167]\n"
"v_accvgpr_read_b32 v219, a241\n"
"v_add_f32_dpp v208, v214, v208 quad_perm:[0,1,2,3] row_mask:0xf bank_mask:0x1\n"
"v_mfma_f32_16x16x32_bf16 v[164:167], a[16:19], v[136:139], v[164:167]\n"
"ds_read_b128 v[220:223], v219 offset:0\n"
"ds_read_b128 v[224:227], v219 offset:1024\n"
"v_mfma_f32_16x16x32_bf16 v[64:67], a[32:35], v[136:139], 0\n"
"v_accvgpr_read_b32 v219, a242\n"
"v_add_f32_dpp v208, v208, v208 row_ror:8 row_mask:0xf bank_mask:0xf\n"
"v_mfma_f32_16x16x32_bf16 v[64:67], a[36:39], v[140:143], v[64:67]\n"
"ds_read2_b32 v[228:229], v219 offset0:0 offset1:4\n"
"v_add_f32_dpp v208, v208, v208 row_ror:4 row_mask:0xf bank_mask:0xf\n"
"v_mfma_f32_16x16x32_bf16 v[64:67], a[40:43], v[144:147], v[64:67]\n"
"v_exp_f32 v124, v124\n"
"v_mfma_f32_16x16x32_bf16 v[64:67], a[44:47], v[148:151], v[64:67]\n"
"v_add_f32_dpp v208, v208, v208 row_ror:2 row_mask:0xf bank_mask:0xf\n"
"v_cvt_pk_bf16_f32 v60, v60, v61\n"
"v_mfma_f32_16x16x32_bf16 v[168:171], a[60:63], v[148:151], 0\n"
"v_add_f32_dpp v208, v208, v208 row_ror:1 row_mask:0xf bank_mask:0xf\n"
"v_cvt_pk_bf16_f32 v61, v62, v63\n"
"v_mfma_f32_16x16x32_bf16 v[168:171], a[56:59], v[144:147], v[168:171]\n"
"buffer_load_dwordx4 v152, s[24:27], s51 offen offset:3264 lds\n"
"v_mfma_f32_16x16x32_bf16 v[168:171], a[52:55], v[140:143], v[168:171]\n"
"v_rcp_f32 v209, v208\n"
"v_mfma_f32_16x16x32_bf16 v[168:171], a[48:51], v[136:139], v[168:171]\n"
"v_maximum3_f32 v159, v159, v208, v209\n"
"v_cvt_pk_bf16_f32 v62, v164, v165\n"
"v_mfma_f32_16x16x32_bf16 v[68:71], a[64:67], v[136:139], 0\n"
"v_exp_f32 v125, v125\n"
"v_mfma_f32_16x16x32_bf16 v[68:71], a[68:71], v[140:143], v[68:71]\n"
"v_exp_f32 v126, v126\n"
"v_mfma_f32_16x16x32_bf16 v[68:71], a[72:75], v[144:147], v[68:71]\n"
"v_exp_f32 v127, v127\n"
"v_mfma_f32_16x16x32_bf16 v[68:71], a[76:79], v[148:151], v[68:71]\n"
"v_cvt_pk_bf16_f32 v102, v124, v125\n"
"v_cvt_pk_bf16_f32 v103, v126, v127\n"
"v_mfma_f32_16x16x32_bf16 v[172:175], a[92:95], v[148:151], 0\n"
"ds_read_b128 v[148:151], v153 offset:8192\n"
"v_cvt_pk_bf16_f32 v63, v166, v167\n"
"v_mfma_f32_16x16x32_bf16 v[172:175], a[88:91], v[144:147], v[172:175]\n"
"ds_read_b128 v[144:147], v153 offset:4096\n"
"v_cvt_pk_bf16_f32 v64, v64, v65\n"
"v_mfma_f32_16x16x32_bf16 v[172:175], a[84:87], v[140:143], v[172:175]\n"
"ds_read_b128 v[140:143], v153 offset:0\n"
"v_cvt_pk_bf16_f32 v65, v66, v67\n"
"v_mfma_f32_16x16x32_bf16 v[172:175], a[80:83], v[136:139], v[172:175]\n"
"s_waitcnt lgkmcnt(3)\n"
"ds_read_b128 v[136:139], v153 offset:32128\n"
"v_cvt_pk_bf16_f32 v66, v168, v169\n"
"v_smfmac_f32_16x16x64_bf16 v[128:131], v[236:239], v[180:187], v157\n"
"v_cvt_pk_bf16_f32 v67, v170, v171\n"
"v_smfmac_f32_16x16x64_bf16 v[128:131], v[240:243], v[188:195], v157\n"
"ds_read_b128 v[240:243], v153 offset:1024\n"
"v_smfmac_f32_16x16x64_bf16 v[132:135], v[236:239], v[196:203], v157\n"
"ds_read_b128 v[236:239], v153 offset:12288\n"
"v_mfma_f32_16x16x32_bf16 v[108:111], v[0:3], v[188:191], 0\n"
"v_exp_f32 v104, v104\n"
"v_mfma_f32_16x16x32_bf16 v[108:111], v[4:7], v[192:195], v[108:111]\n"
"v_exp_f32 v105, v105\n"
"v_mfma_f32_16x16x32_bf16 v[124:127], v[12:15], v[192:195], 0\n"
"v_cvt_pk_bf16_f32 v68, v68, v69\n"
"v_cvt_pk_bf16_f32 v69, v70, v71\n"
"v_mfma_f32_16x16x32_bf16 v[124:127], v[8:11], v[188:191], v[124:127]\n"
"v_cvt_pk_bf16_f32 v70, v172, v173\n"
"v_cvt_pk_bf16_f32 v71, v174, v175\n"
"v_mfma_f32_16x16x32_bf16 v[112:115], v[16:19], v[188:191], 0\n"
"v_exp_f32 v106, v106\n"
"v_mfma_f32_16x16x32_bf16 v[112:115], v[20:23], v[192:195], v[112:115]\n"
"v_exp_f32 v107, v107\n"
"v_mfma_f32_16x16x32_bf16 v[84:87], a[0:3], v[244:247], 0\n"
"v_cvt_pk_bf16_f32 v104, v104, v105\n"
"v_cvt_pk_bf16_f32 v105, v106, v107\n"
"v_mfma_f32_16x16x32_bf16 v[84:87], a[4:7], v[248:251], v[84:87]\n"
"v_exp_f32 v120, v120\n"
"v_mfma_f32_16x16x32_bf16 v[84:87], a[8:11], a[224:227], v[84:87]\n"
"s_waitcnt lgkmcnt(2)\n"
"v_exp_f32 v121, v121\n"
"v_mfma_f32_16x16x32_bf16 v[84:87], a[12:15], v[136:139], v[84:87]\n"
"v_exp_f32 v122, v122\n"
"v_mfma_f32_16x16x32_bf16 v[176:179], a[28:31], v[136:139], 0\n"
"v_exp_f32 v123, v123\n"
"v_mfma_f32_16x16x32_bf16 v[176:179], a[24:27], a[224:227], v[176:179]\n"
"v_add_f32 v128, v128, v129\n"
"v_add_f32 v130, v130, v131\n"
"v_mfma_f32_16x16x32_bf16 v[176:179], a[20:23], v[248:251], v[176:179]\n"
"v_exp_f32 v128, v128\n"
"v_mfma_f32_16x16x32_bf16 v[176:179], a[16:19], v[244:247], v[176:179]\n"
"v_exp_f32 v130, v130\n"
"v_mfma_f32_16x16x32_bf16 v[88:91], a[32:35], v[244:247], 0\n"
"v_cvt_pk_bf16_f32 v129, v128, v130\n"
"v_cvt_pk_bf16_f32 v106, v120, v121\n"
"v_mfma_f32_16x16x32_bf16 v[88:91], a[36:39], v[248:251], v[88:91]\n"
"v_add_f32 v128, v128, v130\n"
"v_cvt_pk_bf16_f32 v107, v122, v123\n"
"v_mfma_f32_16x16x32_bf16 v[88:91], a[40:43], a[224:227], v[88:91]\n"
"v_permlane16_swap_b32 v128, v129\n"
"v_mfma_f32_16x16x32_bf16 v[88:91], a[44:47], v[136:139], v[88:91]\n"
"v_exp_f32 v108, v108\n"
"v_mfma_f32_16x16x32_bf16 v[164:167], a[60:63], v[136:139], 0\n"
"v_cvt_pk_bf16_f32 v84, v84, v85\n"
"v_cvt_pk_bf16_f32 v85, v86, v87\n"
"v_mfma_f32_16x16x32_bf16 v[164:167], a[56:59], a[224:227], v[164:167]\n"
"v_cvt_pk_bf16_f32 v86, v176, v177\n"
"v_cvt_pk_bf16_f32 v87, v178, v179\n"
"v_mfma_f32_16x16x32_bf16 v[164:167], a[52:55], v[248:251], v[164:167]\n"
"v_cvt_pk_bf16_f32 v88, v88, v89\n"
"v_cvt_pk_bf16_f32 v89, v90, v91\n"
"v_mfma_f32_16x16x32_bf16 v[164:167], a[48:51], v[244:247], v[164:167]\n"
"v_exp_f32 v109, v109\n"
"v_mfma_f32_16x16x32_bf16 v[92:95], a[64:67], v[244:247], 0\n"
"s_add_u32 s84, s84, 13107200\n"
"s_addc_u32 s85, s85, 0\n"
"v_mfma_f32_16x16x32_bf16 v[92:95], a[68:71], v[248:251], v[92:95]\n"
"v_exp_f32 v110, v110\n"
"v_mfma_f32_16x16x32_bf16 v[92:95], a[72:75], a[224:227], v[92:95]\n"
"v_cvt_pk_bf16_f32 v90, v164, v165\n"
"v_cvt_pk_bf16_f32 v91, v166, v167\n"
"v_mfma_f32_16x16x32_bf16 v[92:95], a[76:79], v[136:139], v[92:95]\n"
"v_exp_f32 v111, v111\n"
"v_mfma_f32_16x16x32_bf16 v[168:171], a[92:95], v[136:139], 0\n"
"ds_read_b128 v[136:139], v153 offset:2048\n"
"v_cvt_pk_bf16_f32 v108, v108, v109\n"
"v_mfma_f32_16x16x32_bf16 v[168:171], a[88:91], a[224:227], v[168:171]\n"
"ds_read_b128 a[224:227], v153 offset:13312\n"
"v_cvt_pk_bf16_f32 v109, v110, v111\n"
"v_mfma_f32_16x16x32_bf16 v[168:171], a[84:87], v[248:251], v[168:171]\n"
"ds_read_b128 v[248:251], v153 offset:9216\n"
"v_cvt_pk_bf16_f32 v92, v92, v93\n"
"v_mfma_f32_16x16x32_bf16 v[168:171], a[80:83], v[244:247], v[168:171]\n"
"ds_read_b128 v[244:247], v153 offset:5120\n"
"v_cvt_pk_bf16_f32 v93, v94, v95\n"
"v_mfma_f32_16x16x32_bf16 v[120:123], v[28:31], v[192:195], 0\n"
"v_exp_f32 v124, v124\n"
"v_mfma_f32_16x16x32_bf16 v[120:123], v[24:27], v[188:191], v[120:123]\n"
"v_exp_f32 v125, v125\n"
"v_mfma_f32_16x16x32_bf16 v[116:119], v[32:35], v[188:191], 0\n"
"v_cvt_pk_bf16_f32 v94, v168, v169\n"
"v_cvt_pk_bf16_f32 v95, v170, v171\n"
"v_mfma_f32_16x16x32_bf16 v[116:119], v[36:39], v[192:195], v[116:119]\n"
"v_exp_f32 v126, v126\n"
"v_cvt_pk_bf16_f32 v110, v124, v125\n"
"v_mfma_f32_16x16x4_4b_bf16 v[164:179], v[228:229], a[244:245], 0 cbsz:2 abid:0\n"
"v_exp_f32 v127, v127\n"
"v_exp_f32 v112, v112\n"
"v_cvt_pk_bf16_f32 v111, v126, v127\n"
"v_exp_f32 v113, v113\n"
"v_mov_b32 v252, v128\n"
"v_mfma_f32_16x16x32_bf16 v[124:127], v[44:47], v[192:195], 0\n"
"v_exp_f32 v114, v114\n"
"v_mfma_f32_16x16x32_bf16 v[124:127], v[40:43], v[188:191], v[124:127]\n"
"v_exp_f32 v115, v115\n"
"v_smfmac_f32_16x16x64_bf16 v[164:167], v[220:223], v[48:55], v157\n"
"v_mov_b32 v253, v129\n"
"v_smfmac_f32_16x16x64_bf16 v[164:167], v[224:227], v[52:59], v157\n"
"v_cvt_pk_bf16_f32 v112, v112, v113\n"
"v_smfmac_f32_16x16x64_bf16 v[168:171], v[224:227], v[64:71], v157\n"
"v_cvt_pk_bf16_f32 v113, v114, v115\n"
"v_smfmac_f32_16x16x64_bf16 v[168:171], v[220:223], v[60:67], v157\n"
"v_exp_f32 v120, v120\n"
"v_smfmac_f32_16x16x64_bf16 v[172:175], v[220:223], v[72:79], v157\n"
"v_fmac_f32 v204, v209, v164\n"
"v_smfmac_f32_16x16x64_bf16 v[172:175], v[224:227], v[76:83], v157\n"
"v_fmac_f32 v204, v209, v165\n"
"v_smfmac_f32_16x16x64_bf16 v[176:179], v[224:227], v[88:95], v157\n"
"v_fmac_f32 v205, v209, v168\n"
"v_smfmac_f32_16x16x64_bf16 v[176:179], v[220:223], v[84:91], v157\n"
"v_fmac_f32 v205, v209, v169\n"
"v_smfmac_f32_16x16x64_bf16 v[252:255], a[248:251], v[96:103], v157\n"
"v_exp_f32 v121, v121\n"
"v_mfma_f32_16x16x4_4b_bf16 v[220:235], v[128:129], a[244:245], 0 cbsz:2 abid:1\n"
"s_branch .Lp2f%=\n"
".Lepf%=:\n"
"v_mfma_f32_16x16x32_bf16 v[164:167], a[80:83], a[160:163], 0\n"
".Lpad5_%=:\n"
"s_cbranch_vccnz .Lback0_%=\n"
"v_maximum3_f32 v159, v159, v134, v135\n"
"v_mfma_f32_16x16x32_bf16 v[164:167], a[84:87], a[164:167], v[164:167]\n"
"ds_bpermute_b32 v134, v160, v135 offset:0\n"
"v_mfma_f32_16x16x32_bf16 v[164:167], a[88:91], a[168:171], v[164:167]\n"
"v_exp_f32 v116, v116\n"
"v_mfma_f32_16x16x32_bf16 v[164:167], a[92:95], a[172:175], v[164:167]\n"
"buffer_store_dwordx2 v[206:207], v162, s[84:87], s65 offen offset:512 sc0 sc1\n"
"v_exp_f32 v117, v117\n"
"v_mfma_f32_16x16x32_bf16 v[168:171], a[92:95], a[188:191], 0\n"
"v_exp_f32 v115, v115\n"
"v_mfma_f32_16x16x32_bf16 v[168:171], a[88:91], a[184:187], v[168:171]\n"
"v_exp_f32 v118, v118\n"
"v_mfma_f32_16x16x32_bf16 v[168:171], a[84:87], a[180:183], v[168:171]\n"
"v_exp_f32 v119, v119\n"
"v_mfma_f32_16x16x32_bf16 v[168:171], a[80:83], a[176:179], v[168:171]\n"
"buffer_store_dwordx2 v[210:211], v162, s[84:87], s65 offen offset:768 sc0 sc1\n"
"v_exp_f32 v120, v120\n"
"v_mfma_f32_16x16x32_bf16 v[172:175], a[80:83], a[192:195], 0\n"
"v_exp_f32 v121, v121\n"
"v_mfma_f32_16x16x32_bf16 v[172:175], a[84:87], a[196:199], v[172:175]\n"
"v_exp_f32 v122, v122\n"
"v_mfma_f32_16x16x32_bf16 v[172:175], a[88:91], a[200:203], v[172:175]\n"
"v_exp_f32 v123, v123\n"
"v_mfma_f32_16x16x32_bf16 v[172:175], a[92:95], a[204:207], v[172:175]\n"
"s_waitcnt lgkmcnt(0)\n"
"v_exp_f32 v124, v124\n"
"v_mul_f32_dpp v220, v134, v220 quad_perm:[0,0,0,0] row_mask:0xf bank_mask:0xf\n"
"v_mfma_f32_16x16x4_4b_bf16 v[204:219], v[132:133], a[244:245], 0 cbsz:2 abid:3\n"
"v_mul_f32_dpp v221, v134, v221 quad_perm:[1,1,1,1] row_mask:0xf bank_mask:0xf\n"
"v_mul_f32_dpp v222, v134, v222 quad_perm:[2,2,2,2] row_mask:0xf bank_mask:0xf\n"
"v_mul_f32_dpp v223, v134, v223 quad_perm:[3,3,3,3] row_mask:0xf bank_mask:0xf\n"
"v_mul_f32_dpp v224, v134, v224 quad_perm:[0,0,0,0] row_mask:0xf bank_mask:0xf\n"
"v_mul_f32_dpp v225, v134, v225 quad_perm:[1,1,1,1] row_mask:0xf bank_mask:0xf\n"
"v_mul_f32_dpp v226, v134, v226 quad_perm:[2,2,2,2] row_mask:0xf bank_mask:0xf\n"
"v_mul_f32_dpp v227, v134, v227 quad_perm:[3,3,3,3] row_mask:0xf bank_mask:0xf\n"
"v_mul_f32_dpp v228, v134, v228 quad_perm:[0,0,0,0] row_mask:0xf bank_mask:0xf\n"
"v_mfma_f32_16x16x32_bf16 v[220:223], a[32:35], a[160:163], v[220:223]\n"
"v_mul_f32_dpp v229, v134, v229 quad_perm:[1,1,1,1] row_mask:0xf bank_mask:0xf\n"
"v_mul_f32_dpp v230, v134, v230 quad_perm:[2,2,2,2] row_mask:0xf bank_mask:0xf\n"
"v_mfma_f32_16x16x32_bf16 v[220:223], a[36:39], a[164:167], v[220:223]\n"
"v_mul_f32_dpp v231, v134, v231 quad_perm:[3,3,3,3] row_mask:0xf bank_mask:0xf\n"
"v_mul_f32_dpp v232, v134, v232 quad_perm:[0,0,0,0] row_mask:0xf bank_mask:0xf\n"
"v_mfma_f32_16x16x32_bf16 v[220:223], a[40:43], a[168:171], v[220:223]\n"
"v_mul_f32_dpp v233, v134, v233 quad_perm:[1,1,1,1] row_mask:0xf bank_mask:0xf\n"
"v_mul_f32_dpp v234, v134, v234 quad_perm:[2,2,2,2] row_mask:0xf bank_mask:0xf\n"
"v_mfma_f32_16x16x32_bf16 v[220:223], a[44:47], a[172:175], v[220:223]\n"
"v_mul_f32_dpp v235, v134, v235 quad_perm:[3,3,3,3] row_mask:0xf bank_mask:0xf\n"
"v_cvt_pk_bf16_f32 v116, v116, v117\n"
"v_mfma_f32_16x16x32_bf16 v[224:227], a[44:47], a[188:191], v[224:227]\n"
"v_exp_f32 v125, v125\n"
"v_mfma_f32_16x16x32_bf16 v[224:227], a[40:43], a[184:187], v[224:227]\n"
"v_exp_f32 v126, v126\n"
"v_mfma_f32_16x16x32_bf16 v[224:227], a[36:39], a[180:183], v[224:227]\n"
"v_exp_f32 v127, v127\n"
"v_mfma_f32_16x16x32_bf16 v[224:227], a[32:35], a[176:179], v[224:227]\n"
"v_cvt_pk_bf16_f32 v113, v114, v115\n"
"v_cvt_pk_bf16_f32 v117, v118, v119\n"
"v_mfma_f32_16x16x32_bf16 v[228:231], a[32:35], a[192:195], v[228:231]\n"
"v_cvt_pk_bf16_f32 v114, v120, v121\n"
"v_cvt_pk_bf16_f32 v115, v122, v123\n"
"v_mfma_f32_16x16x32_bf16 v[228:231], a[36:39], a[196:199], v[228:231]\n"
"v_cvt_pk_bf16_f32 v118, v124, v125\n"
"v_cvt_pk_bf16_f32 v119, v126, v127\n"
"v_mfma_f32_16x16x32_bf16 v[228:231], a[40:43], a[200:203], v[228:231]\n"
"v_cvt_pk_bf16_f32 v220, v220, v224\n"
"v_cvt_pk_bf16_f32 v222, v222, v226\n"
"v_mfma_f32_16x16x32_bf16 v[228:231], a[44:47], a[204:207], v[228:231]\n"
"v_cvt_pk_bf16_f32 v224, v221, v225\n"
"v_cvt_pk_bf16_f32 v226, v223, v227\n"
"v_mfma_f32_16x16x32_bf16 v[232:235], a[44:47], a[220:223], v[232:235]\n"
"v_pk_max_i16 v220, v220, 0\n"
"v_pk_max_i16 v224, v224, 0\n"
"v_mfma_f32_16x16x32_bf16 v[232:235], a[40:43], a[216:219], v[232:235]\n"
"v_pk_max_i16 v222, v222, 0\n"
"v_pk_max_i16 v226, v226, 0\n"
"v_mfma_f32_16x16x32_bf16 v[232:235], a[36:39], a[212:215], v[232:235]\n"
"v_exp_f32 v96, v96\n"
"v_mfma_f32_16x16x32_bf16 v[232:235], a[32:35], a[208:211], v[232:235]\n"
"v_exp_f32 v97, v97\n"
"v_smfmac_f32_16x16x64_bf16 v[252:255], a[248:251], v[112:119], v157\n"
"v_exp_f32 v100, v100\n"
"v_mfma_f32_16x16x32_bf16 v[120:123], v[12:15], v[192:195], 0\n"
"v_exp_f32 v101, v101\n"
"v_mfma_f32_16x16x32_bf16 v[120:123], v[8:11], v[188:191], v[120:123]\n"
"v_cvt_pk_bf16_f32 v221, v228, v232\n"
"v_cvt_pk_bf16_f32 v225, v229, v233\n"
"v_mfma_f32_16x16x32_bf16 v[124:127], v[28:31], v[192:195], 0\n"
"v_add_f32 v134, v254, v255\n"
"v_cvt_pk_bf16_f32 v223, v230, v234\n"
"v_mfma_f32_16x16x32_bf16 v[124:127], v[24:27], v[188:191], v[124:127]\n"
"v_rcp_f32 v135, v134\n"
"v_mfma_f32_16x16x32_bf16 v[208:211], v[116:119], v[68:71], v[208:211]\n"
"v_cvt_pk_bf16_f32 v227, v231, v235\n"
"v_maximum3_f32 v159, v159, v134, v135\n"
"v_mfma_f32_16x16x32_bf16 v[208:211], v[112:115], v[64:67], v[208:211]\n"
"ds_bpermute_b32 v134, v160, v135 offset:128\n"
"v_mfma_f32_16x16x32_bf16 v[208:211], v[108:111], v[60:63], v[208:211]\n"
"v_pk_max_i16 v221, v221, 0\n"
"v_pk_max_i16 v225, v225, 0\n"
"v_mfma_f32_16x16x32_bf16 v[216:219], v[116:119], v[92:95], v[216:219]\n"
"v_pk_max_i16 v223, v223, 0\n"
"v_pk_max_i16 v227, v227, 0\n"
"v_mfma_f32_16x16x32_bf16 v[216:219], v[112:115], v[88:91], v[216:219]\n"
"v_exp_f32 v98, v98\n"
"v_mfma_f32_16x16x32_bf16 v[216:219], v[108:111], v[84:87], v[216:219]\n"
"buffer_store_dwordx2 v[220:221], v162, s[84:87], s66 offen offset:0 sc0 sc1\n"
"v_exp_f32 v99, v99\n"
"v_mfma_f32_16x16x32_bf16 v[204:207], v[108:111], v[48:51], v[204:207]\n"
"v_exp_f32 v102, v102\n"
"v_mfma_f32_16x16x32_bf16 v[204:207], v[112:115], v[52:55], v[204:207]\n"
"v_exp_f32 v103, v103\n"
"v_mfma_f32_16x16x32_bf16 v[204:207], v[116:119], v[56:59], v[204:207]\n"
"buffer_store_dwordx2 v[224:225], v162, s[84:87], s66 offen offset:256 sc0 sc1\n"
"v_exp_f32 v120, v120\n"
"v_mfma_f32_16x16x32_bf16 v[212:215], v[108:111], v[72:75], v[212:215]\n"
"v_exp_f32 v121, v121\n"
"v_mfma_f32_16x16x32_bf16 v[212:215], v[112:115], v[76:79], v[212:215]\n"
"v_exp_f32 v122, v122\n"
"v_mfma_f32_16x16x32_bf16 v[212:215], v[116:119], v[80:83], v[212:215]\n"
"buffer_store_dwordx2 v[222:223], v162, s[84:87], s66 offen offset:512 sc0 sc1\n"
"v_exp_f32 v123, v123\n"
"v_mfma_f32_16x16x32_bf16 v[108:111], v[0:3], v[196:199], 0\n"
"v_exp_f32 v124, v124\n"
"v_mfma_f32_16x16x32_bf16 v[108:111], v[4:7], v[200:203], v[108:111]\n"
"buffer_store_dwordx2 v[226:227], v162, s[84:87], s66 offen offset:768 sc0 sc1\n"
"v_exp_f32 v125, v125\n"
"v_mfma_f32_16x16x32_bf16 v[112:115], v[16:19], v[196:199], 0\n"
"v_exp_f32 v126, v126\n"
"v_mfma_f32_16x16x32_bf16 v[112:115], v[20:23], v[200:203], v[112:115]\n"
"v_exp_f32 v127, v127\n"
"v_cvt_pk_bf16_f32 v96, v96, v97\n"
"v_mfma_f32_16x16x4_4b_bf16 v[220:235], v[128:129], a[244:245], 0 cbsz:2 abid:1\n"
"v_cvt_pk_bf16_f32 v100, v100, v101\n"
"v_cvt_pk_bf16_f32 v97, v98, v99\n"
"v_cvt_pk_bf16_f32 v101, v102, v103\n"
"v_cvt_pk_bf16_f32 v98, v120, v121\n"
"v_cvt_pk_bf16_f32 v99, v122, v123\n"
"v_cvt_pk_bf16_f32 v102, v124, v125\n"
"v_cvt_pk_bf16_f32 v103, v126, v127\n"
"v_mov_b32 v252, v128\n"
"v_mfma_f32_16x16x32_bf16 v[120:123], v[44:47], v[192:195], 0\n"
"s_waitcnt lgkmcnt(0)\n"
"v_mov_b32 v253, v129\n"
"v_mul_f32_dpp v204, v134, v204 quad_perm:[0,0,0,0] row_mask:0xf bank_mask:0xf\n"
"v_mfma_f32_16x16x32_bf16 v[120:123], v[40:43], v[188:191], v[120:123]\n"
"v_mul_f32_dpp v205, v134, v205 quad_perm:[1,1,1,1] row_mask:0xf bank_mask:0xf\n"
"v_mul_f32_dpp v206, v134, v206 quad_perm:[2,2,2,2] row_mask:0xf bank_mask:0xf\n"
"v_mfma_f32_16x16x32_bf16 v[124:127], v[12:15], v[200:203], 0\n"
"v_mul_f32_dpp v207, v134, v207 quad_perm:[3,3,3,3] row_mask:0xf bank_mask:0xf\n"
"v_mul_f32_dpp v208, v134, v208 quad_perm:[0,0,0,0] row_mask:0xf bank_mask:0xf\n"
"v_mfma_f32_16x16x32_bf16 v[124:127], v[8:11], v[196:199], v[124:127]\n"
"v_mul_f32_dpp v209, v134, v209 quad_perm:[1,1,1,1] row_mask:0xf bank_mask:0xf\n"
"v_mul_f32_dpp v210, v134, v210 quad_perm:[2,2,2,2] row_mask:0xf bank_mask:0xf\n"
"v_smfmac_f32_16x16x64_bf16 v[252:255], a[248:251], v[96:103], v157\n"
"v_mul_f32_dpp v211, v134, v211 quad_perm:[3,3,3,3] row_mask:0xf bank_mask:0xf\n"
"v_mfma_f32_16x16x32_bf16 v[204:207], a[48:51], a[160:163], v[204:207]\n"
"v_mul_f32_dpp v212, v134, v212 quad_perm:[0,0,0,0] row_mask:0xf bank_mask:0xf\n"
"v_mul_f32_dpp v213, v134, v213 quad_perm:[1,1,1,1] row_mask:0xf bank_mask:0xf\n"
"v_mfma_f32_16x16x32_bf16 v[204:207], a[52:55], a[164:167], v[204:207]\n"
"v_mul_f32_dpp v214, v134, v214 quad_perm:[2,2,2,2] row_mask:0xf bank_mask:0xf\n"
"v_mul_f32_dpp v215, v134, v215 quad_perm:[3,3,3,3] row_mask:0xf bank_mask:0xf\n"
"v_mfma_f32_16x16x32_bf16 v[204:207], a[56:59], a[168:171], v[204:207]\n"
"v_mul_f32_dpp v216, v134, v216 quad_perm:[0,0,0,0] row_mask:0xf bank_mask:0xf\n"
"v_mul_f32_dpp v217, v134, v217 quad_perm:[1,1,1,1] row_mask:0xf bank_mask:0xf\n"
"v_mfma_f32_16x16x32_bf16 v[204:207], a[60:63], a[172:175], v[204:207]\n"
"v_mul_f32_dpp v218, v134, v218 quad_perm:[2,2,2,2] row_mask:0xf bank_mask:0xf\n"
"v_mul_f32_dpp v219, v134, v219 quad_perm:[3,3,3,3] row_mask:0xf bank_mask:0xf\n"
"v_mfma_f32_16x16x32_bf16 v[208:211], a[60:63], a[188:191], v[208:211]\n"
"v_exp_f32 v104, v104\n"
"v_mfma_f32_16x16x32_bf16 v[208:211], a[56:59], a[184:187], v[208:211]\n"
"v_exp_f32 v105, v105\n"
"v_mfma_f32_16x16x32_bf16 v[208:211], a[52:55], a[180:183], v[208:211]\n"
"v_exp_f32 v108, v108\n"
"v_mfma_f32_16x16x32_bf16 v[208:211], a[48:51], a[176:179], v[208:211]\n"
"v_exp_f32 v109, v109\n"
"v_mfma_f32_16x16x32_bf16 v[212:215], a[48:51], a[192:195], v[212:215]\n"
"v_exp_f32 v106, v106\n"
"v_mfma_f32_16x16x32_bf16 v[212:215], a[52:55], a[196:199], v[212:215]\n"
"v_exp_f32 v107, v107\n"
"v_mfma_f32_16x16x32_bf16 v[212:215], a[56:59], a[200:203], v[212:215]\n"
"v_exp_f32 v110, v110\n"
"v_mfma_f32_16x16x32_bf16 v[212:215], a[60:63], a[204:207], v[212:215]\n"
"v_exp_f32 v111, v111\n"
"v_mfma_f32_16x16x32_bf16 v[216:219], a[60:63], a[220:223], v[216:219]\n"
"v_exp_f32 v120, v120\n"
"v_mfma_f32_16x16x32_bf16 v[216:219], a[56:59], a[216:219], v[216:219]\n"
"v_exp_f32 v121, v121\n"
"v_mfma_f32_16x16x32_bf16 v[216:219], a[52:55], a[212:215], v[216:219]\n"
"v_exp_f32 v122, v122\n"
"v_mfma_f32_16x16x32_bf16 v[216:219], a[48:51], a[208:211], v[216:219]\n"
"v_exp_f32 v123, v123\n"
"v_mfma_f32_16x16x32_bf16 v[220:223], v[96:99], v[48:51], v[220:223]\n"
"v_exp_f32 v124, v124\n"
"v_mfma_f32_16x16x32_bf16 v[220:223], v[100:103], v[52:55], v[220:223]\n"
"v_exp_f32 v125, v125\n"
"v_mfma_f32_16x16x32_bf16 v[228:231], v[96:99], v[72:75], v[228:231]\n"
"v_exp_f32 v126, v126\n"
"v_mfma_f32_16x16x32_bf16 v[228:231], v[100:103], v[76:79], v[228:231]\n"
"v_exp_f32 v127, v127\n"
"v_mfma_f32_16x16x32_bf16 v[116:119], v[32:35], v[196:199], 0\n"
"v_cvt_pk_bf16_f32 v104, v104, v105\n"
"v_cvt_pk_bf16_f32 v108, v108, v109\n"
"v_mfma_f32_16x16x32_bf16 v[116:119], v[36:39], v[200:203], v[116:119]\n"
"v_cvt_pk_bf16_f32 v105, v106, v107\n"
"v_cvt_pk_bf16_f32 v109, v110, v111\n"
"v_mfma_f32_16x16x32_bf16 v[176:179], a[92:95], a[220:223], 0\n"
"v_cvt_pk_bf16_f32 v106, v120, v121\n"
"v_cvt_pk_bf16_f32 v107, v122, v123\n"
"v_mfma_f32_16x16x32_bf16 v[176:179], a[88:91], a[216:219], v[176:179]\n"
"v_mov_b32 v254, v128\n"
"v_mov_b32 v255, v129\n"
"v_mfma_f32_16x16x32_bf16 v[176:179], a[84:87], a[212:215], v[176:179]\n"
"v_cvt_pk_bf16_f32 v110, v124, v125\n"
"v_cvt_pk_bf16_f32 v111, v126, v127\n"
"v_mfma_f32_16x16x32_bf16 v[176:179], a[80:83], a[208:211], v[176:179]\n"
"v_cvt_pk_bf16_f32 v204, v204, v208\n"
"v_cvt_pk_bf16_f32 v206, v206, v210\n"
"v_smfmac_f32_16x16x64_bf16 v[252:255], a[252:255], v[104:111], v157\n"
"v_cvt_pk_bf16_f32 v208, v205, v209\n"
"v_mfma_f32_16x16x32_bf16 v[224:227], v[104:107], v[68:71], v[224:227]\n"
"v_cvt_pk_bf16_f32 v210, v207, v211\n"
"v_cvt_pk_bf16_f32 v205, v212, v216\n"
"v_mfma_f32_16x16x32_bf16 v[224:227], v[100:103], v[64:67], v[224:227]\n"
"v_cvt_pk_bf16_f32 v209, v213, v217\n"
"v_cvt_pk_bf16_f32 v207, v214, v218\n"
"v_mfma_f32_16x16x32_bf16 v[224:227], v[96:99], v[60:63], v[224:227]\n"
"v_add_f32 v130, v252, v253\n"
"v_cvt_pk_bf16_f32 v211, v215, v219\n"
"v_mfma_f32_16x16x32_bf16 v[232:235], v[104:107], v[92:95], v[232:235]\n"
"v_rcp_f32 v131, v130\n"
"v_mfma_f32_16x16x32_bf16 v[232:235], v[100:103], v[88:91], v[232:235]\n"
"v_maximum3_f32 v159, v159, v130, v131\n"
"v_pk_max_i16 v204, v204, 0\n"
"v_mfma_f32_16x16x32_bf16 v[232:235], v[96:99], v[84:87], v[232:235]\n"
"ds_bpermute_b32 v130, v160, v131 offset:0\n"
"v_mfma_f32_16x16x32_bf16 v[220:223], v[104:107], v[56:59], v[220:223]\n"
"v_pk_max_i16 v205, v205, 0\n"
"v_pk_max_i16 v208, v208, 0\n"
"v_mfma_f32_16x16x32_bf16 v[228:231], v[104:107], v[80:83], v[228:231]\n"
"buffer_store_dwordx2 v[204:205], v162, s[84:87], s67 offen offset:0 sc0 sc1\n"
"v_pk_max_i16 v209, v209, 0\n"
"v_pk_max_i16 v206, v206, 0\n"
"v_mfma_f32_16x16x32_bf16 v[120:123], v[28:31], v[200:203], 0\n"
"v_pk_max_i16 v207, v207, 0\n"
"v_pk_max_i16 v210, v210, 0\n"
"v_mfma_f32_16x16x32_bf16 v[120:123], v[24:27], v[196:199], v[120:123]\n"
"buffer_store_dwordx2 v[208:209], v162, s[84:87], s67 offen offset:256 sc0 sc1\n"
"v_pk_max_i16 v211, v211, 0\n"
"v_exp_f32 v112, v112\n"
"v_mfma_f32_16x16x32_bf16 v[124:127], v[44:47], v[200:203], 0\n"
"v_exp_f32 v113, v113\n"
"v_mfma_f32_16x16x32_bf16 v[124:127], v[40:43], v[196:199], v[124:127]\n"
"buffer_store_dwordx2 v[206:207], v162, s[84:87], s67 offen offset:512 sc0 sc1\n"
"v_exp_f32 v116, v116\n"
"v_exp_f32 v117, v117\n"
"v_exp_f32 v114, v114\n"
"v_exp_f32 v115, v115\n"
"v_exp_f32 v118, v118\n"
"v_exp_f32 v119, v119\n"
"v_exp_f32 v120, v120\n"
"v_exp_f32 v121, v121\n"
"s_waitcnt lgkmcnt(0)\n"
"v_mul_f32_dpp v220, v130, v220 quad_perm:[0,0,0,0] row_mask:0xf bank_mask:0xf\n"
"v_mul_f32_dpp v221, v130, v221 quad_perm:[1,1,1,1] row_mask:0xf bank_mask:0xf\n"
"v_mul_f32_dpp v222, v130, v222 quad_perm:[2,2,2,2] row_mask:0xf bank_mask:0xf\n"
"v_mul_f32_dpp v223, v130, v223 quad_perm:[3,3,3,3] row_mask:0xf bank_mask:0xf\n"
"v_mul_f32_dpp v224, v130, v224 quad_perm:[0,0,0,0] row_mask:0xf bank_mask:0xf\n"
"v_mul_f32_dpp v225, v130, v225 quad_perm:[1,1,1,1] row_mask:0xf bank_mask:0xf\n"
"v_mul_f32_dpp v226, v130, v226 quad_perm:[2,2,2,2] row_mask:0xf bank_mask:0xf\n"
".Lpad6_%=:\n"
"s_cbranch_vccnz .Lback0_%=\n"
"v_mfma_f32_16x16x32_bf16 v[220:223], a[64:67], a[160:163], v[220:223]\n"
"v_mul_f32_dpp v227, v130, v227 quad_perm:[3,3,3,3] row_mask:0xf bank_mask:0xf\n"
"v_mul_f32_dpp v228, v130, v228 quad_perm:[0,0,0,0] row_mask:0xf bank_mask:0xf\n"
"v_mfma_f32_16x16x32_bf16 v[220:223], a[68:71], a[164:167], v[220:223]\n"
"v_mul_f32_dpp v229, v130, v229 quad_perm:[1,1,1,1] row_mask:0xf bank_mask:0xf\n"
"v_mul_f32_dpp v230, v130, v230 quad_perm:[2,2,2,2] row_mask:0xf bank_mask:0xf\n"
"v_mfma_f32_16x16x32_bf16 v[220:223], a[72:75], a[168:171], v[220:223]\n"
"v_mul_f32_dpp v231, v130, v231 quad_perm:[3,3,3,3] row_mask:0xf bank_mask:0xf\n"
"v_mul_f32_dpp v232, v130, v232 quad_perm:[0,0,0,0] row_mask:0xf bank_mask:0xf\n"
"v_mfma_f32_16x16x32_bf16 v[220:223], a[76:79], a[172:175], v[220:223]\n"
"buffer_store_dwordx2 v[210:211], v162, s[84:87], s67 offen offset:768 sc0 sc1\n"
"v_mul_f32_dpp v233, v130, v233 quad_perm:[1,1,1,1] row_mask:0xf bank_mask:0xf\n"
"v_mul_f32_dpp v234, v130, v234 quad_perm:[2,2,2,2] row_mask:0xf bank_mask:0xf\n"
"v_mfma_f32_16x16x32_bf16 v[224:227], a[76:79], a[188:191], v[224:227]\n"
"v_mul_f32_dpp v235, v130, v235 quad_perm:[3,3,3,3] row_mask:0xf bank_mask:0xf\n"
"v_cvt_pk_bf16_f32 v112, v112, v113\n"
"v_mfma_f32_16x16x32_bf16 v[224:227], a[72:75], a[184:187], v[224:227]\n"
"v_exp_f32 v122, v122\n"
"v_mfma_f32_16x16x32_bf16 v[224:227], a[68:71], a[180:183], v[224:227]\n"
"v_exp_f32 v123, v123\n"
"v_mfma_f32_16x16x32_bf16 v[224:227], a[64:67], a[176:179], v[224:227]\n"
"v_exp_f32 v124, v124\n"
"v_mfma_f32_16x16x32_bf16 v[228:231], a[64:67], a[192:195], v[228:231]\n"
"v_exp_f32 v125, v125\n"
"v_mfma_f32_16x16x32_bf16 v[228:231], a[68:71], a[196:199], v[228:231]\n"
"v_exp_f32 v126, v126\n"
"v_mfma_f32_16x16x32_bf16 v[228:231], a[72:75], a[200:203], v[228:231]\n"
"v_cvt_pk_bf16_f32 v220, v220, v224\n"
"v_cvt_pk_bf16_f32 v222, v222, v226\n"
"v_mfma_f32_16x16x32_bf16 v[228:231], a[76:79], a[204:207], v[228:231]\n"
"v_cvt_pk_bf16_f32 v224, v221, v225\n"
"v_cvt_pk_bf16_f32 v226, v223, v227\n"
"v_mfma_f32_16x16x32_bf16 v[232:235], a[76:79], a[220:223], v[232:235]\n"
"v_pk_max_i16 v220, v220, 0\n"
"v_pk_max_i16 v224, v224, 0\n"
"v_mfma_f32_16x16x32_bf16 v[232:235], a[72:75], a[216:219], v[232:235]\n"
"v_pk_max_i16 v222, v222, 0\n"
"v_pk_max_i16 v226, v226, 0\n"
"v_mfma_f32_16x16x32_bf16 v[232:235], a[68:71], a[212:215], v[232:235]\n"
"v_exp_f32 v127, v127\n"
"v_mfma_f32_16x16x32_bf16 v[232:235], a[64:67], a[208:211], v[232:235]\n"
"v_cvt_pk_bf16_f32 v116, v116, v117\n"
"v_cvt_pk_bf16_f32 v113, v114, v115\n"
"v_cvt_pk_bf16_f32 v117, v118, v119\n"
"v_mfma_f32_16x16x4_4b_bf16 v[204:219], v[128:129], a[244:245], 0 cbsz:2 abid:3\n"
"v_cvt_pk_bf16_f32 v114, v120, v121\n"
"v_cvt_pk_bf16_f32 v115, v122, v123\n"
"v_cvt_pk_bf16_f32 v118, v124, v125\n"
"v_cvt_pk_bf16_f32 v119, v126, v127\n"
"v_cvt_pk_bf16_f32 v221, v228, v232\n"
"s_nop 0\n"
"v_smfmac_f32_16x16x64_bf16 v[252:255], a[248:251], v[112:119], v157\n"
"v_cvt_pk_bf16_f32 v225, v229, v233\n"
"v_cvt_pk_bf16_f32 v223, v230, v234\n"
"v_cvt_pk_bf16_f32 v227, v231, v235\n"
"s_nop 0\n"
"v_mfma_f32_16x16x32_bf16 v[204:207], v[108:111], v[48:51], v[204:207]\n"
"v_pk_max_i16 v221, v221, 0\n"
"v_pk_max_i16 v225, v225, 0\n"
"v_mfma_f32_16x16x32_bf16 v[204:207], v[112:115], v[52:55], v[204:207]\n"
"v_pk_max_i16 v223, v223, 0\n"
"v_pk_max_i16 v227, v227, 0\n"
"v_mfma_f32_16x16x32_bf16 v[204:207], v[116:119], v[56:59], v[204:207]\n"
"buffer_store_dwordx2 v[220:221], v162, s[84:87], s68 offen offset:0 sc0 sc1\n"
"v_add_f32 v130, v254, v255\n"
"v_rcp_f32 v131, v130\n"
"v_mfma_f32_16x16x32_bf16 v[208:211], v[116:119], v[68:71], v[208:211]\n"
"v_maximum3_f32 v159, v159, v130, v131\n"
"v_mfma_f32_16x16x32_bf16 v[208:211], v[112:115], v[64:67], v[208:211]\n"
"v_mfma_f32_16x16x32_bf16 v[208:211], v[108:111], v[60:63], v[208:211]\n"
"buffer_store_dwordx2 v[224:225], v162, s[84:87], s68 offen offset:256 sc0 sc1\n"
"ds_bpermute_b32 v130, v160, v131 offset:128\n"
"v_mfma_f32_16x16x32_bf16 v[212:215], v[108:111], v[72:75], v[212:215]\n"
"v_mfma_f32_16x16x32_bf16 v[212:215], v[112:115], v[76:79], v[212:215]\n"
"v_mfma_f32_16x16x32_bf16 v[212:215], v[116:119], v[80:83], v[212:215]\n"
"buffer_store_dwordx2 v[222:223], v162, s[84:87], s68 offen offset:512 sc0 sc1\n"
"v_mfma_f32_16x16x32_bf16 v[216:219], v[116:119], v[92:95], v[216:219]\n"
"v_mfma_f32_16x16x32_bf16 v[216:219], v[112:115], v[88:91], v[216:219]\n"
"v_mfma_f32_16x16x32_bf16 v[216:219], v[108:111], v[84:87], v[216:219]\n"
"s_waitcnt lgkmcnt(0)\n"
"buffer_store_dwordx2 v[226:227], v162, s[84:87], s68 offen offset:768 sc0 sc1\n"
"v_fmac_f32_dpp v164, v130, v204 quad_perm:[0,0,0,0] row_mask:0xf bank_mask:0xf\n"
"v_fmac_f32_dpp v168, v130, v208 quad_perm:[0,0,0,0] row_mask:0xf bank_mask:0xf\n"
"v_fmac_f32_dpp v166, v130, v206 quad_perm:[2,2,2,2] row_mask:0xf bank_mask:0xf\n"
"v_fmac_f32_dpp v170, v130, v210 quad_perm:[2,2,2,2] row_mask:0xf bank_mask:0xf\n"
"v_fmac_f32_dpp v165, v130, v205 quad_perm:[1,1,1,1] row_mask:0xf bank_mask:0xf\n"
"v_fmac_f32_dpp v169, v130, v209 quad_perm:[1,1,1,1] row_mask:0xf bank_mask:0xf\n"
"v_fmac_f32_dpp v167, v130, v207 quad_perm:[3,3,3,3] row_mask:0xf bank_mask:0xf\n"
"v_fmac_f32_dpp v171, v130, v211 quad_perm:[3,3,3,3] row_mask:0xf bank_mask:0xf\n"
"v_cvt_pk_bf16_f32 v164, v164, v168\n"
"v_cvt_pk_bf16_f32 v166, v166, v170\n"
"v_fmac_f32_dpp v172, v130, v212 quad_perm:[0,0,0,0] row_mask:0xf bank_mask:0xf\n"
"v_fmac_f32_dpp v176, v130, v216 quad_perm:[0,0,0,0] row_mask:0xf bank_mask:0xf\n"
"v_fmac_f32_dpp v173, v130, v213 quad_perm:[1,1,1,1] row_mask:0xf bank_mask:0xf\n"
"v_fmac_f32_dpp v177, v130, v217 quad_perm:[1,1,1,1] row_mask:0xf bank_mask:0xf\n"
"v_fmac_f32_dpp v174, v130, v214 quad_perm:[2,2,2,2] row_mask:0xf bank_mask:0xf\n"
"v_fmac_f32_dpp v178, v130, v218 quad_perm:[2,2,2,2] row_mask:0xf bank_mask:0xf\n"
"v_fmac_f32_dpp v175, v130, v215 quad_perm:[3,3,3,3] row_mask:0xf bank_mask:0xf\n"
"v_fmac_f32_dpp v179, v130, v219 quad_perm:[3,3,3,3] row_mask:0xf bank_mask:0xf\n"
"v_cvt_pk_bf16_f32 v168, v165, v169\n"
"v_cvt_pk_bf16_f32 v170, v167, v171\n"
"v_cvt_pk_bf16_f32 v165, v172, v176\n"
"v_cvt_pk_bf16_f32 v169, v173, v177\n"
"v_cvt_pk_bf16_f32 v167, v174, v178\n"
"v_cvt_pk_bf16_f32 v171, v175, v179\n"
"v_pk_max_i16 v164, v164, 0\n"
"v_pk_max_i16 v165, v165, 0\n"
"v_pk_max_i16 v168, v168, 0\n"
"v_pk_max_i16 v169, v169, 0\n"
"v_pk_max_i16 v166, v166, 0\n"
"v_pk_max_i16 v167, v167, 0\n"
"v_pk_max_i16 v170, v170, 0\n"
"v_pk_max_i16 v171, v171, 0\n"
"buffer_store_dwordx2 v[164:165], v162, s[84:87], s69 offen offset:0 sc0 sc1\n"
"buffer_store_dwordx2 v[168:169], v162, s[84:87], s69 offen offset:256 sc0 sc1\n"
"buffer_store_dwordx2 v[166:167], v162, s[84:87], s69 offen offset:512 sc0 sc1\n"
"buffer_store_dwordx2 v[170:171], v162, s[84:87], s69 offen offset:768 sc0 sc1\n"
"v_mov_b32 v0, 0x4e800000\n"
"v_cmp_nle_f32 vcc, v159, v0\n"
"s_nop 4\n"
"s_cmp_eq_u64 vcc, 0\n"
"s_cbranch_scc0 .Lhot%=\n"
"s_endpgm\n"
".Lcend%=:\n"
".Lhot%=:\n"
"s_waitcnt vmcnt(0) lgkmcnt(0)\n"
"s_sub_u32 s24, s8, 192\n"
"s_subb_u32 s25, s9, 0\n"
"s_and_b32 s25, s25, 0xffff\n"
"s_mov_b32 s26, 104857600\n"
"s_mov_b32 s27, 0x20000\n"
"s_sub_u32 s84, s18, 13107200\n"
"s_subb_u32 s85, s19, 0\n"
"s_and_b32 s85, s85, 0xffff\n"
"s_mov_b32 s86, 25472\n"
"s_mov_b32 s87, 0x20000\n"
"v_mov_b32 v0, 0x3f003f00\n"
"v_accvgpr_write_b32 a252, v0\n"
"v_accvgpr_write_b32 a253, v0\n"
"v_accvgpr_write_b32 a254, v0\n"
"v_accvgpr_write_b32 a255, v0\n"
"v_lshrrev_b32 v0, 8, v153\n"
"v_lshl_add_u32 v158, v0, 9, s29\n"
"v_and_b32 v0, 0x30, v153\n"
"v_add_u32 v158, v0, v158\n"
"s_add_u32 s89, s29, 4096\n"
"v_lshrrev_b32 v156, 2, v153\n"
"v_and_b32 v0, 12, v156\n"
"v_and_b32 v156, 0xf0, v156\n"
"v_lshl_add_u32 v156, v0, 20, v156\n"
"v_add_u32 v156, s89, v156\n"
"s_lshl_b32 s89, %[wave], 12\n"
"s_add_u32 s89, s89, 33152\n"
"v_lshrrev_b32 v163, 1, v153\n"
"v_add_u32 v163, s89, v163\n"
"s_nop 11\n"
"ds_read_b128 v[248:251], v153 offset:0\n"
"ds_read_b128 a[224:227], v153 offset:4096\n"
"ds_read_b128 v[236:239], v153 offset:8192\n"
"ds_read_b128 v[240:243], v153 offset:12288\n"
"ds_read_b128 v[244:247], v153 offset:1024\n"
"ds_read_b128 a[228:231], v158 offset:0\n"
"ds_read_b128 a[232:235], v158 offset:2048\n"
"ds_read_b64 a[244:245], v163 offset:0\n"
"ds_read_b32 a236, v156 offset:0\n"
"ds_read_b32 a237, v156 offset:8\n"
"ds_read_b32 a238, v156 offset:4\n"
"ds_read_b32 a239, v156 offset:12\n"
"ds_read_b32 a240, v156 offset:256\n"
"ds_read_b32 a241, v156 offset:264\n"
"ds_read_b32 a242, v156 offset:260\n"
"ds_read_b32 a243, v156 offset:268\n"
"s_add_u32 m0, s28, 0\n"
"v_add_u32 v156, 512, v156\n"
"buffer_load_dwordx4 v152, s[24:27], s40 offen offset:0 lds\n"
"buffer_load_dwordx4 v152, s[24:27], s41 offen offset:1088 lds\n"
"buffer_load_dwordx4 v152, s[24:27], s42 offen offset:2176 lds\n"
"buffer_load_dwordx4 v152, s[24:27], s43 offen offset:3264 lds\n"
"s_add_u32 m0, s28, 4352\n"
"s_nop 0\n"
"buffer_load_dwordx4 v152, s[24:27], s44 offen offset:0 lds\n"
"buffer_load_dwordx4 v152, s[24:27], s45 offen offset:1088 lds\n"
"s_waitcnt vmcnt(2)\n"
"ds_read_b128 a[0:3], v154 offset:0\n"
"ds_read_b128 a[4:7], v154 offset:64\n"
"buffer_load_dwordx4 v152, s[24:27], s46 offen offset:2176 lds\n"
"ds_read_b128 a[8:11], v154 offset:128\n"
"ds_read_b128 a[12:15], v154 offset:192\n"
"s_waitcnt lgkmcnt(3)\n"
"v_mfma_f32_16x16x32_bf16 v[0:3], a[96:99], a[0:3], 0\n"
"s_waitcnt lgkmcnt(2)\n"
"buffer_load_dwordx4 v152, s[24:27], s47 offen offset:3264 lds\n"
"s_add_u32 m0, s28, 8704\n"
"v_mfma_f32_16x16x32_bf16 v[0:3], a[100:103], a[4:7], v[0:3]\n"
"s_waitcnt lgkmcnt(1)\n"
"buffer_load_dwordx4 v152, s[24:27], s48 offen offset:0 lds\n"
"v_mfma_f32_16x16x32_bf16 v[0:3], a[104:107], a[8:11], v[0:3]\n"
"s_waitcnt lgkmcnt(0)\n"
"buffer_load_dwordx4 v152, s[24:27], s49 offen offset:1088 lds\n"
"v_mfma_f32_16x16x32_bf16 v[0:3], a[108:111], a[12:15], v[0:3]\n"
"buffer_load_dwordx4 v152, s[24:27], s50 offen offset:2176 lds\n"
"v_mfma_f32_16x16x32_bf16 v[4:7], a[112:115], a[0:3], 0\n"
"buffer_load_dwordx4 v152, s[24:27], s51 offen offset:3264 lds\n"
"v_mfma_f32_16x16x32_bf16 v[4:7], a[116:119], a[4:7], v[4:7]\n"
"v_mfma_f32_16x16x32_bf16 v[4:7], a[120:123], a[8:11], v[4:7]\n"
"v_cvt_pk_bf16_f32 v0, v0, v1\n"
"v_cvt_pk_bf16_f32 v1, v2, v3\n"
"v_mfma_f32_16x16x32_bf16 v[4:7], a[124:127], a[12:15], v[4:7]\n"
"v_mfma_f32_16x16x32_bf16 v[164:167], a[144:147], a[0:3], 0\n"
"v_mfma_f32_16x16x32_bf16 v[164:167], a[148:151], a[4:7], v[164:167]\n"
"s_add_u32 m0, s28, 0\n"
"v_mfma_f32_16x16x32_bf16 v[164:167], a[152:155], a[8:11], v[164:167]\n"
"buffer_load_dwordx4 v152, s[24:27], s52 offen offset:0 lds\n"
"v_cvt_pk_bf16_f32 v2, v4, v5\n"
"v_mfma_f32_16x16x32_bf16 v[164:167], a[156:159], a[12:15], v[164:167]\n"
"buffer_load_dwordx4 v152, s[24:27], s53 offen offset:1088 lds\n"
"v_cvt_pk_bf16_f32 v3, v6, v7\n"
"v_mfma_f32_16x16x32_bf16 v[4:7], a[128:131], a[0:3], 0\n"
"buffer_load_dwordx4 v152, s[24:27], s54 offen offset:2176 lds\n"
"v_mfma_f32_16x16x32_bf16 v[4:7], a[132:135], a[4:7], v[4:7]\n"
"buffer_load_dwordx4 v152, s[24:27], s55 offen offset:3264 lds\n"
"v_mfma_f32_16x16x32_bf16 v[4:7], a[136:139], a[8:11], v[4:7]\n"
"v_mfma_f32_16x16x32_bf16 v[4:7], a[140:143], a[12:15], v[4:7]\n"
"v_mfma_f32_16x16x32_bf16 v[180:183], v[248:251], a[0:3], 0\n"
"v_mfma_f32_16x16x32_bf16 v[180:183], a[224:227], a[4:7], v[180:183]\n"
"v_mfma_f32_16x16x32_bf16 v[180:183], v[236:239], a[8:11], v[180:183]\n"
"v_cvt_pk_bf16_f32 v4, v4, v5\n"
"v_cvt_pk_bf16_f32 v5, v6, v7\n"
"v_mfma_f32_16x16x32_bf16 v[180:183], v[240:243], a[12:15], v[180:183]\n"
"v_cvt_pk_bf16_f32 v6, v164, v165\n"
"v_cvt_pk_bf16_f32 v7, v166, v167\n"
"v_mfma_f32_16x16x32_bf16 v[184:187], v[244:247], a[0:3], 0\n"
"s_nop 4\n"
"v_cvt_pk_bf16_f32 v180, v180, v181\n"
"v_cvt_pk_bf16_f32 v181, v182, v183\n"
"s_waitcnt vmcnt(8)\n"
"ds_read_b128 a[16:19], v154 offset:4352\n"
"ds_read_b128 a[20:23], v154 offset:4416\n"
"ds_read_b128 a[24:27], v154 offset:4480\n"
"ds_read_b128 a[28:31], v154 offset:4544\n"
"s_waitcnt vmcnt(4)\n"
"ds_read_b128 a[32:35], v154 offset:8704\n"
"ds_read_b128 a[36:39], v154 offset:8768\n"
"ds_read_b128 a[40:43], v154 offset:8832\n"
"ds_read_b128 a[44:47], v154 offset:8896\n"
"s_waitcnt lgkmcnt(4)\n"
"v_mfma_f32_16x16x32_bf16 v[8:11], a[108:111], a[28:31], 0\n"
"v_mfma_f32_16x16x32_bf16 v[8:11], a[104:107], a[24:27], v[8:11]\n"
"s_waitcnt vmcnt(0)\n"
"ds_read_b128 a[48:51], v154 offset:0\n"
"ds_read_b128 a[52:55], v154 offset:64\n"
"v_mfma_f32_16x16x32_bf16 v[8:11], a[100:103], a[20:23], v[8:11]\n"
"ds_read_b128 a[56:59], v154 offset:128\n"
"ds_read_b128 a[60:63], v154 offset:192\n"
"v_mfma_f32_16x16x32_bf16 v[8:11], a[96:99], a[16:19], v[8:11]\n"
"s_waitcnt lgkmcnt(7)\n"
"v_mfma_f32_16x16x32_bf16 v[16:19], a[96:99], a[32:35], 0\n"
"s_waitcnt lgkmcnt(6)\n"
"v_mfma_f32_16x16x32_bf16 v[16:19], a[100:103], a[36:39], v[16:19]\n"
"s_waitcnt lgkmcnt(5)\n"
"v_mfma_f32_16x16x32_bf16 v[16:19], a[104:107], a[40:43], v[16:19]\n"
"s_waitcnt lgkmcnt(4)\n"
"v_cvt_pk_bf16_f32 v8, v8, v9\n"
"v_cvt_pk_bf16_f32 v9, v10, v11\n"
"v_mfma_f32_16x16x32_bf16 v[16:19], a[108:111], a[44:47], v[16:19]\n"
"v_mfma_f32_16x16x32_bf16 v[12:15], a[124:127], a[28:31], 0\n"
"s_add_u32 m0, s28, 4352\n"
"v_mfma_f32_16x16x32_bf16 v[12:15], a[120:123], a[24:27], v[12:15]\n"
"buffer_load_dwordx4 v152, s[24:27], s56 offen offset:0 lds\n"
"v_mfma_f32_16x16x32_bf16 v[12:15], a[116:119], a[20:23], v[12:15]\n"
"buffer_load_dwordx4 v152, s[24:27], s57 offen offset:1088 lds\n"
"v_cvt_pk_bf16_f32 v16, v16, v17\n"
"v_mfma_f32_16x16x32_bf16 v[12:15], a[112:115], a[16:19], v[12:15]\n"
"buffer_load_dwordx4 v152, s[24:27], s58 offen offset:2176 lds\n"
"v_cvt_pk_bf16_f32 v17, v18, v19\n"
"v_mfma_f32_16x16x32_bf16 v[20:23], a[112:115], a[32:35], 0\n"
"buffer_load_dwordx4 v152, s[24:27], s59 offen offset:3264 lds\n"
"s_add_u32 m0, s28, 8704\n"
"v_mfma_f32_16x16x32_bf16 v[20:23], a[116:119], a[36:39], v[20:23]\n"
"buffer_load_dwordx4 v152, s[24:27], s60 offen offset:0 lds\n"
"v_mfma_f32_16x16x32_bf16 v[20:23], a[120:123], a[40:43], v[20:23]\n"
"buffer_load_dwordx4 v152, s[24:27], s61 offen offset:1088 lds\n"
"v_cvt_pk_bf16_f32 v10, v12, v13\n"
"v_mfma_f32_16x16x32_bf16 v[20:23], a[124:127], a[44:47], v[20:23]\n"
"buffer_load_dwordx4 v152, s[24:27], s62 offen offset:2176 lds\n"
"v_cvt_pk_bf16_f32 v11, v14, v15\n"
"v_mfma_f32_16x16x32_bf16 v[12:15], a[140:143], a[28:31], 0\n"
"buffer_load_dwordx4 v152, s[24:27], s63 offen offset:3264 lds\n"
"v_mfma_f32_16x16x32_bf16 v[12:15], a[136:139], a[24:27], v[12:15]\n"
"v_mfma_f32_16x16x32_bf16 v[12:15], a[132:135], a[20:23], v[12:15]\n"
"v_cvt_pk_bf16_f32 v18, v20, v21\n"
"v_cvt_pk_bf16_f32 v19, v22, v23\n"
"v_mfma_f32_16x16x32_bf16 v[12:15], a[128:131], a[16:19], v[12:15]\n"
"s_add_u32 s24, s24, 13107200\n"
"s_addc_u32 s25, s25, 0\n"
"s_sub_u32 s26, s26, 13107200\n"
"s_cselect_b32 s26, 0, s26\n"
"v_mfma_f32_16x16x32_bf16 v[20:23], a[128:131], a[32:35], 0\n"
"v_mfma_f32_16x16x32_bf16 v[20:23], a[132:135], a[36:39], v[20:23]\n"
"s_add_u32 m0, s28, 0\n"
"v_cvt_pk_bf16_f32 v12, v12, v13\n"
"v_mfma_f32_16x16x32_bf16 v[20:23], a[136:139], a[40:43], v[20:23]\n"
"s_waitcnt lgkmcnt(0)\n"
"buffer_load_dwordx4 v152, s[24:27], s40 offen offset:0 lds\n"
"v_cvt_pk_bf16_f32 v13, v14, v15\n"
"v_mfma_f32_16x16x32_bf16 v[20:23], a[140:143], a[44:47], v[20:23]\n"
"buffer_load_dwordx4 v152, s[24:27], s41 offen offset:1088 lds\n"
"v_mfma_f32_16x16x32_bf16 v[168:171], a[156:159], a[28:31], 0\n"
"buffer_load_dwordx4 v152, s[24:27], s42 offen offset:2176 lds\n"
"v_mfma_f32_16x16x32_bf16 v[168:171], a[152:155], a[24:27], v[168:171]\n"
"buffer_load_dwordx4 v152, s[24:27], s43 offen offset:3264 lds\n"
"v_mfma_f32_16x16x32_bf16 v[168:171], a[148:151], a[20:23], v[168:171]\n"
"v_cvt_pk_bf16_f32 v20, v20, v21\n"
"v_cvt_pk_bf16_f32 v21, v22, v23\n"
"v_mfma_f32_16x16x32_bf16 v[168:171], a[144:147], a[16:19], v[168:171]\n"
"v_mfma_f32_16x16x32_bf16 v[172:175], a[144:147], a[32:35], 0\n"
"v_mfma_f32_16x16x32_bf16 v[172:175], a[148:151], a[36:39], v[172:175]\n"
"v_mfma_f32_16x16x32_bf16 v[172:175], a[152:155], a[40:43], v[172:175]\n"
"v_cvt_pk_bf16_f32 v14, v168, v169\n"
"v_cvt_pk_bf16_f32 v15, v170, v171\n"
"v_mfma_f32_16x16x32_bf16 v[172:175], a[156:159], a[44:47], v[172:175]\n"
"v_mfma_f32_16x16x32_bf16 v[24:27], a[96:99], a[48:51], 0\n"
"v_mfma_f32_16x16x32_bf16 v[24:27], a[100:103], a[52:55], v[24:27]\n"
"v_mfma_f32_16x16x32_bf16 v[24:27], a[104:107], a[56:59], v[24:27]\n"
"v_cvt_pk_bf16_f32 v22, v172, v173\n"
"v_cvt_pk_bf16_f32 v23, v174, v175\n"
"v_mfma_f32_16x16x32_bf16 v[24:27], a[108:111], a[60:63], v[24:27]\n"
"v_mfma_f32_16x16x32_bf16 v[176:179], a[112:115], a[48:51], 0\n"
"v_mfma_f32_16x16x32_bf16 v[176:179], a[116:119], a[52:55], v[176:179]\n"
"v_mfma_f32_16x16x32_bf16 v[176:179], a[120:123], a[56:59], v[176:179]\n"
"v_cvt_pk_bf16_f32 v24, v24, v25\n"
"v_cvt_pk_bf16_f32 v25, v26, v27\n"
"v_mfma_f32_16x16x32_bf16 v[176:179], a[124:127], a[60:63], v[176:179]\n"
"v_mfma_f32_16x16x32_bf16 v[188:191], v[240:243], a[28:31], 0\n"
"v_mfma_f32_16x16x32_bf16 v[188:191], v[236:239], a[24:27], v[188:191]\n"
"v_mfma_f32_16x16x32_bf16 v[188:191], a[224:227], a[20:23], v[188:191]\n"
"v_cvt_pk_bf16_f32 v26, v176, v177\n"
"v_cvt_pk_bf16_f32 v27, v178, v179\n"
"v_mfma_f32_16x16x32_bf16 v[188:191], v[248:251], a[16:19], v[188:191]\n"
"v_mfma_f32_16x16x32_bf16 v[196:199], v[248:251], a[32:35], 0\n"
"ds_read_b128 v[248:251], v153 offset:5120\n"
"v_mfma_f32_16x16x32_bf16 v[196:199], a[224:227], a[36:39], v[196:199]\n"
"ds_read_b128 a[224:227], v153 offset:9216\n"
"v_mfma_f32_16x16x32_bf16 v[196:199], v[236:239], a[40:43], v[196:199]\n"
"ds_read_b128 v[236:239], v153 offset:13312\n"
"v_cvt_pk_bf16_f32 v188, v188, v189\n"
"v_mfma_f32_16x16x32_bf16 v[196:199], v[240:243], a[44:47], v[196:199]\n"
"ds_read_b128 v[240:243], v153 offset:2048\n"
"v_cvt_pk_bf16_f32 v189, v190, v191\n"
"v_mfma_f32_16x16x32_bf16 v[28:31], a[128:131], a[48:51], 0\n"
"v_mfma_f32_16x16x32_bf16 v[28:31], a[132:135], a[52:55], v[28:31]\n"
"v_mfma_f32_16x16x32_bf16 v[28:31], a[136:139], a[56:59], v[28:31]\n"
"v_cvt_pk_bf16_f32 v196, v196, v197\n"
"v_cvt_pk_bf16_f32 v197, v198, v199\n"
"v_mfma_f32_16x16x32_bf16 v[28:31], a[140:143], a[60:63], v[28:31]\n"
"v_mfma_f32_16x16x32_bf16 v[200:203], v[244:247], a[32:35], 0\n"
"v_mfma_f32_16x16x32_bf16 v[164:167], a[144:147], a[48:51], 0\n"
"v_mfma_f32_16x16x32_bf16 v[164:167], a[148:151], a[52:55], v[164:167]\n"
"v_cvt_pk_bf16_f32 v28, v28, v29\n"
"v_cvt_pk_bf16_f32 v29, v30, v31\n"
"v_mfma_f32_16x16x32_bf16 v[164:167], a[152:155], a[56:59], v[164:167]\n"
"v_mfma_f32_16x16x32_bf16 v[164:167], a[156:159], a[60:63], v[164:167]\n"
"s_waitcnt lgkmcnt(3)\n"
"v_mfma_f32_16x16x32_bf16 v[184:187], v[248:251], a[4:7], v[184:187]\n"
"s_waitcnt lgkmcnt(2)\n"
"v_mfma_f32_16x16x32_bf16 v[184:187], a[224:227], a[8:11], v[184:187]\n"
"s_waitcnt lgkmcnt(1)\n"
"v_mfma_f32_16x16x32_bf16 v[184:187], v[236:239], a[12:15], v[184:187]\n"
"v_cvt_pk_bf16_f32 v30, v164, v165\n"
"v_cvt_pk_bf16_f32 v31, v166, v167\n"
"v_mfma_f32_16x16x32_bf16 v[192:195], v[236:239], a[28:31], 0\n"
"v_mfma_f32_16x16x32_bf16 v[192:195], a[224:227], a[24:27], v[192:195]\n"
"v_mfma_f32_16x16x32_bf16 v[192:195], v[248:251], a[20:23], v[192:195]\n"
"v_cvt_pk_bf16_f32 v182, v184, v185\n"
"v_cvt_pk_bf16_f32 v183, v186, v187\n"
"v_mfma_f32_16x16x32_bf16 v[192:195], v[244:247], a[16:19], v[192:195]\n"
"ds_read_b128 v[244:247], v153 offset:6144\n"
"v_mfma_f32_16x16x32_bf16 v[200:203], v[248:251], a[36:39], v[200:203]\n"
"ds_read_b128 v[248:251], v153 offset:10240\n"
"v_mfma_f32_16x16x32_bf16 v[200:203], a[224:227], a[40:43], v[200:203]\n"
"ds_read_b128 a[224:227], v153 offset:14336\n"
"v_mfma_f32_16x16x32_bf16 v[200:203], v[236:239], a[44:47], v[200:203]\n"
"s_waitcnt lgkmcnt(3)\n"
"ds_read_b128 v[236:239], v153 offset:3072\n"
"v_cvt_pk_bf16_f32 v190, v192, v193\n"
"v_mfma_f32_16x16x32_bf16 v[184:187], v[240:243], a[0:3], 0\n"
"v_cvt_pk_bf16_f32 v191, v194, v195\n"
"v_mfma_f32_16x16x32_bf16 v[120:123], a[228:231], v[180:183], 0\n"
"ds_read_b128 a[228:231], v158 offset:0\n"
"v_mfma_f32_16x16x32_bf16 v[96:99], v[0:3], v[180:183], 0\n"
"v_cvt_pk_bf16_f32 v198, v200, v201\n"
"v_cvt_pk_bf16_f32 v199, v202, v203\n"
"v_mfma_f32_16x16x32_bf16 v[200:203], v[240:243], a[32:35], 0\n"
"v_mfma_f32_16x16x32_bf16 v[104:107], v[16:19], v[180:183], 0\n"
"s_waitcnt lgkmcnt(4)\n"
"v_mfma_f32_16x16x32_bf16 v[184:187], v[244:247], a[4:7], v[184:187]\n"
"s_waitcnt lgkmcnt(3)\n"
"v_mfma_f32_16x16x32_bf16 v[184:187], v[248:251], a[8:11], v[184:187]\n"
"s_waitcnt lgkmcnt(2)\n"
"v_mfma_f32_16x16x32_bf16 v[184:187], a[224:227], a[12:15], v[184:187]\n"
"v_mfma_f32_16x16x32_bf16 v[192:195], a[224:227], a[28:31], 0\n"
"v_mfma_f32_16x16x32_bf16 v[192:195], v[248:251], a[24:27], v[192:195]\n"
"v_mfma_f32_16x16x32_bf16 v[192:195], v[244:247], a[20:23], v[192:195]\n"
"v_cvt_pk_bf16_f32 v184, v184, v185\n"
"v_cvt_pk_bf16_f32 v185, v186, v187\n"
"v_mfma_f32_16x16x32_bf16 v[192:195], v[240:243], a[16:19], v[192:195]\n"
"ds_read_b128 v[240:243], v153 offset:7168\n"
"v_mfma_f32_16x16x32_bf16 v[200:203], v[244:247], a[36:39], v[200:203]\n"
"ds_read_b128 v[244:247], v153 offset:11264\n"
"v_mfma_f32_16x16x32_bf16 v[200:203], v[248:251], a[40:43], v[200:203]\n"
"ds_read_b128 v[248:251], v153 offset:15360\n"
"v_mfma_f32_16x16x32_bf16 v[200:203], a[224:227], a[44:47], v[200:203]\n"
"s_waitcnt lgkmcnt(3)\n"
"ds_read_b128 a[224:227], v153 offset:18496\n"
"v_cvt_pk_bf16_f32 v192, v192, v193\n"
"v_mfma_f32_16x16x32_bf16 v[164:167], v[236:239], a[0:3], 0\n"
"v_cvt_pk_bf16_f32 v193, v194, v195\n"
"s_nop 2\n"
"v_cvt_pk_bf16_f32 v200, v200, v201\n"
"v_cvt_pk_bf16_f32 v201, v202, v203\n"
"s_waitcnt lgkmcnt(3)\n"
"v_mfma_f32_16x16x32_bf16 v[164:167], v[240:243], a[4:7], v[164:167]\n"
"s_waitcnt lgkmcnt(2)\n"
"v_mfma_f32_16x16x32_bf16 v[164:167], v[244:247], a[8:11], v[164:167]\n"
"s_waitcnt lgkmcnt(1)\n"
"v_mfma_f32_16x16x32_bf16 v[164:167], v[248:251], a[12:15], v[164:167]\n"
"s_nop 7\n"
"v_cvt_pk_bf16_f32 v186, v164, v165\n"
"v_cvt_pk_bf16_f32 v187, v166, v167\n"
"s_nop 1\n"
"v_mfma_f32_16x16x32_bf16 v[120:123], a[232:235], v[184:187], v[120:123]\n"
"ds_read_b128 a[232:235], v158 offset:2048\n"
"v_mfma_f32_16x16x32_bf16 v[96:99], v[4:7], v[184:187], v[96:99]\n"
"v_mfma_f32_16x16x32_bf16 v[100:103], v[12:15], v[184:187], 0\n"
"v_mfma_f32_16x16x32_bf16 v[100:103], v[8:11], v[180:183], v[100:103]\n"
"v_max3_f32 v157, v120, v121, v122\n"
"v_mfma_f32_16x16x32_bf16 v[104:107], v[20:23], v[184:187], v[104:107]\n"
"v_max3_f32 v159, v123, v96, v97\n"
"v_max3_f32 v157, v157, v98, v99\n"
"v_mfma_f32_16x16x32_bf16 v[108:111], v[28:31], v[184:187], 0\n"
"v_mfma_f32_16x16x32_bf16 v[108:111], v[24:27], v[180:183], v[108:111]\n"
"v_max3_f32 v159, v159, v100, v101\n"
"v_max3_f32 v157, v157, v102, v103\n"
"v_max3_f32 v159, v159, v104, v105\n"
"v_max3_f32 v157, v157, v106, v107\n"
"s_nop 3\n"
"v_max3_f32 v159, v159, v108, v109\n"
"s_waitcnt vmcnt(8)\n"
"ds_read_b128 a[64:67], v154 offset:4352\n"
"ds_read_b128 a[68:71], v154 offset:4416\n"
"ds_read_b128 a[72:75], v154 offset:4480\n"
"ds_read_b128 a[76:79], v154 offset:4544\n"
"s_waitcnt vmcnt(4)\n"
"ds_read_b128 a[80:83], v154 offset:8704\n"
"ds_read_b128 a[84:87], v154 offset:8768\n"
"ds_read_b128 a[88:91], v154 offset:8832\n"
"ds_read_b128 a[92:95], v154 offset:8896\n"
"s_waitcnt lgkmcnt(4)\n"
"v_mfma_f32_16x16x32_bf16 v[32:35], a[108:111], a[76:79], 0\n"
"v_mfma_f32_16x16x32_bf16 v[32:35], a[104:107], a[72:75], v[32:35]\n"
"v_mfma_f32_16x16x32_bf16 v[32:35], a[100:103], a[68:71], v[32:35]\n"
"v_mfma_f32_16x16x32_bf16 v[32:35], a[96:99], a[64:67], v[32:35]\n"
"v_mfma_f32_16x16x32_bf16 v[172:175], a[124:127], a[76:79], 0\n"
"v_mfma_f32_16x16x32_bf16 v[172:175], a[120:123], a[72:75], v[172:175]\n"
"v_mfma_f32_16x16x32_bf16 v[172:175], a[116:119], a[68:71], v[172:175]\n"
"v_cvt_pk_bf16_f32 v32, v32, v33\n"
"v_cvt_pk_bf16_f32 v33, v34, v35\n"
"v_mfma_f32_16x16x32_bf16 v[172:175], a[112:115], a[64:67], v[172:175]\n"
"v_mfma_f32_16x16x32_bf16 v[36:39], a[140:143], a[76:79], 0\n"
"s_add_u32 m0, s28, 4352\n"
"v_mfma_f32_16x16x32_bf16 v[36:39], a[136:139], a[72:75], v[36:39]\n"
"buffer_load_dwordx4 v152, s[24:27], s44 offen offset:0 lds\n"
"v_mfma_f32_16x16x32_bf16 v[36:39], a[132:135], a[68:71], v[36:39]\n"
"buffer_load_dwordx4 v152, s[24:27], s45 offen offset:1088 lds\n"
"v_cvt_pk_bf16_f32 v34, v172, v173\n"
"v_mfma_f32_16x16x32_bf16 v[36:39], a[128:131], a[64:67], v[36:39]\n"
"buffer_load_dwordx4 v152, s[24:27], s46 offen offset:2176 lds\n"
"v_cvt_pk_bf16_f32 v35, v174, v175\n"
"v_mfma_f32_16x16x32_bf16 v[168:171], a[156:159], a[76:79], 0\n"
"buffer_load_dwordx4 v152, s[24:27], s47 offen offset:3264 lds\n"
"v_mfma_f32_16x16x32_bf16 v[168:171], a[152:155], a[72:75], v[168:171]\n"
"v_mfma_f32_16x16x32_bf16 v[168:171], a[148:151], a[68:71], v[168:171]\n"
"v_cvt_pk_bf16_f32 v36, v36, v37\n"
"v_cvt_pk_bf16_f32 v37, v38, v39\n"
"v_mfma_f32_16x16x32_bf16 v[168:171], a[144:147], a[64:67], v[168:171]\n"
"v_mfma_f32_16x16x32_bf16 v[172:175], v[236:239], a[32:35], 0\n"
"v_mfma_f32_16x16x32_bf16 v[172:175], v[240:243], a[36:39], v[172:175]\n"
"v_mfma_f32_16x16x32_bf16 v[172:175], v[244:247], a[40:43], v[172:175]\n"
"v_cvt_pk_bf16_f32 v38, v168, v169\n"
"v_cvt_pk_bf16_f32 v39, v170, v171\n"
"v_mfma_f32_16x16x32_bf16 v[172:175], v[248:251], a[44:47], v[172:175]\n"
"v_mfma_f32_16x16x32_bf16 v[168:171], v[248:251], a[28:31], 0\n"
"ds_read_b128 v[248:251], v153 offset:28960\n"
"v_mfma_f32_16x16x32_bf16 v[168:171], v[244:247], a[24:27], v[168:171]\n"
"ds_read_b128 v[244:247], v153 offset:24768\n"
"v_mfma_f32_16x16x32_bf16 v[168:171], v[240:243], a[20:23], v[168:171]\n"
"ds_read_b128 v[240:243], v153 offset:20576\n"
"v_cvt_pk_bf16_f32 v202, v172, v173\n"
"v_mfma_f32_16x16x32_bf16 v[168:171], v[236:239], a[16:19], v[168:171]\n"
"s_waitcnt lgkmcnt(3)\n"
"ds_read_b128 v[236:239], v153 offset:16384\n"
"v_cvt_pk_bf16_f32 v203, v174, v175\n"
"v_mfma_f32_16x16x32_bf16 v[40:43], a[96:99], a[80:83], 0\n"
"v_mfma_f32_16x16x32_bf16 v[40:43], a[100:103], a[84:87], v[40:43]\n"
"v_mfma_f32_16x16x32_bf16 v[40:43], a[104:107], a[88:91], v[40:43]\n"
"v_cvt_pk_bf16_f32 v194, v168, v169\n"
"v_cvt_pk_bf16_f32 v195, v170, v171\n"
"v_mfma_f32_16x16x32_bf16 v[40:43], a[108:111], a[92:95], v[40:43]\n"
"s_waitcnt vmcnt(0) lgkmcnt(0)\n"
"s_mov_b32 s74, 7\n"
"s_nop 11\n"
".Lp2s%=:\n"
"v_mfma_f32_16x16x32_bf16 v[176:179], a[112:115], a[80:83], 0\n"
"s_add_u32 m0, s28, 8704\n"
"v_max3_f32 v157, v157, v110, v111\n"
"v_mfma_f32_16x16x32_bf16 v[176:179], a[116:119], a[84:87], v[176:179]\n"
"buffer_load_dwordx4 v152, s[24:27], s48 offen offset:0 lds\n"
"v_mfma_f32_16x16x32_bf16 v[176:179], a[120:123], a[88:91], v[176:179]\n"
"buffer_load_dwordx4 v152, s[24:27], s49 offen offset:1088 lds\n"
"v_cvt_pk_bf16_f32 v40, v40, v41\n"
"v_mfma_f32_16x16x32_bf16 v[176:179], a[124:127], a[92:95], v[176:179]\n"
"buffer_load_dwordx4 v152, s[24:27], s50 offen offset:2176 lds\n"
"v_cvt_pk_bf16_f32 v41, v42, v43\n"
"v_mfma_f32_16x16x32_bf16 v[44:47], a[128:131], a[80:83], 0\n"
"buffer_load_dwordx4 v152, s[24:27], s51 offen offset:3264 lds\n"
"v_mfma_f32_16x16x32_bf16 v[44:47], a[132:135], a[84:87], v[44:47]\n"
"v_mfma_f32_16x16x32_bf16 v[44:47], a[136:139], a[88:91], v[44:47]\n"
"v_cvt_pk_bf16_f32 v42, v176, v177\n"
"v_cvt_pk_bf16_f32 v43, v178, v179\n"
"v_mfma_f32_16x16x32_bf16 v[44:47], a[140:143], a[92:95], v[44:47]\n"
"v_mfma_f32_16x16x32_bf16 v[164:167], a[144:147], a[80:83], 0\n"
"v_mfma_f32_16x16x32_bf16 v[164:167], a[148:151], a[84:87], v[164:167]\n"
"v_mfma_f32_16x16x32_bf16 v[164:167], a[152:155], a[88:91], v[164:167]\n"
"v_cvt_pk_bf16_f32 v44, v44, v45\n"
"v_cvt_pk_bf16_f32 v45, v46, v47\n"
"v_mfma_f32_16x16x32_bf16 v[164:167], a[156:159], a[92:95], v[164:167]\n"
"v_mfma_f32_16x16x32_bf16 v[112:115], v[32:35], v[180:183], 0\n"
"v_mfma_f32_16x16x32_bf16 v[112:115], v[36:39], v[184:187], v[112:115]\n"
"v_mfma_f32_16x16x32_bf16 v[48:51], a[0:3], v[236:239], 0\n"
"v_cvt_pk_bf16_f32 v46, v164, v165\n"
"v_cvt_pk_bf16_f32 v47, v166, v167\n"
"v_mfma_f32_16x16x32_bf16 v[48:51], a[4:7], v[240:243], v[48:51]\n"
"v_mfma_f32_16x16x32_bf16 v[48:51], a[8:11], v[244:247], v[48:51]\n"
"v_max3_f32 v159, v159, v112, v113\n"
"v_max3_f32 v157, v157, v114, v115\n"
"v_mfma_f32_16x16x32_bf16 v[48:51], a[12:15], v[248:251], v[48:51]\n"
"v_mfma_f32_16x16x32_bf16 v[116:119], v[44:47], v[184:187], 0\n"
"v_mfma_f32_16x16x32_bf16 v[116:119], v[40:43], v[180:183], v[116:119]\n"
"v_mfma_f32_16x16x32_bf16 v[60:63], a[28:31], v[248:251], 0\n"
"v_cvt_pk_bf16_f32 v48, v48, v49\n"
"v_cvt_pk_bf16_f32 v49, v50, v51\n"
"v_mfma_f32_16x16x32_bf16 v[60:63], a[24:27], v[244:247], v[60:63]\n"
"v_mfma_f32_16x16x32_bf16 v[60:63], a[20:23], v[240:243], v[60:63]\n"
"v_max3_f32 v159, v159, v116, v117\n"
"v_max3_f32 v157, v157, v118, v119\n"
"v_mfma_f32_16x16x32_bf16 v[60:63], a[16:19], v[236:239], v[60:63]\n"
"v_max_f32 v157, v157, v159\n"
"v_mov_b32 v161, v157\n"
"v_mfma_f32_16x16x32_bf16 v[52:55], a[32:35], v[236:239], 0\n"
"v_mfma_f32_16x16x32_bf16 v[52:55], a[36:39], v[240:243], v[52:55]\n"
"v_permlane32_swap_b32 v157, v161\n"
"v_mfma_f32_16x16x32_bf16 v[52:55], a[40:43], v[244:247], v[52:55]\n"
"v_cvt_pk_bf16_f32 v50, v60, v61\n"
"v_cvt_pk_bf16_f32 v51, v62, v63\n"
"v_mfma_f32_16x16x32_bf16 v[52:55], a[44:47], v[248:251], v[52:55]\n"
"v_max_f32 v157, v157, v161\n"
"v_mov_b32 v161, v157\n"
"v_mfma_f32_16x16x32_bf16 v[64:67], a[60:63], v[248:251], 0\n"
"v_mfma_f32_16x16x32_bf16 v[64:67], a[56:59], v[244:247], v[64:67]\n"
"v_permlane16_swap_b32 v157, v161\n"
"v_mfma_f32_16x16x32_bf16 v[64:67], a[52:55], v[240:243], v[64:67]\n"
"v_cvt_pk_bf16_f32 v52, v52, v53\n"
"v_cvt_pk_bf16_f32 v53, v54, v55\n"
"v_mfma_f32_16x16x32_bf16 v[64:67], a[48:51], v[236:239], v[64:67]\n"
"v_max_f32 v157, v157, v161\n"
"v_sub_f32 v120, v120, v157\n"
"v_mfma_f32_16x16x32_bf16 v[56:59], a[64:67], v[236:239], 0\n"
"v_sub_f32 v121, v121, v157\n"
"v_sub_f32 v122, v122, v157\n"
"v_mfma_f32_16x16x32_bf16 v[56:59], a[68:71], v[240:243], v[56:59]\n"
"v_sub_f32 v123, v123, v157\n"
"v_sub_f32 v96, v96, v157\n"
"v_mfma_f32_16x16x32_bf16 v[56:59], a[72:75], v[244:247], v[56:59]\n"
"v_cvt_pk_bf16_f32 v54, v64, v65\n"
"v_cvt_pk_bf16_f32 v55, v66, v67\n"
"v_mfma_f32_16x16x32_bf16 v[56:59], a[76:79], v[248:251], v[56:59]\n"
"v_exp_f32 v161, v122\n"
"v_mfma_f32_16x16x32_bf16 v[68:71], a[92:95], v[248:251], 0\n"
"ds_read_b128 v[248:251], v153 offset:17440\n"
"v_sub_f32 v97, v97, v157\n"
"v_mfma_f32_16x16x32_bf16 v[68:71], a[88:91], v[244:247], v[68:71]\n"
"ds_read_b128 v[244:247], v153 offset:31072\n"
"v_sub_f32 v98, v98, v157\n"
"v_mfma_f32_16x16x32_bf16 v[68:71], a[84:87], v[240:243], v[68:71]\n"
"ds_read_b128 v[240:243], v153 offset:26880\n"
"v_cvt_pk_bf16_f32 v56, v56, v57\n"
"v_mfma_f32_16x16x32_bf16 v[68:71], a[80:83], v[236:239], v[68:71]\n"
"s_waitcnt lgkmcnt(3)\n"
"ds_read_b128 v[236:239], v153 offset:22688\n"
"v_cvt_pk_bf16_f32 v57, v58, v59\n"
"v_mfma_f32_16x16x32_bf16 v[148:151], a[228:231], v[188:191], 0\n"
"ds_read_b128 a[228:231], v158 offset:0\n"
"v_sub_f32 v99, v99, v157\n"
"v_mfma_f32_16x16x32_bf16 v[148:151], a[232:235], v[192:195], v[148:151]\n"
"ds_read_b128 a[232:235], v158 offset:2048\n"
"v_exp_f32 v120, v120\n"
"v_mfma_f32_16x16x32_bf16 v[124:127], v[0:3], v[188:191], 0\n"
"v_cvt_pk_bf16_f32 v58, v68, v69\n"
"v_cvt_pk_bf16_f32 v59, v70, v71\n"
"v_mfma_f32_16x16x32_bf16 v[124:127], v[4:7], v[192:195], v[124:127]\n"
"v_exp_f32 v121, v121\n"
"v_mfma_f32_16x16x32_bf16 v[128:131], v[12:15], v[192:195], 0\n"
"v_exp_f32 v123, v123\n"
"v_mfma_f32_16x16x32_bf16 v[128:131], v[8:11], v[188:191], v[128:131]\n"
"v_cvt_pk_bf16_f32 v122, v120, v121\n"
"v_add_f32 v120, v120, v121\n"
"v_mfma_f32_16x16x32_bf16 v[132:135], v[16:19], v[188:191], 0\n"
"v_add_f32 v121, v161, v123\n"
"v_cvt_pk_bf16_f32 v123, v161, v123\n"
"v_mfma_f32_16x16x32_bf16 v[132:135], v[20:23], v[192:195], v[132:135]\n"
"v_exp_f32 v96, v96\n"
"v_mfma_f32_16x16x32_bf16 v[136:139], v[28:31], v[192:195], 0\n"
"v_exp_f32 v97, v97\n"
"v_mfma_f32_16x16x32_bf16 v[136:139], v[24:27], v[188:191], v[136:139]\n"
"v_exp_f32 v98, v98\n"
"v_mfma_f32_16x16x32_bf16 v[140:143], v[32:35], v[188:191], 0\n"
"v_exp_f32 v99, v99\n"
"v_mfma_f32_16x16x32_bf16 v[140:143], v[36:39], v[192:195], v[140:143]\n"
"v_sub_f32 v100, v100, v157\n"
"v_sub_f32 v101, v101, v157\n"
"v_mfma_f32_16x16x32_bf16 v[144:147], v[44:47], v[192:195], 0\n"
"v_exp_f32 v100, v100\n"
"v_mfma_f32_16x16x32_bf16 v[144:147], v[40:43], v[188:191], v[144:147]\n"
"v_cvt_pk_bf16_f32 v96, v96, v97\n"
"v_cvt_pk_bf16_f32 v97, v98, v99\n"
"v_mfma_f32_16x16x32_bf16 v[72:75], a[0:3], a[224:227], 0\n"
"s_waitcnt lgkmcnt(0)\n"
"v_sub_f32 v102, v102, v157\n"
"v_sub_f32 v103, v103, v157\n"
"v_mfma_f32_16x16x32_bf16 v[72:75], a[4:7], v[236:239], v[72:75]\n"
"v_exp_f32 v101, v101\n"
"v_mfma_f32_16x16x32_bf16 v[72:75], a[8:11], v[240:243], v[72:75]\n"
"v_exp_f32 v102, v102\n"
"v_mfma_f32_16x16x32_bf16 v[72:75], a[12:15], v[244:247], v[72:75]\n"
"v_exp_f32 v103, v103\n"
"v_mfma_f32_16x16x32_bf16 v[84:87], a[28:31], v[244:247], 0\n"
"v_sub_f32 v104, v104, v157\n"
"v_sub_f32 v105, v105, v157\n"
"v_mfma_f32_16x16x32_bf16 v[84:87], a[24:27], v[240:243], v[84:87]\n"
"v_exp_f32 v104, v104\n"
"v_mfma_f32_16x16x32_bf16 v[84:87], a[20:23], v[236:239], v[84:87]\n"
"v_cvt_pk_bf16_f32 v72, v72, v73\n"
"v_cvt_pk_bf16_f32 v73, v74, v75\n"
"v_mfma_f32_16x16x32_bf16 v[84:87], a[16:19], a[224:227], v[84:87]\n"
"v_cvt_pk_bf16_f32 v98, v100, v101\n"
"v_cvt_pk_bf16_f32 v99, v102, v103\n"
"v_mfma_f32_16x16x32_bf16 v[76:79], a[32:35], a[224:227], 0\n"
"v_sub_f32 v106, v106, v157\n"
"v_sub_f32 v107, v107, v157\n"
"v_mfma_f32_16x16x32_bf16 v[76:79], a[36:39], v[236:239], v[76:79]\n"
"v_exp_f32 v105, v105\n"
"v_mfma_f32_16x16x32_bf16 v[76:79], a[40:43], v[240:243], v[76:79]\n"
"v_cvt_pk_bf16_f32 v74, v84, v85\n"
"v_cvt_pk_bf16_f32 v75, v86, v87\n"
"v_mfma_f32_16x16x32_bf16 v[76:79], a[44:47], v[244:247], v[76:79]\n"
"v_exp_f32 v106, v106\n"
"v_mfma_f32_16x16x32_bf16 v[88:91], a[60:63], v[244:247], 0\n"
"v_exp_f32 v107, v107\n"
"v_mfma_f32_16x16x32_bf16 v[88:91], a[56:59], v[240:243], v[88:91]\n"
"v_sub_f32 v108, v108, v157\n"
"v_sub_f32 v109, v109, v157\n"
"v_mfma_f32_16x16x32_bf16 v[88:91], a[52:55], v[236:239], v[88:91]\n"
"v_cvt_pk_bf16_f32 v76, v76, v77\n"
"v_cvt_pk_bf16_f32 v77, v78, v79\n"
"v_mfma_f32_16x16x32_bf16 v[88:91], a[48:51], a[224:227], v[88:91]\n"
"v_exp_f32 v108, v108\n"
"v_mfma_f32_16x16x32_bf16 v[80:83], a[64:67], a[224:227], 0\n"
"v_cvt_pk_bf16_f32 v104, v104, v105\n"
"v_cvt_pk_bf16_f32 v105, v106, v107\n"
"v_mfma_f32_16x16x32_bf16 v[80:83], a[68:71], v[236:239], v[80:83]\n"
"v_sub_f32 v110, v110, v157\n"
"v_sub_f32 v111, v111, v157\n"
"v_mfma_f32_16x16x32_bf16 v[80:83], a[72:75], v[240:243], v[80:83]\n"
"v_cvt_pk_bf16_f32 v78, v88, v89\n"
"v_cvt_pk_bf16_f32 v79, v90, v91\n"
"v_mfma_f32_16x16x32_bf16 v[80:83], a[76:79], v[244:247], v[80:83]\n"
"v_exp_f32 v109, v109\n"
"v_mfma_f32_16x16x32_bf16 v[92:95], a[92:95], v[244:247], 0\n"
"ds_read_b128 v[244:247], v153 offset:19552\n"
"v_sub_f32 v112, v112, v157\n"
"v_mfma_f32_16x16x32_bf16 v[92:95], a[88:91], v[240:243], v[92:95]\n"
"ds_read_b128 v[240:243], v153 offset:30016\n"
"v_sub_f32 v113, v113, v157\n"
"v_mfma_f32_16x16x32_bf16 v[92:95], a[84:87], v[236:239], v[92:95]\n"
"ds_read_b128 v[236:239], v153 offset:25824\n"
"v_cvt_pk_bf16_f32 v80, v80, v81\n"
"v_mfma_f32_16x16x32_bf16 v[92:95], a[80:83], a[224:227], v[92:95]\n"
"ds_read_b128 a[224:227], v153 offset:21632\n"
"v_cvt_pk_bf16_f32 v81, v82, v83\n"
"v_mfma_f32_16x16x32_bf16 v[60:63], a[0:3], v[248:251], 0\n"
"v_exp_f32 v110, v110\n"
"v_mfma_f32_16x16x32_bf16 v[64:67], a[32:35], v[248:251], 0\n"
"v_exp_f32 v111, v111\n"
"v_mfma_f32_16x16x32_bf16 v[68:71], a[64:67], v[248:251], 0\n"
"v_cvt_pk_bf16_f32 v82, v92, v93\n"
"v_cvt_pk_bf16_f32 v83, v94, v95\n"
"v_cvt_pk_bf16_f32 v106, v108, v109\n"
"v_mfma_f32_16x16x4_4b_bf16 v[204:219], v[122:123], a[244:245], 0 cbsz:2 abid:0\n"
"v_exp_f32 v112, v112\n"
"v_cvt_pk_bf16_f32 v107, v110, v111\n"
"v_sub_f32 v114, v114, v157\n"
"v_exp_f32 v113, v113\n"
"v_sub_f32 v115, v115, v157\n"
"v_sub_f32 v116, v116, v157\n"
"v_mfma_f32_16x16x32_bf16 v[252:255], a[252:255], v[96:99], v[120:123]\n"
"v_exp_f32 v114, v114\n"
"v_mfma_f32_16x16x32_bf16 v[252:255], a[252:255], v[104:107], v[252:255]\n"
"s_waitcnt lgkmcnt(3)\n"
"v_exp_f32 v115, v115\n"
"v_mfma_f32_16x16x32_bf16 v[84:87], a[0:3], v[244:247], 0\n"
"s_waitcnt lgkmcnt(2)\n"
"v_sub_f32 v117, v117, v157\n"
"v_cvt_pk_bf16_f32 v112, v112, v113\n"
"v_mfma_f32_16x16x32_bf16 v[164:167], a[28:31], v[240:243], 0\n"
"s_waitcnt lgkmcnt(1)\n"
"v_exp_f32 v116, v116\n"
"v_mfma_f32_16x16x32_bf16 v[164:167], a[24:27], v[236:239], v[164:167]\n"
"s_waitcnt lgkmcnt(0)\n"
"v_cvt_pk_bf16_f32 v113, v114, v115\n"
"v_sub_f32 v118, v118, v157\n"
"v_mfma_f32_16x16x32_bf16 v[164:167], a[20:23], a[224:227], v[164:167]\n"
"v_exp_f32 v117, v117\n"
"v_mfma_f32_16x16x32_bf16 v[164:167], a[16:19], v[248:251], v[164:167]\n"
"v_sub_f32 v119, v119, v157\n"
"v_cvt_pk_bf16_f32 v114, v116, v117\n"
"v_mfma_f32_16x16x32_bf16 v[60:63], a[4:7], a[224:227], v[60:63]\n"
"v_exp_f32 v118, v118\n"
"v_mfma_f32_16x16x32_bf16 v[60:63], a[8:11], v[236:239], v[60:63]\n"
"v_exp_f32 v119, v119\n"
"v_mfma_f32_16x16x32_bf16 v[60:63], a[12:15], v[240:243], v[60:63]\n"
"v_cvt_pk_bf16_f32 v115, v118, v119\n"
"v_max3_f32 v157, v148, v149, v150\n"
"v_mfma_f32_16x16x32_bf16 v[64:67], a[36:39], a[224:227], v[64:67]\n"
"s_add_u32 s84, s84, 13107200\n"
"s_addc_u32 s85, s85, 0\n"
"v_mfma_f32_16x16x32_bf16 v[64:67], a[40:43], v[236:239], v[64:67]\n"
"v_max3_f32 v159, v151, v124, v125\n"
"v_max3_f32 v157, v157, v126, v127\n"
"v_mfma_f32_16x16x32_bf16 v[64:67], a[44:47], v[240:243], v[64:67]\n"
"v_cvt_pk_bf16_f32 v60, v60, v61\n"
"v_cvt_pk_bf16_f32 v61, v62, v63\n"
"v_mfma_f32_16x16x32_bf16 v[168:171], a[60:63], v[240:243], 0\n"
"v_cvt_pk_bf16_f32 v62, v164, v165\n"
"v_cvt_pk_bf16_f32 v63, v166, v167\n"
"v_mfma_f32_16x16x32_bf16 v[168:171], a[56:59], v[236:239], v[168:171]\n"
"v_max3_f32 v159, v159, v128, v129\n"
"v_max3_f32 v157, v157, v130, v131\n"
"v_mfma_f32_16x16x32_bf16 v[168:171], a[52:55], a[224:227], v[168:171]\n"
"v_cvt_pk_bf16_f32 v64, v64, v65\n"
"v_cvt_pk_bf16_f32 v65, v66, v67\n"
"v_mfma_f32_16x16x32_bf16 v[168:171], a[48:51], v[248:251], v[168:171]\n"
"v_max3_f32 v159, v159, v132, v133\n"
"v_max3_f32 v157, v157, v134, v135\n"
"v_mfma_f32_16x16x32_bf16 v[68:71], a[68:71], a[224:227], v[68:71]\n"
"v_max3_f32 v159, v159, v136, v137\n"
"v_max3_f32 v157, v157, v138, v139\n"
"v_mfma_f32_16x16x32_bf16 v[68:71], a[72:75], v[236:239], v[68:71]\n"
"v_max3_f32 v159, v159, v140, v141\n"
"v_max3_f32 v157, v157, v142, v143\n"
"v_mfma_f32_16x16x32_bf16 v[68:71], a[76:79], v[240:243], v[68:71]\n"
"v_cvt_pk_bf16_f32 v66, v168, v169\n"
"v_cvt_pk_bf16_f32 v67, v170, v171\n"
"v_mfma_f32_16x16x32_bf16 v[172:175], a[92:95], v[240:243], 0\n"
"ds_read_b128 v[240:243], v153 offset:0\n"
"v_max3_f32 v159, v159, v144, v145\n"
"v_mfma_f32_16x16x32_bf16 v[172:175], a[88:91], v[236:239], v[172:175]\n"
"ds_read_b128 v[236:239], v153 offset:32128\n"
"v_max3_f32 v157, v157, v146, v147\n"
"v_mfma_f32_16x16x32_bf16 v[172:175], a[84:87], a[224:227], v[172:175]\n"
"ds_read_b128 a[224:227], v153 offset:27936\n"
"v_cvt_pk_bf16_f32 v68, v68, v69\n"
"v_mfma_f32_16x16x32_bf16 v[172:175], a[80:83], v[248:251], v[172:175]\n"
"ds_read_b128 v[248:251], v153 offset:23744\n"
"v_cvt_pk_bf16_f32 v69, v70, v71\n"
"v_mfma_f32_16x16x32_bf16 v[88:91], a[32:35], v[244:247], 0\n"
"v_max_f32 v157, v157, v159\n"
"v_mov_b32 v161, v157\n"
"v_mfma_f32_16x16x32_bf16 v[92:95], a[64:67], v[244:247], 0\n"
"s_nop 0\n"
"v_permlane32_swap_b32 v157, v161\n"
"v_mfma_f32_16x16x32_bf16 v[252:255], a[252:255], v[112:115], v[252:255]\n"
"v_cvt_pk_bf16_f32 v70, v172, v173\n"
"v_cvt_pk_bf16_f32 v71, v174, v175\n"
"v_mfma_f32_16x16x32_bf16 v[204:207], v[96:99], v[48:51], v[204:207]\n"
"v_max_f32 v157, v157, v161\n"
"v_mov_b32 v161, v157\n"
"v_mfma_f32_16x16x32_bf16 v[204:207], v[104:107], v[52:55], v[204:207]\n"
"v_mfma_f32_16x16x32_bf16 v[204:207], v[112:115], v[56:59], v[204:207]\n"
"v_permlane16_swap_b32 v157, v161\n"
"v_mfma_f32_16x16x32_bf16 v[208:211], v[112:115], v[68:71], v[208:211]\n"
"v_max_f32 v157, v157, v161\n"
"v_sub_f32 v148, v148, v157\n"
"v_mfma_f32_16x16x32_bf16 v[208:211], v[104:107], v[64:67], v[208:211]\n"
"v_sub_f32 v149, v149, v157\n"
"v_sub_f32 v150, v150, v157\n"
"v_mfma_f32_16x16x32_bf16 v[208:211], v[96:99], v[60:63], v[208:211]\n"
"s_waitcnt lgkmcnt(2)\n"
"v_sub_f32 v151, v151, v157\n"
"v_sub_f32 v124, v124, v157\n"
"v_mfma_f32_16x16x32_bf16 v[176:179], a[28:31], v[236:239], 0\n"
"s_waitcnt lgkmcnt(1)\n"
"v_exp_f32 v161, v150\n"
"v_mfma_f32_16x16x32_bf16 v[176:179], a[24:27], a[224:227], v[176:179]\n"
"s_waitcnt lgkmcnt(0)\n"
"v_exp_f32 v148, v148\n"
"v_mfma_f32_16x16x32_bf16 v[176:179], a[20:23], v[248:251], v[176:179]\n"
"v_exp_f32 v149, v149\n"
"v_mfma_f32_16x16x32_bf16 v[176:179], a[16:19], v[244:247], v[176:179]\n"
"v_exp_f32 v151, v151\n"
"v_mfma_f32_16x16x32_bf16 v[84:87], a[4:7], v[248:251], v[84:87]\n"
"v_cvt_pk_bf16_f32 v150, v148, v149\n"
"v_add_f32 v148, v148, v149\n"
"v_mfma_f32_16x16x32_bf16 v[84:87], a[8:11], a[224:227], v[84:87]\n"
"v_add_f32 v149, v161, v151\n"
"v_cvt_pk_bf16_f32 v151, v161, v151\n"
"v_mfma_f32_16x16x32_bf16 v[84:87], a[12:15], v[236:239], v[84:87]\n"
"v_sub_f32 v125, v125, v157\n"
"v_sub_f32 v126, v126, v157\n"
"v_mfma_f32_16x16x32_bf16 v[88:91], a[36:39], v[248:251], v[88:91]\n"
"v_exp_f32 v124, v124\n"
"v_mfma_f32_16x16x32_bf16 v[88:91], a[40:43], a[224:227], v[88:91]\n"
"v_exp_f32 v125, v125\n"
"v_mfma_f32_16x16x32_bf16 v[88:91], a[44:47], v[236:239], v[88:91]\n"
"v_cvt_pk_bf16_f32 v84, v84, v85\n"
"v_cvt_pk_bf16_f32 v85, v86, v87\n"
"v_mfma_f32_16x16x32_bf16 v[164:167], a[60:63], v[236:239], 0\n"
"v_cvt_pk_bf16_f32 v86, v176, v177\n"
"v_cvt_pk_bf16_f32 v87, v178, v179\n"
"v_mfma_f32_16x16x32_bf16 v[164:167], a[56:59], a[224:227], v[164:167]\n"
"v_sub_f32 v127, v127, v157\n"
"v_sub_f32 v128, v128, v157\n"
"v_mfma_f32_16x16x32_bf16 v[164:167], a[52:55], v[248:251], v[164:167]\n"
"v_cvt_pk_bf16_f32 v88, v88, v89\n"
"v_cvt_pk_bf16_f32 v89, v90, v91\n"
"v_mfma_f32_16x16x32_bf16 v[164:167], a[48:51], v[244:247], v[164:167]\n"
"v_exp_f32 v126, v126\n"
"v_mfma_f32_16x16x32_bf16 v[92:95], a[68:71], v[248:251], v[92:95]\n"
"v_exp_f32 v127, v127\n"
"v_mfma_f32_16x16x32_bf16 v[92:95], a[72:75], a[224:227], v[92:95]\n"
"v_sub_f32 v129, v129, v157\n"
"v_cvt_pk_bf16_f32 v124, v124, v125\n"
"v_mfma_f32_16x16x32_bf16 v[92:95], a[76:79], v[236:239], v[92:95]\n"
"v_cvt_pk_bf16_f32 v90, v164, v165\n"
"v_cvt_pk_bf16_f32 v91, v166, v167\n"
"v_mfma_f32_16x16x32_bf16 v[168:171], a[92:95], v[236:239], 0\n"
"ds_read_b128 v[236:239], v153 offset:1024\n"
"v_cvt_pk_bf16_f32 v125, v126, v127\n"
"v_mfma_f32_16x16x32_bf16 v[168:171], a[88:91], a[224:227], v[168:171]\n"
"ds_read_b128 a[224:227], v153 offset:12288\n"
"v_sub_f32 v130, v130, v157\n"
"v_mfma_f32_16x16x32_bf16 v[168:171], a[84:87], v[248:251], v[168:171]\n"
"ds_read_b128 v[248:251], v153 offset:8192\n"
"v_cvt_pk_bf16_f32 v92, v92, v93\n"
"v_mfma_f32_16x16x32_bf16 v[168:171], a[80:83], v[244:247], v[168:171]\n"
"ds_read_b128 v[244:247], v153 offset:4096\n"
"v_cvt_pk_bf16_f32 v93, v94, v95\n"
"v_mfma_f32_16x16x32_bf16 v[212:215], v[96:99], v[72:75], v[212:215]\n"
"v_exp_f32 v128, v128\n"
"v_mfma_f32_16x16x32_bf16 v[212:215], v[104:107], v[76:79], v[212:215]\n"
"v_exp_f32 v129, v129\n"
"v_mfma_f32_16x16x32_bf16 v[212:215], v[112:115], v[80:83], v[212:215]\n"
"v_cvt_pk_bf16_f32 v94, v168, v169\n"
"v_cvt_pk_bf16_f32 v95, v170, v171\n"
"v_mfma_f32_16x16x32_bf16 v[120:123], a[228:231], v[196:199], 0\n"
"ds_read_b128 a[228:231], v158 offset:0\n"
"v_sub_f32 v131, v131, v157\n"
"v_mfma_f32_16x16x32_bf16 v[120:123], a[232:235], v[200:203], v[120:123]\n"
"ds_read_b128 a[232:235], v158 offset:2048\n"
"v_sub_f32 v132, v132, v157\n"
"v_mfma_f32_16x16x32_bf16 v[216:219], v[112:115], v[92:95], v[216:219]\n"
"v_exp_f32 v130, v130\n"
"v_mfma_f32_16x16x32_bf16 v[216:219], v[104:107], v[88:91], v[216:219]\n"
"v_exp_f32 v131, v131\n"
"v_mfma_f32_16x16x32_bf16 v[216:219], v[96:99], v[84:87], v[216:219]\n"
"v_sub_f32 v133, v133, v157\n"
"v_cvt_pk_bf16_f32 v126, v128, v129\n"
"v_mfma_f32_16x16x32_bf16 v[96:99], v[0:3], v[196:199], 0\n"
"v_exp_f32 v132, v132\n"
"v_mfma_f32_16x16x32_bf16 v[96:99], v[4:7], v[200:203], v[96:99]\n"
"v_cvt_pk_bf16_f32 v127, v130, v131\n"
"v_sub_f32 v134, v134, v157\n"
"v_mfma_f32_16x16x32_bf16 v[100:103], v[12:15], v[200:203], 0\n"
"v_exp_f32 v133, v133\n"
"v_mfma_f32_16x16x32_bf16 v[100:103], v[8:11], v[196:199], v[100:103]\n"
"v_sub_f32 v135, v135, v157\n"
"v_sub_f32 v136, v136, v157\n"
"v_mfma_f32_16x16x32_bf16 v[104:107], v[16:19], v[196:199], 0\n"
"v_exp_f32 v134, v134\n"
"v_mfma_f32_16x16x32_bf16 v[104:107], v[20:23], v[200:203], v[104:107]\n"
"v_exp_f32 v135, v135\n"
"v_mfma_f32_16x16x32_bf16 v[108:111], v[28:31], v[200:203], 0\n"
"v_sub_f32 v137, v137, v157\n"
"v_cvt_pk_bf16_f32 v132, v132, v133\n"
"v_mfma_f32_16x16x32_bf16 v[108:111], v[24:27], v[196:199], v[108:111]\n"
"v_exp_f32 v136, v136\n"
"v_mfma_f32_16x16x32_bf16 v[112:115], v[32:35], v[196:199], 0\n"
"v_cvt_pk_bf16_f32 v133, v134, v135\n"
"v_sub_f32 v138, v138, v157\n"
"v_mfma_f32_16x16x32_bf16 v[112:115], v[36:39], v[200:203], v[112:115]\n"
"v_exp_f32 v137, v137\n"
"v_mfma_f32_16x16x32_bf16 v[116:119], v[44:47], v[200:203], 0\n"
"v_sub_f32 v139, v139, v157\n"
"v_sub_f32 v140, v140, v157\n"
"v_mfma_f32_16x16x32_bf16 v[116:119], v[40:43], v[196:199], v[116:119]\n"
"v_exp_f32 v138, v138\n"
"v_sub_f32 v141, v141, v157\n"
"v_mfma_f32_16x16x4_4b_bf16 v[220:235], v[150:151], a[244:245], 0 cbsz:2 abid:0\n"
"v_exp_f32 v139, v139\n"
"v_exp_f32 v140, v140\n"
"v_cvt_pk_bf16_f32 v134, v136, v137\n"
"v_cvt_pk_bf16_f32 v135, v138, v139\n"
"v_sub_f32 v142, v142, v157\n"
"v_sub_f32 v143, v143, v157\n"
"v_mfma_f32_16x16x32_bf16 v[128:131], v[12:15], a[240:243], 0\n"
"v_exp_f32 v141, v141\n"
"v_mfma_f32_16x16x32_bf16 v[128:131], v[8:11], a[236:239], v[128:131]\n"
"v_exp_f32 v142, v142\n"
"v_mfma_f32_16x16x32_bf16 v[220:223], v[124:127], v[48:51], v[220:223]\n"
"v_exp_f32 v143, v143\n"
"v_mfma_f32_16x16x32_bf16 v[220:223], v[132:135], v[52:55], v[220:223]\n"
"v_sub_f32 v144, v144, v157\n"
"v_sub_f32 v145, v145, v157\n"
"v_mfma_f32_16x16x32_bf16 v[228:231], v[124:127], v[72:75], v[228:231]\n"
"v_exp_f32 v144, v144\n"
"v_mfma_f32_16x16x32_bf16 v[228:231], v[132:135], v[76:79], v[228:231]\n"
"v_cvt_pk_bf16_f32 v140, v140, v141\n"
"v_cvt_pk_bf16_f32 v141, v142, v143\n"
"v_mfma_f32_16x16x32_bf16 v[136:139], v[28:31], a[240:243], 0\n"
"v_sub_f32 v146, v146, v157\n"
"v_sub_f32 v147, v147, v157\n"
"v_mfma_f32_16x16x32_bf16 v[136:139], v[24:27], a[236:239], v[136:139]\n"
"v_exp_f32 v145, v145\n"
"v_mfma_f32_16x16x32_bf16 v[180:183], v[240:243], a[48:51], 0\n"
"s_waitcnt lgkmcnt(0)\n"
"v_exp_f32 v146, v146\n"
"v_mfma_f32_16x16x32_bf16 v[180:183], v[244:247], a[52:55], v[180:183]\n"
"v_exp_f32 v147, v147\n"
"v_mfma_f32_16x16x32_bf16 v[180:183], v[248:251], a[56:59], v[180:183]\n"
"v_cvt_pk_bf16_f32 v142, v144, v145\n"
"v_cvt_pk_bf16_f32 v143, v146, v147\n"
"v_mfma_f32_16x16x32_bf16 v[180:183], a[224:227], a[60:63], v[180:183]\n"
"v_add_f32 v252, v252, v253\n"
"v_max3_f32 v157, v120, v121, v122\n"
"v_mfma_f32_16x16x32_bf16 v[220:223], v[140:143], v[56:59], v[220:223]\n"
"v_rcp_f32 v253, v252\n"
"v_mfma_f32_16x16x32_bf16 v[224:227], v[140:143], v[68:71], v[224:227]\n"
"ds_bpermute_b32 v254, v160, v253 offset:0\n"
"v_mfma_f32_16x16x32_bf16 v[224:227], v[132:135], v[64:67], v[224:227]\n"
"v_max3_f32 v159, v123, v96, v97\n"
"v_max3_f32 v157, v157, v98, v99\n"
"v_mfma_f32_16x16x32_bf16 v[224:227], v[124:127], v[60:63], v[224:227]\n"
"v_max3_f32 v159, v159, v100, v101\n"
"v_max3_f32 v157, v157, v102, v103\n"
"v_mfma_f32_16x16x32_bf16 v[228:231], v[140:143], v[80:83], v[228:231]\n"
"v_max3_f32 v159, v159, v104, v105\n"
"v_max3_f32 v157, v157, v106, v107\n"
"v_mfma_f32_16x16x32_bf16 v[232:235], v[140:143], v[92:95], v[232:235]\n"
"v_max3_f32 v159, v159, v108, v109\n"
"v_max3_f32 v157, v157, v110, v111\n"
"v_mfma_f32_16x16x32_bf16 v[232:235], v[132:135], v[88:91], v[232:235]\n"
"v_max3_f32 v159, v159, v112, v113\n"
"v_max3_f32 v157, v157, v114, v115\n"
"v_mfma_f32_16x16x32_bf16 v[232:235], v[124:127], v[84:87], v[232:235]\n"
"v_max3_f32 v159, v159, v116, v117\n"
"v_max3_f32 v157, v157, v118, v119\n"
"v_mfma_f32_16x16x32_bf16 v[144:147], v[44:47], a[240:243], 0\n"
"v_max_f32 v157, v157, v159\n"
"v_mov_b32 v161, v157\n"
"v_mfma_f32_16x16x32_bf16 v[144:147], v[40:43], a[236:239], v[144:147]\n"
"v_cvt_pk_bf16_f32 v180, v180, v181\n"
"v_cvt_pk_bf16_f32 v181, v182, v183\n"
"v_mfma_f32_16x16x32_bf16 v[188:191], a[224:227], a[76:79], 0\n"
"v_permlane32_swap_b32 v157, v161\n"
"v_mfma_f32_16x16x32_bf16 v[188:191], v[248:251], a[72:75], v[188:191]\n"
"v_max_f32 v157, v157, v161\n"
"v_mov_b32 v161, v157\n"
"v_mfma_f32_16x16x32_bf16 v[188:191], v[244:247], a[68:71], v[188:191]\n"
"s_waitcnt lgkmcnt(0)\n"
"v_mul_f32_dpp v204, v254, v204 quad_perm:[0,0,0,0] row_mask:0xf bank_mask:0xf\n"
"v_mul_f32_dpp v205, v254, v205 quad_perm:[1,1,1,1] row_mask:0xf bank_mask:0xf\n"
"v_mfma_f32_16x16x32_bf16 v[188:191], v[240:243], a[64:67], v[188:191]\n"
"v_mul_f32_dpp v206, v254, v206 quad_perm:[2,2,2,2] row_mask:0xf bank_mask:0xf\n"
"v_mul_f32_dpp v207, v254, v207 quad_perm:[3,3,3,3] row_mask:0xf bank_mask:0xf\n"
"v_mfma_f32_16x16x32_bf16 v[196:199], v[240:243], a[80:83], 0\n"
"ds_read_b128 v[240:243], v153 offset:5120\n"
"v_mul_f32_dpp v208, v254, v208 quad_perm:[0,0,0,0] row_mask:0xf bank_mask:0xf\n"
"v_mfma_f32_16x16x32_bf16 v[196:199], v[244:247], a[84:87], v[196:199]\n"
"ds_read_b128 v[244:247], v153 offset:9216\n"
"v_mul_f32_dpp v209, v254, v209 quad_perm:[1,1,1,1] row_mask:0xf bank_mask:0xf\n"
"v_mfma_f32_16x16x32_bf16 v[196:199], v[248:251], a[88:91], v[196:199]\n"
"ds_read_b128 v[248:251], v153 offset:13312\n"
"v_mul_f32_dpp v210, v254, v210 quad_perm:[2,2,2,2] row_mask:0xf bank_mask:0xf\n"
"v_mfma_f32_16x16x32_bf16 v[196:199], a[224:227], a[92:95], v[196:199]\n"
"ds_read_b128 a[224:227], v153 offset:3072\n"
"v_mul_f32_dpp v211, v254, v211 quad_perm:[3,3,3,3] row_mask:0xf bank_mask:0xf\n"
"v_mfma_f32_16x16x32_bf16 v[204:207], a[0:3], a[160:163], v[204:207]\n"
"v_mul_f32_dpp v212, v254, v212 quad_perm:[0,0,0,0] row_mask:0xf bank_mask:0xf\n"
"v_mul_f32_dpp v213, v254, v213 quad_perm:[1,1,1,1] row_mask:0xf bank_mask:0xf\n"
"v_mfma_f32_16x16x32_bf16 v[204:207], a[4:7], a[164:167], v[204:207]\n"
"v_mul_f32_dpp v214, v254, v214 quad_perm:[2,2,2,2] row_mask:0xf bank_mask:0xf\n"
"v_mul_f32_dpp v215, v254, v215 quad_perm:[3,3,3,3] row_mask:0xf bank_mask:0xf\n"
"v_mfma_f32_16x16x32_bf16 v[204:207], a[8:11], a[168:171], v[204:207]\n"
"v_mul_f32_dpp v216, v254, v216 quad_perm:[0,0,0,0] row_mask:0xf bank_mask:0xf\n"
"v_mul_f32_dpp v217, v254, v217 quad_perm:[1,1,1,1] row_mask:0xf bank_mask:0xf\n"
"v_mfma_f32_16x16x32_bf16 v[204:207], a[12:15], a[172:175], v[204:207]\n"
"v_mul_f32_dpp v218, v254, v218 quad_perm:[2,2,2,2] row_mask:0xf bank_mask:0xf\n"
"v_mul_f32_dpp v219, v254, v219 quad_perm:[3,3,3,3] row_mask:0xf bank_mask:0xf\n"
"v_mfma_f32_16x16x32_bf16 v[208:211], a[12:15], a[188:191], v[208:211]\n"
"v_permlane16_swap_b32 v157, v161\n"
"v_mfma_f32_16x16x32_bf16 v[208:211], a[8:11], a[184:187], v[208:211]\n"
"v_max_f32 v157, v157, v161\n"
"v_sub_f32 v120, v120, v157\n"
"v_mfma_f32_16x16x32_bf16 v[208:211], a[4:7], a[180:183], v[208:211]\n"
"v_sub_f32 v121, v121, v157\n"
"v_sub_f32 v122, v122, v157\n"
"v_mfma_f32_16x16x32_bf16 v[208:211], a[0:3], a[176:179], v[208:211]\n"
"v_sub_f32 v123, v123, v157\n"
"v_sub_f32 v96, v96, v157\n"
"v_mfma_f32_16x16x32_bf16 v[212:215], a[0:3], a[192:195], v[212:215]\n"
"v_exp_f32 v161, v122\n"
"v_mfma_f32_16x16x32_bf16 v[212:215], a[4:7], a[196:199], v[212:215]\n"
"v_exp_f32 v120, v120\n"
"v_mfma_f32_16x16x32_bf16 v[212:215], a[8:11], a[200:203], v[212:215]\n"
"v_cvt_pk_bf16_f32 v204, v204, v208\n"
"v_cvt_pk_bf16_f32 v208, v205, v209\n"
"v_mfma_f32_16x16x32_bf16 v[212:215], a[12:15], a[204:207], v[212:215]\n"
"v_pk_max_i16 v204, v204, 0\n"
"v_pk_max_i16 v208, v208, 0\n"
"v_mfma_f32_16x16x32_bf16 v[216:219], a[12:15], a[220:223], v[216:219]\n"
"s_waitcnt vmcnt(16)\n"
"ds_read_b128 a[12:15], v154 offset:192\n"
"v_cvt_pk_bf16_f32 v206, v206, v210\n"
"v_mfma_f32_16x16x32_bf16 v[216:219], a[8:11], a[216:219], v[216:219]\n"
"ds_read_b128 a[8:11], v154 offset:128\n"
"v_cvt_pk_bf16_f32 v210, v207, v211\n"
"v_mfma_f32_16x16x32_bf16 v[216:219], a[4:7], a[212:215], v[216:219]\n"
"ds_read_b128 a[4:7], v154 offset:64\n"
"v_pk_max_i16 v206, v206, 0\n"
"v_mfma_f32_16x16x32_bf16 v[216:219], a[0:3], a[208:211], v[216:219]\n"
"ds_read_b128 a[0:3], v154 offset:0\n"
"v_pk_max_i16 v210, v210, 0\n"
"v_mfma_f32_16x16x32_bf16 v[252:255], a[252:255], v[124:127], v[148:151]\n"
"v_exp_f32 v121, v121\n"
"v_mfma_f32_16x16x32_bf16 v[252:255], a[252:255], v[132:135], v[252:255]\n"
"v_exp_f32 v123, v123\n"
"v_mfma_f32_16x16x32_bf16 v[252:255], a[252:255], v[140:143], v[252:255]\n"
"v_cvt_pk_bf16_f32 v205, v212, v216\n"
"v_cvt_pk_bf16_f32 v209, v213, v217\n"
"v_mfma_f32_16x16x32_bf16 v[148:151], a[228:231], a[236:239], 0\n"
"ds_read_b128 a[228:231], v158 offset:0\n"
"v_pk_max_i16 v205, v205, 0\n"
"v_mfma_f32_16x16x32_bf16 v[148:151], a[232:235], a[240:243], v[148:151]\n"
"buffer_store_dwordx2 v[204:205], v162, s[84:87], 0 offen offset:0 sc0 sc1\n"
"v_mfma_f32_16x16x32_bf16 v[124:127], v[0:3], a[236:239], 0\n"
"ds_read_b128 a[232:235], v158 offset:2048\n"
"v_pk_max_i16 v209, v209, 0\n"
"v_mfma_f32_16x16x32_bf16 v[124:127], v[4:7], a[240:243], v[124:127]\n"
"buffer_store_dwordx2 v[208:209], v162, s[84:87], 0 offen offset:256 sc0 sc1\n"
"v_mfma_f32_16x16x32_bf16 v[132:135], v[16:19], a[236:239], 0\n"
"v_cvt_pk_bf16_f32 v207, v214, v218\n"
"v_cvt_pk_bf16_f32 v211, v215, v219\n"
"v_mfma_f32_16x16x32_bf16 v[132:135], v[20:23], a[240:243], v[132:135]\n"
"v_pk_max_i16 v207, v207, 0\n"
"v_pk_max_i16 v211, v211, 0\n"
"v_mfma_f32_16x16x32_bf16 v[140:143], v[32:35], a[236:239], 0\n"
"ds_read_b32 a236, v156 offset:0\n"
"ds_read_b32 a237, v156 offset:8\n"
"v_mfma_f32_16x16x32_bf16 v[140:143], v[36:39], a[240:243], v[140:143]\n"
"buffer_store_dwordx2 v[206:207], v162, s[84:87], 0 offen offset:512 sc0 sc1\n"
"v_mfma_f32_16x16x32_bf16 v[184:187], v[236:239], a[48:51], 0\n"
"s_waitcnt lgkmcnt(4)\n"
"ds_read_b32 a238, v156 offset:4\n"
"ds_read_b32 a239, v156 offset:12\n"
"v_mfma_f32_16x16x32_bf16 v[184:187], v[240:243], a[52:55], v[184:187]\n"
"ds_read_b32 a240, v156 offset:256\n"
"ds_read_b32 a241, v156 offset:264\n"
"v_mfma_f32_16x16x32_bf16 v[184:187], v[244:247], a[56:59], v[184:187]\n"
"ds_read_b32 a242, v156 offset:260\n"
"ds_read_b32 a243, v156 offset:268\n"
"v_mfma_f32_16x16x32_bf16 v[184:187], v[248:251], a[60:63], v[184:187]\n"
"buffer_store_dwordx2 v[210:211], v162, s[84:87], 0 offen offset:768 sc0 sc1\n"
"v_mfma_f32_16x16x32_bf16 v[192:195], v[248:251], a[76:79], 0\n"
"s_add_u32 m0, s28, 0\n"
"v_cvt_pk_bf16_f32 v122, v120, v121\n"
"v_mfma_f32_16x16x32_bf16 v[192:195], v[244:247], a[72:75], v[192:195]\n"
"buffer_load_dwordx4 v152, s[24:27], s52 offen offset:0 lds\n"
"v_add_f32 v120, v120, v121\n"
"v_mfma_f32_16x16x32_bf16 v[192:195], v[240:243], a[68:71], v[192:195]\n"
"buffer_load_dwordx4 v152, s[24:27], s53 offen offset:1088 lds\n"
"v_add_f32 v121, v161, v123\n"
"v_mfma_f32_16x16x32_bf16 v[192:195], v[236:239], a[64:67], v[192:195]\n"
"buffer_load_dwordx4 v152, s[24:27], s54 offen offset:2176 lds\n"
"v_cvt_pk_bf16_f32 v123, v161, v123\n"
"v_mfma_f32_16x16x32_bf16 v[200:203], v[236:239], a[80:83], 0\n"
"buffer_load_dwordx4 v152, s[24:27], s55 offen offset:3264 lds\n"
"ds_read_b128 v[236:239], v153 offset:2048\n"
"v_mfma_f32_16x16x32_bf16 v[200:203], v[240:243], a[84:87], v[200:203]\n"
"ds_read_b128 v[240:243], v153 offset:6144\n"
"v_sub_f32 v97, v97, v157\n"
"v_mfma_f32_16x16x32_bf16 v[200:203], v[244:247], a[88:91], v[200:203]\n"
"ds_read_b128 v[244:247], v153 offset:10240\n"
"v_add_f32 v252, v252, v253\n"
"v_mfma_f32_16x16x32_bf16 v[200:203], v[248:251], a[92:95], v[200:203]\n"
"ds_read_b128 v[248:251], v153 offset:14336\n"
"v_exp_f32 v96, v96\n"
"v_mfma_f32_16x16x4_4b_bf16 v[204:219], v[122:123], a[244:245], 0 cbsz:2 abid:0\n"
"v_sub_f32 v98, v98, v157\n"
"v_exp_f32 v97, v97\n"
"v_rcp_f32 v253, v252\n"
"v_sub_f32 v99, v99, v157\n"
"v_exp_f32 v98, v98\n"
"v_mfma_f32_16x16x32_bf16 v[164:167], a[224:227], a[48:51], 0\n"
"ds_bpermute_b32 v254, v160, v253 offset:0\n"
"v_mfma_f32_16x16x32_bf16 v[172:175], a[224:227], a[80:83], 0\n"
"v_exp_f32 v99, v99\n"
"v_sub_f32 v100, v100, v157\n"
"v_sub_f32 v101, v101, v157\n"
"v_exp_f32 v100, v100\n"
"v_cvt_pk_bf16_f32 v96, v96, v97\n"
"v_cvt_pk_bf16_f32 v97, v98, v99\n"
"v_sub_f32 v102, v102, v157\n"
"v_exp_f32 v101, v101\n"
"v_sub_f32 v103, v103, v157\n"
"v_exp_f32 v102, v102\n"
"v_exp_f32 v103, v103\n"
"v_sub_f32 v104, v104, v157\n"
"v_sub_f32 v105, v105, v157\n"
"v_exp_f32 v104, v104\n"
"v_cvt_pk_bf16_f32 v98, v100, v101\n"
"v_cvt_pk_bf16_f32 v99, v102, v103\n"
"v_sub_f32 v106, v106, v157\n"
"v_exp_f32 v105, v105\n"
"v_mfma_f32_16x16x32_bf16 v[204:207], v[96:99], v[48:51], v[204:207]\n"
"v_sub_f32 v107, v107, v157\n"
"v_sub_f32 v108, v108, v157\n"
"v_mfma_f32_16x16x32_bf16 v[212:215], v[96:99], v[72:75], v[212:215]\n"
"v_exp_f32 v106, v106\n"
"v_exp_f32 v107, v107\n"
"v_sub_f32 v109, v109, v157\n"
"v_exp_f32 v108, v108\n"
"v_cvt_pk_bf16_f32 v104, v104, v105\n"
"s_waitcnt lgkmcnt(0)\n"
"v_mul_f32_dpp v220, v254, v220 quad_perm:[0,0,0,0] row_mask:0xf bank_mask:0xf\n"
"v_mul_f32_dpp v221, v254, v221 quad_perm:[1,1,1,1] row_mask:0xf bank_mask:0xf\n"
"v_mul_f32_dpp v222, v254, v222 quad_perm:[2,2,2,2] row_mask:0xf bank_mask:0xf\n"
"v_mul_f32_dpp v223, v254, v223 quad_perm:[3,3,3,3] row_mask:0xf bank_mask:0xf\n"
"v_mul_f32_dpp v224, v254, v224 quad_perm:[0,0,0,0] row_mask:0xf bank_mask:0xf\n"
"v_mul_f32_dpp v225, v254, v225 quad_perm:[1,1,1,1] row_mask:0xf bank_mask:0xf\n"
"v_mul_f32_dpp v226, v254, v226 quad_perm:[2,2,2,2] row_mask:0xf bank_mask:0xf\n"
"v_mfma_f32_16x16x32_bf16 v[220:223], a[16:19], a[160:163], v[220:223]\n"
"v_mul_f32_dpp v227, v254, v227 quad_perm:[3,3,3,3] row_mask:0xf bank_mask:0xf\n"
"v_mul_f32_dpp v228, v254, v228 quad_perm:[0,0,0,0] row_mask:0xf bank_mask:0xf\n"
"v_mfma_f32_16x16x32_bf16 v[220:223], a[20:23], a[164:167], v[220:223]\n"
"v_mul_f32_dpp v229, v254, v229 quad_perm:[1,1,1,1] row_mask:0xf bank_mask:0xf\n"
"v_mul_f32_dpp v230, v254, v230 quad_perm:[2,2,2,2] row_mask:0xf bank_mask:0xf\n"
"v_mfma_f32_16x16x32_bf16 v[220:223], a[24:27], a[168:171], v[220:223]\n"
"v_mul_f32_dpp v231, v254, v231 quad_perm:[3,3,3,3] row_mask:0xf bank_mask:0xf\n"
"v_mul_f32_dpp v232, v254, v232 quad_perm:[0,0,0,0] row_mask:0xf bank_mask:0xf\n"
"v_mfma_f32_16x16x32_bf16 v[220:223], a[28:31], a[172:175], v[220:223]\n"
"v_mul_f32_dpp v233, v254, v233 quad_perm:[1,1,1,1] row_mask:0xf bank_mask:0xf\n"
"v_cvt_pk_bf16_f32 v105, v106, v107\n"
"v_mfma_f32_16x16x32_bf16 v[224:227], a[28:31], a[188:191], v[224:227]\n"
"v_mul_f32_dpp v234, v254, v234 quad_perm:[2,2,2,2] row_mask:0xf bank_mask:0xf\n"
"v_sub_f32 v110, v110, v157\n"
"v_mfma_f32_16x16x32_bf16 v[224:227], a[24:27], a[184:187], v[224:227]\n"
"v_exp_f32 v109, v109\n"
"v_mfma_f32_16x16x32_bf16 v[224:227], a[20:23], a[180:183], v[224:227]\n"
"v_mul_f32_dpp v235, v254, v235 quad_perm:[3,3,3,3] row_mask:0xf bank_mask:0xf\n"
"v_sub_f32 v111, v111, v157\n"
"v_mfma_f32_16x16x32_bf16 v[224:227], a[16:19], a[176:179], v[224:227]\n"
"v_exp_f32 v110, v110\n"
"v_mfma_f32_16x16x32_bf16 v[228:231], a[16:19], a[192:195], v[228:231]\n"
"v_exp_f32 v111, v111\n"
"v_mfma_f32_16x16x32_bf16 v[228:231], a[20:23], a[196:199], v[228:231]\n"
"v_sub_f32 v112, v112, v157\n"
"v_sub_f32 v113, v113, v157\n"
"v_mfma_f32_16x16x32_bf16 v[228:231], a[24:27], a[200:203], v[228:231]\n"
"v_exp_f32 v112, v112\n"
"v_mfma_f32_16x16x32_bf16 v[228:231], a[28:31], a[204:207], v[228:231]\n"
"v_cvt_pk_bf16_f32 v106, v108, v109\n"
"v_cvt_pk_bf16_f32 v107, v110, v111\n"
"v_mfma_f32_16x16x32_bf16 v[232:235], a[28:31], a[220:223], v[232:235]\n"
"s_waitcnt vmcnt(17)\n"
"ds_read_b128 a[28:31], v154 offset:4544\n"
"v_sub_f32 v114, v114, v157\n"
"v_mfma_f32_16x16x32_bf16 v[232:235], a[24:27], a[216:219], v[232:235]\n"
"ds_read_b128 a[24:27], v154 offset:4480\n"
"v_sub_f32 v115, v115, v157\n"
"v_mfma_f32_16x16x32_bf16 v[232:235], a[20:23], a[212:215], v[232:235]\n"
"ds_read_b128 a[20:23], v154 offset:4416\n"
"v_sub_f32 v116, v116, v157\n"
"v_mfma_f32_16x16x32_bf16 v[232:235], a[16:19], a[208:211], v[232:235]\n"
"ds_read_b128 a[16:19], v154 offset:4352\n"
"v_sub_f32 v117, v117, v157\n"
"v_mfma_f32_16x16x32_bf16 v[252:255], a[252:255], v[96:99], v[120:123]\n"
"v_exp_f32 v113, v113\n"
"v_mfma_f32_16x16x32_bf16 v[252:255], a[252:255], v[104:107], v[252:255]\n"
"v_exp_f32 v114, v114\n"
"v_mfma_f32_16x16x32_bf16 v[204:207], v[104:107], v[52:55], v[204:207]\n"
"v_exp_f32 v115, v115\n"
"v_mfma_f32_16x16x32_bf16 v[212:215], v[104:107], v[76:79], v[212:215]\n"
"v_exp_f32 v116, v116\n"
"v_cvt_pk_bf16_f32 v112, v112, v113\n"
"v_cvt_pk_bf16_f32 v113, v114, v115\n"
"v_sub_f32 v118, v118, v157\n"
"v_exp_f32 v117, v117\n"
"v_sub_f32 v119, v119, v157\n"
"v_exp_f32 v118, v118\n"
"v_exp_f32 v119, v119\n"
"v_cvt_pk_bf16_f32 v220, v220, v224\n"
"v_cvt_pk_bf16_f32 v224, v221, v225\n"
"v_cvt_pk_bf16_f32 v221, v228, v232\n"
"v_cvt_pk_bf16_f32 v225, v229, v233\n"
"v_pk_max_i16 v220, v220, 0\n"
"v_pk_max_i16 v221, v221, 0\n"
"v_cvt_pk_bf16_f32 v114, v116, v117\n"
"v_cvt_pk_bf16_f32 v115, v118, v119\n"
"v_pk_max_i16 v224, v224, 0\n"
"v_pk_max_i16 v225, v225, 0\n"
"v_cvt_pk_bf16_f32 v222, v222, v226\n"
"v_mfma_f32_16x16x32_bf16 v[252:255], a[252:255], v[112:115], v[252:255]\n"
"buffer_store_dwordx2 v[220:221], v162, s[84:87], s65 offen offset:0 sc0 sc1\n"
"v_mfma_f32_16x16x32_bf16 v[204:207], v[112:115], v[56:59], v[204:207]\n"
"buffer_store_dwordx2 v[224:225], v162, s[84:87], s65 offen offset:256 sc0 sc1\n"
"v_mfma_f32_16x16x32_bf16 v[208:211], v[112:115], v[68:71], v[208:211]\n"
"v_cvt_pk_bf16_f32 v226, v223, v227\n"
"v_cvt_pk_bf16_f32 v223, v230, v234\n"
"v_mfma_f32_16x16x32_bf16 v[208:211], v[104:107], v[64:67], v[208:211]\n"
"v_cvt_pk_bf16_f32 v227, v231, v235\n"
"v_pk_max_i16 v222, v222, 0\n"
"v_mfma_f32_16x16x32_bf16 v[208:211], v[96:99], v[60:63], v[208:211]\n"
"v_pk_max_i16 v223, v223, 0\n"
"v_pk_max_i16 v226, v226, 0\n"
"v_mfma_f32_16x16x32_bf16 v[212:215], v[112:115], v[80:83], v[212:215]\n"
"buffer_store_dwordx2 v[222:223], v162, s[84:87], s65 offen offset:512 sc0 sc1\n"
"v_mfma_f32_16x16x32_bf16 v[216:219], v[112:115], v[92:95], v[216:219]\n"
"s_nop 0\n"
"ds_read_b64 v[222:223], v163 offset:16384\n"
"s_add_u32 m0, s28, 4352\n"
"v_mfma_f32_16x16x32_bf16 v[216:219], v[104:107], v[88:91], v[216:219]\n"
"s_waitcnt lgkmcnt(1)\n"
"buffer_load_dwordx4 v152, s[24:27], s56 offen offset:0 lds\n"
"v_pk_max_i16 v227, v227, 0\n"
"v_mfma_f32_16x16x32_bf16 v[216:219], v[96:99], v[84:87], v[216:219]\n"
"buffer_load_dwordx4 v152, s[24:27], s57 offen offset:1088 lds\n"
"v_cvt_pk_bf16_f32 v188, v188, v189\n"
"v_cvt_pk_bf16_f32 v189, v190, v191\n"
"v_cvt_pk_bf16_f32 v196, v196, v197\n"
"v_cvt_pk_bf16_f32 v197, v198, v199\n"
"v_cvt_pk_bf16_f32 v182, v184, v185\n"
"v_cvt_pk_bf16_f32 v183, v186, v187\n"
"v_mfma_f32_16x16x32_bf16 v[184:187], v[236:239], a[48:51], 0\n"
"buffer_load_dwordx4 v152, s[24:27], s58 offen offset:2176 lds\n"
"v_cvt_pk_bf16_f32 v190, v192, v193\n"
"v_mfma_f32_16x16x32_bf16 v[184:187], v[240:243], a[52:55], v[184:187]\n"
"buffer_load_dwordx4 v152, s[24:27], s59 offen offset:3264 lds\n"
"v_cvt_pk_bf16_f32 v191, v194, v195\n"
"v_mfma_f32_16x16x32_bf16 v[184:187], v[244:247], a[56:59], v[184:187]\n"
"v_cvt_pk_bf16_f32 v198, v200, v201\n"
"v_cvt_pk_bf16_f32 v199, v202, v203\n"
"v_mfma_f32_16x16x32_bf16 v[184:187], v[248:251], a[60:63], v[184:187]\n"
"buffer_store_dwordx2 v[226:227], v162, s[84:87], s65 offen offset:768 sc0 sc1\n"
"v_mfma_f32_16x16x32_bf16 v[192:195], v[248:251], a[76:79], 0\n"
"v_max3_f32 v157, v148, v149, v150\n"
"v_max3_f32 v159, v151, v124, v125\n"
"v_mfma_f32_16x16x32_bf16 v[192:195], v[244:247], a[72:75], v[192:195]\n"
"v_max3_f32 v157, v157, v126, v127\n"
"v_max3_f32 v159, v159, v128, v129\n"
"v_mfma_f32_16x16x32_bf16 v[192:195], v[240:243], a[68:71], v[192:195]\n"
"v_max3_f32 v157, v157, v130, v131\n"
"v_add_u32 v163, 512, v163\n"
"v_mfma_f32_16x16x32_bf16 v[192:195], v[236:239], a[64:67], v[192:195]\n"
"v_max3_f32 v159, v159, v132, v133\n"
"v_max3_f32 v157, v157, v134, v135\n"
"v_mfma_f32_16x16x32_bf16 v[200:203], v[236:239], a[80:83], 0\n"
"ds_read_b128 v[236:239], v153 offset:7168\n"
"v_max3_f32 v159, v159, v136, v137\n"
"v_mfma_f32_16x16x32_bf16 v[200:203], v[240:243], a[84:87], v[200:203]\n"
"ds_read_b128 v[240:243], v153 offset:11264\n"
"v_max3_f32 v157, v157, v138, v139\n"
"v_mfma_f32_16x16x32_bf16 v[200:203], v[244:247], a[88:91], v[200:203]\n"
"ds_read_b128 v[244:247], v153 offset:15360\n"
"v_max3_f32 v159, v159, v140, v141\n"
"v_mfma_f32_16x16x32_bf16 v[200:203], v[248:251], a[92:95], v[200:203]\n"
"ds_read_b128 v[248:251], v153 offset:0\n"
"v_max3_f32 v157, v157, v142, v143\n"
"v_mfma_f32_16x16x32_bf16 v[120:123], a[228:231], v[180:183], 0\n"
"ds_read_b128 a[228:231], v158 offset:0\n"
"v_max3_f32 v159, v159, v144, v145\n"
"v_mfma_f32_16x16x32_bf16 v[96:99], v[0:3], v[180:183], 0\n"
"v_cvt_pk_bf16_f32 v184, v184, v185\n"
"v_cvt_pk_bf16_f32 v185, v186, v187\n"
"v_mfma_f32_16x16x32_bf16 v[104:107], v[16:19], v[180:183], 0\n"
"v_cvt_pk_bf16_f32 v192, v192, v193\n"
"v_cvt_pk_bf16_f32 v193, v194, v195\n"
"v_mfma_f32_16x16x32_bf16 v[112:115], v[32:35], v[180:183], 0\n"
"v_cvt_pk_bf16_f32 v200, v200, v201\n"
"v_cvt_pk_bf16_f32 v201, v202, v203\n"
"v_max3_f32 v157, v157, v146, v147\n"
"v_max_f32 v157, v157, v159\n"
"v_mov_b32 v161, v157\n"
"v_add_f32 v252, v252, v253\n"
"v_rcp_f32 v253, v252\n"
"v_permlane32_swap_b32 v157, v161\n"
"v_max_f32 v157, v157, v161\n"
"v_mov_b32 v161, v157\n"
"s_waitcnt lgkmcnt(5)\n"
"v_lshlrev_b32 v220, 16, v222\n"
"v_and_b32 v221, 0xffff0000, v222\n"
"s_waitcnt lgkmcnt(4)\n"
"v_mfma_f32_16x16x32_bf16 v[164:167], v[236:239], a[52:55], v[164:167]\n"
"s_waitcnt lgkmcnt(3)\n"
"ds_bpermute_b32 v254, v160, v253 offset:0\n"
"v_mfma_f32_16x16x32_bf16 v[164:167], v[240:243], a[56:59], v[164:167]\n"
"s_waitcnt lgkmcnt(3)\n"
"v_permlane16_swap_b32 v157, v161\n"
"v_mfma_f32_16x16x32_bf16 v[164:167], v[244:247], a[60:63], v[164:167]\n"
"v_max_f32 v157, v157, v161\n"
"v_sub_f32 v148, v148, v157\n"
"v_mfma_f32_16x16x32_bf16 v[168:171], v[244:247], a[76:79], 0\n"
"v_sub_f32 v149, v149, v157\n"
"v_sub_f32 v150, v150, v157\n"
"v_mfma_f32_16x16x32_bf16 v[168:171], v[240:243], a[72:75], v[168:171]\n"
"v_sub_f32 v151, v151, v157\n"
"v_sub_f32 v124, v124, v157\n"
"v_mfma_f32_16x16x32_bf16 v[168:171], v[236:239], a[68:71], v[168:171]\n"
"v_exp_f32 v161, v150\n"
"v_mfma_f32_16x16x32_bf16 v[168:171], a[224:227], a[64:67], v[168:171]\n"
"ds_read_b128 a[224:227], v153 offset:4096\n"
"v_cvt_pk_bf16_f32 v186, v164, v165\n"
"v_mfma_f32_16x16x32_bf16 v[172:175], v[236:239], a[84:87], v[172:175]\n"
"ds_read_b128 v[236:239], v153 offset:8192\n"
"v_cvt_pk_bf16_f32 v187, v166, v167\n"
"v_mfma_f32_16x16x32_bf16 v[172:175], v[240:243], a[88:91], v[172:175]\n"
"ds_read_b128 v[240:243], v153 offset:12288\n"
"v_sub_f32 v125, v125, v157\n"
"v_mfma_f32_16x16x32_bf16 v[172:175], v[244:247], a[92:95], v[172:175]\n"
"ds_read_b128 v[244:247], v153 offset:1024\n"
"v_cvt_pk_bf16_f32 v194, v168, v169\n"
"v_mfma_f32_16x16x32_bf16 v[120:123], a[232:235], v[184:187], v[120:123]\n"
"ds_read_b128 a[232:235], v158 offset:2048\n"
"v_cvt_pk_bf16_f32 v195, v170, v171\n"
"v_mfma_f32_16x16x32_bf16 v[96:99], v[4:7], v[184:187], v[96:99]\n"
"v_exp_f32 v148, v148\n"
"v_mfma_f32_16x16x32_bf16 v[100:103], v[12:15], v[184:187], 0\n"
"v_exp_f32 v149, v149\n"
"v_mfma_f32_16x16x32_bf16 v[100:103], v[8:11], v[180:183], v[100:103]\n"
"v_exp_f32 v151, v151\n"
"v_mfma_f32_16x16x32_bf16 v[104:107], v[20:23], v[184:187], v[104:107]\n"
"v_cvt_pk_bf16_f32 v202, v172, v173\n"
"v_cvt_pk_bf16_f32 v203, v174, v175\n"
"v_mfma_f32_16x16x32_bf16 v[108:111], v[28:31], v[184:187], 0\n"
"v_cvt_pk_bf16_f32 v150, v148, v149\n"
"v_add_f32 v148, v148, v149\n"
"v_mfma_f32_16x16x32_bf16 v[108:111], v[24:27], v[180:183], v[108:111]\n"
"v_add_f32 v149, v161, v151\n"
"v_cvt_pk_bf16_f32 v151, v161, v151\n"
"v_mfma_f32_16x16x32_bf16 v[112:115], v[36:39], v[184:187], v[112:115]\n"
"v_exp_f32 v124, v124\n"
"v_mfma_f32_16x16x32_bf16 v[116:119], v[44:47], v[184:187], 0\n"
"v_sub_f32 v126, v126, v157\n"
"v_sub_f32 v127, v127, v157\n"
"v_mfma_f32_16x16x32_bf16 v[116:119], v[40:43], v[180:183], v[116:119]\n"
"s_waitcnt lgkmcnt(3)\n"
"v_exp_f32 v125, v125\n"
"v_mul_f32_dpp v204, v254, v204 quad_perm:[0,0,0,0] row_mask:0xf bank_mask:0xf\n"
"v_mfma_f32_16x16x4_4b_bf16 v[164:179], v[150:151], a[244:245], 0 cbsz:2 abid:0\n"
"s_sub_u32 s74, s74, 1\n"
"v_exp_f32 v126, v126\n"
"v_mul_f32_dpp v205, v254, v205 quad_perm:[1,1,1,1] row_mask:0xf bank_mask:0xf\n"
"v_mul_f32_dpp v206, v254, v206 quad_perm:[2,2,2,2] row_mask:0xf bank_mask:0xf\n"
"v_exp_f32 v127, v127\n"
"v_mul_f32_dpp v207, v254, v207 quad_perm:[3,3,3,3] row_mask:0xf bank_mask:0xf\n"
"v_sub_f32 v128, v128, v157\n"
"s_cbranch_scc1 .Leps%=\n"
"v_mfma_f32_16x16x32_bf16 v[180:183], v[248:251], a[0:3], 0\n"
"v_mul_f32_dpp v208, v254, v208 quad_perm:[0,0,0,0] row_mask:0xf bank_mask:0xf\n"
"v_mul_f32_dpp v209, v254, v209 quad_perm:[1,1,1,1] row_mask:0xf bank_mask:0xf\n"
"v_mfma_f32_16x16x32_bf16 v[180:183], a[224:227], a[4:7], v[180:183]\n"
"v_mul_f32_dpp v210, v254, v210 quad_perm:[2,2,2,2] row_mask:0xf bank_mask:0xf\n"
"v_sub_f32 v129, v129, v157\n"
"v_mfma_f32_16x16x32_bf16 v[180:183], v[236:239], a[8:11], v[180:183]\n"
"s_waitcnt lgkmcnt(0)\n"
"v_exp_f32 v128, v128\n"
"v_mfma_f32_16x16x32_bf16 v[180:183], v[240:243], a[12:15], v[180:183]\n"
"v_mul_f32_dpp v211, v254, v211 quad_perm:[3,3,3,3] row_mask:0xf bank_mask:0xf\n"
"v_cvt_pk_bf16_f32 v124, v124, v125\n"
"v_mfma_f32_16x16x32_bf16 v[204:207], a[32:35], a[160:163], v[204:207]\n"
"v_mul_f32_dpp v212, v254, v212 quad_perm:[0,0,0,0] row_mask:0xf bank_mask:0xf\n"
"v_cvt_pk_bf16_f32 v125, v126, v127\n"
"v_mfma_f32_16x16x32_bf16 v[204:207], a[36:39], a[164:167], v[204:207]\n"
"v_sub_f32 v130, v130, v157\n"
"v_mul_f32_dpp v213, v254, v213 quad_perm:[1,1,1,1] row_mask:0xf bank_mask:0xf\n"
"v_mfma_f32_16x16x32_bf16 v[204:207], a[40:43], a[168:171], v[204:207]\n"
"v_exp_f32 v129, v129\n"
"v_mfma_f32_16x16x32_bf16 v[204:207], a[44:47], a[172:175], v[204:207]\n"
"v_mul_f32_dpp v214, v254, v214 quad_perm:[2,2,2,2] row_mask:0xf bank_mask:0xf\n"
"v_mul_f32_dpp v215, v254, v215 quad_perm:[3,3,3,3] row_mask:0xf bank_mask:0xf\n"
"v_mfma_f32_16x16x32_bf16 v[208:211], a[44:47], a[188:191], v[208:211]\n"
"v_sub_f32 v131, v131, v157\n"
"v_mul_f32_dpp v216, v254, v216 quad_perm:[0,0,0,0] row_mask:0xf bank_mask:0xf\n"
"v_mfma_f32_16x16x32_bf16 v[208:211], a[40:43], a[184:187], v[208:211]\n"
"v_exp_f32 v130, v130\n"
"v_mfma_f32_16x16x32_bf16 v[208:211], a[36:39], a[180:183], v[208:211]\n"
"v_mul_f32_dpp v217, v254, v217 quad_perm:[1,1,1,1] row_mask:0xf bank_mask:0xf\n"
"v_mul_f32_dpp v218, v254, v218 quad_perm:[2,2,2,2] row_mask:0xf bank_mask:0xf\n"
"v_mfma_f32_16x16x32_bf16 v[208:211], a[32:35], a[176:179], v[208:211]\n"
"v_exp_f32 v131, v131\n"
"v_mfma_f32_16x16x32_bf16 v[212:215], a[32:35], a[192:195], v[212:215]\n"
"v_mul_f32_dpp v219, v254, v219 quad_perm:[3,3,3,3] row_mask:0xf bank_mask:0xf\n"
"v_sub_f32 v132, v132, v157\n"
"v_mfma_f32_16x16x32_bf16 v[212:215], a[36:39], a[196:199], v[212:215]\n"
"v_sub_f32 v133, v133, v157\n"
"v_cvt_pk_bf16_f32 v126, v128, v129\n"
"v_mfma_f32_16x16x32_bf16 v[212:215], a[40:43], a[200:203], v[212:215]\n"
"v_exp_f32 v132, v132\n"
"v_mfma_f32_16x16x32_bf16 v[212:215], a[44:47], a[204:207], v[212:215]\n"
"v_cvt_pk_bf16_f32 v127, v130, v131\n"
"v_sub_f32 v134, v134, v157\n"
"v_mfma_f32_16x16x32_bf16 v[216:219], a[44:47], a[220:223], v[216:219]\n"
"s_waitcnt vmcnt(16)\n"
"ds_read_b128 a[44:47], v154 offset:8896\n"
"v_sub_f32 v135, v135, v157\n"
"v_mfma_f32_16x16x32_bf16 v[216:219], a[40:43], a[216:219], v[216:219]\n"
"ds_read_b128 a[40:43], v154 offset:8832\n"
"v_sub_f32 v136, v136, v157\n"
"v_mfma_f32_16x16x32_bf16 v[216:219], a[36:39], a[212:215], v[216:219]\n"
"ds_read_b128 a[36:39], v154 offset:8768\n"
"v_sub_f32 v137, v137, v157\n"
"v_mfma_f32_16x16x32_bf16 v[216:219], a[32:35], a[208:211], v[216:219]\n"
"ds_read_b128 a[32:35], v154 offset:8704\n"
"v_sub_f32 v138, v138, v157\n"
"v_mfma_f32_16x16x32_bf16 v[252:255], a[252:255], v[124:127], v[148:151]\n"
"v_exp_f32 v133, v133\n"
"v_mfma_f32_16x16x32_bf16 v[164:167], v[124:127], v[48:51], v[164:167]\n"
"v_exp_f32 v134, v134\n"
"v_mfma_f32_16x16x32_bf16 v[172:175], v[124:127], v[72:75], v[172:175]\n"
"v_exp_f32 v135, v135\n"
"v_mfma_f32_16x16x32_bf16 v[148:151], a[228:231], v[188:191], 0\n"
"ds_read_b128 a[228:231], v158 offset:0\n"
"v_cvt_pk_bf16_f32 v132, v132, v133\n"
"v_mfma_f32_16x16x32_bf16 v[148:151], a[232:235], v[192:195], v[148:151]\n"
"ds_read_b128 a[232:235], v158 offset:2048\n"
"v_cvt_pk_bf16_f32 v133, v134, v135\n"
"v_mfma_f32_16x16x32_bf16 v[128:131], v[12:15], v[192:195], 0\n"
"v_exp_f32 v136, v136\n"
"v_mfma_f32_16x16x32_bf16 v[128:131], v[8:11], v[188:191], v[128:131]\n"
"v_exp_f32 v137, v137\n"
"v_mfma_f32_16x16x32_bf16 v[184:187], v[244:247], a[0:3], 0\n"
"v_sub_f32 v139, v139, v157\n"
"v_exp_f32 v138, v138\n"
"v_exp_f32 v139, v139\n"
"v_sub_f32 v140, v140, v157\n"
"v_sub_f32 v141, v141, v157\n"
"v_exp_f32 v140, v140\n"
"v_cvt_pk_bf16_f32 v134, v136, v137\n"
"v_cvt_pk_bf16_f32 v135, v138, v139\n"
"v_mfma_f32_16x16x32_bf16 v[136:139], v[28:31], v[192:195], 0\n"
"v_cvt_pk_bf16_f32 v204, v204, v208\n"
"v_sub_f32 v142, v142, v157\n"
"v_mfma_f32_16x16x32_bf16 v[136:139], v[24:27], v[188:191], v[136:139]\n"
"v_exp_f32 v141, v141\n"
"v_mfma_f32_16x16x32_bf16 v[252:255], a[252:255], v[132:135], v[252:255]\n"
"v_cvt_pk_bf16_f32 v208, v205, v209\n"
"v_cvt_pk_bf16_f32 v205, v212, v216\n"
"v_mfma_f32_16x16x32_bf16 v[164:167], v[132:135], v[52:55], v[164:167]\n"
"v_cvt_pk_bf16_f32 v209, v213, v217\n"
"v_sub_f32 v143, v143, v157\n"
"v_mfma_f32_16x16x32_bf16 v[172:175], v[132:135], v[76:79], v[172:175]\n"
"v_pk_max_i16 v204, v204, 0\n"
"v_exp_f32 v142, v142\n"
"s_add_u32 m0, s28, 8704\n"
"v_pk_max_i16 v205, v205, 0\n"
"v_pk_max_i16 v208, v208, 0\n"
"v_exp_f32 v143, v143\n"
"v_pk_max_i16 v209, v209, 0\n"
"v_sub_f32 v144, v144, v157\n"
"v_cvt_pk_bf16_f32 v206, v206, v210\n"
"v_cvt_pk_bf16_f32 v210, v207, v211\n"
"v_cvt_pk_bf16_f32 v207, v214, v218\n"
"v_sub_f32 v145, v145, v157\n"
"v_cvt_pk_bf16_f32 v211, v215, v219\n"
"v_exp_f32 v144, v144\n"
"v_pk_max_i16 v206, v206, 0\n"
"v_cvt_pk_bf16_f32 v140, v140, v141\n"
"v_pk_max_i16 v207, v207, 0\n"
"v_cvt_pk_bf16_f32 v141, v142, v143\n"
"v_sub_f32 v146, v146, v157\n"
"v_exp_f32 v145, v145\n"
"v_pk_max_i16 v210, v210, 0\n"
"v_pk_max_i16 v211, v211, 0\n"
"v_sub_f32 v147, v147, v157\n"
"v_exp_f32 v146, v146\n"
"v_exp_f32 v147, v147\n"
"v_cvt_pk_bf16_f32 v142, v144, v145\n"
"v_max3_f32 v157, v120, v121, v122\n"
"v_cvt_pk_bf16_f32 v143, v146, v147\n"
"v_mfma_f32_16x16x32_bf16 v[144:147], v[44:47], v[192:195], 0\n"
"s_waitcnt lgkmcnt(0)\n"
"buffer_load_dwordx4 v152, s[24:27], s60 offen offset:0 lds\n"
"v_lshlrev_b32 v222, 16, v223\n"
"v_mfma_f32_16x16x32_bf16 v[144:147], v[40:43], v[188:191], v[144:147]\n"
"buffer_load_dwordx4 v152, s[24:27], s61 offen offset:1088 lds\n"
"v_and_b32 v223, 0xffff0000, v223\n"
"v_mfma_f32_16x16x32_bf16 v[252:255], a[252:255], v[140:143], v[252:255]\n"
"buffer_load_dwordx4 v152, s[24:27], s62 offen offset:2176 lds\n"
"v_max3_f32 v159, v123, v96, v97\n"
"v_mfma_f32_16x16x32_bf16 v[164:167], v[140:143], v[56:59], v[164:167]\n"
"buffer_load_dwordx4 v152, s[24:27], s63 offen offset:3264 lds\n"
"v_max3_f32 v157, v157, v98, v99\n"
"v_mfma_f32_16x16x32_bf16 v[168:171], v[140:143], v[68:71], v[168:171]\n"
"v_max3_f32 v159, v159, v100, v101\n"
"v_max3_f32 v157, v157, v102, v103\n"
"v_mfma_f32_16x16x32_bf16 v[168:171], v[132:135], v[64:67], v[168:171]\n"
"v_max3_f32 v159, v159, v104, v105\n"
"v_max3_f32 v157, v157, v106, v107\n"
"v_mfma_f32_16x16x32_bf16 v[168:171], v[124:127], v[60:63], v[168:171]\n"
"buffer_store_dwordx2 v[204:205], v162, s[84:87], s66 offen offset:0 sc0 sc1\n"
"v_mfma_f32_16x16x32_bf16 v[172:175], v[140:143], v[80:83], v[172:175]\n"
"buffer_store_dwordx2 v[208:209], v162, s[84:87], s66 offen offset:256 sc0 sc1\n"
"v_mfma_f32_16x16x32_bf16 v[176:179], v[140:143], v[92:95], v[176:179]\n"
"v_max3_f32 v159, v159, v108, v109\n"
"v_max3_f32 v157, v157, v110, v111\n"
"v_mfma_f32_16x16x32_bf16 v[176:179], v[132:135], v[88:91], v[176:179]\n"
"v_max3_f32 v159, v159, v112, v113\n"
"v_max3_f32 v157, v157, v114, v115\n"
"v_mfma_f32_16x16x32_bf16 v[176:179], v[124:127], v[84:87], v[176:179]\n"
"buffer_store_dwordx2 v[206:207], v162, s[84:87], s66 offen offset:512 sc0 sc1\n"
"v_mfma_f32_16x16x32_bf16 v[124:127], v[0:3], v[188:191], 0\n"
"v_max3_f32 v159, v159, v116, v117\n"
"v_max3_f32 v157, v157, v118, v119\n"
"v_mfma_f32_16x16x32_bf16 v[124:127], v[4:7], v[192:195], v[124:127]\n"
"buffer_store_dwordx2 v[210:211], v162, s[84:87], s66 offen offset:768 sc0 sc1\n"
"v_mfma_f32_16x16x32_bf16 v[132:135], v[16:19], v[188:191], 0\n"
"v_max_f32 v157, v157, v159\n"
"v_mov_b32 v161, v157\n"
"v_mfma_f32_16x16x32_bf16 v[132:135], v[20:23], v[192:195], v[132:135]\n"
"v_add_f32 v252, v252, v253\n"
"v_add_u32 v158, 64, v158\n"
"v_mfma_f32_16x16x32_bf16 v[140:143], v[32:35], v[188:191], 0\n"
"v_permlane32_swap_b32 v157, v161\n"
"v_mfma_f32_16x16x32_bf16 v[140:143], v[36:39], v[192:195], v[140:143]\n"
"v_max_f32 v157, v157, v161\n"
"v_mov_b32 v161, v157\n"
"v_mfma_f32_16x16x32_bf16 v[188:191], v[240:243], a[28:31], 0\n"
"v_rcp_f32 v253, v252\n"
"v_mfma_f32_16x16x32_bf16 v[188:191], v[236:239], a[24:27], v[188:191]\n"
"ds_bpermute_b32 v254, v160, v253 offset:0\n"
"v_mfma_f32_16x16x32_bf16 v[188:191], a[224:227], a[20:23], v[188:191]\n"
"v_permlane16_swap_b32 v157, v161\n"
"v_mfma_f32_16x16x32_bf16 v[188:191], v[248:251], a[16:19], v[188:191]\n"
"v_max_f32 v157, v157, v161\n"
"v_sub_f32 v120, v120, v157\n"
"v_sub_f32 v121, v121, v157\n"
"v_sub_f32 v122, v122, v157\n"
"v_sub_f32 v123, v123, v157\n"
"v_exp_f32 v161, v122\n"
"v_exp_f32 v120, v120\n"
"v_exp_f32 v121, v121\n"
"v_exp_f32 v123, v123\n"
"v_cvt_pk_bf16_f32 v122, v120, v121\n"
"v_add_f32 v120, v120, v121\n"
"v_add_f32 v121, v161, v123\n"
"v_cvt_pk_bf16_f32 v123, v161, v123\n"
"v_sub_f32 v96, v96, v157\n"
"v_sub_f32 v97, v97, v157\n"
"v_exp_f32 v96, v96\n"
"v_mfma_f32_16x16x4_4b_bf16 v[204:219], v[122:123], a[244:245], 0 cbsz:2 abid:0\n"
"v_sub_f32 v98, v98, v157\n"
"v_exp_f32 v97, v97\n"
"v_sub_f32 v99, v99, v157\n"
"v_exp_f32 v98, v98\n"
"v_exp_f32 v99, v99\n"
"v_sub_f32 v100, v100, v157\n"
"v_sub_f32 v101, v101, v157\n"
"v_exp_f32 v100, v100\n"
"v_cvt_pk_bf16_f32 v96, v96, v97\n"
"s_waitcnt lgkmcnt(0)\n"
"v_fmac_f32_dpp v220, v254, v164 quad_perm:[0,0,0,0] row_mask:0xf bank_mask:0xf\n"
"v_mfma_f32_16x16x32_bf16 v[164:167], a[144:147], a[0:3], 0\n"
"v_fmac_f32_dpp v221, v254, v168 quad_perm:[0,0,0,0] row_mask:0xf bank_mask:0xf\n"
"v_fmac_f32_dpp v222, v254, v172 quad_perm:[0,0,0,0] row_mask:0xf bank_mask:0xf\n"
"v_mfma_f32_16x16x32_bf16 v[164:167], a[148:151], a[4:7], v[164:167]\n"
"v_fmac_f32_dpp v223, v254, v176 quad_perm:[0,0,0,0] row_mask:0xf bank_mask:0xf\n"
"v_cvt_pk_bf16_f32 v220, v220, v221\n"
"v_mfma_f32_16x16x32_bf16 v[164:167], a[152:155], a[8:11], v[164:167]\n"
"v_cvt_pk_bf16_f32 v221, v222, v223\n"
"v_cvt_pk_bf16_f32 v97, v98, v99\n"
"v_mfma_f32_16x16x32_bf16 v[164:167], a[156:159], a[12:15], v[164:167]\n"
"v_pk_max_i16 v220, v220, 0\n"
"v_sub_f32 v102, v102, v157\n"
"v_mfma_f32_16x16x32_bf16 v[168:171], a[156:159], a[28:31], 0\n"
"v_pk_max_i16 v221, v221, 0\n"
"v_sub_f32 v103, v103, v157\n"
"v_mfma_f32_16x16x32_bf16 v[168:171], a[152:155], a[24:27], v[168:171]\n"
"v_exp_f32 v101, v101\n"
"v_mfma_f32_16x16x32_bf16 v[168:171], a[148:151], a[20:23], v[168:171]\n"
"v_exp_f32 v102, v102\n"
"v_mfma_f32_16x16x32_bf16 v[168:171], a[144:147], a[16:19], v[168:171]\n"
"buffer_store_dwordx2 v[220:221], v155, s[84:87], s70 offen offset:0 sc0 sc1\n"
"v_mfma_f32_16x16x32_bf16 v[172:175], a[144:147], a[32:35], 0\n"
"v_exp_f32 v103, v103\n"
"v_mfma_f32_16x16x32_bf16 v[172:175], a[148:151], a[36:39], v[172:175]\n"
"v_sub_f32 v104, v104, v157\n"
"v_sub_f32 v105, v105, v157\n"
"v_mfma_f32_16x16x32_bf16 v[172:175], a[152:155], a[40:43], v[172:175]\n"
"v_exp_f32 v104, v104\n"
"v_mfma_f32_16x16x32_bf16 v[172:175], a[156:159], a[44:47], v[172:175]\n"
"v_cvt_pk_bf16_f32 v98, v100, v101\n"
"v_cvt_pk_bf16_f32 v99, v102, v103\n"
"v_mfma_f32_16x16x32_bf16 v[100:103], v[12:15], v[200:203], 0\n"
"v_sub_f32 v106, v106, v157\n"
"v_sub_f32 v107, v107, v157\n"
"v_mfma_f32_16x16x32_bf16 v[100:103], v[8:11], v[196:199], v[100:103]\n"
"v_exp_f32 v105, v105\n"
"v_mfma_f32_16x16x32_bf16 v[252:255], a[252:255], v[96:99], v[120:123]\n"
"v_exp_f32 v106, v106\n"
"v_mfma_f32_16x16x32_bf16 v[204:207], v[96:99], v[48:51], v[204:207]\n"
"v_exp_f32 v107, v107\n"
"v_mfma_f32_16x16x32_bf16 v[212:215], v[96:99], v[72:75], v[212:215]\n"
"v_sub_f32 v108, v108, v157\n"
"v_sub_f32 v109, v109, v157\n"
"v_mfma_f32_16x16x32_bf16 v[120:123], a[228:231], v[196:199], 0\n"
"ds_read_b128 a[228:231], v158 offset:0\n"
"v_cvt_pk_bf16_f32 v104, v104, v105\n"
"v_mfma_f32_16x16x32_bf16 v[120:123], a[232:235], v[200:203], v[120:123]\n"
"ds_read_b128 a[232:235], v158 offset:2048\n"
"v_cvt_pk_bf16_f32 v105, v106, v107\n"
"v_mfma_f32_16x16x32_bf16 v[8:11], a[108:111], a[28:31], 0\n"
"v_exp_f32 v108, v108\n"
"v_mfma_f32_16x16x32_bf16 v[8:11], a[104:107], a[24:27], v[8:11]\n"
"v_sub_f32 v110, v110, v157\n"
"v_sub_f32 v111, v111, v157\n"
"v_mfma_f32_16x16x32_bf16 v[8:11], a[100:103], a[20:23], v[8:11]\n"
"v_exp_f32 v109, v109\n"
"v_mfma_f32_16x16x32_bf16 v[8:11], a[96:99], a[16:19], v[8:11]\n"
"v_exp_f32 v110, v110\n"
"v_mfma_f32_16x16x32_bf16 v[12:15], a[124:127], a[28:31], 0\n"
"v_exp_f32 v111, v111\n"
"v_mfma_f32_16x16x32_bf16 v[12:15], a[120:123], a[24:27], v[12:15]\n"
"v_sub_f32 v112, v112, v157\n"
"v_sub_f32 v113, v113, v157\n"
"v_mfma_f32_16x16x32_bf16 v[12:15], a[116:119], a[20:23], v[12:15]\n"
"v_exp_f32 v112, v112\n"
"v_mfma_f32_16x16x32_bf16 v[12:15], a[112:115], a[16:19], v[12:15]\n"
"v_cvt_pk_bf16_f32 v106, v108, v109\n"
"v_cvt_pk_bf16_f32 v107, v110, v111\n"
"v_mfma_f32_16x16x32_bf16 v[108:111], v[28:31], v[200:203], 0\n"
"v_sub_f32 v114, v114, v157\n"
"v_sub_f32 v115, v115, v157\n"
"v_mfma_f32_16x16x32_bf16 v[108:111], v[24:27], v[196:199], v[108:111]\n"
"v_exp_f32 v113, v113\n"
"v_mfma_f32_16x16x32_bf16 v[252:255], a[252:255], v[104:107], v[252:255]\n"
"v_exp_f32 v114, v114\n"
"v_mfma_f32_16x16x32_bf16 v[204:207], v[104:107], v[52:55], v[204:207]\n"
"v_exp_f32 v115, v115\n"
"v_mfma_f32_16x16x32_bf16 v[212:215], v[104:107], v[76:79], v[212:215]\n"
"v_sub_f32 v116, v116, v157\n"
"v_sub_f32 v117, v117, v157\n"
"v_exp_f32 v116, v116\n"
"v_cvt_pk_bf16_f32 v112, v112, v113\n"
"v_cvt_pk_bf16_f32 v113, v114, v115\n"
"v_sub_f32 v118, v118, v157\n"
"v_exp_f32 v117, v117\n"
"v_sub_f32 v119, v119, v157\n"
"v_exp_f32 v118, v118\n"
"v_exp_f32 v119, v119\n"
"v_cvt_pk_bf16_f32 v114, v116, v117\n"
"v_max3_f32 v157, v148, v149, v150\n"
"v_cvt_pk_bf16_f32 v115, v118, v119\n"
"v_mfma_f32_16x16x32_bf16 v[116:119], v[44:47], v[200:203], 0\n"
"v_max3_f32 v159, v151, v124, v125\n"
"v_max3_f32 v157, v157, v126, v127\n"
"v_mfma_f32_16x16x32_bf16 v[116:119], v[40:43], v[196:199], v[116:119]\n"
"v_max3_f32 v159, v159, v128, v129\n"
"v_max3_f32 v157, v157, v130, v131\n"
"v_mfma_f32_16x16x32_bf16 v[252:255], a[252:255], v[112:115], v[252:255]\n"
"v_max3_f32 v159, v159, v132, v133\n"
"v_max3_f32 v157, v157, v134, v135\n"
"v_mfma_f32_16x16x32_bf16 v[204:207], v[112:115], v[56:59], v[204:207]\n"
"v_max3_f32 v159, v159, v136, v137\n"
"v_max3_f32 v157, v157, v138, v139\n"
"v_mfma_f32_16x16x32_bf16 v[208:211], v[112:115], v[68:71], v[208:211]\n"
"v_max3_f32 v159, v159, v140, v141\n"
"v_max3_f32 v157, v157, v142, v143\n"
"v_mfma_f32_16x16x32_bf16 v[208:211], v[104:107], v[64:67], v[208:211]\n"
"v_max3_f32 v159, v159, v144, v145\n"
"v_max3_f32 v157, v157, v146, v147\n"
"v_mfma_f32_16x16x32_bf16 v[208:211], v[96:99], v[60:63], v[208:211]\n"
"v_max_f32 v157, v157, v159\n"
"v_mov_b32 v161, v157\n"
"v_mfma_f32_16x16x32_bf16 v[212:215], v[112:115], v[80:83], v[212:215]\n"
"v_add_f32 v252, v252, v253\n"
"v_cvt_pk_bf16_f32 v8, v8, v9\n"
"v_mfma_f32_16x16x32_bf16 v[216:219], v[112:115], v[92:95], v[216:219]\n"
"v_permlane32_swap_b32 v157, v161\n"
"v_mfma_f32_16x16x32_bf16 v[216:219], v[104:107], v[88:91], v[216:219]\n"
"v_max_f32 v157, v157, v161\n"
"v_mov_b32 v161, v157\n"
"v_mfma_f32_16x16x32_bf16 v[216:219], v[96:99], v[84:87], v[216:219]\n"
"v_rcp_f32 v253, v252\n"
"v_mfma_f32_16x16x32_bf16 v[96:99], v[0:3], v[196:199], 0\n"
"ds_bpermute_b32 v254, v160, v253 offset:0\n"
"v_mfma_f32_16x16x32_bf16 v[96:99], v[4:7], v[200:203], v[96:99]\n"
"v_permlane16_swap_b32 v157, v161\n"
"v_mfma_f32_16x16x32_bf16 v[104:107], v[16:19], v[196:199], 0\n"
"v_max_f32 v157, v157, v161\n"
"v_sub_f32 v148, v148, v157\n"
"v_mfma_f32_16x16x32_bf16 v[104:107], v[20:23], v[200:203], v[104:107]\n"
"v_sub_f32 v149, v149, v157\n"
"v_sub_f32 v150, v150, v157\n"
"v_mfma_f32_16x16x32_bf16 v[112:115], v[32:35], v[196:199], 0\n"
"v_sub_f32 v151, v151, v157\n"
"v_sub_f32 v124, v124, v157\n"
"v_mfma_f32_16x16x32_bf16 v[112:115], v[36:39], v[200:203], v[112:115]\n"
"v_exp_f32 v161, v150\n"
"v_mfma_f32_16x16x32_bf16 v[0:3], a[96:99], a[0:3], 0\n"
"v_exp_f32 v148, v148\n"
"v_mfma_f32_16x16x32_bf16 v[0:3], a[100:103], a[4:7], v[0:3]\n"
"v_exp_f32 v149, v149\n"
"v_mfma_f32_16x16x32_bf16 v[0:3], a[104:107], a[8:11], v[0:3]\n"
"v_exp_f32 v151, v151\n"
"v_mfma_f32_16x16x32_bf16 v[0:3], a[108:111], a[12:15], v[0:3]\n"
"v_cvt_pk_bf16_f32 v150, v148, v149\n"
"v_add_f32 v148, v148, v149\n"
"v_mfma_f32_16x16x32_bf16 v[16:19], a[96:99], a[32:35], 0\n"
"v_add_f32 v149, v161, v151\n"
"v_cvt_pk_bf16_f32 v151, v161, v151\n"
"v_mfma_f32_16x16x32_bf16 v[16:19], a[100:103], a[36:39], v[16:19]\n"
"v_sub_f32 v125, v125, v157\n"
"v_sub_f32 v126, v126, v157\n"
"v_mfma_f32_16x16x32_bf16 v[16:19], a[104:107], a[40:43], v[16:19]\n"
"v_exp_f32 v124, v124\n"
"v_mfma_f32_16x16x32_bf16 v[16:19], a[108:111], a[44:47], v[16:19]\n"
"v_exp_f32 v125, v125\n"
"v_mfma_f32_16x16x32_bf16 v[4:7], a[112:115], a[0:3], 0\n"
"v_sub_f32 v127, v127, v157\n"
"v_sub_f32 v128, v128, v157\n"
"v_mfma_f32_16x16x32_bf16 v[4:7], a[116:119], a[4:7], v[4:7]\n"
"v_exp_f32 v126, v126\n"
"v_mfma_f32_16x16x32_bf16 v[4:7], a[120:123], a[8:11], v[4:7]\n"
"v_exp_f32 v127, v127\n"
"v_mfma_f32_16x16x32_bf16 v[4:7], a[124:127], a[12:15], v[4:7]\n"
"v_sub_f32 v129, v129, v157\n"
"v_cvt_pk_bf16_f32 v124, v124, v125\n"
"v_mfma_f32_16x16x32_bf16 v[20:23], a[112:115], a[32:35], 0\n"
"v_exp_f32 v128, v128\n"
"v_mfma_f32_16x16x32_bf16 v[20:23], a[116:119], a[36:39], v[20:23]\n"
"v_cvt_pk_bf16_f32 v125, v126, v127\n"
"v_sub_f32 v130, v130, v157\n"
"v_mfma_f32_16x16x32_bf16 v[20:23], a[120:123], a[40:43], v[20:23]\n"
"v_exp_f32 v129, v129\n"
"v_mfma_f32_16x16x32_bf16 v[20:23], a[124:127], a[44:47], v[20:23]\n"
"v_sub_f32 v131, v131, v157\n"
"v_exp_f32 v130, v130\n"
"v_mfma_f32_16x16x4_4b_bf16 v[220:235], v[150:151], a[244:245], 0 cbsz:2 abid:0\n"
"v_exp_f32 v131, v131\n"
"v_sub_f32 v132, v132, v157\n"
"v_sub_f32 v133, v133, v157\n"
"v_exp_f32 v132, v132\n"
"v_cvt_pk_bf16_f32 v126, v128, v129\n"
"v_cvt_pk_bf16_f32 v127, v130, v131\n"
"v_mfma_f32_16x16x32_bf16 v[196:199], v[248:251], a[32:35], 0\n"
"ds_read_b128 v[248:251], v153 offset:5120\n"
"v_sub_f32 v134, v134, v157\n"
"v_mfma_f32_16x16x32_bf16 v[196:199], a[224:227], a[36:39], v[196:199]\n"
"ds_read_b128 a[224:227], v153 offset:9216\n"
"v_sub_f32 v135, v135, v157\n"
"v_mfma_f32_16x16x32_bf16 v[196:199], v[236:239], a[40:43], v[196:199]\n"
"ds_read_b128 v[236:239], v153 offset:13312\n"
"v_sub_f32 v136, v136, v157\n"
"v_mfma_f32_16x16x32_bf16 v[196:199], v[240:243], a[44:47], v[196:199]\n"
"ds_read_b128 v[240:243], v153 offset:2048\n"
"v_sub_f32 v137, v137, v157\n"
"v_mfma_f32_16x16x32_bf16 v[220:223], v[124:127], v[48:51], v[220:223]\n"
"v_exp_f32 v133, v133\n"
"v_mfma_f32_16x16x32_bf16 v[228:231], v[124:127], v[72:75], v[228:231]\n"
"v_exp_f32 v134, v134\n"
"v_mfma_f32_16x16x32_bf16 v[200:203], v[244:247], a[32:35], 0\n"
"v_exp_f32 v135, v135\n"
"v_exp_f32 v136, v136\n"
"v_cvt_pk_bf16_f32 v132, v132, v133\n"
"v_cvt_pk_bf16_f32 v133, v134, v135\n"
"v_sub_f32 v138, v138, v157\n"
"v_exp_f32 v137, v137\n"
"v_sub_f32 v139, v139, v157\n"
"v_exp_f32 v138, v138\n"
"s_waitcnt lgkmcnt(4)\n"
"v_mul_f32_dpp v204, v254, v204 quad_perm:[0,0,0,0] row_mask:0xf bank_mask:0xf\n"
"v_exp_f32 v139, v139\n"
"v_mul_f32_dpp v205, v254, v205 quad_perm:[1,1,1,1] row_mask:0xf bank_mask:0xf\n"
"v_sub_f32 v140, v140, v157\n"
"v_mul_f32_dpp v206, v254, v206 quad_perm:[2,2,2,2] row_mask:0xf bank_mask:0xf\n"
"s_waitcnt lgkmcnt(3)\n"
"v_mfma_f32_16x16x32_bf16 v[184:187], v[248:251], a[4:7], v[184:187]\n"
"s_waitcnt lgkmcnt(2)\n"
"v_mul_f32_dpp v207, v254, v207 quad_perm:[3,3,3,3] row_mask:0xf bank_mask:0xf\n"
"v_mul_f32_dpp v208, v254, v208 quad_perm:[0,0,0,0] row_mask:0xf bank_mask:0xf\n"
"v_mfma_f32_16x16x32_bf16 v[184:187], a[224:227], a[8:11], v[184:187]\n"
"s_waitcnt lgkmcnt(1)\n"
"v_sub_f32 v141, v141, v157\n"
"v_mul_f32_dpp v209, v254, v209 quad_perm:[1,1,1,1] row_mask:0xf bank_mask:0xf\n"
"v_mfma_f32_16x16x32_bf16 v[184:187], v[236:239], a[12:15], v[184:187]\n"
"v_exp_f32 v140, v140\n"
"v_mfma_f32_16x16x32_bf16 v[204:207], a[48:51], a[160:163], v[204:207]\n"
"v_cvt_pk_bf16_f32 v134, v136, v137\n"
"v_mul_f32_dpp v210, v254, v210 quad_perm:[2,2,2,2] row_mask:0xf bank_mask:0xf\n"
"v_mfma_f32_16x16x32_bf16 v[204:207], a[52:55], a[164:167], v[204:207]\n"
"v_cvt_pk_bf16_f32 v135, v138, v139\n"
"v_sub_f32 v142, v142, v157\n"
"v_mfma_f32_16x16x32_bf16 v[204:207], a[56:59], a[168:171], v[204:207]\n"
"v_mul_f32_dpp v211, v254, v211 quad_perm:[3,3,3,3] row_mask:0xf bank_mask:0xf\n"
"v_mul_f32_dpp v212, v254, v212 quad_perm:[0,0,0,0] row_mask:0xf bank_mask:0xf\n"
"v_mfma_f32_16x16x32_bf16 v[204:207], a[60:63], a[172:175], v[204:207]\n"
"v_exp_f32 v141, v141\n"
"v_mfma_f32_16x16x32_bf16 v[208:211], a[60:63], a[188:191], v[208:211]\n"
"v_mul_f32_dpp v213, v254, v213 quad_perm:[1,1,1,1] row_mask:0xf bank_mask:0xf\n"
"v_sub_f32 v143, v143, v157\n"
"v_mfma_f32_16x16x32_bf16 v[208:211], a[56:59], a[184:187], v[208:211]\n"
"v_mul_f32_dpp v214, v254, v214 quad_perm:[2,2,2,2] row_mask:0xf bank_mask:0xf\n"
"v_mul_f32_dpp v215, v254, v215 quad_perm:[3,3,3,3] row_mask:0xf bank_mask:0xf\n"
"v_mfma_f32_16x16x32_bf16 v[208:211], a[52:55], a[180:183], v[208:211]\n"
"v_exp_f32 v142, v142\n"
"v_mfma_f32_16x16x32_bf16 v[208:211], a[48:51], a[176:179], v[208:211]\n"
"v_mul_f32_dpp v216, v254, v216 quad_perm:[0,0,0,0] row_mask:0xf bank_mask:0xf\n"
"v_mul_f32_dpp v217, v254, v217 quad_perm:[1,1,1,1] row_mask:0xf bank_mask:0xf\n"
"v_mfma_f32_16x16x32_bf16 v[212:215], a[48:51], a[192:195], v[212:215]\n"
"v_exp_f32 v143, v143\n"
"v_mfma_f32_16x16x32_bf16 v[212:215], a[52:55], a[196:199], v[212:215]\n"
"v_mul_f32_dpp v218, v254, v218 quad_perm:[2,2,2,2] row_mask:0xf bank_mask:0xf\n"
"v_sub_f32 v144, v144, v157\n"
"v_mfma_f32_16x16x32_bf16 v[212:215], a[56:59], a[200:203], v[212:215]\n"
"v_mul_f32_dpp v219, v254, v219 quad_perm:[3,3,3,3] row_mask:0xf bank_mask:0xf\n"
"v_sub_f32 v145, v145, v157\n"
"v_mfma_f32_16x16x32_bf16 v[212:215], a[60:63], a[204:207], v[212:215]\n"
"v_exp_f32 v144, v144\n"
"v_mfma_f32_16x16x32_bf16 v[216:219], a[60:63], a[220:223], v[216:219]\n"
"s_waitcnt vmcnt(17)\n"
"ds_read_b128 a[60:63], v154 offset:192\n"
"v_cvt_pk_bf16_f32 v140, v140, v141\n"
"v_mfma_f32_16x16x32_bf16 v[216:219], a[56:59], a[216:219], v[216:219]\n"
"ds_read_b128 a[56:59], v154 offset:128\n"
"v_cvt_pk_bf16_f32 v141, v142, v143\n"
"v_mfma_f32_16x16x32_bf16 v[216:219], a[52:55], a[212:215], v[216:219]\n"
"ds_read_b128 a[52:55], v154 offset:64\n"
"v_sub_f32 v146, v146, v157\n"
"v_mfma_f32_16x16x32_bf16 v[216:219], a[48:51], a[208:211], v[216:219]\n"
"ds_read_b128 a[48:51], v154 offset:0\n"
"v_sub_f32 v147, v147, v157\n"
"v_mfma_f32_16x16x32_bf16 v[252:255], a[252:255], v[124:127], v[148:151]\n"
"v_exp_f32 v145, v145\n"
"v_mfma_f32_16x16x32_bf16 v[252:255], a[252:255], v[132:135], v[252:255]\n"
"v_exp_f32 v146, v146\n"
"v_mfma_f32_16x16x32_bf16 v[220:223], v[132:135], v[52:55], v[220:223]\n"
"v_exp_f32 v147, v147\n"
"v_mfma_f32_16x16x32_bf16 v[228:231], v[132:135], v[76:79], v[228:231]\n"
"v_cvt_pk_bf16_f32 v142, v144, v145\n"
"v_cvt_pk_bf16_f32 v143, v146, v147\n"
"v_mfma_f32_16x16x32_bf16 v[192:195], v[236:239], a[28:31], 0\n"
"v_cvt_pk_bf16_f32 v0, v0, v1\n"
"v_cvt_pk_bf16_f32 v1, v2, v3\n"
"v_mfma_f32_16x16x32_bf16 v[192:195], a[224:227], a[24:27], v[192:195]\n"
"v_cvt_pk_bf16_f32 v9, v10, v11\n"
"v_cvt_pk_bf16_f32 v16, v16, v17\n"
"v_mfma_f32_16x16x32_bf16 v[192:195], v[248:251], a[20:23], v[192:195]\n"
"v_cvt_pk_bf16_f32 v17, v18, v19\n"
"v_cvt_pk_bf16_f32 v204, v204, v208\n"
"v_mfma_f32_16x16x32_bf16 v[192:195], v[244:247], a[16:19], v[192:195]\n"
"ds_read_b128 v[244:247], v153 offset:6144\n"
"v_cvt_pk_bf16_f32 v208, v205, v209\n"
"v_mfma_f32_16x16x32_bf16 v[252:255], a[252:255], v[140:143], v[252:255]\n"
"v_cvt_pk_bf16_f32 v205, v212, v216\n"
"v_cvt_pk_bf16_f32 v209, v213, v217\n"
"v_mfma_f32_16x16x32_bf16 v[220:223], v[140:143], v[56:59], v[220:223]\n"
"v_pk_max_i16 v204, v204, 0\n"
"v_pk_max_i16 v205, v205, 0\n"
"v_mfma_f32_16x16x32_bf16 v[224:227], v[140:143], v[68:71], v[224:227]\n"
"v_pk_max_i16 v208, v208, 0\n"
"v_pk_max_i16 v209, v209, 0\n"
"v_mfma_f32_16x16x32_bf16 v[224:227], v[132:135], v[64:67], v[224:227]\n"
"v_cvt_pk_bf16_f32 v206, v206, v210\n"
"v_cvt_pk_bf16_f32 v210, v207, v211\n"
"v_mfma_f32_16x16x32_bf16 v[224:227], v[124:127], v[60:63], v[224:227]\n"
"buffer_store_dwordx2 v[204:205], v162, s[84:87], s67 offen offset:0 sc0 sc1\n"
"v_mfma_f32_16x16x32_bf16 v[228:231], v[140:143], v[80:83], v[228:231]\n"
"buffer_store_dwordx2 v[208:209], v162, s[84:87], s67 offen offset:256 sc0 sc1\n"
"v_mfma_f32_16x16x32_bf16 v[232:235], v[140:143], v[92:95], v[232:235]\n"
"v_max3_f32 v157, v120, v121, v122\n"
"v_cvt_pk_bf16_f32 v207, v214, v218\n"
"v_mfma_f32_16x16x32_bf16 v[232:235], v[132:135], v[88:91], v[232:235]\n"
"s_add_u32 m0, s28, 0\n"
"v_max3_f32 v159, v123, v96, v97\n"
"v_mfma_f32_16x16x32_bf16 v[232:235], v[124:127], v[84:87], v[232:235]\n"
"s_waitcnt lgkmcnt(1)\n"
"v_cvt_pk_bf16_f32 v211, v215, v219\n"
"v_max3_f32 v157, v157, v98, v99\n"
"v_mfma_f32_16x16x32_bf16 v[24:27], a[96:99], a[48:51], 0\n"
"v_pk_max_i16 v206, v206, 0\n"
"v_max3_f32 v159, v159, v100, v101\n"
"v_mfma_f32_16x16x32_bf16 v[24:27], a[100:103], a[52:55], v[24:27]\n"
"v_pk_max_i16 v207, v207, 0\n"
"v_max3_f32 v157, v157, v102, v103\n"
"v_mfma_f32_16x16x32_bf16 v[24:27], a[104:107], a[56:59], v[24:27]\n"
"v_pk_max_i16 v210, v210, 0\n"
"v_max3_f32 v159, v159, v104, v105\n"
"v_mfma_f32_16x16x32_bf16 v[24:27], a[108:111], a[60:63], v[24:27]\n"
"buffer_store_dwordx2 v[206:207], v162, s[84:87], s67 offen offset:512 sc0 sc1\n"
"v_mfma_f32_16x16x32_bf16 v[176:179], a[112:115], a[48:51], 0\n"
"v_cvt_pk_bf16_f32 v2, v4, v5\n"
"v_cvt_pk_bf16_f32 v3, v6, v7\n"
"v_mfma_f32_16x16x32_bf16 v[176:179], a[116:119], a[52:55], v[176:179]\n"
"v_cvt_pk_bf16_f32 v10, v12, v13\n"
"v_cvt_pk_bf16_f32 v11, v14, v15\n"
"v_mfma_f32_16x16x32_bf16 v[176:179], a[120:123], a[56:59], v[176:179]\n"
"v_cvt_pk_bf16_f32 v18, v20, v21\n"
"v_cvt_pk_bf16_f32 v19, v22, v23\n"
"v_mfma_f32_16x16x32_bf16 v[176:179], a[124:127], a[60:63], v[176:179]\n"
"v_pk_max_i16 v211, v211, 0\n"
"v_max3_f32 v157, v157, v106, v107\n"
"v_mfma_f32_16x16x32_bf16 v[4:7], a[128:131], a[0:3], 0\n"
"v_max3_f32 v159, v159, v108, v109\n"
"v_max3_f32 v157, v157, v110, v111\n"
"v_mfma_f32_16x16x32_bf16 v[4:7], a[132:135], a[4:7], v[4:7]\n"
"v_max3_f32 v159, v159, v112, v113\n"
"v_max3_f32 v157, v157, v114, v115\n"
"v_mfma_f32_16x16x32_bf16 v[4:7], a[136:139], a[8:11], v[4:7]\n"
"v_max3_f32 v159, v159, v116, v117\n"
"v_max3_f32 v157, v157, v118, v119\n"
"v_mfma_f32_16x16x32_bf16 v[4:7], a[140:143], a[12:15], v[4:7]\n"
"buffer_store_dwordx2 v[210:211], v162, s[84:87], s67 offen offset:768 sc0 sc1\n"
"v_mfma_f32_16x16x32_bf16 v[12:15], a[140:143], a[28:31], 0\n"
"v_max_f32 v157, v157, v159\n"
"v_mov_b32 v161, v157\n"
"v_mfma_f32_16x16x32_bf16 v[12:15], a[136:139], a[24:27], v[12:15]\n"
"v_add_f32 v252, v252, v253\n"
"v_cvt_pk_bf16_f32 v24, v24, v25\n"
"v_mfma_f32_16x16x32_bf16 v[12:15], a[132:135], a[20:23], v[12:15]\n"
"v_permlane32_swap_b32 v157, v161\n"
"v_mfma_f32_16x16x32_bf16 v[12:15], a[128:131], a[16:19], v[12:15]\n"
"v_max_f32 v157, v157, v161\n"
"v_mov_b32 v161, v157\n"
"v_mfma_f32_16x16x32_bf16 v[20:23], a[128:131], a[32:35], 0\n"
"v_cvt_pk_bf16_f32 v4, v4, v5\n"
"v_cvt_pk_bf16_f32 v5, v6, v7\n"
"v_mfma_f32_16x16x32_bf16 v[20:23], a[132:135], a[36:39], v[20:23]\n"
"v_permlane16_swap_b32 v157, v161\n"
"v_mfma_f32_16x16x32_bf16 v[20:23], a[136:139], a[40:43], v[20:23]\n"
"v_max_f32 v157, v157, v161\n"
"v_sub_f32 v120, v120, v157\n"
"v_mfma_f32_16x16x32_bf16 v[20:23], a[140:143], a[44:47], v[20:23]\n"
"v_sub_f32 v121, v121, v157\n"
"v_sub_f32 v122, v122, v157\n"
"v_mfma_f32_16x16x32_bf16 v[28:31], a[128:131], a[48:51], 0\n"
"v_sub_f32 v123, v123, v157\n"
"v_cvt_pk_bf16_f32 v12, v12, v13\n"
"v_mfma_f32_16x16x32_bf16 v[28:31], a[132:135], a[52:55], v[28:31]\n"
"v_exp_f32 v161, v122\n"
"v_mfma_f32_16x16x32_bf16 v[28:31], a[136:139], a[56:59], v[28:31]\n"
"v_exp_f32 v120, v120\n"
"v_mfma_f32_16x16x32_bf16 v[28:31], a[140:143], a[60:63], v[28:31]\n"
"v_exp_f32 v121, v121\n"
"v_mfma_f32_16x16x32_bf16 v[200:203], v[248:251], a[36:39], v[200:203]\n"
"ds_read_b128 v[248:251], v153 offset:10240\n"
"v_cvt_pk_bf16_f32 v13, v14, v15\n"
"v_mfma_f32_16x16x32_bf16 v[200:203], a[224:227], a[40:43], v[200:203]\n"
"ds_read_b128 a[224:227], v153 offset:14336\n"
"v_cvt_pk_bf16_f32 v20, v20, v21\n"
"v_mfma_f32_16x16x32_bf16 v[200:203], v[236:239], a[44:47], v[200:203]\n"
"ds_read_b128 v[236:239], v153 offset:3072\n"
"s_add_u32 s24, s24, 13107200\n"
"s_addc_u32 s25, s25, 0\n"
"s_sub_u32 s26, s26, 13107200\n"
"s_cselect_b32 s26, 0, s26\n"
"v_exp_f32 v123, v123\n"
"v_cvt_pk_bf16_f32 v21, v22, v23\n"
"v_cvt_pk_bf16_f32 v122, v120, v121\n"
"v_add_f32 v120, v120, v121\n"
"v_add_f32 v121, v161, v123\n"
"v_cvt_pk_bf16_f32 v123, v161, v123\n"
"v_sub_f32 v96, v96, v157\n"
"v_sub_f32 v97, v97, v157\n"
"v_exp_f32 v96, v96\n"
"v_mfma_f32_16x16x4_4b_bf16 v[204:219], v[122:123], a[244:245], 0 cbsz:2 abid:0\n"
"buffer_load_dwordx4 v152, s[24:27], s40 offen offset:0 lds\n"
"v_sub_f32 v98, v98, v157\n"
"v_exp_f32 v97, v97\n"
"v_sub_f32 v99, v99, v157\n"
"v_exp_f32 v98, v98\n"
"v_exp_f32 v99, v99\n"
"v_sub_f32 v100, v100, v157\n"
"v_sub_f32 v101, v101, v157\n"
"v_exp_f32 v100, v100\n"
"v_cvt_pk_bf16_f32 v96, v96, v97\n"
"v_cvt_pk_bf16_f32 v97, v98, v99\n"
"v_sub_f32 v102, v102, v157\n"
"v_exp_f32 v101, v101\n"
"v_cvt_pk_bf16_f32 v6, v164, v165\n"
"v_cvt_pk_bf16_f32 v7, v166, v167\n"
"v_mfma_f32_16x16x32_bf16 v[164:167], a[144:147], a[48:51], 0\n"
"buffer_load_dwordx4 v152, s[24:27], s41 offen offset:1088 lds\n"
"ds_read_b64 a[244:245], v163 offset:0\n"
"v_mfma_f32_16x16x32_bf16 v[164:167], a[148:151], a[52:55], v[164:167]\n"
"buffer_load_dwordx4 v152, s[24:27], s42 offen offset:2176 lds\n"
"v_cvt_pk_bf16_f32 v14, v168, v169\n"
"v_mfma_f32_16x16x32_bf16 v[164:167], a[152:155], a[56:59], v[164:167]\n"
"buffer_load_dwordx4 v152, s[24:27], s43 offen offset:3264 lds\n"
"v_cvt_pk_bf16_f32 v15, v170, v171\n"
"v_mfma_f32_16x16x32_bf16 v[164:167], a[156:159], a[60:63], v[164:167]\n"
"v_cvt_pk_bf16_f32 v22, v172, v173\n"
"v_cvt_pk_bf16_f32 v23, v174, v175\n"
"v_sub_f32 v103, v103, v157\n"
"v_exp_f32 v102, v102\n"
"v_exp_f32 v103, v103\n"
"v_sub_f32 v104, v104, v157\n"
"v_sub_f32 v105, v105, v157\n"
"v_exp_f32 v104, v104\n"
"v_cvt_pk_bf16_f32 v98, v100, v101\n"
"v_cvt_pk_bf16_f32 v99, v102, v103\n"
"v_sub_f32 v106, v106, v157\n"
"v_exp_f32 v105, v105\n"
"v_mfma_f32_16x16x32_bf16 v[204:207], v[96:99], v[48:51], v[204:207]\n"
"v_sub_f32 v107, v107, v157\n"
"v_sub_f32 v108, v108, v157\n"
"v_mfma_f32_16x16x32_bf16 v[212:215], v[96:99], v[72:75], v[212:215]\n"
"v_exp_f32 v106, v106\n"
"v_exp_f32 v107, v107\n"
"v_sub_f32 v109, v109, v157\n"
"v_exp_f32 v108, v108\n"
"v_cvt_pk_bf16_f32 v104, v104, v105\n"
"v_cvt_pk_bf16_f32 v105, v106, v107\n"
"v_sub_f32 v110, v110, v157\n"
"v_exp_f32 v109, v109\n"
"v_sub_f32 v111, v111, v157\n"
"v_rcp_f32 v253, v252\n"
"v_exp_f32 v110, v110\n"
"v_exp_f32 v111, v111\n"
"v_sub_f32 v112, v112, v157\n"
"v_sub_f32 v113, v113, v157\n"
"v_exp_f32 v112, v112\n"
"v_cvt_pk_bf16_f32 v106, v108, v109\n"
"v_cvt_pk_bf16_f32 v107, v110, v111\n"
"v_sub_f32 v114, v114, v157\n"
"v_exp_f32 v113, v113\n"
"v_mfma_f32_16x16x32_bf16 v[204:207], v[104:107], v[52:55], v[204:207]\n"
"ds_bpermute_b32 v254, v160, v253 offset:0\n"
"v_mfma_f32_16x16x32_bf16 v[212:215], v[104:107], v[76:79], v[212:215]\n"
"v_sub_f32 v115, v115, v157\n"
"v_exp_f32 v114, v114\n"
"v_exp_f32 v115, v115\n"
"v_sub_f32 v116, v116, v157\n"
"v_sub_f32 v117, v117, v157\n"
"v_exp_f32 v116, v116\n"
"v_cvt_pk_bf16_f32 v112, v112, v113\n"
"v_cvt_pk_bf16_f32 v113, v114, v115\n"
"v_sub_f32 v118, v118, v157\n"
"v_exp_f32 v117, v117\n"
"v_sub_f32 v119, v119, v157\n"
"v_exp_f32 v118, v118\n"
"v_exp_f32 v119, v119\n"
"v_cvt_pk_bf16_f32 v114, v116, v117\n"
"v_cvt_pk_bf16_f32 v25, v26, v27\n"
"v_cvt_pk_bf16_f32 v115, v118, v119\n"
"v_cvt_pk_bf16_f32 v180, v180, v181\n"
"v_cvt_pk_bf16_f32 v26, v176, v177\n"
"v_cvt_pk_bf16_f32 v181, v182, v183\n"
"v_mfma_f32_16x16x32_bf16 v[204:207], v[112:115], v[56:59], v[204:207]\n"
"v_cvt_pk_bf16_f32 v27, v178, v179\n"
"v_cvt_pk_bf16_f32 v188, v188, v189\n"
"v_mfma_f32_16x16x32_bf16 v[208:211], v[112:115], v[68:71], v[208:211]\n"
"v_cvt_pk_bf16_f32 v189, v190, v191\n"
"v_cvt_pk_bf16_f32 v196, v196, v197\n"
"v_mfma_f32_16x16x32_bf16 v[208:211], v[104:107], v[64:67], v[208:211]\n"
"v_cvt_pk_bf16_f32 v197, v198, v199\n"
"v_cvt_pk_bf16_f32 v28, v28, v29\n"
"v_mfma_f32_16x16x32_bf16 v[208:211], v[96:99], v[60:63], v[208:211]\n"
"s_waitcnt lgkmcnt(0)\n"
"v_mul_f32_dpp v220, v254, v220 quad_perm:[0,0,0,0] row_mask:0xf bank_mask:0xf\n"
"v_mul_f32_dpp v221, v254, v221 quad_perm:[1,1,1,1] row_mask:0xf bank_mask:0xf\n"
"v_mfma_f32_16x16x32_bf16 v[212:215], v[112:115], v[80:83], v[212:215]\n"
"v_mul_f32_dpp v222, v254, v222 quad_perm:[2,2,2,2] row_mask:0xf bank_mask:0xf\n"
"v_mul_f32_dpp v223, v254, v223 quad_perm:[3,3,3,3] row_mask:0xf bank_mask:0xf\n"
"v_mfma_f32_16x16x32_bf16 v[216:219], v[112:115], v[92:95], v[216:219]\n"
"v_mul_f32_dpp v224, v254, v224 quad_perm:[0,0,0,0] row_mask:0xf bank_mask:0xf\n"
"v_mul_f32_dpp v225, v254, v225 quad_perm:[1,1,1,1] row_mask:0xf bank_mask:0xf\n"
"v_mfma_f32_16x16x32_bf16 v[216:219], v[104:107], v[88:91], v[216:219]\n"
"v_mul_f32_dpp v226, v254, v226 quad_perm:[2,2,2,2] row_mask:0xf bank_mask:0xf\n"
"v_mul_f32_dpp v227, v254, v227 quad_perm:[3,3,3,3] row_mask:0xf bank_mask:0xf\n"
"v_mfma_f32_16x16x32_bf16 v[216:219], v[96:99], v[84:87], v[216:219]\n"
"v_mul_f32_dpp v228, v254, v228 quad_perm:[0,0,0,0] row_mask:0xf bank_mask:0xf\n"
"v_mul_f32_dpp v229, v254, v229 quad_perm:[1,1,1,1] row_mask:0xf bank_mask:0xf\n"
"v_mfma_f32_16x16x32_bf16 v[220:223], a[64:67], a[160:163], v[220:223]\n"
"v_mul_f32_dpp v230, v254, v230 quad_perm:[2,2,2,2] row_mask:0xf bank_mask:0xf\n"
"v_mul_f32_dpp v231, v254, v231 quad_perm:[3,3,3,3] row_mask:0xf bank_mask:0xf\n"
"v_mfma_f32_16x16x32_bf16 v[220:223], a[68:71], a[164:167], v[220:223]\n"
"v_mul_f32_dpp v232, v254, v232 quad_perm:[0,0,0,0] row_mask:0xf bank_mask:0xf\n"
"v_mul_f32_dpp v233, v254, v233 quad_perm:[1,1,1,1] row_mask:0xf bank_mask:0xf\n"
"v_mfma_f32_16x16x32_bf16 v[220:223], a[72:75], a[168:171], v[220:223]\n"
"v_mul_f32_dpp v234, v254, v234 quad_perm:[2,2,2,2] row_mask:0xf bank_mask:0xf\n"
"v_mul_f32_dpp v235, v254, v235 quad_perm:[3,3,3,3] row_mask:0xf bank_mask:0xf\n"
"v_mfma_f32_16x16x32_bf16 v[220:223], a[76:79], a[172:175], v[220:223]\n"
"v_cvt_pk_bf16_f32 v182, v184, v185\n"
"v_cvt_pk_bf16_f32 v29, v30, v31\n"
"v_mfma_f32_16x16x32_bf16 v[224:227], a[76:79], a[188:191], v[224:227]\n"
"v_cvt_pk_bf16_f32 v183, v186, v187\n"
"v_cvt_pk_bf16_f32 v190, v192, v193\n"
"v_mfma_f32_16x16x32_bf16 v[224:227], a[72:75], a[184:187], v[224:227]\n"
"v_cvt_pk_bf16_f32 v191, v194, v195\n"
"v_cvt_pk_bf16_f32 v198, v200, v201\n"
"v_mfma_f32_16x16x32_bf16 v[224:227], a[68:71], a[180:183], v[224:227]\n"
"v_cvt_pk_bf16_f32 v199, v202, v203\n"
"v_cvt_pk_bf16_f32 v30, v164, v165\n"
"v_mfma_f32_16x16x32_bf16 v[224:227], a[64:67], a[176:179], v[224:227]\n"
"v_cvt_pk_bf16_f32 v31, v166, v167\n"
"v_add_u32 v156, 512, v156\n"
"v_mfma_f32_16x16x32_bf16 v[228:231], a[64:67], a[192:195], v[228:231]\n"
"v_mfma_f32_16x16x32_bf16 v[228:231], a[68:71], a[196:199], v[228:231]\n"
"v_mfma_f32_16x16x32_bf16 v[228:231], a[72:75], a[200:203], v[228:231]\n"
"v_cvt_pk_bf16_f32 v220, v220, v224\n"
"v_cvt_pk_bf16_f32 v224, v221, v225\n"
"v_mfma_f32_16x16x32_bf16 v[228:231], a[76:79], a[204:207], v[228:231]\n"
"v_pk_max_i16 v220, v220, 0\n"
"v_pk_max_i16 v224, v224, 0\n"
"v_mfma_f32_16x16x32_bf16 v[232:235], a[76:79], a[220:223], v[232:235]\n"
"s_waitcnt vmcnt(18)\n"
"ds_read_b128 a[76:79], v154 offset:4544\n"
"v_cvt_pk_bf16_f32 v222, v222, v226\n"
"v_mfma_f32_16x16x32_bf16 v[232:235], a[72:75], a[216:219], v[232:235]\n"
"ds_read_b128 a[72:75], v154 offset:4480\n"
"v_cvt_pk_bf16_f32 v226, v223, v227\n"
"v_mfma_f32_16x16x32_bf16 v[232:235], a[68:71], a[212:215], v[232:235]\n"
"ds_read_b128 a[68:71], v154 offset:4416\n"
"v_pk_max_i16 v222, v222, 0\n"
"v_mfma_f32_16x16x32_bf16 v[232:235], a[64:67], a[208:211], v[232:235]\n"
"ds_read_b128 a[64:67], v154 offset:4352\n"
"v_pk_max_i16 v226, v226, 0\n"
"v_mfma_f32_16x16x32_bf16 v[252:255], a[252:255], v[96:99], v[120:123]\n"
"v_mfma_f32_16x16x32_bf16 v[252:255], a[252:255], v[104:107], v[252:255]\n"
"v_mfma_f32_16x16x32_bf16 v[252:255], a[252:255], v[112:115], v[252:255]\n"
"v_cvt_pk_bf16_f32 v221, v228, v232\n"
"v_cvt_pk_bf16_f32 v225, v229, v233\n"
"v_mfma_f32_16x16x32_bf16 v[184:187], v[240:243], a[0:3], 0\n"
"v_pk_max_i16 v221, v221, 0\n"
"v_pk_max_i16 v225, v225, 0\n"
"v_mfma_f32_16x16x32_bf16 v[184:187], v[244:247], a[4:7], v[184:187]\n"
"v_cvt_pk_bf16_f32 v223, v230, v234\n"
"v_cvt_pk_bf16_f32 v227, v231, v235\n"
"v_mfma_f32_16x16x32_bf16 v[184:187], v[248:251], a[8:11], v[184:187]\n"
"v_pk_max_i16 v223, v223, 0\n"
"v_pk_max_i16 v227, v227, 0\n"
"v_mfma_f32_16x16x32_bf16 v[184:187], a[224:227], a[12:15], v[184:187]\n"
"s_waitcnt lgkmcnt(3)\n"
"buffer_store_dwordx2 v[220:221], v162, s[84:87], s68 offen offset:0 sc0 sc1\n"
"v_mfma_f32_16x16x32_bf16 v[32:35], a[108:111], a[76:79], 0\n"
"s_waitcnt lgkmcnt(2)\n"
"v_add_f32 v252, v252, v253\n"
"v_mfma_f32_16x16x32_bf16 v[32:35], a[104:107], a[72:75], v[32:35]\n"
"s_waitcnt lgkmcnt(1)\n"
"v_rcp_f32 v253, v252\n"
"v_mfma_f32_16x16x32_bf16 v[32:35], a[100:103], a[68:71], v[32:35]\n"
"s_waitcnt lgkmcnt(0)\n"
"v_cvt_pk_bf16_f32 v184, v184, v185\n"
"v_cvt_pk_bf16_f32 v185, v186, v187\n"
"v_mfma_f32_16x16x32_bf16 v[32:35], a[96:99], a[64:67], v[32:35]\n"
"buffer_store_dwordx2 v[224:225], v162, s[84:87], s68 offen offset:256 sc0 sc1\n"
"v_mfma_f32_16x16x32_bf16 v[172:175], a[124:127], a[76:79], 0\n"
"ds_bpermute_b32 v254, v160, v253 offset:0\n"
"v_mfma_f32_16x16x32_bf16 v[172:175], a[120:123], a[72:75], v[172:175]\n"
"v_mfma_f32_16x16x32_bf16 v[172:175], a[116:119], a[68:71], v[172:175]\n"
"v_cvt_pk_bf16_f32 v32, v32, v33\n"
"v_cvt_pk_bf16_f32 v33, v34, v35\n"
"v_mfma_f32_16x16x32_bf16 v[172:175], a[112:115], a[64:67], v[172:175]\n"
"buffer_store_dwordx2 v[222:223], v162, s[84:87], s68 offen offset:512 sc0 sc1\n"
"v_mfma_f32_16x16x32_bf16 v[36:39], a[140:143], a[76:79], 0\n"
"s_add_u32 m0, s28, 4352\n"
"v_mfma_f32_16x16x32_bf16 v[36:39], a[136:139], a[72:75], v[36:39]\n"
"buffer_load_dwordx4 v152, s[24:27], s44 offen offset:0 lds\n"
"v_mfma_f32_16x16x32_bf16 v[36:39], a[132:135], a[68:71], v[36:39]\n"
"buffer_load_dwordx4 v152, s[24:27], s45 offen offset:1088 lds\n"
"v_cvt_pk_bf16_f32 v34, v172, v173\n"
"v_mfma_f32_16x16x32_bf16 v[36:39], a[128:131], a[64:67], v[36:39]\n"
"buffer_load_dwordx4 v152, s[24:27], s46 offen offset:2176 lds\n"
"v_cvt_pk_bf16_f32 v35, v174, v175\n"
"v_mfma_f32_16x16x32_bf16 v[192:195], a[224:227], a[28:31], 0\n"
"buffer_load_dwordx4 v152, s[24:27], s47 offen offset:3264 lds\n"
"v_mfma_f32_16x16x32_bf16 v[192:195], v[248:251], a[24:27], v[192:195]\n"
"v_mfma_f32_16x16x32_bf16 v[192:195], v[244:247], a[20:23], v[192:195]\n"
"v_cvt_pk_bf16_f32 v36, v36, v37\n"
"v_cvt_pk_bf16_f32 v37, v38, v39\n"
"v_mfma_f32_16x16x32_bf16 v[192:195], v[240:243], a[16:19], v[192:195]\n"
"buffer_store_dwordx2 v[226:227], v162, s[84:87], s68 offen offset:768 sc0 sc1\n"
"v_mfma_f32_16x16x32_bf16 v[168:171], a[156:159], a[76:79], 0\n"
"s_waitcnt lgkmcnt(0)\n"
"v_mul_f32_dpp v204, v254, v204 quad_perm:[0,0,0,0] row_mask:0xf bank_mask:0xf\n"
"v_mul_f32_dpp v205, v254, v205 quad_perm:[1,1,1,1] row_mask:0xf bank_mask:0xf\n"
"v_mfma_f32_16x16x32_bf16 v[168:171], a[152:155], a[72:75], v[168:171]\n"
"v_mul_f32_dpp v206, v254, v206 quad_perm:[2,2,2,2] row_mask:0xf bank_mask:0xf\n"
"v_mul_f32_dpp v207, v254, v207 quad_perm:[3,3,3,3] row_mask:0xf bank_mask:0xf\n"
"v_mfma_f32_16x16x32_bf16 v[168:171], a[148:151], a[68:71], v[168:171]\n"
"v_cvt_pk_bf16_f32 v192, v192, v193\n"
"v_cvt_pk_bf16_f32 v193, v194, v195\n"
"v_mfma_f32_16x16x32_bf16 v[168:171], a[144:147], a[64:67], v[168:171]\n"
"v_mul_f32_dpp v208, v254, v208 quad_perm:[0,0,0,0] row_mask:0xf bank_mask:0xf\n"
"v_mul_f32_dpp v209, v254, v209 quad_perm:[1,1,1,1] row_mask:0xf bank_mask:0xf\n"
"v_mfma_f32_16x16x32_bf16 v[200:203], v[240:243], a[32:35], 0\n"
"ds_read_b128 v[240:243], v153 offset:7168\n"
"v_mul_f32_dpp v210, v254, v210 quad_perm:[2,2,2,2] row_mask:0xf bank_mask:0xf\n"
"v_mfma_f32_16x16x32_bf16 v[200:203], v[244:247], a[36:39], v[200:203]\n"
"ds_read_b128 v[244:247], v153 offset:11264\n"
"v_mul_f32_dpp v211, v254, v211 quad_perm:[3,3,3,3] row_mask:0xf bank_mask:0xf\n"
"v_mfma_f32_16x16x32_bf16 v[200:203], v[248:251], a[40:43], v[200:203]\n"
"ds_read_b128 v[248:251], v153 offset:15360\n"
"v_cvt_pk_bf16_f32 v38, v168, v169\n"
"v_mfma_f32_16x16x32_bf16 v[200:203], a[224:227], a[44:47], v[200:203]\n"
"ds_read_b128 a[224:227], v153 offset:18496\n"
"v_cvt_pk_bf16_f32 v39, v170, v171\n"
"v_mfma_f32_16x16x32_bf16 v[164:167], v[236:239], a[0:3], 0\n"
"v_mul_f32_dpp v212, v254, v212 quad_perm:[0,0,0,0] row_mask:0xf bank_mask:0xf\n"
"v_mul_f32_dpp v213, v254, v213 quad_perm:[1,1,1,1] row_mask:0xf bank_mask:0xf\n"
"v_mfma_f32_16x16x32_bf16 v[172:175], v[236:239], a[32:35], 0\n"
"v_mul_f32_dpp v214, v254, v214 quad_perm:[2,2,2,2] row_mask:0xf bank_mask:0xf\n"
"v_mul_f32_dpp v215, v254, v215 quad_perm:[3,3,3,3] row_mask:0xf bank_mask:0xf\n"
"v_mfma_f32_16x16x32_bf16 v[204:207], a[80:83], a[160:163], v[204:207]\n"
"v_cvt_pk_bf16_f32 v200, v200, v201\n"
"v_cvt_pk_bf16_f32 v201, v202, v203\n"
"v_mfma_f32_16x16x32_bf16 v[204:207], a[84:87], a[164:167], v[204:207]\n"
"v_mul_f32_dpp v216, v254, v216 quad_perm:[0,0,0,0] row_mask:0xf bank_mask:0xf\n"
"v_mul_f32_dpp v217, v254, v217 quad_perm:[1,1,1,1] row_mask:0xf bank_mask:0xf\n"
"v_mfma_f32_16x16x32_bf16 v[204:207], a[88:91], a[168:171], v[204:207]\n"
"v_mul_f32_dpp v218, v254, v218 quad_perm:[2,2,2,2] row_mask:0xf bank_mask:0xf\n"
"v_mul_f32_dpp v219, v254, v219 quad_perm:[3,3,3,3] row_mask:0xf bank_mask:0xf\n"
"v_mfma_f32_16x16x32_bf16 v[204:207], a[92:95], a[172:175], v[204:207]\n"
"v_mfma_f32_16x16x32_bf16 v[208:211], a[92:95], a[188:191], v[208:211]\n"
"v_mfma_f32_16x16x32_bf16 v[208:211], a[88:91], a[184:187], v[208:211]\n"
"v_mfma_f32_16x16x32_bf16 v[208:211], a[84:87], a[180:183], v[208:211]\n"
"v_mfma_f32_16x16x32_bf16 v[208:211], a[80:83], a[176:179], v[208:211]\n"
"s_waitcnt lgkmcnt(3)\n"
"v_mfma_f32_16x16x32_bf16 v[164:167], v[240:243], a[4:7], v[164:167]\n"
"s_waitcnt lgkmcnt(2)\n"
"v_mfma_f32_16x16x32_bf16 v[164:167], v[244:247], a[8:11], v[164:167]\n"
"s_waitcnt lgkmcnt(1)\n"
"v_mfma_f32_16x16x32_bf16 v[164:167], v[248:251], a[12:15], v[164:167]\n"
"v_cvt_pk_bf16_f32 v204, v204, v208\n"
"v_cvt_pk_bf16_f32 v208, v205, v209\n"
"v_mfma_f32_16x16x32_bf16 v[168:171], v[248:251], a[28:31], 0\n"
"v_pk_max_i16 v204, v204, 0\n"
"v_pk_max_i16 v208, v208, 0\n"
"v_mfma_f32_16x16x32_bf16 v[168:171], v[244:247], a[24:27], v[168:171]\n"
"v_cvt_pk_bf16_f32 v206, v206, v210\n"
"v_cvt_pk_bf16_f32 v210, v207, v211\n"
"v_mfma_f32_16x16x32_bf16 v[168:171], v[240:243], a[20:23], v[168:171]\n"
"v_cvt_pk_bf16_f32 v186, v164, v165\n"
"v_cvt_pk_bf16_f32 v187, v166, v167\n"
"v_mfma_f32_16x16x32_bf16 v[168:171], v[236:239], a[16:19], v[168:171]\n"
"ds_read_b128 v[236:239], v153 offset:16384\n"
"v_pk_max_i16 v206, v206, 0\n"
"v_mfma_f32_16x16x32_bf16 v[172:175], v[240:243], a[36:39], v[172:175]\n"
"ds_read_b128 v[240:243], v153 offset:20576\n"
"v_pk_max_i16 v210, v210, 0\n"
"v_mfma_f32_16x16x32_bf16 v[172:175], v[244:247], a[40:43], v[172:175]\n"
"ds_read_b128 v[244:247], v153 offset:24768\n"
"v_mfma_f32_16x16x32_bf16 v[172:175], v[248:251], a[44:47], v[172:175]\n"
"ds_read_b128 v[248:251], v153 offset:28960\n"
"v_cvt_pk_bf16_f32 v194, v168, v169\n"
"v_mfma_f32_16x16x32_bf16 v[212:215], a[80:83], a[192:195], v[212:215]\n"
"v_cvt_pk_bf16_f32 v195, v170, v171\n"
"v_mfma_f32_16x16x32_bf16 v[212:215], a[84:87], a[196:199], v[212:215]\n"
"v_mfma_f32_16x16x32_bf16 v[212:215], a[88:91], a[200:203], v[212:215]\n"
"v_cvt_pk_bf16_f32 v202, v172, v173\n"
"v_cvt_pk_bf16_f32 v203, v174, v175\n"
"v_mfma_f32_16x16x32_bf16 v[212:215], a[92:95], a[204:207], v[212:215]\n"
"v_mfma_f32_16x16x32_bf16 v[216:219], a[92:95], a[220:223], v[216:219]\n"
"s_waitcnt vmcnt(21)\n"
"ds_read_b128 a[92:95], v154 offset:8896\n"
"v_mfma_f32_16x16x32_bf16 v[216:219], a[88:91], a[216:219], v[216:219]\n"
"ds_read_b128 a[88:91], v154 offset:8832\n"
"v_mfma_f32_16x16x32_bf16 v[216:219], a[84:87], a[212:215], v[216:219]\n"
"ds_read_b128 a[84:87], v154 offset:8768\n"
"v_mfma_f32_16x16x32_bf16 v[216:219], a[80:83], a[208:211], v[216:219]\n"
"ds_read_b128 a[80:83], v154 offset:8704\n"
"v_mfma_f32_16x16x32_bf16 v[120:123], a[228:231], v[180:183], 0\n"
"ds_read_b128 a[228:231], v158 offset:0\n"
"v_mfma_f32_16x16x32_bf16 v[120:123], a[232:235], v[184:187], v[120:123]\n"
"ds_read_b128 a[232:235], v158 offset:2048\n"
"v_mfma_f32_16x16x32_bf16 v[96:99], v[0:3], v[180:183], 0\n"
"v_cvt_pk_bf16_f32 v205, v212, v216\n"
"v_cvt_pk_bf16_f32 v209, v213, v217\n"
"v_mfma_f32_16x16x32_bf16 v[96:99], v[4:7], v[184:187], v[96:99]\n"
"v_pk_max_i16 v205, v205, 0\n"
"v_pk_max_i16 v209, v209, 0\n"
"v_mfma_f32_16x16x32_bf16 v[100:103], v[12:15], v[184:187], 0\n"
"v_cvt_pk_bf16_f32 v207, v214, v218\n"
"v_cvt_pk_bf16_f32 v211, v215, v219\n"
"v_mfma_f32_16x16x32_bf16 v[100:103], v[8:11], v[180:183], v[100:103]\n"
"buffer_store_dwordx2 v[204:205], v162, s[84:87], s69 offen offset:0 sc0 sc1\n"
"v_mfma_f32_16x16x32_bf16 v[104:107], v[16:19], v[180:183], 0\n"
"v_pk_max_i16 v207, v207, 0\n"
"v_pk_max_i16 v211, v211, 0\n"
"v_mfma_f32_16x16x32_bf16 v[104:107], v[20:23], v[184:187], v[104:107]\n"
"buffer_store_dwordx2 v[208:209], v162, s[84:87], s69 offen offset:256 sc0 sc1\n"
"v_mfma_f32_16x16x32_bf16 v[108:111], v[28:31], v[184:187], 0\n"
"v_max3_f32 v157, v120, v121, v122\n"
"v_max3_f32 v159, v123, v96, v97\n"
"v_mfma_f32_16x16x32_bf16 v[108:111], v[24:27], v[180:183], v[108:111]\n"
"s_waitcnt lgkmcnt(2)\n"
"buffer_store_dwordx2 v[206:207], v162, s[84:87], s69 offen offset:512 sc0 sc1\n"
"v_mfma_f32_16x16x32_bf16 v[40:43], a[96:99], a[80:83], 0\n"
"v_max3_f32 v157, v157, v98, v99\n"
"v_max3_f32 v159, v159, v100, v101\n"
"v_mfma_f32_16x16x32_bf16 v[40:43], a[100:103], a[84:87], v[40:43]\n"
"v_max3_f32 v157, v157, v102, v103\n"
"v_max3_f32 v159, v159, v104, v105\n"
"v_mfma_f32_16x16x32_bf16 v[40:43], a[104:107], a[88:91], v[40:43]\n"
"v_max3_f32 v157, v157, v106, v107\n"
"v_max3_f32 v159, v159, v108, v109\n"
"v_mfma_f32_16x16x32_bf16 v[40:43], a[108:111], a[92:95], v[40:43]\n"
"buffer_store_dwordx2 v[210:211], v162, s[84:87], s69 offen offset:768 sc0 sc1\n"
"s_branch .Lp2s%=\n"
".Leps%=:\n"
"s_nop 0\n"
"v_mfma_f32_16x16x32_bf16 v[204:207], a[32:35], a[160:163], v[204:207]\n"
"v_sub_f32 v129, v129, v157\n"
"v_sub_f32 v130, v130, v157\n"
"v_mfma_f32_16x16x32_bf16 v[204:207], a[36:39], a[164:167], v[204:207]\n"
"v_sub_f32 v131, v131, v157\n"
"v_sub_f32 v132, v132, v157\n"
"v_mfma_f32_16x16x32_bf16 v[204:207], a[40:43], a[168:171], v[204:207]\n"
"v_exp_f32 v128, v128\n"
"v_mfma_f32_16x16x32_bf16 v[204:207], a[44:47], a[172:175], v[204:207]\n"
"v_exp_f32 v129, v129\n"
"v_exp_f32 v130, v130\n"
"v_exp_f32 v131, v131\n"
"v_sub_f32 v133, v133, v157\n"
"v_cvt_pk_bf16_f32 v124, v124, v125\n"
"v_exp_f32 v132, v132\n"
"v_sub_f32 v134, v134, v157\n"
"v_exp_f32 v133, v133\n"
"v_sub_f32 v135, v135, v157\n"
"v_cvt_pk_bf16_f32 v125, v126, v127\n"
"v_exp_f32 v134, v134\n"
"v_exp_f32 v135, v135\n"
"v_sub_f32 v136, v136, v157\n"
"v_sub_f32 v137, v137, v157\n"
"v_sub_f32 v138, v138, v157\n"
"v_sub_f32 v139, v139, v157\n"
"v_cvt_pk_bf16_f32 v126, v128, v129\n"
"v_cvt_pk_bf16_f32 v127, v130, v131\n"
"v_mfma_f32_16x16x32_bf16 v[128:131], v[12:15], v[192:195], 0\n"
"v_exp_f32 v136, v136\n"
"v_mfma_f32_16x16x32_bf16 v[128:131], v[8:11], v[188:191], v[128:131]\n"
"v_exp_f32 v137, v137\n"
"v_mfma_f32_16x16x32_bf16 v[164:167], v[124:127], v[48:51], v[164:167]\n"
"v_exp_f32 v138, v138\n"
"v_mfma_f32_16x16x32_bf16 v[172:175], v[124:127], v[72:75], v[172:175]\n"
"v_exp_f32 v139, v139\n"
"v_sub_f32 v140, v140, v157\n"
"v_sub_f32 v141, v141, v157\n"
"v_cvt_pk_bf16_f32 v132, v132, v133\n"
"v_exp_f32 v140, v140\n"
"v_sub_f32 v142, v142, v157\n"
"v_exp_f32 v141, v141\n"
"v_sub_f32 v143, v143, v157\n"
"v_mul_f32_dpp v208, v254, v208 quad_perm:[0,0,0,0] row_mask:0xf bank_mask:0xf\n"
"v_mul_f32_dpp v209, v254, v209 quad_perm:[1,1,1,1] row_mask:0xf bank_mask:0xf\n"
"v_mul_f32_dpp v210, v254, v210 quad_perm:[2,2,2,2] row_mask:0xf bank_mask:0xf\n"
"v_mul_f32_dpp v211, v254, v211 quad_perm:[3,3,3,3] row_mask:0xf bank_mask:0xf\n"
"v_mul_f32_dpp v212, v254, v212 quad_perm:[0,0,0,0] row_mask:0xf bank_mask:0xf\n"
"v_mul_f32_dpp v213, v254, v213 quad_perm:[1,1,1,1] row_mask:0xf bank_mask:0xf\n"
"v_mul_f32_dpp v214, v254, v214 quad_perm:[2,2,2,2] row_mask:0xf bank_mask:0xf\n"
"v_mfma_f32_16x16x32_bf16 v[208:211], a[44:47], a[188:191], v[208:211]\n"
"v_mul_f32_dpp v215, v254, v215 quad_perm:[3,3,3,3] row_mask:0xf bank_mask:0xf\n"
"v_mul_f32_dpp v216, v254, v216 quad_perm:[0,0,0,0] row_mask:0xf bank_mask:0xf\n"
"v_mfma_f32_16x16x32_bf16 v[208:211], a[40:43], a[184:187], v[208:211]\n"
"v_mul_f32_dpp v217, v254, v217 quad_perm:[1,1,1,1] row_mask:0xf bank_mask:0xf\n"
"v_mul_f32_dpp v218, v254, v218 quad_perm:[2,2,2,2] row_mask:0xf bank_mask:0xf\n"
"v_mfma_f32_16x16x32_bf16 v[208:211], a[36:39], a[180:183], v[208:211]\n"
"v_mul_f32_dpp v219, v254, v219 quad_perm:[3,3,3,3] row_mask:0xf bank_mask:0xf\n"
"v_cvt_pk_bf16_f32 v133, v134, v135\n"
"v_mfma_f32_16x16x32_bf16 v[208:211], a[32:35], a[176:179], v[208:211]\n"
"v_exp_f32 v142, v142\n"
"v_mfma_f32_16x16x32_bf16 v[252:255], a[252:255], v[124:127], v[148:151]\n"
"v_exp_f32 v143, v143\n"
"v_mfma_f32_16x16x32_bf16 v[212:215], a[32:35], a[192:195], v[212:215]\n"
"v_sub_f32 v144, v144, v157\n"
"v_sub_f32 v145, v145, v157\n"
"v_mfma_f32_16x16x32_bf16 v[212:215], a[36:39], a[196:199], v[212:215]\n"
"v_sub_f32 v146, v146, v157\n"
"v_sub_f32 v147, v147, v157\n"
"v_mfma_f32_16x16x32_bf16 v[212:215], a[40:43], a[200:203], v[212:215]\n"
"v_cvt_pk_bf16_f32 v134, v136, v137\n"
"v_cvt_pk_bf16_f32 v135, v138, v139\n"
"v_mfma_f32_16x16x32_bf16 v[212:215], a[44:47], a[204:207], v[212:215]\n"
"v_exp_f32 v144, v144\n"
"v_mfma_f32_16x16x32_bf16 v[252:255], a[252:255], v[132:135], v[252:255]\n"
"v_exp_f32 v145, v145\n"
"v_mfma_f32_16x16x32_bf16 v[164:167], v[132:135], v[52:55], v[164:167]\n"
"v_exp_f32 v146, v146\n"
"v_mfma_f32_16x16x32_bf16 v[172:175], v[132:135], v[76:79], v[172:175]\n"
"v_exp_f32 v147, v147\n"
"v_mfma_f32_16x16x32_bf16 v[216:219], a[44:47], a[220:223], v[216:219]\n"
"v_cvt_pk_bf16_f32 v140, v140, v141\n"
"v_cvt_pk_bf16_f32 v141, v142, v143\n"
"v_mfma_f32_16x16x32_bf16 v[216:219], a[40:43], a[216:219], v[216:219]\n"
"v_cvt_pk_bf16_f32 v142, v144, v145\n"
"v_cvt_pk_bf16_f32 v143, v146, v147\n"
"v_mfma_f32_16x16x32_bf16 v[216:219], a[36:39], a[212:215], v[216:219]\n"
"v_max3_f32 v157, v120, v121, v122\n"
"v_max3_f32 v159, v123, v96, v97\n"
"v_mfma_f32_16x16x32_bf16 v[216:219], a[32:35], a[208:211], v[216:219]\n"
"v_max3_f32 v157, v157, v98, v99\n"
"v_max3_f32 v159, v159, v100, v101\n"
"v_mfma_f32_16x16x32_bf16 v[252:255], a[252:255], v[140:143], v[252:255]\n"
"v_max3_f32 v157, v157, v102, v103\n"
"v_max3_f32 v159, v159, v104, v105\n"
"v_mfma_f32_16x16x32_bf16 v[168:171], v[140:143], v[68:71], v[168:171]\n"
"v_max3_f32 v157, v157, v106, v107\n"
"v_max3_f32 v159, v159, v108, v109\n"
"v_mfma_f32_16x16x32_bf16 v[168:171], v[132:135], v[64:67], v[168:171]\n"
"v_max3_f32 v157, v157, v110, v111\n"
"v_max3_f32 v159, v159, v112, v113\n"
"v_mfma_f32_16x16x32_bf16 v[168:171], v[124:127], v[60:63], v[168:171]\n"
"v_add_f32 v252, v252, v253\n"
"v_max3_f32 v157, v157, v114, v115\n"
"v_mfma_f32_16x16x32_bf16 v[176:179], v[140:143], v[92:95], v[176:179]\n"
"v_rcp_f32 v253, v252\n"
"v_mfma_f32_16x16x32_bf16 v[176:179], v[132:135], v[88:91], v[176:179]\n"
"ds_bpermute_b32 v254, v160, v253 offset:0\n"
"v_mfma_f32_16x16x32_bf16 v[176:179], v[124:127], v[84:87], v[176:179]\n"
"v_max3_f32 v159, v159, v116, v117\n"
"v_max3_f32 v157, v157, v118, v119\n"
"v_mfma_f32_16x16x32_bf16 v[164:167], v[140:143], v[56:59], v[164:167]\n"
"v_max_f32 v157, v157, v159\n"
"v_mov_b32 v161, v157\n"
"v_mfma_f32_16x16x32_bf16 v[172:175], v[140:143], v[80:83], v[172:175]\n"
"v_lshlrev_b32 v222, 16, v223\n"
"v_and_b32 v223, 0xffff0000, v223\n"
"v_mfma_f32_16x16x32_bf16 v[148:151], a[228:231], v[188:191], 0\n"
"s_waitcnt lgkmcnt(1)\n"
"ds_read_b128 a[228:231], v158 offset:0\n"
"v_cvt_pk_bf16_f32 v204, v204, v208\n"
"v_mfma_f32_16x16x32_bf16 v[148:151], a[232:235], v[192:195], v[148:151]\n"
"ds_read_b128 a[232:235], v158 offset:2048\n"
"v_cvt_pk_bf16_f32 v206, v206, v210\n"
"v_mfma_f32_16x16x32_bf16 v[124:127], v[0:3], v[188:191], 0\n"
"v_permlane32_swap_b32 v157, v161\n"
"v_mfma_f32_16x16x32_bf16 v[124:127], v[4:7], v[192:195], v[124:127]\n"
"v_max_f32 v157, v157, v161\n"
"v_mov_b32 v161, v157\n"
"v_mfma_f32_16x16x32_bf16 v[132:135], v[16:19], v[188:191], 0\n"
"v_cvt_pk_bf16_f32 v208, v205, v209\n"
"v_cvt_pk_bf16_f32 v210, v207, v211\n"
"v_mfma_f32_16x16x32_bf16 v[132:135], v[20:23], v[192:195], v[132:135]\n"
"v_permlane16_swap_b32 v157, v161\n"
"v_mfma_f32_16x16x32_bf16 v[136:139], v[28:31], v[192:195], 0\n"
"v_max_f32 v157, v157, v161\n"
"v_sub_f32 v120, v120, v157\n"
"v_mfma_f32_16x16x32_bf16 v[136:139], v[24:27], v[188:191], v[136:139]\n"
"v_sub_f32 v121, v121, v157\n"
"v_sub_f32 v96, v96, v157\n"
"v_mfma_f32_16x16x32_bf16 v[140:143], v[32:35], v[188:191], 0\n"
"v_exp_f32 v120, v120\n"
"v_mfma_f32_16x16x32_bf16 v[140:143], v[36:39], v[192:195], v[140:143]\n"
"v_exp_f32 v121, v121\n"
"v_mfma_f32_16x16x32_bf16 v[144:147], v[44:47], v[192:195], 0\n"
"v_sub_f32 v97, v97, v157\n"
"v_sub_f32 v122, v122, v157\n"
"v_mfma_f32_16x16x32_bf16 v[144:147], v[40:43], v[188:191], v[144:147]\n"
"v_sub_f32 v123, v123, v157\n"
"v_exp_f32 v96, v96\n"
"v_sub_f32 v98, v98, v157\n"
"v_exp_f32 v97, v97\n"
"v_sub_f32 v99, v99, v157\n"
"v_exp_f32 v161, v122\n"
"v_exp_f32 v123, v123\n"
"v_exp_f32 v98, v98\n"
"v_exp_f32 v99, v99\n"
"v_sub_f32 v100, v100, v157\n"
"v_sub_f32 v101, v101, v157\n"
"v_sub_f32 v102, v102, v157\n"
"v_sub_f32 v103, v103, v157\n"
"v_exp_f32 v100, v100\n"
"v_exp_f32 v101, v101\n"
"v_exp_f32 v102, v102\n"
"v_exp_f32 v103, v103\n"
"v_cvt_pk_bf16_f32 v122, v120, v121\n"
"v_sub_f32 v104, v104, v157\n"
"v_sub_f32 v105, v105, v157\n"
"v_add_f32 v120, v120, v121\n"
"v_cvt_pk_bf16_f32 v96, v96, v97\n"
"v_exp_f32 v104, v104\n"
"v_sub_f32 v106, v106, v157\n"
"v_exp_f32 v105, v105\n"
"v_sub_f32 v107, v107, v157\n"
"v_add_f32 v121, v161, v123\n"
"v_cvt_pk_bf16_f32 v97, v98, v99\n"
"v_exp_f32 v106, v106\n"
"v_exp_f32 v107, v107\n"
"v_sub_f32 v108, v108, v157\n"
"v_sub_f32 v109, v109, v157\n"
"v_sub_f32 v110, v110, v157\n"
"v_sub_f32 v111, v111, v157\n"
"v_cvt_pk_bf16_f32 v123, v161, v123\n"
"v_cvt_pk_bf16_f32 v98, v100, v101\n"
"v_cvt_pk_bf16_f32 v99, v102, v103\n"
"v_mfma_f32_16x16x32_bf16 v[100:103], v[12:15], v[200:203], 0\n"
"v_exp_f32 v108, v108\n"
"v_mfma_f32_16x16x32_bf16 v[100:103], v[8:11], v[196:199], v[100:103]\n"
"v_exp_f32 v109, v109\n"
"v_exp_f32 v110, v110\n"
"v_exp_f32 v111, v111\n"
"v_sub_f32 v112, v112, v157\n"
"v_sub_f32 v113, v113, v157\n"
"v_cvt_pk_bf16_f32 v104, v104, v105\n"
"v_exp_f32 v112, v112\n"
"v_sub_f32 v114, v114, v157\n"
"v_exp_f32 v113, v113\n"
"v_sub_f32 v115, v115, v157\n"
"s_waitcnt lgkmcnt(0)\n"
"v_fmac_f32_dpp v220, v254, v164 quad_perm:[0,0,0,0] row_mask:0xf bank_mask:0xf\n"
"v_fmac_f32_dpp v221, v254, v168 quad_perm:[0,0,0,0] row_mask:0xf bank_mask:0xf\n"
"v_fmac_f32_dpp v222, v254, v172 quad_perm:[0,0,0,0] row_mask:0xf bank_mask:0xf\n"
"v_fmac_f32_dpp v223, v254, v176 quad_perm:[0,0,0,0] row_mask:0xf bank_mask:0xf\n"
"v_mfma_f32_16x16x32_bf16 v[252:255], a[252:255], v[96:99], v[120:123]\n"
"v_cvt_pk_bf16_f32 v105, v106, v107\n"
"v_exp_f32 v114, v114\n"
"v_exp_f32 v115, v115\n"
"v_sub_f32 v116, v116, v157\n"
"v_sub_f32 v117, v117, v157\n"
"v_sub_f32 v118, v118, v157\n"
"v_sub_f32 v119, v119, v157\n"
"v_cvt_pk_bf16_f32 v106, v108, v109\n"
"v_cvt_pk_bf16_f32 v107, v110, v111\n"
"v_mfma_f32_16x16x32_bf16 v[108:111], v[28:31], v[200:203], 0\n"
"v_exp_f32 v116, v116\n"
"v_mfma_f32_16x16x32_bf16 v[108:111], v[24:27], v[196:199], v[108:111]\n"
"v_exp_f32 v117, v117\n"
"v_mfma_f32_16x16x32_bf16 v[252:255], a[252:255], v[104:107], v[252:255]\n"
"v_exp_f32 v118, v118\n"
"v_exp_f32 v119, v119\n"
"v_cvt_pk_bf16_f32 v112, v112, v113\n"
"v_cvt_pk_bf16_f32 v113, v114, v115\n"
"v_cvt_pk_bf16_f32 v114, v116, v117\n"
"v_cvt_pk_bf16_f32 v115, v118, v119\n"
"v_mfma_f32_16x16x32_bf16 v[116:119], v[44:47], v[200:203], 0\n"
"v_cvt_pk_bf16_f32 v205, v212, v216\n"
"v_cvt_pk_bf16_f32 v209, v213, v217\n"
"v_mfma_f32_16x16x32_bf16 v[116:119], v[40:43], v[196:199], v[116:119]\n"
"v_cvt_pk_bf16_f32 v207, v214, v218\n"
"v_cvt_pk_bf16_f32 v211, v215, v219\n"
"v_mfma_f32_16x16x32_bf16 v[252:255], a[252:255], v[112:115], v[252:255]\n"
"v_pk_max_i16 v204, v204, 0\n"
"v_pk_max_i16 v205, v205, 0\n"
"v_pk_max_i16 v208, v208, 0\n"
"v_pk_max_i16 v209, v209, 0\n"
"v_pk_max_i16 v206, v206, 0\n"
"v_pk_max_i16 v207, v207, 0\n"
"v_pk_max_i16 v210, v210, 0\n"
"v_pk_max_i16 v211, v211, 0\n"
"v_max3_f32 v157, v148, v149, v150\n"
"v_max3_f32 v159, v151, v124, v125\n"
"v_max3_f32 v157, v157, v126, v127\n"
"v_add_f32 v252, v252, v253\n"
"v_rcp_f32 v253, v252\n"
"v_max3_f32 v159, v159, v128, v129\n"
"v_max3_f32 v157, v157, v130, v131\n"
"v_max3_f32 v159, v159, v132, v133\n"
"v_max3_f32 v157, v157, v134, v135\n"
"v_max3_f32 v159, v159, v136, v137\n"
"v_max3_f32 v157, v157, v138, v139\n"
"v_max3_f32 v159, v159, v140, v141\n"
"v_max3_f32 v157, v157, v142, v143\n"
"v_max3_f32 v159, v159, v144, v145\n"
"v_max3_f32 v157, v157, v146, v147\n"
"v_max_f32 v157, v157, v159\n"
"v_mov_b32 v161, v157\n"
"v_cvt_pk_bf16_f32 v220, v220, v221\n"
"v_cvt_pk_bf16_f32 v221, v222, v223\n"
"v_pk_max_i16 v220, v220, 0\n"
"v_permlane32_swap_b32 v157, v161\n"
"v_max_f32 v157, v157, v161\n"
"v_mov_b32 v161, v157\n"
"v_pk_max_i16 v221, v221, 0\n"
"v_add_u32 v158, 64, v158\n"
"ds_bpermute_b32 v254, v160, v253 offset:0\n"
"buffer_store_dwordx2 v[204:205], v162, s[84:87], s66 offen offset:0 sc0 sc1\n"
"v_permlane16_swap_b32 v157, v161\n"
"v_max_f32 v157, v157, v161\n"
"v_sub_f32 v148, v148, v157\n"
"v_sub_f32 v149, v149, v157\n"
"v_exp_f32 v148, v148\n"
"v_exp_f32 v149, v149\n"
"v_sub_f32 v124, v124, v157\n"
"v_sub_f32 v125, v125, v157\n"
"v_sub_f32 v150, v150, v157\n"
"v_sub_f32 v151, v151, v157\n"
"v_exp_f32 v124, v124\n"
"v_sub_f32 v126, v126, v157\n"
"v_exp_f32 v125, v125\n"
"v_sub_f32 v127, v127, v157\n"
"v_exp_f32 v161, v150\n"
"v_exp_f32 v151, v151\n"
"v_exp_f32 v126, v126\n"
"v_exp_f32 v127, v127\n"
"v_sub_f32 v128, v128, v157\n"
"v_sub_f32 v129, v129, v157\n"
"v_sub_f32 v130, v130, v157\n"
"v_sub_f32 v131, v131, v157\n"
"v_exp_f32 v128, v128\n"
"v_exp_f32 v129, v129\n"
"v_exp_f32 v130, v130\n"
"v_exp_f32 v131, v131\n"
"v_cvt_pk_bf16_f32 v150, v148, v149\n"
"v_sub_f32 v132, v132, v157\n"
"v_sub_f32 v133, v133, v157\n"
"v_add_f32 v148, v148, v149\n"
"v_cvt_pk_bf16_f32 v124, v124, v125\n"
"v_exp_f32 v132, v132\n"
"v_sub_f32 v134, v134, v157\n"
"v_exp_f32 v133, v133\n"
"v_sub_f32 v135, v135, v157\n"
"v_add_f32 v149, v161, v151\n"
"v_cvt_pk_bf16_f32 v125, v126, v127\n"
"v_exp_f32 v134, v134\n"
"v_exp_f32 v135, v135\n"
"v_sub_f32 v136, v136, v157\n"
"v_sub_f32 v137, v137, v157\n"
"v_sub_f32 v138, v138, v157\n"
"v_sub_f32 v139, v139, v157\n"
"v_cvt_pk_bf16_f32 v151, v161, v151\n"
"v_cvt_pk_bf16_f32 v126, v128, v129\n"
"v_cvt_pk_bf16_f32 v127, v130, v131\n"
"v_exp_f32 v136, v136\n"
"v_exp_f32 v137, v137\n"
"v_exp_f32 v138, v138\n"
"v_exp_f32 v139, v139\n"
"v_sub_f32 v140, v140, v157\n"
"v_sub_f32 v141, v141, v157\n"
"v_cvt_pk_bf16_f32 v132, v132, v133\n"
"v_exp_f32 v140, v140\n"
"v_sub_f32 v142, v142, v157\n"
"v_exp_f32 v141, v141\n"
"v_sub_f32 v143, v143, v157\n"
"v_cvt_pk_bf16_f32 v133, v134, v135\n"
"v_exp_f32 v142, v142\n"
"v_exp_f32 v143, v143\n"
"v_sub_f32 v144, v144, v157\n"
"v_sub_f32 v145, v145, v157\n"
"v_sub_f32 v146, v146, v157\n"
"v_sub_f32 v147, v147, v157\n"
"v_cvt_pk_bf16_f32 v134, v136, v137\n"
"v_cvt_pk_bf16_f32 v135, v138, v139\n"
"v_exp_f32 v144, v144\n"
"v_exp_f32 v145, v145\n"
"v_exp_f32 v146, v146\n"
"v_exp_f32 v147, v147\n"
"v_cvt_pk_bf16_f32 v140, v140, v141\n"
"v_cvt_pk_bf16_f32 v141, v142, v143\n"
"v_cvt_pk_bf16_f32 v142, v144, v145\n"
"v_cvt_pk_bf16_f32 v143, v146, v147\n"
"buffer_store_dwordx2 v[208:209], v162, s[84:87], s66 offen offset:256 sc0 sc1\n"
"buffer_store_dwordx2 v[206:207], v162, s[84:87], s66 offen offset:512 sc0 sc1\n"
"buffer_store_dwordx2 v[210:211], v162, s[84:87], s66 offen offset:768 sc0 sc1\n"
"s_nop 1\n"
"v_mfma_f32_16x16x4_4b_bf16 v[204:219], v[122:123], a[244:245], 0 cbsz:2 abid:0\n"
"buffer_store_dwordx2 v[220:221], v155, s[84:87], s70 offen offset:0 sc0 sc1\n"
"v_mfma_f32_16x16x32_bf16 v[120:123], a[228:231], v[196:199], 0\n"
"v_mfma_f32_16x16x32_bf16 v[120:123], a[232:235], v[200:203], v[120:123]\n"
"v_mfma_f32_16x16x32_bf16 v[204:207], v[96:99], v[48:51], v[204:207]\n"
"v_mfma_f32_16x16x32_bf16 v[204:207], v[104:107], v[52:55], v[204:207]\n"
"v_mfma_f32_16x16x32_bf16 v[204:207], v[112:115], v[56:59], v[204:207]\n"
"v_max3_f32 v157, v120, v121, v122\n"
"v_mfma_f32_16x16x32_bf16 v[208:211], v[112:115], v[68:71], v[208:211]\n"
"v_mfma_f32_16x16x32_bf16 v[208:211], v[104:107], v[64:67], v[208:211]\n"
"v_mfma_f32_16x16x32_bf16 v[208:211], v[96:99], v[60:63], v[208:211]\n"
"s_waitcnt lgkmcnt(0)\n"
"v_mul_f32_dpp v204, v254, v204 quad_perm:[0,0,0,0] row_mask:0xf bank_mask:0xf\n"
"v_mul_f32_dpp v205, v254, v205 quad_perm:[1,1,1,1] row_mask:0xf bank_mask:0xf\n"
"v_mfma_f32_16x16x32_bf16 v[212:215], v[96:99], v[72:75], v[212:215]\n"
"v_mul_f32_dpp v206, v254, v206 quad_perm:[2,2,2,2] row_mask:0xf bank_mask:0xf\n"
"v_mul_f32_dpp v207, v254, v207 quad_perm:[3,3,3,3] row_mask:0xf bank_mask:0xf\n"
"v_mfma_f32_16x16x32_bf16 v[212:215], v[104:107], v[76:79], v[212:215]\n"
"v_mfma_f32_16x16x32_bf16 v[212:215], v[112:115], v[80:83], v[212:215]\n"
"v_mul_f32_dpp v208, v254, v208 quad_perm:[0,0,0,0] row_mask:0xf bank_mask:0xf\n"
"v_mul_f32_dpp v209, v254, v209 quad_perm:[1,1,1,1] row_mask:0xf bank_mask:0xf\n"
"v_mfma_f32_16x16x32_bf16 v[216:219], v[112:115], v[92:95], v[216:219]\n"
"v_mul_f32_dpp v210, v254, v210 quad_perm:[2,2,2,2] row_mask:0xf bank_mask:0xf\n"
"v_mul_f32_dpp v211, v254, v211 quad_perm:[3,3,3,3] row_mask:0xf bank_mask:0xf\n"
"v_mfma_f32_16x16x32_bf16 v[216:219], v[104:107], v[88:91], v[216:219]\n"
"v_mfma_f32_16x16x32_bf16 v[216:219], v[96:99], v[84:87], v[216:219]\n"
"v_mul_f32_dpp v212, v254, v212 quad_perm:[0,0,0,0] row_mask:0xf bank_mask:0xf\n"
"v_mul_f32_dpp v213, v254, v213 quad_perm:[1,1,1,1] row_mask:0xf bank_mask:0xf\n"
"v_mfma_f32_16x16x32_bf16 v[96:99], v[0:3], v[196:199], 0\n"
"v_mul_f32_dpp v214, v254, v214 quad_perm:[2,2,2,2] row_mask:0xf bank_mask:0xf\n"
"v_mul_f32_dpp v215, v254, v215 quad_perm:[3,3,3,3] row_mask:0xf bank_mask:0xf\n"
"v_mfma_f32_16x16x32_bf16 v[96:99], v[4:7], v[200:203], v[96:99]\n"
"v_mfma_f32_16x16x32_bf16 v[104:107], v[16:19], v[196:199], 0\n"
"v_mul_f32_dpp v216, v254, v216 quad_perm:[0,0,0,0] row_mask:0xf bank_mask:0xf\n"
"v_mul_f32_dpp v217, v254, v217 quad_perm:[1,1,1,1] row_mask:0xf bank_mask:0xf\n"
"v_mfma_f32_16x16x32_bf16 v[104:107], v[20:23], v[200:203], v[104:107]\n"
"v_mul_f32_dpp v218, v254, v218 quad_perm:[2,2,2,2] row_mask:0xf bank_mask:0xf\n"
"v_mul_f32_dpp v219, v254, v219 quad_perm:[3,3,3,3] row_mask:0xf bank_mask:0xf\n"
"v_mfma_f32_16x16x32_bf16 v[252:255], a[252:255], v[124:127], v[148:151]\n"
"v_max3_f32 v159, v123, v96, v97\n"
"v_max3_f32 v157, v157, v98, v99\n"
"v_mfma_f32_16x16x32_bf16 v[252:255], a[252:255], v[132:135], v[252:255]\n"
"v_max3_f32 v159, v159, v100, v101\n"
"v_max3_f32 v157, v157, v102, v103\n"
"v_mfma_f32_16x16x32_bf16 v[252:255], a[252:255], v[140:143], v[252:255]\n"
"v_max3_f32 v159, v159, v104, v105\n"
"v_max3_f32 v157, v157, v106, v107\n"
"v_mfma_f32_16x16x32_bf16 v[112:115], v[32:35], v[196:199], 0\n"
"v_max3_f32 v159, v159, v108, v109\n"
"v_max3_f32 v157, v157, v110, v111\n"
"v_mfma_f32_16x16x32_bf16 v[112:115], v[36:39], v[200:203], v[112:115]\n"
"v_mfma_f32_16x16x4_4b_bf16 v[220:235], v[150:151], a[244:245], 0 cbsz:2 abid:0\n"
"v_add_f32 v252, v252, v253\n"
"v_rcp_f32 v253, v252\n"
"s_nop 0\n"
"ds_bpermute_b32 v254, v160, v253 offset:0\n"
"v_mfma_f32_16x16x32_bf16 v[204:207], a[48:51], a[160:163], v[204:207]\n"
"v_max3_f32 v159, v159, v112, v113\n"
"v_max3_f32 v157, v157, v114, v115\n"
"v_mfma_f32_16x16x32_bf16 v[204:207], a[52:55], a[164:167], v[204:207]\n"
"v_max3_f32 v159, v159, v116, v117\n"
"v_max3_f32 v157, v157, v118, v119\n"
"v_mfma_f32_16x16x32_bf16 v[204:207], a[56:59], a[168:171], v[204:207]\n"
"v_max_f32 v157, v157, v159\n"
"v_mov_b32 v161, v157\n"
"v_mfma_f32_16x16x32_bf16 v[204:207], a[60:63], a[172:175], v[204:207]\n"
"s_nop 0\n"
"v_permlane32_swap_b32 v157, v161\n"
"v_mfma_f32_16x16x32_bf16 v[220:223], v[124:127], v[48:51], v[220:223]\n"
"v_max_f32 v157, v157, v161\n"
"v_mov_b32 v161, v157\n"
"v_mfma_f32_16x16x32_bf16 v[220:223], v[132:135], v[52:55], v[220:223]\n"
"v_mfma_f32_16x16x32_bf16 v[220:223], v[140:143], v[56:59], v[220:223]\n"
"v_permlane16_swap_b32 v157, v161\n"
"v_mfma_f32_16x16x32_bf16 v[224:227], v[140:143], v[68:71], v[224:227]\n"
"v_max_f32 v157, v157, v161\n"
"v_sub_f32 v120, v120, v157\n"
"v_mfma_f32_16x16x32_bf16 v[224:227], v[132:135], v[64:67], v[224:227]\n"
"v_sub_f32 v121, v121, v157\n"
"v_sub_f32 v96, v96, v157\n"
"v_mfma_f32_16x16x32_bf16 v[224:227], v[124:127], v[60:63], v[224:227]\n"
"v_exp_f32 v120, v120\n"
"v_mfma_f32_16x16x32_bf16 v[228:231], v[124:127], v[72:75], v[228:231]\n"
"v_exp_f32 v121, v121\n"
"v_mfma_f32_16x16x32_bf16 v[228:231], v[132:135], v[76:79], v[228:231]\n"
"v_sub_f32 v97, v97, v157\n"
"v_sub_f32 v122, v122, v157\n"
"v_mfma_f32_16x16x32_bf16 v[228:231], v[140:143], v[80:83], v[228:231]\n"
"v_sub_f32 v123, v123, v157\n"
"v_sub_f32 v98, v98, v157\n"
"v_mfma_f32_16x16x32_bf16 v[232:235], v[140:143], v[92:95], v[232:235]\n"
"v_exp_f32 v96, v96\n"
"v_mfma_f32_16x16x32_bf16 v[232:235], v[132:135], v[88:91], v[232:235]\n"
"v_exp_f32 v97, v97\n"
"v_mfma_f32_16x16x32_bf16 v[232:235], v[124:127], v[84:87], v[232:235]\n"
"v_sub_f32 v99, v99, v157\n"
"v_sub_f32 v100, v100, v157\n"
"v_mfma_f32_16x16x32_bf16 v[208:211], a[60:63], a[188:191], v[208:211]\n"
"v_exp_f32 v161, v122\n"
"v_mfma_f32_16x16x32_bf16 v[208:211], a[56:59], a[184:187], v[208:211]\n"
"v_exp_f32 v123, v123\n"
"v_mfma_f32_16x16x32_bf16 v[208:211], a[52:55], a[180:183], v[208:211]\n"
"v_exp_f32 v98, v98\n"
"v_mfma_f32_16x16x32_bf16 v[208:211], a[48:51], a[176:179], v[208:211]\n"
"v_exp_f32 v99, v99\n"
"v_mfma_f32_16x16x32_bf16 v[212:215], a[48:51], a[192:195], v[212:215]\n"
"v_sub_f32 v101, v101, v157\n"
"v_sub_f32 v102, v102, v157\n"
"v_mfma_f32_16x16x32_bf16 v[212:215], a[52:55], a[196:199], v[212:215]\n"
"v_sub_f32 v103, v103, v157\n"
"v_cvt_pk_bf16_f32 v122, v120, v121\n"
"v_mfma_f32_16x16x32_bf16 v[212:215], a[56:59], a[200:203], v[212:215]\n"
"v_exp_f32 v100, v100\n"
"v_mfma_f32_16x16x32_bf16 v[212:215], a[60:63], a[204:207], v[212:215]\n"
"v_exp_f32 v101, v101\n"
"v_mfma_f32_16x16x32_bf16 v[216:219], a[60:63], a[220:223], v[216:219]\n"
"v_exp_f32 v102, v102\n"
"v_mfma_f32_16x16x32_bf16 v[216:219], a[56:59], a[216:219], v[216:219]\n"
"v_exp_f32 v103, v103\n"
"v_mfma_f32_16x16x32_bf16 v[216:219], a[52:55], a[212:215], v[216:219]\n"
"v_sub_f32 v104, v104, v157\n"
"v_sub_f32 v105, v105, v157\n"
"v_mfma_f32_16x16x32_bf16 v[216:219], a[48:51], a[208:211], v[216:219]\n"
"v_add_f32 v120, v120, v121\n"
"v_cvt_pk_bf16_f32 v96, v96, v97\n"
"v_exp_f32 v104, v104\n"
"v_sub_f32 v106, v106, v157\n"
"v_exp_f32 v105, v105\n"
"v_sub_f32 v107, v107, v157\n"
"v_add_f32 v121, v161, v123\n"
"v_cvt_pk_bf16_f32 v97, v98, v99\n"
"v_exp_f32 v106, v106\n"
"v_exp_f32 v107, v107\n"
"v_sub_f32 v108, v108, v157\n"
"v_sub_f32 v109, v109, v157\n"
"v_sub_f32 v110, v110, v157\n"
"v_sub_f32 v111, v111, v157\n"
"v_cvt_pk_bf16_f32 v123, v161, v123\n"
"v_cvt_pk_bf16_f32 v98, v100, v101\n"
"v_cvt_pk_bf16_f32 v99, v102, v103\n"
"v_exp_f32 v108, v108\n"
"v_exp_f32 v109, v109\n"
"v_exp_f32 v110, v110\n"
"v_exp_f32 v111, v111\n"
"v_sub_f32 v112, v112, v157\n"
"v_sub_f32 v113, v113, v157\n"
"v_cvt_pk_bf16_f32 v104, v104, v105\n"
"v_exp_f32 v112, v112\n"
"v_sub_f32 v114, v114, v157\n"
"v_exp_f32 v113, v113\n"
"v_sub_f32 v115, v115, v157\n"
"v_cvt_pk_bf16_f32 v105, v106, v107\n"
"s_waitcnt lgkmcnt(0)\n"
"v_mul_f32_dpp v220, v254, v220 quad_perm:[0,0,0,0] row_mask:0xf bank_mask:0xf\n"
"v_mul_f32_dpp v221, v254, v221 quad_perm:[1,1,1,1] row_mask:0xf bank_mask:0xf\n"
"v_mul_f32_dpp v222, v254, v222 quad_perm:[2,2,2,2] row_mask:0xf bank_mask:0xf\n"
"v_mul_f32_dpp v223, v254, v223 quad_perm:[3,3,3,3] row_mask:0xf bank_mask:0xf\n"
"v_mul_f32_dpp v224, v254, v224 quad_perm:[0,0,0,0] row_mask:0xf bank_mask:0xf\n"
"v_mul_f32_dpp v225, v254, v225 quad_perm:[1,1,1,1] row_mask:0xf bank_mask:0xf\n"
"v_mul_f32_dpp v226, v254, v226 quad_perm:[2,2,2,2] row_mask:0xf bank_mask:0xf\n"
"v_mfma_f32_16x16x32_bf16 v[220:223], a[64:67], a[160:163], v[220:223]\n"
"v_mul_f32_dpp v227, v254, v227 quad_perm:[3,3,3,3] row_mask:0xf bank_mask:0xf\n"
"v_mul_f32_dpp v228, v254, v228 quad_perm:[0,0,0,0] row_mask:0xf bank_mask:0xf\n"
"v_mfma_f32_16x16x32_bf16 v[220:223], a[68:71], a[164:167], v[220:223]\n"
"v_mul_f32_dpp v229, v254, v229 quad_perm:[1,1,1,1] row_mask:0xf bank_mask:0xf\n"
"v_mul_f32_dpp v230, v254, v230 quad_perm:[2,2,2,2] row_mask:0xf bank_mask:0xf\n"
"v_mfma_f32_16x16x32_bf16 v[220:223], a[72:75], a[168:171], v[220:223]\n"
"v_mul_f32_dpp v231, v254, v231 quad_perm:[3,3,3,3] row_mask:0xf bank_mask:0xf\n"
"v_mul_f32_dpp v232, v254, v232 quad_perm:[0,0,0,0] row_mask:0xf bank_mask:0xf\n"
"v_mfma_f32_16x16x32_bf16 v[220:223], a[76:79], a[172:175], v[220:223]\n"
"v_mul_f32_dpp v233, v254, v233 quad_perm:[1,1,1,1] row_mask:0xf bank_mask:0xf\n"
"v_mul_f32_dpp v234, v254, v234 quad_perm:[2,2,2,2] row_mask:0xf bank_mask:0xf\n"
"v_mfma_f32_16x16x32_bf16 v[224:227], a[76:79], a[188:191], v[224:227]\n"
"v_mul_f32_dpp v235, v254, v235 quad_perm:[3,3,3,3] row_mask:0xf bank_mask:0xf\n"
"v_sub_f32 v116, v116, v157\n"
"v_mfma_f32_16x16x32_bf16 v[224:227], a[72:75], a[184:187], v[224:227]\n"
"v_exp_f32 v114, v114\n"
"v_mfma_f32_16x16x32_bf16 v[224:227], a[68:71], a[180:183], v[224:227]\n"
"v_exp_f32 v115, v115\n"
"v_mfma_f32_16x16x32_bf16 v[224:227], a[64:67], a[176:179], v[224:227]\n"
"v_sub_f32 v117, v117, v157\n"
"v_sub_f32 v118, v118, v157\n"
"v_mfma_f32_16x16x32_bf16 v[252:255], a[252:255], v[96:99], v[120:123]\n"
"v_sub_f32 v119, v119, v157\n"
"v_cvt_pk_bf16_f32 v106, v108, v109\n"
"v_mfma_f32_16x16x32_bf16 v[228:231], a[64:67], a[192:195], v[228:231]\n"
"v_cvt_pk_bf16_f32 v107, v110, v111\n"
"v_cvt_pk_bf16_f32 v112, v112, v113\n"
"v_mfma_f32_16x16x32_bf16 v[228:231], a[68:71], a[196:199], v[228:231]\n"
"v_exp_f32 v116, v116\n"
"v_mfma_f32_16x16x32_bf16 v[228:231], a[72:75], a[200:203], v[228:231]\n"
"v_exp_f32 v117, v117\n"
"v_mfma_f32_16x16x32_bf16 v[228:231], a[76:79], a[204:207], v[228:231]\n"
"v_exp_f32 v118, v118\n"
"v_mfma_f32_16x16x32_bf16 v[252:255], a[252:255], v[104:107], v[252:255]\n"
"v_exp_f32 v119, v119\n"
"v_mfma_f32_16x16x32_bf16 v[232:235], a[76:79], a[220:223], v[232:235]\n"
"v_cvt_pk_bf16_f32 v113, v114, v115\n"
"v_cvt_pk_bf16_f32 v114, v116, v117\n"
"v_mfma_f32_16x16x32_bf16 v[232:235], a[72:75], a[216:219], v[232:235]\n"
"v_cvt_pk_bf16_f32 v115, v118, v119\n"
"v_cvt_pk_bf16_f32 v204, v204, v208\n"
"v_mfma_f32_16x16x32_bf16 v[232:235], a[68:71], a[212:215], v[232:235]\n"
"v_cvt_pk_bf16_f32 v206, v206, v210\n"
"v_cvt_pk_bf16_f32 v208, v205, v209\n"
"v_mfma_f32_16x16x32_bf16 v[232:235], a[64:67], a[208:211], v[232:235]\n"
"v_cvt_pk_bf16_f32 v210, v207, v211\n"
"v_cvt_pk_bf16_f32 v205, v212, v216\n"
"v_mfma_f32_16x16x32_bf16 v[252:255], a[252:255], v[112:115], v[252:255]\n"
"v_cvt_pk_bf16_f32 v209, v213, v217\n"
"v_cvt_pk_bf16_f32 v207, v214, v218\n"
"v_cvt_pk_bf16_f32 v211, v215, v219\n"
"v_pk_max_i16 v204, v204, 0\n"
"v_pk_max_i16 v205, v205, 0\n"
"v_pk_max_i16 v208, v208, 0\n"
"v_pk_max_i16 v209, v209, 0\n"
"v_pk_max_i16 v206, v206, 0\n"
"v_pk_max_i16 v207, v207, 0\n"
"v_pk_max_i16 v210, v210, 0\n"
"v_pk_max_i16 v211, v211, 0\n"
"v_add_f32 v252, v252, v253\n"
"v_rcp_f32 v253, v252\n"
"v_cvt_pk_bf16_f32 v220, v220, v224\n"
"v_cvt_pk_bf16_f32 v222, v222, v226\n"
"v_cvt_pk_bf16_f32 v224, v221, v225\n"
"v_cvt_pk_bf16_f32 v226, v223, v227\n"
"v_cvt_pk_bf16_f32 v221, v228, v232\n"
"v_cvt_pk_bf16_f32 v225, v229, v233\n"
"v_cvt_pk_bf16_f32 v223, v230, v234\n"
"v_cvt_pk_bf16_f32 v227, v231, v235\n"
"v_pk_max_i16 v220, v220, 0\n"
"v_pk_max_i16 v221, v221, 0\n"
"v_pk_max_i16 v224, v224, 0\n"
"v_pk_max_i16 v225, v225, 0\n"
"v_pk_max_i16 v222, v222, 0\n"
"v_pk_max_i16 v223, v223, 0\n"
"v_pk_max_i16 v226, v226, 0\n"
"v_pk_max_i16 v227, v227, 0\n"
"ds_bpermute_b32 v254, v160, v253 offset:0\n"
"buffer_store_dwordx2 v[204:205], v162, s[84:87], s67 offen offset:0 sc0 sc1\n"
"buffer_store_dwordx2 v[208:209], v162, s[84:87], s67 offen offset:256 sc0 sc1\n"
"buffer_store_dwordx2 v[206:207], v162, s[84:87], s67 offen offset:512 sc0 sc1\n"
"buffer_store_dwordx2 v[210:211], v162, s[84:87], s67 offen offset:768 sc0 sc1\n"
"s_nop 1\n"
"v_mfma_f32_16x16x4_4b_bf16 v[204:219], v[122:123], a[244:245], 0 cbsz:2 abid:0\n"
"buffer_store_dwordx2 v[220:221], v162, s[84:87], s68 offen offset:0 sc0 sc1\n"
"buffer_store_dwordx2 v[224:225], v162, s[84:87], s68 offen offset:256 sc0 sc1\n"
"s_nop 5\n"
"v_mfma_f32_16x16x32_bf16 v[204:207], v[96:99], v[48:51], v[204:207]\n"
"s_nop 1\n"
"v_mfma_f32_16x16x32_bf16 v[204:207], v[104:107], v[52:55], v[204:207]\n"
"v_mfma_f32_16x16x32_bf16 v[204:207], v[112:115], v[56:59], v[204:207]\n"
"buffer_store_dwordx2 v[222:223], v162, s[84:87], s68 offen offset:512 sc0 sc1\n"
"v_mfma_f32_16x16x32_bf16 v[208:211], v[112:115], v[68:71], v[208:211]\n"
"v_mfma_f32_16x16x32_bf16 v[208:211], v[104:107], v[64:67], v[208:211]\n"
"v_mfma_f32_16x16x32_bf16 v[208:211], v[96:99], v[60:63], v[208:211]\n"
"buffer_store_dwordx2 v[226:227], v162, s[84:87], s68 offen offset:768 sc0 sc1\n"
"v_mfma_f32_16x16x32_bf16 v[212:215], v[96:99], v[72:75], v[212:215]\n"
"s_waitcnt lgkmcnt(0)\n"
"v_mul_f32_dpp v204, v254, v204 quad_perm:[0,0,0,0] row_mask:0xf bank_mask:0xf\n"
"v_mul_f32_dpp v205, v254, v205 quad_perm:[1,1,1,1] row_mask:0xf bank_mask:0xf\n"
"v_mfma_f32_16x16x32_bf16 v[212:215], v[104:107], v[76:79], v[212:215]\n"
"v_mul_f32_dpp v206, v254, v206 quad_perm:[2,2,2,2] row_mask:0xf bank_mask:0xf\n"
"v_mul_f32_dpp v207, v254, v207 quad_perm:[3,3,3,3] row_mask:0xf bank_mask:0xf\n"
"v_mfma_f32_16x16x32_bf16 v[212:215], v[112:115], v[80:83], v[212:215]\n"
"v_mul_f32_dpp v208, v254, v208 quad_perm:[0,0,0,0] row_mask:0xf bank_mask:0xf\n"
"v_mul_f32_dpp v209, v254, v209 quad_perm:[1,1,1,1] row_mask:0xf bank_mask:0xf\n"
"v_mfma_f32_16x16x32_bf16 v[216:219], v[112:115], v[92:95], v[216:219]\n"
"v_mul_f32_dpp v210, v254, v210 quad_perm:[2,2,2,2] row_mask:0xf bank_mask:0xf\n"
"v_mul_f32_dpp v211, v254, v211 quad_perm:[3,3,3,3] row_mask:0xf bank_mask:0xf\n"
"v_mfma_f32_16x16x32_bf16 v[216:219], v[104:107], v[88:91], v[216:219]\n"
"v_mfma_f32_16x16x32_bf16 v[216:219], v[96:99], v[84:87], v[216:219]\n"
"v_mul_f32_dpp v212, v254, v212 quad_perm:[0,0,0,0] row_mask:0xf bank_mask:0xf\n"
"v_mul_f32_dpp v213, v254, v213 quad_perm:[1,1,1,1] row_mask:0xf bank_mask:0xf\n"
"v_mfma_f32_16x16x32_bf16 v[204:207], a[80:83], a[160:163], v[204:207]\n"
"v_mul_f32_dpp v214, v254, v214 quad_perm:[2,2,2,2] row_mask:0xf bank_mask:0xf\n"
"v_mul_f32_dpp v215, v254, v215 quad_perm:[3,3,3,3] row_mask:0xf bank_mask:0xf\n"
"v_mfma_f32_16x16x32_bf16 v[204:207], a[84:87], a[164:167], v[204:207]\n"
"v_mfma_f32_16x16x32_bf16 v[204:207], a[88:91], a[168:171], v[204:207]\n"
"v_mul_f32_dpp v216, v254, v216 quad_perm:[0,0,0,0] row_mask:0xf bank_mask:0xf\n"
"v_mul_f32_dpp v217, v254, v217 quad_perm:[1,1,1,1] row_mask:0xf bank_mask:0xf\n"
"v_mfma_f32_16x16x32_bf16 v[204:207], a[92:95], a[172:175], v[204:207]\n"
"v_mul_f32_dpp v218, v254, v218 quad_perm:[2,2,2,2] row_mask:0xf bank_mask:0xf\n"
"v_mul_f32_dpp v219, v254, v219 quad_perm:[3,3,3,3] row_mask:0xf bank_mask:0xf\n"
"v_mfma_f32_16x16x32_bf16 v[208:211], a[92:95], a[188:191], v[208:211]\n"
"v_mfma_f32_16x16x32_bf16 v[208:211], a[88:91], a[184:187], v[208:211]\n"
"v_mfma_f32_16x16x32_bf16 v[208:211], a[84:87], a[180:183], v[208:211]\n"
"v_mfma_f32_16x16x32_bf16 v[208:211], a[80:83], a[176:179], v[208:211]\n"
"v_mfma_f32_16x16x32_bf16 v[212:215], a[80:83], a[192:195], v[212:215]\n"
"v_mfma_f32_16x16x32_bf16 v[212:215], a[84:87], a[196:199], v[212:215]\n"
"v_mfma_f32_16x16x32_bf16 v[212:215], a[88:91], a[200:203], v[212:215]\n"
"v_cvt_pk_bf16_f32 v204, v204, v208\n"
"v_cvt_pk_bf16_f32 v206, v206, v210\n"
"v_mfma_f32_16x16x32_bf16 v[212:215], a[92:95], a[204:207], v[212:215]\n"
"v_cvt_pk_bf16_f32 v208, v205, v209\n"
"v_cvt_pk_bf16_f32 v210, v207, v211\n"
"v_mfma_f32_16x16x32_bf16 v[216:219], a[92:95], a[220:223], v[216:219]\n"
"v_pk_max_i16 v204, v204, 0\n"
"v_pk_max_i16 v208, v208, 0\n"
"v_mfma_f32_16x16x32_bf16 v[216:219], a[88:91], a[216:219], v[216:219]\n"
"v_pk_max_i16 v206, v206, 0\n"
"v_pk_max_i16 v210, v210, 0\n"
"v_mfma_f32_16x16x32_bf16 v[216:219], a[84:87], a[212:215], v[216:219]\n"
"v_mfma_f32_16x16x32_bf16 v[216:219], a[80:83], a[208:211], v[216:219]\n"
"s_nop 7\n"
"v_cvt_pk_bf16_f32 v205, v212, v216\n"
"v_cvt_pk_bf16_f32 v209, v213, v217\n"
"v_cvt_pk_bf16_f32 v207, v214, v218\n"
"v_cvt_pk_bf16_f32 v211, v215, v219\n"
"v_pk_max_i16 v205, v205, 0\n"
"v_pk_max_i16 v209, v209, 0\n"
"v_pk_max_i16 v207, v207, 0\n"
"v_pk_max_i16 v211, v211, 0\n"
"buffer_store_dwordx2 v[204:205], v162, s[84:87], s69 offen offset:0 sc0 sc1\n"
"buffer_store_dwordx2 v[208:209], v162, s[84:87], s69 offen offset:256 sc0 sc1\n"
"buffer_store_dwordx2 v[206:207], v162, s[84:87], s69 offen offset:512 sc0 sc1\n"
"buffer_store_dwordx2 v[210:211], v162, s[84:87], s69 offen offset:768 sc0 sc1\n"
".Lend%=:\n"
    :
    : [wave] "s"(wave), [lds] "s"(lds), [ka] "s"(ka), [bid] "s"(bid)
    : "v0","v1","v2","v3","v4","v5","v6","v7","v8","v9","v10","v11","v12","v13","v14","v15","v16","v17","v18","v19","v20","v21","v22","v23","v24","v25","v26","v27","v28","v29","v30","v31","v32","v33","v34","v35","v36","v37","v38","v39","v40","v41","v42","v43","v44","v45","v46","v47","v48","v49","v50","v51","v52","v53","v54","v55","v56","v57","v58","v59","v60","v61","v62","v63","v64","v65","v66","v67","v68","v69","v70","v71","v72","v73","v74","v75","v76","v77","v78","v79","v80","v81","v82","v83","v84","v85","v86","v87","v88","v89","v90","v91","v92","v93","v94","v95","v96","v97","v98","v99","v100","v101","v102","v103","v104","v105","v106","v107","v108","v109","v110","v111","v112","v113","v114","v115","v116","v117","v118","v119","v120","v121","v122","v123","v124","v125","v126","v127","v128","v129","v130","v131","v132","v133","v134","v135","v136","v137","v138","v139","v140","v141","v142","v143","v144","v145","v146","v147","v148","v149","v150","v151","v152","v153","v154","v155","v156","v157","v158","v159","v160","v161","v162","v163","v164","v165","v166","v167","v168","v169","v170","v171","v172","v173","v174","v175","v176","v177","v178","v179","v180","v181","v182","v183","v184","v185","v186","v187","v188","v189","v190","v191","v192","v193","v194","v195","v196","v197","v198","v199","v200","v201","v202","v203","v204","v205","v206","v207","v208","v209","v210","v211","v212","v213","v214","v215","v216","v217","v218","v219","v220","v221","v222","v223","v224","v225","v226","v227","v228","v229","v230","v231","v232","v233","v234","v235","v236","v237","v238","v239","v240","v241","v242","v243","v244","v245","v246","v247","v248","v249","v250","v251","v252","v253","v254","v255","a0","a1","a2","a3","a4","a5","a6","a7","a8","a9","a10","a11","a12","a13","a14","a15","a16","a17","a18","a19","a20","a21","a22","a23","a24","a25","a26","a27","a28","a29","a30","a31","a32","a33","a34","a35","a36","a37","a38","a39","a40","a41","a42","a43","a44","a45","a46","a47","a48","a49","a50","a51","a52","a53","a54","a55","a56","a57","a58","a59","a60","a61","a62","a63","a64","a65","a66","a67","a68","a69","a70","a71","a72","a73","a74","a75","a76","a77","a78","a79","a80","a81","a82","a83","a84","a85","a86","a87","a88","a89","a90","a91","a92","a93","a94","a95","a96","a97","a98","a99","a100","a101","a102","a103","a104","a105","a106","a107","a108","a109","a110","a111","a112","a113","a114","a115","a116","a117","a118","a119","a120","a121","a122","a123","a124","a125","a126","a127","a128","a129","a130","a131","a132","a133","a134","a135","a136","a137","a138","a139","a140","a141","a142","a143","a144","a145","a146","a147","a148","a149","a150","a151","a152","a153","a154","a155","a156","a157","a158","a159","a160","a161","a162","a163","a164","a165","a166","a167","a168","a169","a170","a171","a172","a173","a174","a175","a176","a177","a178","a179","a180","a181","a182","a183","a184","a185","a186","a187","a188","a189","a190","a191","a192","a193","a194","a195","a196","a197","a198","a199","a200","a201","a202","a203","a204","a205","a206","a207","a208","a209","a210","a211","a212","a213","a214","a215","a216","a217","a218","a219","a220","a221","a222","a223","a224","a225","a226","a227","a228","a229","a230","a231","a232","a233","a234","a235","a236","a237","a238","a239","a240","a241","a242","a243","a244","a245","a246","a247","a248","a249","a250","a251","a252","a253","a254","a255","s8","s9","s10","s11","s12","s13","s14","s15","s16","s17","s18","s19","s20","s21","s22","s23","s24","s25","s26","s27","s28","s29","s40","s41","s42","s43","s44","s45","s46","s47","s48","s49","s50","s51","s52","s53","s54","s55","s56","s57","s58","s59","s60","s61","s62","s63","s64","s65","s66","s67","s68","s69","s70","s71","s72","s73","s74","s75","s76","s77","s78","s79","s80","s81","s82","s83","s84","s85","s86","s87","s88","s89","s90","s91","s92","s93","s94","s95","scc","vcc","memory");
  STAMP_END();
}

extern "C" void kernel_launch(void* const* d_in, const int* in_sizes, int n_in, void* d_out, int out_size, void* d_ws,
                              size_t ws_size, hipStream_t stream) {
  autoint_r_kernel<<<256, 256, 0, stream>>>((const char*)d_in[0], (const char*)d_in[1], (const char*)d_in[2],
                                            (const char*)d_in[3], (const char*)d_in[4], (char*)d_out, (char*)d_ws);
}
```
